```python
import torch

def _softmax(x, axis=-1):
    return torch.softmax(x, dim=axis)
def _gelu(x):
    return torch.nn.functional.gelu(x, approximate="tanh")
def _silu(x):
    return torch.nn.functional.silu(x)
def _sigmoid(x):
    return torch.sigmoid(x)
def _layernorm(x, axis=-1, eps=1e-5):
    m = x.mean(dim=axis, keepdim=True)
    v = x.var(dim=axis, keepdim=True, unbiased=False)
    return (x - m) / torch.sqrt(v + eps)
def _scatter_add(tgt, idx, src, axis=0):
    return tgt.clone().index_add_(axis, idx, src)
def _segment_sum(src, seg, n):
    out = torch.zeros((n,) + src.shape[1:], dtype=src.dtype)
    return out.index_add_(0, seg, src)
def _pad(x, pw):
    flat = tuple(v for pair in reversed(pw) for v in pair)
    return torch.nn.functional.pad(x, flat)

kernel_name = 'sequential_preLN__softmax_full_mha__geglu_ffn__prompt'

def setup_inputs():

    _w = (lambda a: (lambda t: t.to(torch.bfloat16) if t.is_floating_point() else t)(torch.as_tensor(a)))
    def _A_setup():
        B, S, D = 1, 2048, 640
        return {
            "x": _w((lambda *s: torch.randn(*s, dtype=torch.bfloat16) * 0.05)(B, S, D)),
            "wqkv": _w((lambda *s: torch.randn(*s, dtype=torch.bfloat16) * 0.05)(D, 3 * D)),
            "wo": _w((lambda *s: torch.randn(*s, dtype=torch.bfloat16) * 0.05)(D, D)),
        }
    _A_in = _A_setup()

    def _B_setup():
        B, S, D, F = 1, 2048, 640, 1536
        return {
            "x": _w((lambda *s: torch.randn(*s, dtype=torch.bfloat16) * 0.05)(B, S, D)),
            "w_gate": _w((lambda *s: torch.randn(*s, dtype=torch.bfloat16) * 0.05)(D, F)),
            "w_up": _w((lambda *s: torch.randn(*s, dtype=torch.bfloat16) * 0.05)(D, F)),
            "w_down": _w((lambda *s: torch.randn(*s, dtype=torch.bfloat16) * 0.05)(F, D)),
        }
    _B_in = _B_setup()

    return {
        "A_x": _A_in["x"],
        "A_wqkv": _A_in["wqkv"],
        "A_wo": _A_in["wo"],
        "B_x": _B_in["x"],
        "B_w_gate": _B_in["w_gate"],
        "B_w_up": _B_in["w_up"],
        "B_w_down": _B_in["w_down"],
    }

def reference(A_x, A_wqkv, A_wo, B_x, B_w_gate, B_w_up, B_w_down):
    def _A_ref(x, wqkv, wo):
        B, S, D = x.shape
        H = 32; Dh = D // H
        qkv = (lambda x, *d: x.permute(*d))((x @ wqkv).reshape(B, S, 3, H, Dh), 2, 0, 3, 1, 4)
        q, k, v = qkv[0], qkv[1], qkv[2]
        scale = Dh ** -0.5
        scores = torch.einsum("bhqd,bhkd->bhqk", q, k) * scale
        p = _softmax(scores, axis=-1)
        o = torch.einsum("bhqk,bhkd->bhqd", p, v)
        return (lambda x, *d: x.permute(*d))(o, 0, 2, 1, 3).reshape(B, S, D) @ wo

    def _B_ref(x, w_gate, w_up, w_down):
        return (_gelu(x @ w_gate) * (x @ w_up)) @ w_down

    x = A_x
    h = x + _A_ref(_layernorm(x, axis=-1), A_wqkv, A_wo)
    h2 = h + _B_ref(_layernorm(h, axis=-1), B_w_gate, B_w_up, B_w_down)
    return h2
```

```cpp
#include <hip/hip_runtime.h>
#include <hip/hip_bf16.h>
#include <stdint.h>
typedef unsigned short u16;
static __device__ __forceinline__ float bf2f(u16 v){ return __uint_as_float(((unsigned)v)<<16); }
static __device__ __forceinline__ u16 f2bf(float f){ unsigned u=__float_as_uint(f); u += 0x7fff + ((u>>16)&1); return (u16)(u>>16); }
#define S_ 2048
#define D_ 640
#define F_ 1536
__global__ void n_ln(const u16* x, float* y){
  int r=blockIdx.x, t=threadIdx.x; float v[10]; float s=0;
  for(int i=0;i<10;i++){ v[i]=bf2f(x[r*D_+t+64*i]); s+=v[i]; }
  for(int o=32;o;o>>=1) s+=__shfl_xor(s,o);
  float m=s/D_; float q=0;
  for(int i=0;i<10;i++){ float d=v[i]-m; q+=d*d; }
  for(int o=32;o;o>>=1) q+=__shfl_xor(q,o);
  float rs=rsqrtf(q/D_+1e-5f);
  for(int i=0;i<10;i++) y[r*D_+t+64*i]=bf2f(f2bf((v[i]-m)*rs));
}
__global__ void n_gemm(const float* A, const u16* B, float* C, int K, int N){
  int n=blockIdx.x*64+threadIdx.x; int m0=blockIdx.y*8;
  float acc[8]={0,0,0,0,0,0,0,0};
  for(int k=0;k<K;k++){ float b=bf2f(B[k*N+n]);
    #pragma unroll
    for(int i=0;i<8;i++) acc[i]+=A[(m0+i)*K+k]*b; }
  #pragma unroll
  for(int i=0;i<8;i++) C[(m0+i)*N+n]=bf2f(f2bf(acc[i]));
}
__global__ void n_attn(const float* qkv, float* o){
  int h=blockIdx.y; int q=blockIdx.x*64+threadIdx.x;
  float qv[20], acc[20];
  #pragma unroll
  for(int d=0;d<20;d++){ qv[d]=qkv[q*1920+h*20+d]*0.22360679775f; acc[d]=0; }
  float m=-1e30f,l=0;
  for(int k=0;k<S_;k++){ float s=0;
    #pragma unroll
    for(int d=0;d<20;d++) s+=qv[d]*qkv[k*1920+640+h*20+d];
    float mn=fmaxf(m,s); float c=__expf(m-mn); float p=__expf(s-mn); l=l*c+p;
    #pragma unroll
    for(int d=0;d<20;d++) acc[d]=acc[d]*c+p*qkv[k*1920+1280+h*20+d];
    m=mn; }
  #pragma unroll
  for(int d=0;d<20;d++) o[q*D_+h*20+d]=bf2f(f2bf(acc[d]/l));
}
__global__ void n_add(const u16* x, const float* a, u16* h){ int i=blockIdx.x*256+threadIdx.x; h[i]=f2bf(bf2f(x[i])+a[i]); }
__global__ void n_glu(const float* g, const float* u, float* H){ int i=blockIdx.x*256+threadIdx.x; float x=g[i];
  float t=tanhf(0.7978845608028654f*(x+0.044715f*x*x*x)); float ge=bf2f(f2bf(0.5f*x*(1.f+t))); H[i]=bf2f(f2bf(ge*u[i])); }

extern "C" void kernel_launch(void* const* d_in, const int* in_sizes, int n_in,
                              void* d_out, int out_size, void* d_ws, size_t ws_size,
                              hipStream_t stream) {
  const u16* x=(const u16*)d_in[0]; const u16* wqkv=(const u16*)d_in[1]; const u16* wo=(const u16*)d_in[2];
  const u16* wg=(const u16*)d_in[4]; const u16* wu=(const u16*)d_in[5]; const u16* wd=(const u16*)d_in[6];
  float* ws=(float*)d_ws;
  float* ln=ws; float* qkv=ln+S_*D_; float* o=qkv+S_*1920; float* a=o+S_*D_; float* g=a+S_*D_; float* u=g+S_*F_; float* H=u+S_*F_; u16* h=(u16*)(H+S_*F_);
  n_ln<<<S_,64,0,stream>>>(x,ln);
  n_gemm<<<dim3(1920/64,S_/8),64,0,stream>>>(ln,wqkv,qkv,D_,1920);
  n_attn<<<dim3(S_/64,32),64,0,stream>>>(qkv,o);
  n_gemm<<<dim3(640/64,S_/8),64,0,stream>>>(o,wo,a,D_,640);
  n_add<<<S_*D_/256,256,0,stream>>>(x,a,h);
  n_ln<<<S_,64,0,stream>>>(h,ln);
  n_gemm<<<dim3(F_/64,S_/8),64,0,stream>>>(ln,wg,g,D_,F_);
  n_gemm<<<dim3(F_/64,S_/8),64,0,stream>>>(ln,wu,u,D_,F_);
  n_glu<<<S_*F_/256,256,0,stream>>>(g,u,H);
  n_gemm<<<dim3(640/64,S_/8),64,0,stream>>>(H,wd,a,F_,640);
  n_add<<<S_*D_/256,256,0,stream>>>(h,a,(u16*)d_out);
}
```

```cpp
#include <hip/hip_runtime.h>
#include <stdint.h>

typedef unsigned short u16;
typedef __bf16 bf16x8 __attribute__((ext_vector_type(8)));
typedef __bf16 bf16x2 __attribute__((ext_vector_type(2)));
typedef short s16x4 __attribute__((ext_vector_type(4)));
typedef short s16x8 __attribute__((ext_vector_type(8)));
typedef float f32x2 __attribute__((ext_vector_type(2)));
typedef float f32x4 __attribute__((ext_vector_type(4)));
typedef float f32x16 __attribute__((ext_vector_type(16)));
typedef int i32x4 __attribute__((ext_vector_type(4)));
typedef unsigned u32x2 __attribute__((ext_vector_type(2)));
typedef unsigned u32x4 __attribute__((ext_vector_type(4)));
#define LDS3 __attribute__((address_space(3)))

constexpr int S_ = 2048, D_ = 640, F_ = 1536;
constexpr int REC = 2688;
constexpr int REC_HEAD = 64 * REC;
constexpr size_t WS_REC = 0;
constexpr size_t WS_O = WS_REC + (size_t)64 * REC_HEAD;
constexpr size_t WS_H1 = WS_O + (size_t)S_ * D_ * 2;
constexpr size_t WS_HH = WS_H1 + (size_t)S_ * D_ * 2;
constexpr size_t WS_ST = WS_HH + (size_t)S_ * F_ * 2;
constexpr size_t WS_CS = WS_ST + (size_t)S_ * 64;
constexpr float LN_EPS = 1e-5f;

#define SB() __builtin_amdgcn_sched_barrier(0)
#define WAIT_VL(n) asm volatile("s_waitcnt vmcnt(%0) lgkmcnt(0)" ::"n"(n) : "memory")
#define BARRIER() asm volatile("s_barrier" ::: "memory")
#define LBARRIER() asm volatile("s_waitcnt lgkmcnt(0)\n\ts_barrier" ::: "memory")

__device__ __forceinline__ i32x4 mk_srd(const void* p, unsigned bytes) {
  unsigned long long a = (unsigned long long)p;
  i32x4 r;
  r.x = (int)(unsigned)a; r.y = (int)((unsigned)(a >> 32) & 0xffffu); r.z = (int)bytes; r.w = 0x00020000;
  return r;
}
__device__ __forceinline__ void dma16(unsigned lds, unsigned voff, i32x4 srd, unsigned soff) {
  asm volatile("s_mov_b32 m0, %0\n\ts_nop 0\n\tbuffer_load_dwordx4 %1, %2, %3 offen lds"
               ::"s"(lds), "v"(voff), "s"(srd), "s"(soff) : "memory");
}
__device__ __forceinline__ void dma16_masked(unsigned lds, unsigned voff, i32x4 srd, unsigned soff, unsigned long long mask) {
  asm volatile("s_mov_b32 m0, %0\n\ts_mov_b64 exec, %4\n\tbuffer_load_dwordx4 %1, %2, %3 offen lds\n\ts_mov_b64 exec, -1"
               ::"s"(lds), "v"(voff), "s"(srd), "s"(soff), "s"(mask) : "memory");
}
template <typename T> __device__ __forceinline__ T ldsr(unsigned a) { return *(LDS3 const T*)(uintptr_t)a; }
template <typename T> __device__ __forceinline__ void ldsw(unsigned a, T v) { *(LDS3 T*)(uintptr_t)a = v; }
__device__ __forceinline__ s16x4 ldstr(unsigned a) { return __builtin_amdgcn_ds_read_tr16_b64_v4i16((LDS3 s16x4*)(uintptr_t)a); }
__device__ __forceinline__ bf16x8 ldstr2(unsigned a0, unsigned a1) {
  s16x4 lo = ldstr(a0), hi = ldstr(a1);
  s16x8 r = __builtin_shufflevector(lo, hi, 0, 1, 2, 3, 4, 5, 6, 7);
  return __builtin_bit_cast(bf16x8, r);
}
__device__ __forceinline__ unsigned pk2(float a, float b) {
  f32x2 v = {a, b};
  return __builtin_bit_cast(unsigned, __builtin_convertvector(v, bf16x2));
}
__device__ __forceinline__ float bflo(unsigned v) { return __uint_as_float(v << 16); }
__device__ __forceinline__ float bfhi(unsigned v) { return __uint_as_float(v & 0xffff0000u); }
__device__ __forceinline__ s16x4 lo4(bf16x8 v) { s16x8 t = __builtin_bit_cast(s16x8, v); return __builtin_shufflevector(t, t, 0, 1, 2, 3); }
__device__ __forceinline__ s16x4 hi4(bf16x8 v) { s16x8 t = __builtin_bit_cast(s16x8, v); return __builtin_shufflevector(t, t, 4, 5, 6, 7); }
#define MFMA32(a, b, c) __builtin_amdgcn_mfma_f32_32x32x16_bf16(a, b, c, 0, 0, 0)
#define MFMA16(a, b, c) __builtin_amdgcn_mfma_f32_16x16x32_bf16(a, b, c, 0, 0, 0)
#define MFMA32K8(a, b, c) __builtin_amdgcn_mfma_f32_32x32x8bf16_1k(a, b, c, 0, 0, 0)
#define MFMA4(a, b, c) __builtin_amdgcn_mfma_f32_4x4x4bf16_1k(a, b, c, 0, 0, 0)

struct Frag1 { bf16x8 a[2], b[2]; };

template <bool SWAP>
__device__ __forceinline__ void k1_body(const u16* __restrict__ x, const u16* __restrict__ w, char* __restrict__ rec,
                                        unsigned L, int rt, int ct) {
  const int tid = threadIdx.x, lane = tid & 63, wave = __builtin_amdgcn_readfirstlane(tid >> 6);
  const int wm = wave >> 1, wn = wave & 1, r = lane & 31, hh = lane >> 5;
  const int m0 = rt * 128, n0 = ct * 120;
  const i32x4 sx = mk_srd(x, S_ * D_ * 2), sw = mk_srd(w, D_ * 1920 * 2);
  unsigned vA[4], vB[4];
#pragma unroll
  for (int i = 0; i < 4; ++i) {
    int row = (wave * 4 + i) * 8 + (lane >> 3);
    vA[i] = row * 1280 + (((lane & 7) ^ ((row >> 1) & 7)) << 4);
    int k = (wave * 4 + i) * 4 + (lane >> 4);
    vB[i] = k * 3840 + (((lane & 15) ^ ((k & 3) << 2)) << 4);
  }
  const unsigned long long bmask = ~((1ull << 15) | (1ull << 27) | (1ull << 39) | (1ull << 51));
  const unsigned oA = m0 * 1280, oB = n0 * 2;
  auto issueA = [&](int s, int i) { dma16(L + (s & 3) * 32768 + (wave * 4 + i) * 1024, vA[i], sx, oA + s * 128); };
  auto issueB = [&](int s, int i) { dma16_masked(L + (s & 3) * 32768 + 16384 + (wave * 4 + i) * 1024, vB[i], sw, oB + s * (64 * 3840), bmask); };
#pragma unroll
  for (int s = 0; s < 4; ++s) {
#pragma unroll
    for (int i = 0; i < 4; ++i) { issueA(s, i); issueB(s, i); }
  }
  {
    int k = tid & 63;
    ldsw<u32x4>(L + wave * 32768 + 16384 + k * 256 + ((15 ^ ((k & 3) << 2)) << 4), u32x4{0x3f80u, 0u, 0u, 0u});
  }
  unsigned aA[2][4], aB[2];
#pragma unroll
  for (int i = 0; i < 2; ++i) {
    int row = wm * 64 + ((i ^ wn) * 32) + r;
#pragma unroll
    for (int ks = 0; ks < 4; ++ks) aA[i][ks] = L + row * 128 + (((2 * ks + hh) ^ ((row >> 1) & 7)) << 4);
    int q = (lane >> 2) & 3, p = lane & 3, ch = (lane >> 4) & 1;
    int c = wn * 8 + ((i ^ wm) * 4) + 2 * ch + (p >> 1);
    aB[i] = L + 16384 + (8 * hh + q) * 256 + ((c ^ (q << 2)) << 4) + 8 * (p & 1);
  }
  auto rd = [&](Frag1& f, unsigned sl, int ks) {
    f.a[0] = ldsr<bf16x8>(aA[0][ks] + sl);
    f.b[0] = ldstr2(aB[0] + sl + ks * 4096, aB[0] + sl + ks * 4096 + 1024);
    f.a[1] = ldsr<bf16x8>(aA[1][ks] + sl);
    f.b[1] = ldstr2(aB[1] + sl + ks * 4096, aB[1] + sl + ks * 4096 + 1024);
  };
  f32x16 acc[2][2] = {};
  f32x4 sq0 = {}, sq1 = {}, cs0 = {}, cs1 = {};
  const s16x4 ones = {0x3f80, 0x3f80, 0x3f80, 0x3f80};
  auto mm = [&](const Frag1& f, int t) {
    const int i = t >> 1, j = t & 1;
    acc[i][j] = SWAP ? MFMA32(f.b[j], f.a[i], acc[i][j]) : MFMA32(f.a[i], f.b[j], acc[i][j]);
    if (t == 0) sq0 = MFMA4(lo4(f.a[0]), lo4(f.a[0]), sq0);
    if (t == 1) cs0 = MFMA4(ones, lo4(f.b[0]), cs0);
    if (t == 2) sq1 = MFMA4(hi4(f.a[0]), hi4(f.a[0]), sq1);
    if (t == 3) cs1 = MFMA4(ones, hi4(f.b[0]), cs1);
  };
  Frag1 f0, f1;
  WAIT_VL(24); BARRIER();
  rd(f0, 0, 0);
  for (int s = 0; s < 10; ++s) {
    const unsigned sl = (s & 3) * 32768, nsl = ((s + 1) & 3) * 32768;
    SB(); rd(f1, sl, 1); SB(); mm(f0, 0); mm(f0, 1); mm(f0, 2); mm(f0, 3);
    SB(); rd(f0, sl, 2); SB(); mm(f1, 0); mm(f1, 1); mm(f1, 2); mm(f1, 3);
    SB(); rd(f1, sl, 3); SB(); mm(f0, 0); mm(f0, 1); mm(f0, 2); mm(f0, 3);
    SB();
    if (s < 9) {
      if (s < 7) WAIT_VL(16); else if (s == 7) WAIT_VL(8); else WAIT_VL(0);
      BARRIER();
      rd(f0, nsl, 0);
    }
    SB();
    mm(f1, 0); SB();
    if (s < 6) { issueA(s + 4, 0); issueB(s + 4, 0); issueA(s + 4, 1); }
    SB(); mm(f1, 1); SB();
    if (s < 6) { issueB(s + 4, 1); issueA(s + 4, 2); issueB(s + 4, 2); }
    SB(); mm(f1, 2); SB();
    if (s < 6) { issueA(s + 4, 3); issueB(s + 4, 3); }
    SB(); mm(f1, 3);
  }
  SB();
  const unsigned T = L + 131072;
  {
    f32x4 sq = sq0 + sq1;
    int e = lane & 3;
    float sv = e == 0 ? sq[0] : e == 1 ? sq[1] : e == 2 ? sq[2] : sq[3];
    ldsw<float>(T + hh * 512 + (wm * 64 + wn * 32 + r) * 4, sv);
    ldsw<float>(T + 1024 + hh * 512 + (wn * 64 + wm * 32 + r) * 4, cs0[0] + cs1[0]);
    if (wn == 1) {
#pragma unroll
      for (int i = 0; i < 2; ++i) {
        const int rb = wm * 64 + (i ^ 1) * 32;
        if (SWAP) {
          float v = wm ? acc[i][0][12] : acc[i][1][12];
          if (hh == 0) ldsw<float>(T + 2048 + (rb + r) * 4, v);
        } else if (r == 24) {
#pragma unroll
          for (int g = 0; g < 4; ++g) {
            f32x4 v;
#pragma unroll
            for (int e2 = 0; e2 < 4; ++e2) v[e2] = wm ? acc[i][0][4 * g + e2] : acc[i][1][4 * g + e2];
            ldsw<f32x4>(T + 2048 + (rb + 8 * g + 4 * hh) * 4, v);
          }
        }
      }
    }
  }
  LBARRIER();
  if (tid < 128) {
    float sum = ldsr<float>(T + 2048 + tid * 4);
    float sq = ldsr<float>(T + tid * 4) + ldsr<float>(T + 512 + tid * 4);
    float mean = sum * (1.f / D_);
    float var = fmaxf(sq * (1.f / D_) - mean * mean, 0.f);
    float rs = __builtin_amdgcn_rsqf(var + LN_EPS);
    if (SWAP) ldsw<f32x2>(T + 2560 + tid * 8, f32x2{rs, -rs * mean});
    else { ldsw<float>(T + 2560 + tid * 4, rs); ldsw<float>(T + 3072 + tid * 4, -rs * mean); }
  } else {
    int c = tid - 128;
    ldsw<float>(T + 3584 + c * 4, ldsr<float>(T + 1024 + c * 4) + ldsr<float>(T + 1536 + c * 4));
  }
  LBARRIER();
  if (SWAP) {
    const float qs = 0.22360679774997896f * 1.4426950408889634f;
    f32x2 ab[2];
#pragma unroll
    for (int i = 0; i < 2; ++i) ab[i] = ldsr<f32x2>(T + 2560 + (wm * 64 + (i ^ wn) * 32 + r) * 8);
#pragma unroll
    for (int j = 0; j < 2; ++j) {
#pragma unroll
      for (int g = 0; g < 4; ++g) {
        const int lc = wn * 64 + (j ^ wm) * 32 + 8 * g + 4 * hh;
        if (lc < 120) {
          const f32x4 cs = ldsr<f32x4>(T + 3584 + lc * 4);
          const int qd_all = (n0 + lc) >> 2;
          const int hall = (qd_all * 205) >> 10, qd = qd_all - hall * 5;
          const float sc = hall < 32 ? qs : 1.f;
#pragma unroll
          for (int i = 0; i < 2; ++i) {
            const int tile = rt * 4 + wm * 2 + (i ^ wn);
            const float a = ab[i].x * sc, b = ab[i].y * sc;
            float y0 = a * acc[i][j][4 * g + 0] + b * cs[0], y1 = a * acc[i][j][4 * g + 1] + b * cs[1];
            float y2 = a * acc[i][j][4 * g + 2] + b * cs[2], y3 = a * acc[i][j][4 * g + 3] + b * cs[3];
            u32x2 v = {pk2(y0, y1), pk2(y2, y3)};
            if (hall < 64) {
              unsigned off = qd < 4 ? (qd >> 1) * 512 + r * 16 + (qd & 1) * 8 : 1024 + r * 8;
              *(u32x2*)(rec + (size_t)(hall * 64 + tile) * REC + off) = v;
            } else {
              const int k16 = r & 15;
              char* p = rec + (size_t)((hall - 32) * 64 + tile) * REC + 1280 + (r >> 4) * 672 + ((k16 >> 2) & 1) * 336 +
                        ((k16 & 3) + 4 * (k16 >> 3)) * 2 + qd * 64;
              *(u16*)(p) = (u16)v.x; *(u16*)(p + 16) = (u16)(v.x >> 16);
              *(u16*)(p + 32) = (u16)v.y; *(u16*)(p + 48) = (u16)(v.y >> 16);
              if (qd == 4) *(u16*)(p + 64) = 0x3f80;
            }
          }
        }
      }
    }
  } else {
#pragma unroll
    for (int i = 0; i < 2; ++i) {
      const int rb = wm * 64 + (i ^ wn) * 32, tile = rt * 4 + wm * 2 + (i ^ wn);
      f32x4 av[4], bv[4];
#pragma unroll
      for (int g = 0; g < 4; ++g) {
        av[g] = ldsr<f32x4>(T + 2560 + (rb + 8 * g + 4 * hh) * 4);
        bv[g] = ldsr<f32x4>(T + 3072 + (rb + 8 * g + 4 * hh) * 4);
      }
#pragma unroll
      for (int j = 0; j < 2; ++j) {
        const int lc = wn * 64 + (j ^ wm) * 32 + r;
        if (lc < 120) {
          const float cs = ldsr<float>(T + 3584 + lc * 4);
          const int gc = n0 + lc - 1280;
          const int hd = (gc * 205) >> 12, d = gc - hd * 20;
          u32x4 v[2];
#pragma unroll
          for (int g = 0; g < 4; ++g) {
            float y0 = av[g][0] * acc[i][j][4 * g + 0] + bv[g][0] * cs, y1 = av[g][1] * acc[i][j][4 * g + 1] + bv[g][1] * cs;
            float y2 = av[g][2] * acc[i][j][4 * g + 2] + bv[g][2] * cs, y3 = av[g][3] * acc[i][j][4 * g + 3] + bv[g][3] * cs;
            v[g >> 1][(g & 1) * 2] = pk2(y0, y1); v[g >> 1][(g & 1) * 2 + 1] = pk2(y2, y3);
          }
          char* p = rec + (size_t)((32 + hd) * 64 + tile) * REC + 1280 + hh * 336 + d * 16;
          *(u32x4*)p = v[0]; *(u32x4*)(p + 672) = v[1];
          if (d == 19) {
            const u32x4 one = {0x3f803f80u, 0x3f803f80u, 0x3f803f80u, 0x3f803f80u};
            *(u32x4*)(p + 16) = one; *(u32x4*)(p + 672 + 16) = one;
          }
        }
      }
    }
  }
}
constexpr bool chk_div() {
  for (int i = 0; i < 480; ++i) if (((i * 205) >> 10) != i / 5) return false;
  for (int i = 0; i < 640; ++i) if (((i * 205) >> 12) != i / 20) return false;
  return true;
}
static_assert(chk_div(), "magic division");

__global__ __launch_bounds__(256, 1) void k1_qkv(const u16* __restrict__ x, const u16* __restrict__ w, char* __restrict__ ws) {
  __shared__ __attribute__((aligned(1024))) char smem[131072 + 4096];
  const unsigned L = (unsigned)(uintptr_t)(LDS3 char*)smem;
  const int b = blockIdx.x, xcd = b & 7, i = b >> 3;
  const int rt = (xcd >> 1) * 4 + (i & 3), ct = (xcd & 1) * 8 + (i >> 2);
  if (ct <= 10) k1_body<true>(x, w, ws + WS_REC, L, rt, ct);
  else k1_body<false>(x, w, ws + WS_REC, L, rt, ct);
}

constexpr int K2_STAGE = 8 * REC;

__device__ __forceinline__ void exp4(const f32x16& s, int g, unsigned* p) {
  float e0 = __builtin_amdgcn_exp2f(s[4 * g + 0]), e1 = __builtin_amdgcn_exp2f(s[4 * g + 1]);
  float e2 = __builtin_amdgcn_exp2f(s[4 * g + 2]), e3 = __builtin_amdgcn_exp2f(s[4 * g + 3]);
  p[0] = pk2(e0, e1); p[1] = pk2(e2, e3);
}
__device__ __forceinline__ bf16x8 px8(const unsigned* p) { u32x4 v = {p[0], p[1], p[2], p[3]}; return __builtin_bit_cast(bf16x8, v); }
__device__ __forceinline__ float max16(const f32x16& s) {
  float m = fmaxf(fmaxf(fmaxf(s[0], s[1]), fmaxf(s[2], s[3])), fmaxf(fmaxf(s[4], s[5]), fmaxf(s[6], s[7])));
  float n = fmaxf(fmaxf(fmaxf(s[8], s[9]), fmaxf(s[10], s[11])), fmaxf(fmaxf(s[12], s[13]), fmaxf(s[14], s[15])));
  m = fmaxf(m, n);
  return fmaxf(m, __shfl_xor(m, 32));
}

__global__ __launch_bounds__(512, 2) void k2_attn(char* __restrict__ ws, const u16* __restrict__ wg, const u16* __restrict__ wu) {
  __shared__ __attribute__((aligned(1024))) char smem[3 * K2_STAGE + 16384];
  const unsigned L = (unsigned)(uintptr_t)(LDS3 char*)smem;
  const int tid = threadIdx.x, lane = tid & 63, wave = __builtin_amdgcn_readfirstlane(tid >> 6);
  const int b = blockIdx.x, xcd = b & 7, bi = b >> 3;
  const int head = xcd * 4 + (bi & 3), qb = bi >> 2;
  const int r = lane & 31, hh = lane >> 5;
  const char* rec = ws + WS_REC;
  const i32x4 skv = mk_srd(rec + (size_t)(32 + head) * REC_HEAD, REC_HEAD);
  const unsigned vL = lane * 16;
  auto issue = [&](int s, unsigned slot) {
#pragma unroll
    for (int i = 0; i < 3; ++i) {
      int p = wave * 3 + i; p = p > 20 ? 20 : p;
      dma16(slot + p * 1024, vL, skv, s * K2_STAGE + p * 1024);
    }
  };
  issue(0, L);
  const char* qrec = rec + (size_t)(head * 64 + qb * 8 + wave) * REC;
  const bf16x8 qlo = *(const bf16x8*)(qrec + hh * 512 + r * 16);
  u32x2 qh = *(const u32x2*)(qrec + 1024 + r * 8);
  if (hh) qh = u32x2{0u, 0u};
  const s16x4 qhi = __builtin_bit_cast(s16x4, qh);
  WAIT_VL(0); BARRIER();
  issue(1, L + K2_STAGE);
  const int kr = b / 48, cg = b - kr * 48;
  if (b < 240) {
    const i32x4 sw = mk_srd(cg < 24 ? wg : wu, D_ * F_ * 2);
    const int c0 = (cg < 24 ? cg : cg - 24) * 64;
#pragma unroll
    for (int i = 0; i < 2; ++i) {
      int p = wave * 2 + i;
      dma16(L + 3 * K2_STAGE + p * 1024, (lane >> 3) * 3072 + (lane & 7) * 16, sw, (kr * 128 + p * 8) * 3072 + c0 * 2);
    }
  }
  const unsigned aK = hh * 512 + r * 16, aKh = 1024 + r * 8;
  const unsigned aV = 1280 + hh * 336 + (r <= 20 ? r : (r <= 27 ? 20 : 19)) * 16;
  f32x16 negm;
  bf16x8 klo = ldsr<bf16x8>(L + aK);
  s16x4 khi = ldsr<s16x4>(L + aKh);
  {
    f32x16 z = {};
    f32x16 s0 = MFMA32(klo, qlo, z);
    s0 = MFMA32K8(khi, qhi, s0);
    float m = -max16(s0);
#pragma unroll
    for (int i = 0; i < 16; ++i) negm[i] = m;
  }
  f32x16 sc = MFMA32(klo, qlo, negm), sn;
  sc = MFMA32K8(khi, qhi, sc);
  klo = ldsr<bf16x8>(L + aK + REC);
  khi = ldsr<s16x4>(L + aKh + REC);
  f32x16 o = {};
  unsigned p[8] = {};
  bf16x8 v0, v1 = {};
  unsigned cur = L, nxt = L + K2_STAGE, fre = L + 2 * K2_STAGE;
  for (int s = 0; s < 8; ++s) {
    const unsigned cK = cur + aK, cKh = cur + aKh, cV = cur + aV, nK = nxt + aK, nKh = nxt + aKh;
#pragma unroll
    for (int t = 0; t < 8; ++t) {
      f32x16& S = (t & 1) ? sn : sc;
      f32x16& N = (t & 1) ? sc : sn;
      SB();
      v0 = ldsr<bf16x8>(cV + t * REC);
      o = MFMA32(v1, px8(p + 4), o);
      exp4(S, 0, p);
      SB();
      v1 = ldsr<bf16x8>(cV + t * REC + 672);
      N = MFMA32(klo, qlo, negm);
      exp4(S, 1, p + 2);
      SB();
      N = MFMA32K8(khi, qhi, N);
      exp4(S, 2, p + 4);
      SB();
      if (t == 6) {
        if (s < 7) {
          WAIT_VL(0); BARRIER();
          if (s < 6) issue(s + 2, fre);
        }
      }
      if (t < 6) { klo = ldsr<bf16x8>(cK + (t + 2) * REC); khi = ldsr<s16x4>(cKh + (t + 2) * REC); }
      else { klo = ldsr<bf16x8>(nK + (t - 6) * REC); khi = ldsr<s16x4>(nKh + (t - 6) * REC); }
      o = MFMA32(v0, px8(p), o);
      exp4(S, 3, p + 6);
    }
    SB();
    if (s == 3 && wave < 4 && b < 240) {
      const unsigned a0 = L + 3 * K2_STAGE + (8 * (lane >> 4) + ((lane >> 2) & 3)) * 128 + wave * 32 + (lane & 3) * 8;
      const s16x8 one8 = {0x3f80, 0x3f80, 0x3f80, 0x3f80, 0x3f80, 0x3f80, 0x3f80, 0x3f80};
      f32x4 c = {};
#pragma unroll
      for (int ks = 0; ks < 4; ++ks) c = MFMA16(__builtin_bit_cast(bf16x8, one8), ldstr2(a0 + ks * 4096, a0 + ks * 4096 + 512), c);
      if (lane < 16) {
        const int col = (cg < 24 ? cg : cg - 24) * 64 + wave * 16 + lane;
        const int j = col / 96, idx = col - j * 96 + (cg < 24 ? 0 : 96);
        *(float*)(ws + WS_CS + j * 4096 + (kr * 192 + idx) * 4) = c[0];
      }
    }
    unsigned tmp = cur; cur = nxt; nxt = fre; fre = tmp;
  }
  SB();
  o = MFMA32(v1, px8(p + 4), o);
  float l = __shfl(o[8], r + 32);
  bool ok = l <= 0x1p100f;
#pragma unroll
  for (int i = 0; i < 12; ++i) ok = ok && (__builtin_fabsf(o[i]) <= 0x1p126f);
  if (__builtin_amdgcn_ballot_w64(!ok) != 0ull) {
    const char* kv = rec + (size_t)(32 + head) * REC_HEAD;
    float m = -__builtin_inff();
    o = f32x16{};
    for (int t = 0; t < 64; ++t) {
      const char* q = kv + t * REC;
      f32x16 z = {};
      f32x16 s = MFMA32(*(const bf16x8*)(q + aK), qlo, z);
      s = MFMA32K8(*(const s16x4*)(q + aKh), qhi, s);
      float mn = fmaxf(m, max16(s));
      float c = __builtin_amdgcn_exp2f(m - mn);
      m = mn;
#pragma unroll
      for (int i = 0; i < 16; ++i) { s[i] -= mn; o[i] *= c; }
      unsigned pp[8];
#pragma unroll
      for (int g = 0; g < 4; ++g) exp4(s, g, pp + 2 * g);
      o = MFMA32(*(const bf16x8*)(q + aV), px8(pp), o);
      o = MFMA32(*(const bf16x8*)(q + aV + 672), px8(pp + 4), o);
    }
    l = __shfl(o[8], r + 32);
  }
  const float inv = 1.f / l;
  const int row = qb * 256 + wave * 32 + r;
#pragma unroll
  for (int g = 0; g < 3; ++g) {
    if (g < 2 || hh == 0) {
      const int col = head * 20 + 8 * g + 4 * hh;
      u32x2 v = {pk2(o[4 * g] * inv, o[4 * g + 1] * inv), pk2(o[4 * g + 2] * inv, o[4 * g + 3] * inv)};
      *(u32x2*)(ws + WS_O + (size_t)(col >> 3) * 32768 + row * 16 + (col & 7) * 2) = v;
    }
  }
}

struct Frag3 { bf16x8 a[2], b[5]; };
constexpr int K3_STAGE = 16384 + 20480;

template <int KD, bool FIRST>
__device__ __forceinline__ void proj_body(const char* __restrict__ A, const u16* __restrict__ W, const char* __restrict__ R,
                                          char* __restrict__ C, char* __restrict__ stats, unsigned L) {
  constexpr int NS = KD / 128;
  const int tid = threadIdx.x, lane = tid & 63, wave = __builtin_amdgcn_readfirstlane(tid >> 6);
  const int b = blockIdx.x, xcd = b & 7, bi = b >> 3;
  const int rt = xcd * 4 + (bi & 3), ct = bi >> 2;
  const int m0 = rt * 64, n0 = ct * 80;
  const int wm = wave & 1, wk = wave >> 1;
  const i32x4 sa = mk_srd(A, S_ * KD * 2), sw = mk_srd(W, KD * D_ * 2), sr = mk_srd(R, S_ * D_ * 2);
  unsigned vB[5];
#pragma unroll
  for (int i = 0; i < 5; ++i) {
    int idx = (wave * 5 + i) * 64 + lane, pr = idx / 10, c = idx - pr * 10;
    vB[i] = (pr ^ (((pr >> 3) & 1) << 2)) * 1280 + c * 16;
  }
  const unsigned vL = lane * 16;
  auto issueA = [&](int s, int i) { dma16(L + (s & 3) * K3_STAGE + (wave * 4 + i) * 1024, vL, sa, (s * 16 + wave * 4 + i) * 32768 + m0 * 16); };
  auto issueB = [&](int s, int i) { dma16(L + (s & 3) * K3_STAGE + 16384 + (wave * 5 + i) * 1024, vB[i], sw, s * (128 * 1280) + n0 * 2); };
  auto issue = [&](int s) {
#pragma unroll
    for (int i = 0; i < 4; ++i) { issueA(s, i); issueB(s, i); }
    issueB(s, 4);
  };
  const unsigned RS = L + 4 * K3_STAGE;
  issue(0);
#pragma unroll
  for (int i = 0; i < 3; ++i) {
    int p = wave * 3 + i; p = p > 9 ? 9 : p;
    if (FIRST) { int idx = p * 64 + lane, row = idx / 10, c = idx - row * 10; dma16(RS + p * 1024, row * 1280 + c * 16, sr, m0 * 1280 + n0 * 2); }
    else dma16(RS + p * 1024, vL, sr, (ct * 10 + p) * 32768 + m0 * 16);
  }
  issue(1); issue(2); issue(3);
  const int g = lane >> 4;
  const unsigned aA = L + (wk * 8 + g) * 1024 + (wm * 32 + (lane & 15)) * 16;
  unsigned aB[2];
#pragma unroll
  for (int t = 0; t < 2; ++t) aB[t] = L + 16384 + (wk * 64 + 8 * g + ((4 * t) ^ (4 * (g & 1))) + ((lane >> 2) & 3)) * 160 + (lane & 3) * 8;
  auto rd = [&](Frag3& f, unsigned sl, int ks) {
    f.a[0] = ldsr<bf16x8>(aA + sl + ks * 4096);
    f.a[1] = ldsr<bf16x8>(aA + sl + ks * 4096 + 256);
#pragma unroll
    for (int j = 0; j < 5; ++j) f.b[j] = ldstr2(aB[0] + sl + ks * 5120 + j * 32, aB[1] + sl + ks * 5120 + j * 32);
  };
  f32x4 acc[2][5] = {};
  auto mm = [&](const Frag3& f) {
#pragma unroll
    for (int j = 0; j < 5; ++j) { acc[0][j] = MFMA16(f.b[j], f.a[0], acc[0][j]); acc[1][j] = MFMA16(f.b[j], f.a[1], acc[1][j]); }
  };
  Frag3 f0, f1;
  WAIT_VL(30); BARRIER();
  rd(f0, 0, 0);
  for (int s = 0; s < NS; ++s) {
    const unsigned sl = (s & 3) * K3_STAGE, nsl = ((s + 1) & 3) * K3_STAGE;
    SB(); rd(f1, sl, 1); SB(); mm(f0); SB();
    if (s < NS - 1) {
      if (s < NS - 3) WAIT_VL(18); else if (s == NS - 3) WAIT_VL(9); else WAIT_VL(0);
      BARRIER();
      if (s < NS - 4) issue(s + 4);
      rd(f0, nsl, 0);
    }
    SB(); mm(f1);
  }
  SB();
  const unsigned X = L + (NS & 3) * K3_STAGE;
#pragma unroll
  for (int j = 0; j < 5; ++j) ldsw<f32x4>(X + (wave * 5 + j) * 1024 + lane * 16, wk ? acc[0][j] : acc[1][j]);
  LBARRIER();
  const int lr = wm * 32 + wk * 16 + (lane & 15), row = m0 + lr;
  float sum = 0.f, sq = 0.f;
#pragma unroll
  for (int j = 0; j < 5; ++j) {
    f32x4 v = (wk ? acc[1][j] : acc[0][j]) + ldsr<f32x4>(X + ((wave ^ 2) * 5 + j) * 1024 + lane * 16);
    u32x2 rr = FIRST ? ldsr<u32x2>(RS + lr * 160 + j * 32 + g * 8) : ldsr<u32x2>(RS + (2 * j + (g >> 1)) * 1024 + lr * 16 + (g & 1) * 8);
    u32x2 o = {pk2(v[0] + bflo(rr.x), v[1] + bfhi(rr.x)), pk2(v[2] + bflo(rr.y), v[3] + bfhi(rr.y))};
    if (FIRST) {
      *(u32x2*)(C + (size_t)(ct * 10 + 2 * j + (g >> 1)) * 32768 + row * 16 + (g & 1) * 8) = o;
      float h0 = bflo(o.x), h1 = bfhi(o.x), h2 = bflo(o.y), h3 = bfhi(o.y);
      sum += (h0 + h1) + (h2 + h3);
      sq += (h0 * h0 + h1 * h1) + (h2 * h2 + h3 * h3);
    } else {
      *(u32x2*)(C + (size_t)row * 1280 + (n0 + 16 * j + 4 * g) * 2) = o;
    }
  }
  if (FIRST) {
    sum += __shfl_xor(sum, 16); sq += __shfl_xor(sq, 16);
    sum += __shfl_xor(sum, 32); sq += __shfl_xor(sq, 32);
    if (lane < 16) *(f32x2*)(stats + row * 64 + ct * 8) = f32x2{sum, sq};
  }
}

__global__ __launch_bounds__(256, 1) void k3_oproj(const u16* __restrict__ x, const u16* __restrict__ wo, char* __restrict__ ws) {
  __shared__ __attribute__((aligned(1024))) char smem[4 * K3_STAGE + 10240];
  proj_body<D_, true>(ws + WS_O, wo, (const char*)x, ws + WS_H1, ws + WS_ST, (unsigned)(uintptr_t)(LDS3 char*)smem);
}
__global__ __launch_bounds__(256, 1) void k5_down(const u16* __restrict__ wd, char* __restrict__ ws, u16* __restrict__ out) {
  __shared__ __attribute__((aligned(1024))) char smem[4 * K3_STAGE + 10240];
  proj_body<F_, false>(ws + WS_HH, wd, ws + WS_H1, (char*)out, nullptr, (unsigned)(uintptr_t)(LDS3 char*)smem);
}

struct Frag4 { bf16x8 a[2], b[3]; };
constexpr int K4_STAGE = 16384 + 24576;

__global__ __launch_bounds__(256, 1) void k4_gateup(const u16* __restrict__ wg, const u16* __restrict__ wu, char* __restrict__ ws) {
  __shared__ __attribute__((aligned(1024))) char smem[3 * K4_STAGE + 8192 + 4096 + 2048];
  const unsigned L = (unsigned)(uintptr_t)(LDS3 char*)smem;
  const int tid = threadIdx.x, lane = tid & 63, wave = __builtin_amdgcn_readfirstlane(tid >> 6);
  const int b = blockIdx.x, xcd = b & 7, bi = b >> 3;
  const int rt = (xcd >> 1) * 4 + (bi & 3), ct = (xcd & 1) * 8 + (bi >> 2);
  const int m0 = rt * 128, n0 = ct * 96;
  const int wm = wave >> 1, wn = wave & 1, r = lane & 31, hh = lane >> 5;
  const i32x4 sa = mk_srd(ws + WS_H1, S_ * D_ * 2), sg = mk_srd(wg, D_ * F_ * 2), su = mk_srd(wu, D_ * F_ * 2);
  const i32x4 sst = mk_srd(ws + WS_ST, S_ * 64 + 65536);
  unsigned vG[3], vU[3];
#pragma unroll
  for (int i = 0; i < 3; ++i) {
    int idx = (wave * 3 + i) * 64 + lane, pr = idx / 12, c = idx - pr * 12;
    vG[i] = pr * 3072 + c * 16;
    vU[i] = vG[i] ^ 32;
  }
  const unsigned vL = lane * 16;
  auto issueA = [&](int s, unsigned sl, int i) {
    const int p = wave * 4 + i;
    dma16(sl + p * 1024, vL, sa, (s * 8 + (p >> 1)) * 32768 + (m0 + (p & 1) * 64) * 16);
  };
  auto issueB = [&](int s, unsigned sl, int i) {
    dma16(sl + 16384 + (wave * 3 + i) * 1024, vG[i], sg, s * (64 * 3072) + n0 * 2);
    dma16(sl + 16384 + 12288 + (wave * 3 + i) * 1024, vU[i], su, s * (64 * 3072) + n0 * 2);
  };
  auto issue = [&](int s, unsigned sl) {
#pragma unroll
    for (int i = 0; i < 3; ++i) { issueA(s, sl, i); issueB(s, sl, i); }
    issueA(s, sl, 3);
  };
  const unsigned T = L + 3 * K4_STAGE;
  issue(0, L);
  dma16(T + wave * 2048, vL, sst, m0 * 64 + wave * 2048);
  dma16(T + wave * 2048 + 1024, vL, sst, m0 * 64 + wave * 2048 + 1024);
  dma16(T + 8192 + wave * 1024, vL, sst, S_ * 64 + ct * 4096 + wave * 1024);
  issue(1, L + K4_STAGE); issue(2, L + 2 * K4_STAGE);
  const unsigned aA = hh * 2048 + (wm * 64 + r) * 16;
  unsigned aB[3];
  {
    const int q = (lane >> 2) & 3, p = lane & 3, up = (lane >> 4) & 1;
#pragma unroll
    for (int j = 0; j < 3; ++j) aB[j] = 16384 + up * 12288 + (8 * hh + q) * 192 + (((wn * 3 + j) ^ up) * 32) + p * 8;
  }
  auto rd = [&](Frag4& f, unsigned sl, int ks) {
    f.a[0] = ldsr<bf16x8>(aA + sl + ks * 4096);
    f.a[1] = ldsr<bf16x8>(aA + sl + ks * 4096 + 512);
#pragma unroll
    for (int j = 0; j < 3; ++j) f.b[j] = ldstr2(aB[j] + sl + ks * 3072, aB[j] + sl + ks * 3072 + 768);
  };
  f32x16 acc[2][3] = {};
  auto mm = [&](const Frag4& f, int j) { acc[0][j] = MFMA32(f.b[j], f.a[0], acc[0][j]); acc[1][j] = MFMA32(f.b[j], f.a[1], acc[1][j]); };
  Frag4 f0, f1;
  WAIT_VL(23); BARRIER();
  rd(f0, L, 0);
  unsigned cur = L, nxt = L + K4_STAGE, n2 = L + 2 * K4_STAGE;
  for (int s = 0; s < 10; ++s) {
    SB(); rd(f1, cur, 1); SB(); mm(f0, 0); mm(f0, 1); mm(f0, 2);
    SB(); rd(f0, cur, 2); SB(); mm(f1, 0); mm(f1, 1); mm(f1, 2);
    SB(); rd(f1, cur, 3); SB(); mm(f0, 0); mm(f0, 1); mm(f0, 2);
    SB();
    if (s < 9) {
      if (s < 8) WAIT_VL(10); else WAIT_VL(0);
      BARRIER();
      rd(f0, nxt, 0);
    }
    SB();
    mm(f1, 0); SB();
    if (s < 7) { issueA(s + 3, cur, 0); issueB(s + 3, cur, 0); issueA(s + 3, cur, 1); issueA(s + 3, cur, 2); }
    SB(); mm(f1, 1); SB();
    if (s < 7) { issueB(s + 3, cur, 1); issueA(s + 3, cur, 3); issueB(s + 3, cur, 2); }
    SB(); mm(f1, 2); SB();
    if (s == 1) {
      if (tid < 128) {
        f32x4 t0 = ldsr<f32x4>(T + tid * 64), t1 = ldsr<f32x4>(T + tid * 64 + 16), t2 = ldsr<f32x4>(T + tid * 64 + 32), t3 = ldsr<f32x4>(T + tid * 64 + 48);
        f32x4 t = (t0 + t1) + (t2 + t3);
        float mean = (t[0] + t[2]) * (1.f / D_);
        float var = fmaxf((t[1] + t[3]) * (1.f / D_) - mean * mean, 0.f);
        float rs = __builtin_amdgcn_rsqf(var + LN_EPS);
        ldsw<f32x2>(T + 12288 + tid * 8, f32x2{rs, -rs * mean});
      } else if (tid < 128 + 96) {
        const unsigned a = T + 8192 + (tid - 128) * 8;
        f32x2 c = (ldsr<f32x2>(a) + ldsr<f32x2>(a + 768)) + (ldsr<f32x2>(a + 1536) + ldsr<f32x2>(a + 2304)) + ldsr<f32x2>(a + 3072);
        ldsw<f32x2>(T + 13312 + (tid - 128) * 8, c);
      }
    }
    unsigned tmp = cur; cur = nxt; nxt = n2; n2 = tmp;
  }
  SB();
  const float k1 = -2.f * 0.7978845608028654f * 1.4426950408889634f, k2 = k1 * 0.044715f;
#pragma unroll
  for (int j = 0; j < 3; ++j) {
    const int fc = (wn * 3 + j) * 16 + 4 * hh;
    f32x4 cg[2], cu[2];
#pragma unroll
    for (int g = 0; g < 2; ++g) { cg[g] = ldsr<f32x4>(T + 13312 + (fc + 8 * g) * 4); cu[g] = ldsr<f32x4>(T + 13312 + 384 + (fc + 8 * g) * 4); }
#pragma unroll
    for (int i = 0; i < 2; ++i) {
      const f32x2 ab = ldsr<f32x2>(T + 12288 + (wm * 64 + i * 32 + r) * 8);
      unsigned pk[4];
#pragma unroll
      for (int g = 0; g < 2; ++g) {
        float y[4];
#pragma unroll
        for (int e = 0; e < 4; ++e) {
          float gt = ab.x * acc[i][j][4 * g + e] + ab.y * cg[g][e];
          float up = ab.x * acc[i][j][8 + 4 * g + e] + ab.y * cu[g][e];
          float z = gt * (k1 + k2 * gt * gt);
          float sg2 = __builtin_amdgcn_rcpf(1.f + __builtin_amdgcn_exp2f(z));
          y[e] = gt * up * sg2;
        }
        pk[2 * g] = pk2(y[0], y[1]); pk[2 * g + 1] = pk2(y[2], y[3]);
      }
      auto s0 = __builtin_amdgcn_permlane32_swap(pk[0], pk[2], false, false);
      auto s1 = __builtin_amdgcn_permlane32_swap(pk[1], pk[3], false, false);
      u32x4 v = {s0[0], s1[0], s0[1], s1[1]};
      const int chunk = ct * 12 + (wn * 3 + j) * 2 + hh;
      *(u32x4*)(ws + WS_HH + (size_t)chunk * 32768 + (m0 + wm * 64 + i * 32 + r) * 16) = v;
    }
  }
}

extern "C" void kernel_launch(void* const* d_in, const int* in_sizes, int n_in, void* d_out, int out_size, void* d_ws,
                              size_t ws_size, hipStream_t stream) {
  const u16* x = (const u16*)d_in[0];
  const u16* wqkv = (const u16*)d_in[1];
  const u16* wo = (const u16*)d_in[2];
  const u16* wg = (const u16*)d_in[4];
  const u16* wu = (const u16*)d_in[5];
  const u16* wd = (const u16*)d_in[6];
  char* ws = (char*)d_ws;
  k1_qkv<<<256, 256, 0, stream>>>(x, wqkv, ws);
  k2_attn<<<256, 512, 0, stream>>>(ws, wg, wu);
  k3_oproj<<<256, 256, 0, stream>>>(x, wo, ws);
  k4_gateup<<<256, 256, 0, stream>>>(wg, wu, ws);
  k5_down<<<256, 256, 0, stream>>>(wd, ws, (u16*)d_out);
}
```

```cpp
#include <hip/hip_runtime.h>
#include <stdint.h>

typedef unsigned short u16;
typedef __bf16 bf16x8 __attribute__((ext_vector_type(8)));
typedef __bf16 bf16x2 __attribute__((ext_vector_type(2)));
typedef short s16x4 __attribute__((ext_vector_type(4)));
typedef short s16x8 __attribute__((ext_vector_type(8)));
typedef float f32x2 __attribute__((ext_vector_type(2)));
typedef float f32x4 __attribute__((ext_vector_type(4)));
typedef float f32x16 __attribute__((ext_vector_type(16)));
typedef int i32x4 __attribute__((ext_vector_type(4)));
typedef unsigned u32x2 __attribute__((ext_vector_type(2)));
typedef unsigned u32x4 __attribute__((ext_vector_type(4)));
#define LDS3 __attribute__((address_space(3)))

constexpr int S_ = 2048, D_ = 640, F_ = 1536;
constexpr int REC = 2688;
constexpr int REC_HEAD = 64 * REC;
constexpr size_t WS_REC = 0;
constexpr size_t WS_O = WS_REC + (size_t)64 * REC_HEAD;
constexpr size_t WS_H1 = WS_O + (size_t)S_ * D_ * 2;
constexpr size_t WS_HH = WS_H1 + (size_t)S_ * D_ * 2;
constexpr size_t WS_ST = WS_HH + (size_t)S_ * F_ * 2;
constexpr size_t WS_CS = WS_ST + (size_t)S_ * 64;
constexpr float LN_EPS = 1e-5f;

#define TS(k, i)
#define SB() __builtin_amdgcn_sched_barrier(0)
#define WAIT_VL(n) asm volatile("s_waitcnt vmcnt(%0) lgkmcnt(0)" ::"n"(n) : "memory")
#define BARRIER() asm volatile("s_barrier" ::: "memory")
#define LBARRIER() asm volatile("s_waitcnt lgkmcnt(0)\n\ts_barrier" ::: "memory")

__device__ __forceinline__ i32x4 mk_srd(const void* p, unsigned bytes) {
  unsigned long long a = (unsigned long long)p;
  i32x4 r;
  r.x = (int)(unsigned)a; r.y = (int)((unsigned)(a >> 32) & 0xffffu); r.z = (int)bytes; r.w = 0x00020000;
  return r;
}
__device__ __forceinline__ void dma16(unsigned lds, unsigned voff, i32x4 srd, unsigned soff) {
  asm volatile("s_mov_b32 m0, %0\n\ts_nop 0\n\tbuffer_load_dwordx4 %1, %2, %3 offen lds"
               ::"s"(lds), "v"(voff), "s"(srd), "s"(soff) : "memory");
}
__device__ __forceinline__ void dma16_masked(unsigned lds, unsigned voff, i32x4 srd, unsigned soff, unsigned long long mask) {
  asm volatile("s_mov_b32 m0, %0\n\ts_mov_b64 exec, %4\n\tbuffer_load_dwordx4 %1, %2, %3 offen lds\n\ts_mov_b64 exec, -1"
               ::"s"(lds), "v"(voff), "s"(srd), "s"(soff), "s"(mask) : "memory");
}
template <typename T> __device__ __forceinline__ T ldsr(unsigned a) { return *(LDS3 const T*)(uintptr_t)a; }
template <typename T> __device__ __forceinline__ void ldsw(unsigned a, T v) { *(LDS3 T*)(uintptr_t)a = v; }
__device__ __forceinline__ s16x4 ldstr(unsigned a) { return __builtin_amdgcn_ds_read_tr16_b64_v4i16((LDS3 s16x4*)(uintptr_t)a); }
__device__ __forceinline__ bf16x8 ldstr2(unsigned a0, unsigned a1) {
  s16x4 lo = ldstr(a0), hi = ldstr(a1);
  s16x8 r = __builtin_shufflevector(lo, hi, 0, 1, 2, 3, 4, 5, 6, 7);
  return __builtin_bit_cast(bf16x8, r);
}
__device__ __forceinline__ unsigned pk2(float a, float b) {
  f32x2 v = {a, b};
  return __builtin_bit_cast(unsigned, __builtin_convertvector(v, bf16x2));
}
__device__ __forceinline__ float bflo(unsigned v) { return __uint_as_float(v << 16); }
__device__ __forceinline__ float bfhi(unsigned v) { return __uint_as_float(v & 0xffff0000u); }
__device__ __forceinline__ s16x4 lo4(bf16x8 v) { s16x8 t = __builtin_bit_cast(s16x8, v); return __builtin_shufflevector(t, t, 0, 1, 2, 3); }
__device__ __forceinline__ s16x4 hi4(bf16x8 v) { s16x8 t = __builtin_bit_cast(s16x8, v); return __builtin_shufflevector(t, t, 4, 5, 6, 7); }
#define MFMA32(a, b, c) __builtin_amdgcn_mfma_f32_32x32x16_bf16(a, b, c, 0, 0, 0)
#define MFMA16(a, b, c) __builtin_amdgcn_mfma_f32_16x16x32_bf16(a, b, c, 0, 0, 0)
#define MFMA32K8(a, b, c) __builtin_amdgcn_mfma_f32_32x32x8bf16_1k(a, b, c, 0, 0, 0)
#define MFMA4(a, b, c) __builtin_amdgcn_mfma_f32_4x4x4bf16_1k(a, b, c, 0, 0, 0)

struct Frag1 { bf16x8 a[2], b[2]; };

template <bool SWAP>
__device__ __forceinline__ void k1_body(const u16* __restrict__ x, const u16* __restrict__ w, char* __restrict__ rec,
                                        unsigned L, int rt, int ct) {
  const int tid = threadIdx.x, lane = tid & 63, wave = __builtin_amdgcn_readfirstlane(tid >> 6);
  const int wm = wave >> 1, wn = wave & 1, r = lane & 31, hh = lane >> 5;
  const int m0 = rt * 128, n0 = ct * 120;
  TS(0, 0);
  const i32x4 sx = mk_srd(x, S_ * D_ * 2), sw = mk_srd(w, D_ * 1920 * 2);
  unsigned vA[4], vB[4];
#pragma unroll
  for (int i = 0; i < 4; ++i) {
    int row = (wave * 4 + i) * 8 + (lane >> 3);
    vA[i] = row * 1280 + (((lane & 7) ^ ((row >> 1) & 7)) << 4);
    int k = (wave * 4 + i) * 4 + (lane >> 4);
    vB[i] = k * 3840 + (((lane & 15) ^ ((k & 3) << 2)) << 4);
  }
  const unsigned long long bmask = ~((1ull << 15) | (1ull << 27) | (1ull << 39) | (1ull << 51));
  const unsigned oA = m0 * 1280, oB = n0 * 2;
  auto issueA = [&](int s, int i) { dma16(L + (s & 3) * 32768 + (wave * 4 + i) * 1024, vA[i], sx, oA + s * 128); };
  auto issueB = [&](int s, int i) { dma16_masked(L + (s & 3) * 32768 + 16384 + (wave * 4 + i) * 1024, vB[i], sw, oB + s * (64 * 3840), bmask); };
#pragma unroll
  for (int s = 0; s < 4; ++s) {
#pragma unroll
    for (int i = 0; i < 4; ++i) { issueA(s, i); issueB(s, i); }
  }
  {
    int k = tid & 63;
    ldsw<u32x4>(L + wave * 32768 + 16384 + k * 256 + ((15 ^ ((k & 3) << 2)) << 4), u32x4{0x3f80u, 0u, 0u, 0u});
  }
  unsigned aA[2][4], aB[2];
#pragma unroll
  for (int i = 0; i < 2; ++i) {
    int row = wm * 64 + ((i ^ wn) * 32) + r;
#pragma unroll
    for (int ks = 0; ks < 4; ++ks) aA[i][ks] = L + row * 128 + (((2 * ks + hh) ^ ((row >> 1) & 7)) << 4);
    int q = (lane >> 2) & 3, p = lane & 3, ch = (lane >> 4) & 1;
    int c = wn * 8 + ((i ^ wm) * 4) + 2 * ch + (p >> 1);
    aB[i] = L + 16384 + (8 * hh + q) * 256 + ((c ^ (q << 2)) << 4) + 8 * (p & 1);
  }
  auto rd = [&](Frag1& f, unsigned sl, int ks) {
    f.a[0] = ldsr<bf16x8>(aA[0][ks] + sl);
    f.b[0] = ldstr2(aB[0] + sl + ks * 4096, aB[0] + sl + ks * 4096 + 1024);
    f.a[1] = ldsr<bf16x8>(aA[1][ks] + sl);
    f.b[1] = ldstr2(aB[1] + sl + ks * 4096, aB[1] + sl + ks * 4096 + 1024);
  };
  f32x16 acc[2][2] = {};
  f32x4 sq0 = {}, sq1 = {}, cs0 = {}, cs1 = {};
  const s16x4 ones = {0x3f80, 0x3f80, 0x3f80, 0x3f80};
  auto mm = [&](const Frag1& f, int t) {
    const int i = t >> 1, j = t & 1;
    acc[i][j] = SWAP ? MFMA32(f.b[j], f.a[i], acc[i][j]) : MFMA32(f.a[i], f.b[j], acc[i][j]);
    if (t == 0) sq0 = MFMA4(lo4(f.a[0]), lo4(f.a[0]), sq0);
    if (t == 1) cs0 = MFMA4(ones, lo4(f.b[0]), cs0);
    if (t == 2) sq1 = MFMA4(hi4(f.a[0]), hi4(f.a[0]), sq1);
    if (t == 3) cs1 = MFMA4(ones, hi4(f.b[0]), cs1);
  };
  Frag1 f0, f1;
  WAIT_VL(24); BARRIER();
  TS(0, 1);
  rd(f0, 0, 0);
  for (int s = 0; s < 10; ++s) {
    const unsigned sl = (s & 3) * 32768, nsl = ((s + 1) & 3) * 32768;
    SB(); rd(f1, sl, 1); SB(); mm(f0, 0); mm(f0, 1); mm(f0, 2); mm(f0, 3);
    SB(); rd(f0, sl, 2); SB(); mm(f1, 0); mm(f1, 1); mm(f1, 2); mm(f1, 3);
    SB(); rd(f1, sl, 3); SB(); mm(f0, 0); mm(f0, 1); mm(f0, 2); mm(f0, 3);
    SB();
    if (s < 9) {
      if (s < 7) WAIT_VL(16); else if (s == 7) WAIT_VL(8); else WAIT_VL(0);
      BARRIER();
      rd(f0, nsl, 0);
    }
    SB();
    mm(f1, 0); SB();
    if (s < 6) { issueA(s + 4, 0); issueB(s + 4, 0); issueA(s + 4, 1); }
    SB(); mm(f1, 1); SB();
    if (s < 6) { issueB(s + 4, 1); issueA(s + 4, 2); issueB(s + 4, 2); }
    SB(); mm(f1, 2); SB();
    if (s < 6) { issueA(s + 4, 3); issueB(s + 4, 3); }
    SB(); mm(f1, 3);
  }
  SB();
  TS(0, 2);
  const unsigned T = L + 131072;
  {
    f32x4 sq = sq0 + sq1;
    int e = lane & 3;
    float sv = e == 0 ? sq[0] : e == 1 ? sq[1] : e == 2 ? sq[2] : sq[3];
    ldsw<float>(T + hh * 512 + (wm * 64 + wn * 32 + r) * 4, sv);
    ldsw<float>(T + 1024 + hh * 512 + (wn * 64 + wm * 32 + r) * 4, cs0[0] + cs1[0]);
    if (wn == 1) {
#pragma unroll
      for (int i = 0; i < 2; ++i) {
        const int rb = wm * 64 + (i ^ 1) * 32;
        if (SWAP) {
          float v = wm ? acc[i][0][12] : acc[i][1][12];
          if (hh == 0) ldsw<float>(T + 2048 + (rb + r) * 4, v);
        } else if (r == 24) {
#pragma unroll
          for (int g = 0; g < 4; ++g) {
            f32x4 v;
#pragma unroll
            for (int e2 = 0; e2 < 4; ++e2) v[e2] = wm ? acc[i][0][4 * g + e2] : acc[i][1][4 * g + e2];
            ldsw<f32x4>(T + 2048 + (rb + 8 * g + 4 * hh) * 4, v);
          }
        }
      }
    }
  }
  LBARRIER();
  if (tid < 128) {
    float sum = ldsr<float>(T + 2048 + tid * 4);
    float sq = ldsr<float>(T + tid * 4) + ldsr<float>(T + 512 + tid * 4);
    float mean = sum * (1.f / D_);
    float var = fmaxf(sq * (1.f / D_) - mean * mean, 0.f);
    float rs = __builtin_amdgcn_rsqf(var + LN_EPS);
    if (SWAP) ldsw<f32x2>(T + 2560 + tid * 8, f32x2{rs, -rs * mean});
    else { ldsw<float>(T + 2560 + tid * 4, rs); ldsw<float>(T + 3072 + tid * 4, -rs * mean); }
  } else {
    int c = tid - 128;
    ldsw<float>(T + 3584 + c * 4, ldsr<float>(T + 1024 + c * 4) + ldsr<float>(T + 1536 + c * 4));
  }
  LBARRIER();
  if (SWAP) {
    const float qs = 0.22360679774997896f * 1.4426950408889634f;
    f32x2 ab[2];
#pragma unroll
    for (int i = 0; i < 2; ++i) ab[i] = ldsr<f32x2>(T + 2560 + (wm * 64 + (i ^ wn) * 32 + r) * 8);
#pragma unroll
    for (int j = 0; j < 2; ++j) {
#pragma unroll
      for (int g = 0; g < 4; ++g) {
        const int lc = wn * 64 + (j ^ wm) * 32 + 8 * g + 4 * hh;
        if (lc < 120) {
          const f32x4 cs = ldsr<f32x4>(T + 3584 + lc * 4);
          const int qd_all = (n0 + lc) >> 2;
          const int hall = (qd_all * 205) >> 10, qd = qd_all - hall * 5;
          const float sc = hall < 32 ? qs : 1.f;
#pragma unroll
          for (int i = 0; i < 2; ++i) {
            const int tile = rt * 4 + wm * 2 + (i ^ wn);
            const float a = ab[i].x * sc, b = ab[i].y * sc;
            float y0 = a * acc[i][j][4 * g + 0] + b * cs[0], y1 = a * acc[i][j][4 * g + 1] + b * cs[1];
            float y2 = a * acc[i][j][4 * g + 2] + b * cs[2], y3 = a * acc[i][j][4 * g + 3] + b * cs[3];
            u32x2 v = {pk2(y0, y1), pk2(y2, y3)};
            if (hall < 64) {
              unsigned off = qd < 4 ? (qd >> 1) * 512 + r * 16 + (qd & 1) * 8 : 1024 + r * 8;
              *(u32x2*)(rec + (size_t)(hall * 64 + tile) * REC + off) = v;
            } else {
              const int k16 = r & 15;
              char* p = rec + (size_t)((hall - 32) * 64 + tile) * REC + 1280 + (r >> 4) * 672 + ((k16 >> 2) & 1) * 336 +
                        ((k16 & 3) + 4 * (k16 >> 3)) * 2 + qd * 64;
              *(u16*)(p) = (u16)v.x; *(u16*)(p + 16) = (u16)(v.x >> 16);
              *(u16*)(p + 32) = (u16)v.y; *(u16*)(p + 48) = (u16)(v.y >> 16);
              if (qd == 4) *(u16*)(p + 64) = 0x3f80;
            }
          }
        }
      }
    }
  } else {
#pragma unroll
    for (int i = 0; i < 2; ++i) {
      const int rb = wm * 64 + (i ^ wn) * 32, tile = rt * 4 + wm * 2 + (i ^ wn);
      f32x4 av[4], bv[4];
#pragma unroll
      for (int g = 0; g < 4; ++g) {
        av[g] = ldsr<f32x4>(T + 2560 + (rb + 8 * g + 4 * hh) * 4);
        bv[g] = ldsr<f32x4>(T + 3072 + (rb + 8 * g + 4 * hh) * 4);
      }
#pragma unroll
      for (int j = 0; j < 2; ++j) {
        const int lc = wn * 64 + (j ^ wm) * 32 + r;
        if (lc < 120) {
          const float cs = ldsr<float>(T + 3584 + lc * 4);
          const int gc = n0 + lc - 1280;
          const int hd = (gc * 205) >> 12, d = gc - hd * 20;
          u32x4 v[2];
#pragma unroll
          for (int g = 0; g < 4; ++g) {
            float y0 = av[g][0] * acc[i][j][4 * g + 0] + bv[g][0] * cs, y1 = av[g][1] * acc[i][j][4 * g + 1] + bv[g][1] * cs;
            float y2 = av[g][2] * acc[i][j][4 * g + 2] + bv[g][2] * cs, y3 = av[g][3] * acc[i][j][4 * g + 3] + bv[g][3] * cs;
            v[g >> 1][(g & 1) * 2] = pk2(y0, y1); v[g >> 1][(g & 1) * 2 + 1] = pk2(y2, y3);
          }
          char* p = rec + (size_t)((32 + hd) * 64 + tile) * REC + 1280 + hh * 336 + d * 16;
          *(u32x4*)p = v[0]; *(u32x4*)(p + 672) = v[1];
          if (d == 19) {
            const u32x4 one = {0x3f803f80u, 0x3f803f80u, 0x3f803f80u, 0x3f803f80u};
            *(u32x4*)(p + 16) = one; *(u32x4*)(p + 672 + 16) = one;
          }
        }
      }
    }
  }
}
constexpr bool chk_div() {
  for (int i = 0; i < 480; ++i) if (((i * 205) >> 10) != i / 5) return false;
  for (int i = 0; i < 640; ++i) if (((i * 205) >> 12) != i / 20) return false;
  return true;
}
static_assert(chk_div(), "magic division");

__global__ __launch_bounds__(256, 1) void k1_qkv(const u16* __restrict__ x, const u16* __restrict__ w, char* __restrict__ ws) {
  __shared__ __attribute__((aligned(1024))) char smem[131072 + 4096];
  const unsigned L = (unsigned)(uintptr_t)(LDS3 char*)smem;
  const int b = blockIdx.x, xcd = b & 7, i = b >> 3;
  const int rt = (xcd >> 1) * 4 + (i & 3), ct = (xcd & 1) * 8 + (i >> 2);
  if (ct <= 10) k1_body<true>(x, w, ws + WS_REC, L, rt, ct);
  else k1_body<false>(x, w, ws + WS_REC, L, rt, ct);
  TS(0, 3);
}

constexpr int K2_STAGE = 8 * REC;

__device__ __forceinline__ void exp4(const f32x16& s, int g, unsigned* p) {
  float e0 = __builtin_amdgcn_exp2f(s[4 * g + 0]), e1 = __builtin_amdgcn_exp2f(s[4 * g + 1]);
  float e2 = __builtin_amdgcn_exp2f(s[4 * g + 2]), e3 = __builtin_amdgcn_exp2f(s[4 * g + 3]);
  p[0] = pk2(e0, e1); p[1] = pk2(e2, e3);
}
__device__ __forceinline__ bf16x8 px8(const unsigned* p) { u32x4 v = {p[0], p[1], p[2], p[3]}; return __builtin_bit_cast(bf16x8, v); }
__device__ __forceinline__ float max16(const f32x16& s) {
  float m = fmaxf(fmaxf(fmaxf(s[0], s[1]), fmaxf(s[2], s[3])), fmaxf(fmaxf(s[4], s[5]), fmaxf(s[6], s[7])));
  float n = fmaxf(fmaxf(fmaxf(s[8], s[9]), fmaxf(s[10], s[11])), fmaxf(fmaxf(s[12], s[13]), fmaxf(s[14], s[15])));
  m = fmaxf(m, n);
  return fmaxf(m, __shfl_xor(m, 32));
}

__global__ __launch_bounds__(512, 2) void k2_attn(char* __restrict__ ws, const u16* __restrict__ wg, const u16* __restrict__ wu) {
  __shared__ __attribute__((aligned(1024))) char smem[3 * K2_STAGE + 16384];
  const unsigned L = (unsigned)(uintptr_t)(LDS3 char*)smem;
  const int tid = threadIdx.x, lane = tid & 63, wave = __builtin_amdgcn_readfirstlane(tid >> 6);
  const int b = blockIdx.x, xcd = b & 7, bi = b >> 3;
  const int head = xcd * 4 + (bi & 3), qb = bi >> 2;
  const int r = lane & 31, hh = lane >> 5;
  const char* rec = ws + WS_REC;
  const i32x4 skv = mk_srd(rec + (size_t)(32 + head) * REC_HEAD, REC_HEAD);
  const unsigned vL = lane * 16;
  auto issue = [&](int s, unsigned slot) {
#pragma unroll
    for (int i = 0; i < 3; ++i) {
      int p = wave * 3 + i; p = p > 20 ? 20 : p;
      dma16(slot + p * 1024, vL, skv, s * K2_STAGE + p * 1024);
    }
  };
  TS(1, 0);
  issue(0, L);
  const char* qrec = rec + (size_t)(head * 64 + qb * 8 + wave) * REC;
  const bf16x8 qlo = *(const bf16x8*)(qrec + hh * 512 + r * 16);
  u32x2 qh = *(const u32x2*)(qrec + 1024 + r * 8);
  if (hh) qh = u32x2{0u, 0u};
  const s16x4 qhi = __builtin_bit_cast(s16x4, qh);
  WAIT_VL(0); BARRIER();
  TS(1, 1);
  issue(1, L + K2_STAGE);
  const int kr = b / 48, cg = b - kr * 48;
  if (b < 240) {
    const i32x4 sw = mk_srd(cg < 24 ? wg : wu, D_ * F_ * 2);
    const int c0 = (cg < 24 ? cg : cg - 24) * 64;
#pragma unroll
    for (int i = 0; i < 2; ++i) {
      int p = wave * 2 + i;
      dma16(L + 3 * K2_STAGE + p * 1024, (lane >> 3) * 3072 + (lane & 7) * 16, sw, (kr * 128 + p * 8) * 3072 + c0 * 2);
    }
  }
  const unsigned aK = hh * 512 + r * 16, aKh = 1024 + r * 8;
  const unsigned aV = 1280 + hh * 336 + (r <= 20 ? r : (r <= 27 ? 20 : 19)) * 16;
  f32x16 negm;
  bf16x8 klo = ldsr<bf16x8>(L + aK);
  s16x4 khi = ldsr<s16x4>(L + aKh);
  {
    f32x16 z = {};
    f32x16 s0 = MFMA32(klo, qlo, z);
    s0 = MFMA32K8(khi, qhi, s0);
    float m = -max16(s0);
#pragma unroll
    for (int i = 0; i < 16; ++i) negm[i] = m;
  }
  f32x16 sc = MFMA32(klo, qlo, negm), sn;
  sc = MFMA32K8(khi, qhi, sc);
  klo = ldsr<bf16x8>(L + aK + REC);
  khi = ldsr<s16x4>(L + aKh + REC);
  f32x16 o = {};
  unsigned p[8] = {};
  bf16x8 v0, v1 = {};
  unsigned cur = L, nxt = L + K2_STAGE, fre = L + 2 * K2_STAGE;
  for (int s = 0; s < 8; ++s) {
    const unsigned cK = cur + aK, cKh = cur + aKh, cV = cur + aV, nK = nxt + aK, nKh = nxt + aKh;
#pragma unroll
    for (int t = 0; t < 8; ++t) {
      f32x16& S = (t & 1) ? sn : sc;
      f32x16& N = (t & 1) ? sc : sn;
      SB();
      v0 = ldsr<bf16x8>(cV + t * REC);
      o = MFMA32(v1, px8(p + 4), o);
      exp4(S, 0, p);
      SB();
      v1 = ldsr<bf16x8>(cV + t * REC + 672);
      N = MFMA32(klo, qlo, negm);
      exp4(S, 1, p + 2);
      SB();
      N = MFMA32K8(khi, qhi, N);
      exp4(S, 2, p + 4);
      SB();
      if (t == 6) {
        if (s < 7) {
          WAIT_VL(0); BARRIER();
          if (s < 6) issue(s + 2, fre);
        }
      }
      if (t < 6) { klo = ldsr<bf16x8>(cK + (t + 2) * REC); khi = ldsr<s16x4>(cKh + (t + 2) * REC); }
      else { klo = ldsr<bf16x8>(nK + (t - 6) * REC); khi = ldsr<s16x4>(nKh + (t - 6) * REC); }
      o = MFMA32(v0, px8(p), o);
      exp4(S, 3, p + 6);
    }
    SB();
    if (s == 3 && wave < 4 && b < 240) {
      const unsigned a0 = L + 3 * K2_STAGE + (8 * (lane >> 4) + ((lane >> 2) & 3)) * 128 + wave * 32 + (lane & 3) * 8;
      const s16x8 one8 = {0x3f80, 0x3f80, 0x3f80, 0x3f80, 0x3f80, 0x3f80, 0x3f80, 0x3f80};
      f32x4 c = {};
#pragma unroll
      for (int ks = 0; ks < 4; ++ks) c = MFMA16(__builtin_bit_cast(bf16x8, one8), ldstr2(a0 + ks * 4096, a0 + ks * 4096 + 512), c);
      if (lane < 16) {
        const int col = (cg < 24 ? cg : cg - 24) * 64 + wave * 16 + lane;
        const int j = col / 96, idx = col - j * 96 + (cg < 24 ? 0 : 96);
        *(float*)(ws + WS_CS + j * 4096 + (kr * 192 + idx) * 4) = c[0];
      }
    }
    unsigned tmp = cur; cur = nxt; nxt = fre; fre = tmp;
  }
  SB();
  TS(1, 2);
  o = MFMA32(v1, px8(p + 4), o);
  float l = __shfl(o[8], r + 32);
  bool ok = l <= 0x1p100f;
#pragma unroll
  for (int i = 0; i < 12; ++i) ok = ok && (__builtin_fabsf(o[i]) <= 0x1p126f);
  if (__builtin_amdgcn_ballot_w64(!ok) != 0ull) {
    const char* kv = rec + (size_t)(32 + head) * REC_HEAD;
    float m = -__builtin_inff();
    o = f32x16{};
    for (int t = 0; t < 64; ++t) {
      const char* q = kv + t * REC;
      f32x16 z = {};
      f32x16 s = MFMA32(*(const bf16x8*)(q + aK), qlo, z);
      s = MFMA32K8(*(const s16x4*)(q + aKh), qhi, s);
      float mn = fmaxf(m, max16(s));
      float c = __builtin_amdgcn_exp2f(m - mn);
      m = mn;
#pragma unroll
      for (int i = 0; i < 16; ++i) { s[i] -= mn; o[i] *= c; }
      unsigned pp[8];
#pragma unroll
      for (int g = 0; g < 4; ++g) exp4(s, g, pp + 2 * g);
      o = MFMA32(*(const bf16x8*)(q + aV), px8(pp), o);
      o = MFMA32(*(const bf16x8*)(q + aV + 672), px8(pp + 4), o);
    }
    l = __shfl(o[8], r + 32);
  }
  const float inv = 1.f / l;
  const int row = qb * 256 + wave * 32 + r;
#pragma unroll
  for (int g = 0; g < 3; ++g) {
    if (g < 2 || hh == 0) {
      const int col = head * 20 + 8 * g + 4 * hh;
      u32x2 v = {pk2(o[4 * g] * inv, o[4 * g + 1] * inv), pk2(o[4 * g + 2] * inv, o[4 * g + 3] * inv)};
      *(u32x2*)(ws + WS_O + (size_t)(col >> 3) * 32768 + row * 16 + (col & 7) * 2) = v;
    }
  }
  TS(1, 3);
}

struct Frag3 { bf16x8 a[2], b[5]; };
constexpr int K3_STAGE = 16384 + 20480;

template <int KD, bool FIRST>
__device__ __forceinline__ void proj_body(const char* __restrict__ A, const u16* __restrict__ W, const char* __restrict__ R,
                                          char* __restrict__ C, char* __restrict__ stats, unsigned L) {
  constexpr int NS = KD / 128;
  const int tid = threadIdx.x, lane = tid & 63, wave = __builtin_amdgcn_readfirstlane(tid >> 6);
  const int b = blockIdx.x, xcd = b & 7, bi = b >> 3;
  const int rt = xcd * 4 + (bi & 3), ct = bi >> 2;
  const int m0 = rt * 64, n0 = ct * 80;
  const int wm = wave & 1, wk = wave >> 1;
  const i32x4 sa = mk_srd(A, S_ * KD * 2), sw = mk_srd(W, KD * D_ * 2), sr = mk_srd(R, S_ * D_ * 2);
  unsigned vB[5];
#pragma unroll
  for (int i = 0; i < 5; ++i) {
    int idx = (wave * 5 + i) * 64 + lane, pr = idx / 10, c = idx - pr * 10;
    vB[i] = (pr ^ (((pr >> 3) & 1) << 2)) * 1280 + c * 16;
  }
  const unsigned vL = lane * 16;
  auto issueA = [&](int s, int i) { dma16(L + (s & 3) * K3_STAGE + (wave * 4 + i) * 1024, vL, sa, (s * 16 + wave * 4 + i) * 32768 + m0 * 16); };
  auto issueB = [&](int s, int i) { dma16(L + (s & 3) * K3_STAGE + 16384 + (wave * 5 + i) * 1024, vB[i], sw, s * (128 * 1280) + n0 * 2); };
  auto issue = [&](int s) {
#pragma unroll
    for (int i = 0; i < 4; ++i) { issueA(s, i); issueB(s, i); }
    issueB(s, 4);
  };
  const unsigned RS = L + 4 * K3_STAGE;
  TS(FIRST ? 2 : 4, 0);
  issue(0);
#pragma unroll
  for (int i = 0; i < 3; ++i) {
    int p = wave * 3 + i; p = p > 9 ? 9 : p;
    if (FIRST) { int idx = p * 64 + lane, row = idx / 10, c = idx - row * 10; dma16(RS + p * 1024, row * 1280 + c * 16, sr, m0 * 1280 + n0 * 2); }
    else dma16(RS + p * 1024, vL, sr, (ct * 10 + p) * 32768 + m0 * 16);
  }
  issue(1); issue(2); issue(3);
  const int g = lane >> 4;
  const unsigned aA = L + (wk * 8 + g) * 1024 + (wm * 32 + (lane & 15)) * 16;
  unsigned aB[2];
#pragma unroll
  for (int t = 0; t < 2; ++t) aB[t] = L + 16384 + (wk * 64 + 8 * g + ((4 * t) ^ (4 * (g & 1))) + ((lane >> 2) & 3)) * 160 + (lane & 3) * 8;
  auto rd = [&](Frag3& f, unsigned sl, int ks) {
    f.a[0] = ldsr<bf16x8>(aA + sl + ks * 4096);
    f.a[1] = ldsr<bf16x8>(aA + sl + ks * 4096 + 256);
#pragma unroll
    for (int j = 0; j < 5; ++j) f.b[j] = ldstr2(aB[0] + sl + ks * 5120 + j * 32, aB[1] + sl + ks * 5120 + j * 32);
  };
  f32x4 acc[2][5] = {};
  auto mm = [&](const Frag3& f) {
#pragma unroll
    for (int j = 0; j < 5; ++j) { acc[0][j] = MFMA16(f.b[j], f.a[0], acc[0][j]); acc[1][j] = MFMA16(f.b[j], f.a[1], acc[1][j]); }
  };
  Frag3 f0, f1;
  WAIT_VL(30); BARRIER();
  TS(FIRST ? 2 : 4, 1);
  rd(f0, 0, 0);
  for (int s = 0; s < NS; ++s) {
    const unsigned sl = (s & 3) * K3_STAGE, nsl = ((s + 1) & 3) * K3_STAGE;
    SB(); rd(f1, sl, 1); SB(); mm(f0); SB();
    if (s < NS - 1) {
      if (s < NS - 3) WAIT_VL(18); else if (s == NS - 3) WAIT_VL(9); else WAIT_VL(0);
      BARRIER();
      if (s < NS - 4) issue(s + 4);
      rd(f0, nsl, 0);
    }
    SB(); mm(f1);
  }
  SB();
  TS(FIRST ? 2 : 4, 2);
  const unsigned X = L + (NS & 3) * K3_STAGE;
#pragma unroll
  for (int j = 0; j < 5; ++j) ldsw<f32x4>(X + (wave * 5 + j) * 1024 + lane * 16, wk ? acc[0][j] : acc[1][j]);
  LBARRIER();
  const int lr = wm * 32 + wk * 16 + (lane & 15), row = m0 + lr;
  float sum = 0.f, sq = 0.f;
#pragma unroll
  for (int j = 0; j < 5; ++j) {
    f32x4 v = (wk ? acc[1][j] : acc[0][j]) + ldsr<f32x4>(X + ((wave ^ 2) * 5 + j) * 1024 + lane * 16);
    u32x2 rr = FIRST ? ldsr<u32x2>(RS + lr * 160 + j * 32 + g * 8) : ldsr<u32x2>(RS + (2 * j + (g >> 1)) * 1024 + lr * 16 + (g & 1) * 8);
    u32x2 o = {pk2(v[0] + bflo(rr.x), v[1] + bfhi(rr.x)), pk2(v[2] + bflo(rr.y), v[3] + bfhi(rr.y))};
    if (FIRST) {
      *(u32x2*)(C + (size_t)(ct * 10 + 2 * j + (g >> 1)) * 32768 + row * 16 + (g & 1) * 8) = o;
      float h0 = bflo(o.x), h1 = bfhi(o.x), h2 = bflo(o.y), h3 = bfhi(o.y);
      sum += (h0 + h1) + (h2 + h3);
      sq += (h0 * h0 + h1 * h1) + (h2 * h2 + h3 * h3);
    } else {
      *(u32x2*)(C + (size_t)row * 1280 + (n0 + 16 * j + 4 * g) * 2) = o;
    }
  }
  if (FIRST) {
    sum += __shfl_xor(sum, 16); sq += __shfl_xor(sq, 16);
    sum += __shfl_xor(sum, 32); sq += __shfl_xor(sq, 32);
    if (lane < 16) *(f32x2*)(stats + row * 64 + ct * 8) = f32x2{sum, sq};
  }
  TS(FIRST ? 2 : 4, 3);
}

__global__ __launch_bounds__(256, 1) void k3_oproj(const u16* __restrict__ x, const u16* __restrict__ wo, char* __restrict__ ws) {
  __shared__ __attribute__((aligned(1024))) char smem[4 * K3_STAGE + 10240];
  proj_body<D_, true>(ws + WS_O, wo, (const char*)x, ws + WS_H1, ws + WS_ST, (unsigned)(uintptr_t)(LDS3 char*)smem);
}
__global__ __launch_bounds__(256, 1) void k5_down(const u16* __restrict__ wd, char* __restrict__ ws, u16* __restrict__ out) {
  __shared__ __attribute__((aligned(1024))) char smem[4 * K3_STAGE + 10240];
  proj_body<F_, false>(ws + WS_HH, wd, ws + WS_H1, (char*)out, nullptr, (unsigned)(uintptr_t)(LDS3 char*)smem);
}

struct Frag4 { bf16x8 a[2], b[3]; };
constexpr int K4_STAGE = 16384 + 24576;

__global__ __launch_bounds__(256, 1) void k4_gateup(const u16* __restrict__ wg, const u16* __restrict__ wu, char* __restrict__ ws) {
  __shared__ __attribute__((aligned(1024))) char smem[3 * K4_STAGE + 8192 + 4096 + 2048];
  const unsigned L = (unsigned)(uintptr_t)(LDS3 char*)smem;
  const int tid = threadIdx.x, lane = tid & 63, wave = __builtin_amdgcn_readfirstlane(tid >> 6);
  const int b = blockIdx.x, xcd = b & 7, bi = b >> 3;
  const int rt = (xcd >> 1) * 4 + (bi & 3), ct = (xcd & 1) * 8 + (bi >> 2);
  const int m0 = rt * 128, n0 = ct * 96;
  const int wm = wave >> 1, wn = wave & 1, r = lane & 31, hh = lane >> 5;
  const i32x4 sa = mk_srd(ws + WS_H1, S_ * D_ * 2), sg = mk_srd(wg, D_ * F_ * 2), su = mk_srd(wu, D_ * F_ * 2);
  const i32x4 sst = mk_srd(ws + WS_ST, S_ * 64 + 65536);
  unsigned vG[3], vU[3];
#pragma unroll
  for (int i = 0; i < 3; ++i) {
    int idx = (wave * 3 + i) * 64 + lane, pr = idx / 12, c = idx - pr * 12;
    vG[i] = pr * 3072 + c * 16;
    vU[i] = vG[i] ^ 32;
  }
  const unsigned vL = lane * 16;
  auto issueA = [&](int s, unsigned sl, int i) {
    const int p = wave * 4 + i;
    dma16(sl + p * 1024, vL, sa, (s * 8 + (p >> 1)) * 32768 + (m0 + (p & 1) * 64) * 16);
  };
  auto issueB = [&](int s, unsigned sl, int i) {
    dma16(sl + 16384 + (wave * 3 + i) * 1024, vG[i], sg, s * (64 * 3072) + n0 * 2);
    dma16(sl + 16384 + 12288 + (wave * 3 + i) * 1024, vU[i], su, s * (64 * 3072) + n0 * 2);
  };
  auto issue = [&](int s, unsigned sl) {
#pragma unroll
    for (int i = 0; i < 3; ++i) { issueA(s, sl, i); issueB(s, sl, i); }
    issueA(s, sl, 3);
  };
  const unsigned T = L + 3 * K4_STAGE;
  TS(3, 0);
  issue(0, L);
  dma16(T + wave * 2048, vL, sst, m0 * 64 + wave * 2048);
  dma16(T + wave * 2048 + 1024, vL, sst, m0 * 64 + wave * 2048 + 1024);
  dma16(T + 8192 + wave * 1024, vL, sst, S_ * 64 + ct * 4096 + wave * 1024);
  issue(1, L + K4_STAGE); issue(2, L + 2 * K4_STAGE);
  const unsigned aA = hh * 2048 + (wm * 64 + r) * 16;
  unsigned aB[3];
  {
    const int q = (lane >> 2) & 3, p = lane & 3, up = (lane >> 4) & 1;
#pragma unroll
    for (int j = 0; j < 3; ++j) aB[j] = 16384 + up * 12288 + (8 * hh + q) * 192 + (((wn * 3 + j) ^ up) * 32) + p * 8;
  }
  auto rd = [&](Frag4& f, unsigned sl, int ks) {
    f.a[0] = ldsr<bf16x8>(aA + sl + ks * 4096);
    f.a[1] = ldsr<bf16x8>(aA + sl + ks * 4096 + 512);
#pragma unroll
    for (int j = 0; j < 3; ++j) f.b[j] = ldstr2(aB[j] + sl + ks * 3072, aB[j] + sl + ks * 3072 + 768);
  };
  f32x16 acc[2][3] = {};
  auto mm = [&](const Frag4& f, int j) { acc[0][j] = MFMA32(f.b[j], f.a[0], acc[0][j]); acc[1][j] = MFMA32(f.b[j], f.a[1], acc[1][j]); };
  Frag4 f0, f1;
  WAIT_VL(23); BARRIER();
  TS(3, 1);
  rd(f0, L, 0);
  unsigned cur = L, nxt = L + K4_STAGE, n2 = L + 2 * K4_STAGE;
  for (int s = 0; s < 10; ++s) {
    SB(); rd(f1, cur, 1); SB(); mm(f0, 0); mm(f0, 1); mm(f0, 2);
    SB(); rd(f0, cur, 2); SB(); mm(f1, 0); mm(f1, 1); mm(f1, 2);
    SB(); rd(f1, cur, 3); SB(); mm(f0, 0); mm(f0, 1); mm(f0, 2);
    SB();
    if (s < 9) {
      if (s < 8) WAIT_VL(10); else WAIT_VL(0);
      BARRIER();
      rd(f0, nxt, 0);
    }
    SB();
    mm(f1, 0); SB();
    if (s < 7) { issueA(s + 3, cur, 0); issueB(s + 3, cur, 0); issueA(s + 3, cur, 1); issueA(s + 3, cur, 2); }
    SB(); mm(f1, 1); SB();
    if (s < 7) { issueB(s + 3, cur, 1); issueA(s + 3, cur, 3); issueB(s + 3, cur, 2); }
    SB(); mm(f1, 2); SB();
    if (s == 1) {
      if (tid < 128) {
        f32x4 t0 = ldsr<f32x4>(T + tid * 64), t1 = ldsr<f32x4>(T + tid * 64 + 16), t2 = ldsr<f32x4>(T + tid * 64 + 32), t3 = ldsr<f32x4>(T + tid * 64 + 48);
        f32x4 t = (t0 + t1) + (t2 + t3);
        float mean = (t[0] + t[2]) * (1.f / D_);
        float var = fmaxf((t[1] + t[3]) * (1.f / D_) - mean * mean, 0.f);
        float rs = __builtin_amdgcn_rsqf(var + LN_EPS);
        ldsw<f32x2>(T + 12288 + tid * 8, f32x2{rs, -rs * mean});
      } else if (tid < 128 + 96) {
        const unsigned a = T + 8192 + (tid - 128) * 8;
        f32x2 c = (ldsr<f32x2>(a) + ldsr<f32x2>(a + 768)) + (ldsr<f32x2>(a + 1536) + ldsr<f32x2>(a + 2304)) + ldsr<f32x2>(a + 3072);
        ldsw<f32x2>(T + 13312 + (tid - 128) * 8, c);
      }
    }
    unsigned tmp = cur; cur = nxt; nxt = n2; n2 = tmp;
  }
  SB();
  TS(3, 2);
  const float k1 = -2.f * 0.7978845608028654f * 1.4426950408889634f, k2 = k1 * 0.044715f;
#pragma unroll
  for (int j = 0; j < 3; ++j) {
    const int fc = (wn * 3 + j) * 16 + 4 * hh;
    f32x4 cg[2], cu[2];
#pragma unroll
    for (int g = 0; g < 2; ++g) { cg[g] = ldsr<f32x4>(T + 13312 + (fc + 8 * g) * 4); cu[g] = ldsr<f32x4>(T + 13312 + 384 + (fc + 8 * g) * 4); }
#pragma unroll
    for (int i = 0; i < 2; ++i) {
      const f32x2 ab = ldsr<f32x2>(T + 12288 + (wm * 64 + i * 32 + r) * 8);
      unsigned pk[4];
#pragma unroll
      for (int g = 0; g < 2; ++g) {
        float y[4];
#pragma unroll
        for (int e = 0; e < 4; ++e) {
          float gt = ab.x * acc[i][j][4 * g + e] + ab.y * cg[g][e];
          float up = ab.x * acc[i][j][8 + 4 * g + e] + ab.y * cu[g][e];
          float z = gt * (k1 + k2 * gt * gt);
          float sg2 = __builtin_amdgcn_rcpf(1.f + __builtin_amdgcn_exp2f(z));
          y[e] = gt * up * sg2;
        }
        pk[2 * g] = pk2(y[0], y[1]); pk[2 * g + 1] = pk2(y[2], y[3]);
      }
      auto s0 = __builtin_amdgcn_permlane32_swap(pk[0], pk[2], false, false);
      auto s1 = __builtin_amdgcn_permlane32_swap(pk[1], pk[3], false, false);
      u32x4 v = {s0[0], s1[0], s0[1], s1[1]};
      const int chunk = ct * 12 + (wn * 3 + j) * 2 + hh;
      *(u32x4*)(ws + WS_HH + (size_t)chunk * 32768 + (m0 + wm * 64 + i * 32 + r) * 16) = v;
    }
  }
  TS(3, 3);
}

extern "C" void kernel_launch(void* const* d_in, const int* in_sizes, int n_in, void* d_out, int out_size, void* d_ws,
                              size_t ws_size, hipStream_t stream) {
  const u16* x = (const u16*)d_in[0];
  const u16* wqkv = (const u16*)d_in[1];
  const u16* wo = (const u16*)d_in[2];
  const u16* wg = (const u16*)d_in[4];
  const u16* wu = (const u16*)d_in[5];
  const u16* wd = (const u16*)d_in[6];
  char* ws = (char*)d_ws;
  k1_qkv<<<256, 256, 0, stream>>>(x, wqkv, ws);
  k2_attn<<<256, 512, 0, stream>>>(ws, wg, wu);
  k3_oproj<<<256, 256, 0, stream>>>(x, wo, ws);
  k4_gateup<<<256, 256, 0, stream>>>(wg, wu, ws);
  k5_down<<<256, 256, 0, stream>>>(wd, ws, (u16*)d_out);
}
```

```cpp
#define XCODEPF
#include <hip/hip_runtime.h>
#include <stdint.h>

typedef unsigned short u16;
typedef __bf16 bf16x8 __attribute__((ext_vector_type(8)));
typedef __bf16 bf16x2 __attribute__((ext_vector_type(2)));
typedef short s16x4 __attribute__((ext_vector_type(4)));
typedef short s16x8 __attribute__((ext_vector_type(8)));
typedef float f32x2 __attribute__((ext_vector_type(2)));
typedef float f32x4 __attribute__((ext_vector_type(4)));
typedef float f32x16 __attribute__((ext_vector_type(16)));
typedef int i32x4 __attribute__((ext_vector_type(4)));
typedef unsigned u32x2 __attribute__((ext_vector_type(2)));
typedef unsigned u32x4 __attribute__((ext_vector_type(4)));
#define LDS3 __attribute__((address_space(3)))

constexpr int S_ = 2048, D_ = 640, F_ = 1536;
constexpr int REC = 2688;
constexpr int REC_HEAD = 64 * REC;
constexpr size_t WS_REC = 0;
constexpr size_t WS_O = WS_REC + (size_t)64 * REC_HEAD;
constexpr size_t WS_H1 = WS_O + (size_t)S_ * D_ * 2;
constexpr size_t WS_HH = WS_H1 + (size_t)S_ * D_ * 2;
constexpr size_t WS_ST = WS_HH + (size_t)S_ * F_ * 2;
constexpr size_t WS_CS = WS_ST + (size_t)S_ * 64;
constexpr float LN_EPS = 1e-5f;

#define TS(k, i)
#define SB() __builtin_amdgcn_sched_barrier(0)
#define WAIT_VL(n) asm volatile("s_waitcnt vmcnt(%0) lgkmcnt(0)" ::"n"(n) : "memory")
#define BARRIER() asm volatile("s_barrier" ::: "memory")
#define LBARRIER() asm volatile("s_waitcnt lgkmcnt(0)\n\ts_barrier" ::: "memory")

__device__ __forceinline__ i32x4 mk_srd(const void* p, unsigned bytes) {
  unsigned long long a = (unsigned long long)p;
  i32x4 r;
  r.x = (int)(unsigned)a; r.y = (int)((unsigned)(a >> 32) & 0xffffu); r.z = (int)bytes; r.w = 0x00020000;
  return r;
}
__device__ __forceinline__ void dma16(unsigned lds, unsigned voff, i32x4 srd, unsigned soff) {
  asm volatile("s_mov_b32 m0, %0\n\ts_nop 0\n\tbuffer_load_dwordx4 %1, %2, %3 offen lds"
               ::"s"(lds), "v"(voff), "s"(srd), "s"(soff) : "memory");
}
__device__ __forceinline__ void dma16_masked(unsigned lds, unsigned voff, i32x4 srd, unsigned soff, unsigned long long mask) {
  asm volatile("s_mov_b32 m0, %0\n\ts_mov_b64 exec, %4\n\tbuffer_load_dwordx4 %1, %2, %3 offen lds\n\ts_mov_b64 exec, -1"
               ::"s"(lds), "v"(voff), "s"(srd), "s"(soff), "s"(mask) : "memory");
}
template <typename T> __device__ __forceinline__ T ldsr(unsigned a) { return *(LDS3 const T*)(uintptr_t)a; }
template <typename T> __device__ __forceinline__ void ldsw(unsigned a, T v) { *(LDS3 T*)(uintptr_t)a = v; }
__device__ __forceinline__ s16x4 ldstr(unsigned a) { return __builtin_amdgcn_ds_read_tr16_b64_v4i16((LDS3 s16x4*)(uintptr_t)a); }
__device__ __forceinline__ bf16x8 ldstr2(unsigned a0, unsigned a1) {
  s16x4 lo = ldstr(a0), hi = ldstr(a1);
  s16x8 r = __builtin_shufflevector(lo, hi, 0, 1, 2, 3, 4, 5, 6, 7);
  return __builtin_bit_cast(bf16x8, r);
}
__device__ __forceinline__ unsigned pk2(float a, float b) {
  f32x2 v = {a, b};
  return __builtin_bit_cast(unsigned, __builtin_convertvector(v, bf16x2));
}
__device__ __forceinline__ float bflo(unsigned v) { return __uint_as_float(v << 16); }
__device__ __forceinline__ float bfhi(unsigned v) { return __uint_as_float(v & 0xffff0000u); }
__device__ __forceinline__ s16x4 lo4(bf16x8 v) { s16x8 t = __builtin_bit_cast(s16x8, v); return __builtin_shufflevector(t, t, 0, 1, 2, 3); }
__device__ __forceinline__ s16x4 hi4(bf16x8 v) { s16x8 t = __builtin_bit_cast(s16x8, v); return __builtin_shufflevector(t, t, 4, 5, 6, 7); }
#define MFMA32(a, b, c) __builtin_amdgcn_mfma_f32_32x32x16_bf16(a, b, c, 0, 0, 0)
#define MFMA16(a, b, c) __builtin_amdgcn_mfma_f32_16x16x32_bf16(a, b, c, 0, 0, 0)
#define MFMA32K8(a, b, c) __builtin_amdgcn_mfma_f32_32x32x8bf16_1k(a, b, c, 0, 0, 0)
#define MFMA4(a, b, c) __builtin_amdgcn_mfma_f32_4x4x4bf16_1k(a, b, c, 0, 0, 0)

struct Frag1 { bf16x8 a[2], b[2]; };

template <bool SWAP>
__device__ __forceinline__ void k1_body(const u16* __restrict__ x, const u16* __restrict__ w, char* __restrict__ rec,
                                        unsigned L, int rt, int ct) {
  const int tid = threadIdx.x, lane = tid & 63, wave = __builtin_amdgcn_readfirstlane(tid >> 6);
  const int wm = wave >> 1, wn = wave & 1, r = lane & 31, hh = lane >> 5;
  const int m0 = rt * 128, n0 = ct * 120;
  TS(0, 0);
  const i32x4 sx = mk_srd(x, S_ * D_ * 2), sw = mk_srd(w, D_ * 1920 * 2);
  unsigned vA[4], vB[4];
#pragma unroll
  for (int i = 0; i < 4; ++i) {
    int row = (wave * 4 + i) * 8 + (lane >> 3);
    vA[i] = row * 1280 + (((lane & 7) ^ ((row >> 1) & 7)) << 4);
    int k = (wave * 4 + i) * 4 + (lane >> 4);
    vB[i] = k * 3840 + (((lane & 15) ^ ((k & 3) << 2)) << 4);
  }
  const unsigned long long bmask = ~((1ull << 15) | (1ull << 27) | (1ull << 39) | (1ull << 51));
  const unsigned oA = m0 * 1280, oB = n0 * 2;
  auto issueA = [&](int s, int i) { dma16(L + (s & 3) * 32768 + (wave * 4 + i) * 1024, vA[i], sx, oA + s * 128); };
  auto issueB = [&](int s, int i) { dma16_masked(L + (s & 3) * 32768 + 16384 + (wave * 4 + i) * 1024, vB[i], sw, oB + s * (64 * 3840), bmask); };
#pragma unroll
  for (int s = 0; s < 4; ++s) {
#pragma unroll
    for (int i = 0; i < 4; ++i) { issueA(s, i); issueB(s, i); }
  }
  {
    int k = tid & 63;
    ldsw<u32x4>(L + wave * 32768 + 16384 + k * 256 + ((15 ^ ((k & 3) << 2)) << 4), u32x4{0x3f80u, 0u, 0u, 0u});
  }
  unsigned aA[2][4], aB[2];
#pragma unroll
  for (int i = 0; i < 2; ++i) {
    int row = wm * 64 + ((i ^ wn) * 32) + r;
#pragma unroll
    for (int ks = 0; ks < 4; ++ks) aA[i][ks] = L + row * 128 + (((2 * ks + hh) ^ ((row >> 1) & 7)) << 4);
    int q = (lane >> 2) & 3, p = lane & 3, ch = (lane >> 4) & 1;
    int c = wn * 8 + ((i ^ wm) * 4) + 2 * ch + (p >> 1);
    aB[i] = L + 16384 + (8 * hh + q) * 256 + ((c ^ (q << 2)) << 4) + 8 * (p & 1);
  }
  auto rd = [&](Frag1& f, unsigned sl, int ks) {
    f.a[0] = ldsr<bf16x8>(aA[0][ks] + sl);
    f.b[0] = ldstr2(aB[0] + sl + ks * 4096, aB[0] + sl + ks * 4096 + 1024);
    f.a[1] = ldsr<bf16x8>(aA[1][ks] + sl);
    f.b[1] = ldstr2(aB[1] + sl + ks * 4096, aB[1] + sl + ks * 4096 + 1024);
  };
  f32x16 acc[2][2] = {};
  f32x4 sq0 = {}, sq1 = {}, cs0 = {}, cs1 = {};
  const s16x4 ones = {0x3f80, 0x3f80, 0x3f80, 0x3f80};
  auto mm = [&](const Frag1& f, int t) {
    const int i = t >> 1, j = t & 1;
    acc[i][j] = SWAP ? MFMA32(f.b[j], f.a[i], acc[i][j]) : MFMA32(f.a[i], f.b[j], acc[i][j]);
    if (t == 0) sq0 = MFMA4(lo4(f.a[0]), lo4(f.a[0]), sq0);
    if (t == 1) cs0 = MFMA4(ones, lo4(f.b[0]), cs0);
    if (t == 2) sq1 = MFMA4(hi4(f.a[0]), hi4(f.a[0]), sq1);
    if (t == 3) cs1 = MFMA4(ones, hi4(f.b[0]), cs1);
  };
  Frag1 f0, f1;
  WAIT_VL(24); BARRIER();
  TS(0, 1);
  rd(f0, 0, 0);
  for (int s = 0; s < 10; ++s) {
    const unsigned sl = (s & 3) * 32768, nsl = ((s + 1) & 3) * 32768;
    SB(); rd(f1, sl, 1); SB(); mm(f0, 0); mm(f0, 1); mm(f0, 2); mm(f0, 3);
    SB(); rd(f0, sl, 2); SB(); mm(f1, 0); mm(f1, 1); mm(f1, 2); mm(f1, 3);
    SB(); rd(f1, sl, 3); SB(); mm(f0, 0); mm(f0, 1); mm(f0, 2); mm(f0, 3);
    SB();
    if (s < 9) {
      if (s < 7) WAIT_VL(16); else if (s == 7) WAIT_VL(8); else WAIT_VL(0);
      BARRIER();
      rd(f0, nsl, 0);
    }
    SB();
    mm(f1, 0); SB();
    if (s < 6) { issueA(s + 4, 0); issueB(s + 4, 0); issueA(s + 4, 1); }
    SB(); mm(f1, 1); SB();
    if (s < 6) { issueB(s + 4, 1); issueA(s + 4, 2); issueB(s + 4, 2); }
    SB(); mm(f1, 2); SB();
    if (s < 6) { issueA(s + 4, 3); issueB(s + 4, 3); }
    SB(); mm(f1, 3);
  }
  SB();
  TS(0, 2);
  const unsigned T = L + 131072;
  {
    f32x4 sq = sq0 + sq1;
    int e = lane & 3;
    float sv = e == 0 ? sq[0] : e == 1 ? sq[1] : e == 2 ? sq[2] : sq[3];
    ldsw<float>(T + hh * 512 + (wm * 64 + wn * 32 + r) * 4, sv);
    ldsw<float>(T + 1024 + hh * 512 + (wn * 64 + wm * 32 + r) * 4, cs0[0] + cs1[0]);
    if (wn == 1) {
#pragma unroll
      for (int i = 0; i < 2; ++i) {
        const int rb = wm * 64 + (i ^ 1) * 32;
        if (SWAP) {
          float v = wm ? acc[i][0][12] : acc[i][1][12];
          if (hh == 0) ldsw<float>(T + 2048 + (rb + r) * 4, v);
        } else if (r == 24) {
#pragma unroll
          for (int g = 0; g < 4; ++g) {
            f32x4 v;
#pragma unroll
            for (int e2 = 0; e2 < 4; ++e2) v[e2] = wm ? acc[i][0][4 * g + e2] : acc[i][1][4 * g + e2];
            ldsw<f32x4>(T + 2048 + (rb + 8 * g + 4 * hh) * 4, v);
          }
        }
      }
    }
  }
  LBARRIER();
  if (tid < 128) {
    float sum = ldsr<float>(T + 2048 + tid * 4);
    float sq = ldsr<float>(T + tid * 4) + ldsr<float>(T + 512 + tid * 4);
    float mean = sum * (1.f / D_);
    float var = fmaxf(sq * (1.f / D_) - mean * mean, 0.f);
    float rs = __builtin_amdgcn_rsqf(var + LN_EPS);
    if (SWAP) ldsw<f32x2>(T + 2560 + tid * 8, f32x2{rs, -rs * mean});
    else { ldsw<float>(T + 2560 + tid * 4, rs); ldsw<float>(T + 3072 + tid * 4, -rs * mean); }
  } else {
    int c = tid - 128;
    ldsw<float>(T + 3584 + c * 4, ldsr<float>(T + 1024 + c * 4) + ldsr<float>(T + 1536 + c * 4));
  }
  LBARRIER();
  if (SWAP) {
    const float qs = 0.22360679774997896f * 1.4426950408889634f;
    f32x2 ab[2];
#pragma unroll
    for (int i = 0; i < 2; ++i) ab[i] = ldsr<f32x2>(T + 2560 + (wm * 64 + (i ^ wn) * 32 + r) * 8);
#pragma unroll
    for (int j = 0; j < 2; ++j) {
#pragma unroll
      for (int g = 0; g < 4; ++g) {
        const int lc = wn * 64 + (j ^ wm) * 32 + 8 * g + 4 * hh;
        if (lc < 120) {
          const f32x4 cs = ldsr<f32x4>(T + 3584 + lc * 4);
          const int qd_all = (n0 + lc) >> 2;
          const int hall = (qd_all * 205) >> 10, qd = qd_all - hall * 5;
          const float sc = hall < 32 ? qs : 1.f;
#pragma unroll
          for (int i = 0; i < 2; ++i) {
            const int tile = rt * 4 + wm * 2 + (i ^ wn);
            const float a = ab[i].x * sc, b = ab[i].y * sc;
            float y0 = a * acc[i][j][4 * g + 0] + b * cs[0], y1 = a * acc[i][j][4 * g + 1] + b * cs[1];
            float y2 = a * acc[i][j][4 * g + 2] + b * cs[2], y3 = a * acc[i][j][4 * g + 3] + b * cs[3];
            u32x2 v = {pk2(y0, y1), pk2(y2, y3)};
            if (hall < 64) {
              unsigned off = qd < 4 ? (qd >> 1) * 512 + r * 16 + (qd & 1) * 8 : 1024 + r * 8;
              *(u32x2*)(rec + (size_t)(hall * 64 + tile) * REC + off) = v;
            } else {
              const int k16 = r & 15;
              char* p = rec + (size_t)((hall - 32) * 64 + tile) * REC + 1280 + (r >> 4) * 672 + ((k16 >> 2) & 1) * 336 +
                        ((k16 & 3) + 4 * (k16 >> 3)) * 2 + qd * 64;
              *(u16*)(p) = (u16)v.x; *(u16*)(p + 16) = (u16)(v.x >> 16);
              *(u16*)(p + 32) = (u16)v.y; *(u16*)(p + 48) = (u16)(v.y >> 16);
              if (qd == 4) *(u16*)(p + 64) = 0x3f80;
            }
          }
        }
      }
    }
  } else {
#pragma unroll
    for (int i = 0; i < 2; ++i) {
      const int rb = wm * 64 + (i ^ wn) * 32, tile = rt * 4 + wm * 2 + (i ^ wn);
      f32x4 av[4], bv[4];
#pragma unroll
      for (int g = 0; g < 4; ++g) {
        av[g] = ldsr<f32x4>(T + 2560 + (rb + 8 * g + 4 * hh) * 4);
        bv[g] = ldsr<f32x4>(T + 3072 + (rb + 8 * g + 4 * hh) * 4);
      }
#pragma unroll
      for (int j = 0; j < 2; ++j) {
        const int lc = wn * 64 + (j ^ wm) * 32 + r;
        if (lc < 120) {
          const float cs = ldsr<float>(T + 3584 + lc * 4);
          const int gc = n0 + lc - 1280;
          const int hd = (gc * 205) >> 12, d = gc - hd * 20;
          u32x4 v[2];
#pragma unroll
          for (int g = 0; g < 4; ++g) {
            float y0 = av[g][0] * acc[i][j][4 * g + 0] + bv[g][0] * cs, y1 = av[g][1] * acc[i][j][4 * g + 1] + bv[g][1] * cs;
            float y2 = av[g][2] * acc[i][j][4 * g + 2] + bv[g][2] * cs, y3 = av[g][3] * acc[i][j][4 * g + 3] + bv[g][3] * cs;
            v[g >> 1][(g & 1) * 2] = pk2(y0, y1); v[g >> 1][(g & 1) * 2 + 1] = pk2(y2, y3);
          }
          char* p = rec + (size_t)((32 + hd) * 64 + tile) * REC + 1280 + hh * 336 + d * 16;
          *(u32x4*)p = v[0]; *(u32x4*)(p + 672) = v[1];
          if (d == 19) {
            const u32x4 one = {0x3f803f80u, 0x3f803f80u, 0x3f803f80u, 0x3f803f80u};
            *(u32x4*)(p + 16) = one; *(u32x4*)(p + 672 + 16) = one;
          }
        }
      }
    }
  }
}
constexpr bool chk_div() {
  for (int i = 0; i < 480; ++i) if (((i * 205) >> 10) != i / 5) return false;
  for (int i = 0; i < 640; ++i) if (((i * 205) >> 12) != i / 20) return false;
  return true;
}
static_assert(chk_div(), "magic division");

__global__ __launch_bounds__(256, 1) void k1_qkv(const u16* __restrict__ x, const u16* __restrict__ w, char* __restrict__ ws) {
  __shared__ __attribute__((aligned(1024))) char smem[131072 + 4096];
  const unsigned L = (unsigned)(uintptr_t)(LDS3 char*)smem;
  const int b = blockIdx.x, xcd = b & 7, i = b >> 3;
  const int rt = (xcd >> 1) * 4 + (i & 3), ct = (xcd & 1) * 8 + (i >> 2);
  { unsigned long long pc; asm volatile("s_getpc_b64 %0" : "=s"(pc));
    i32x4 sc = mk_srd((const void*)(pc & ~255ull), 44032);
    const int wave = __builtin_amdgcn_readfirstlane(threadIdx.x >> 6);
    for (int j = 0; j < 3; ++j)
      asm volatile("s_mov_b32 m0, %0\n\ts_nop 0\n\tbuffer_load_dword %1, %2, %3 offen lds" ::"s"(L + 131072 + wave * 256), "v"((threadIdx.x & 63) * 64), "s"(sc), "s"((wave * 3 + j) * 4096) : "memory");
  }
  if (ct <= 10) k1_body<true>(x, w, ws + WS_REC, L, rt, ct);
  else k1_body<false>(x, w, ws + WS_REC, L, rt, ct);
  TS(0, 3);
}

constexpr int K2_STAGE = 8 * REC;

__device__ __forceinline__ void exp4(const f32x16& s, int g, unsigned* p) {
  float e0 = __builtin_amdgcn_exp2f(s[4 * g + 0]), e1 = __builtin_amdgcn_exp2f(s[4 * g + 1]);
  float e2 = __builtin_amdgcn_exp2f(s[4 * g + 2]), e3 = __builtin_amdgcn_exp2f(s[4 * g + 3]);
  p[0] = pk2(e0, e1); p[1] = pk2(e2, e3);
}
__device__ __forceinline__ bf16x8 px8(const unsigned* p) { u32x4 v = {p[0], p[1], p[2], p[3]}; return __builtin_bit_cast(bf16x8, v); }
__device__ __forceinline__ float max16(const f32x16& s) {
  float m = fmaxf(fmaxf(fmaxf(s[0], s[1]), fmaxf(s[2], s[3])), fmaxf(fmaxf(s[4], s[5]), fmaxf(s[6], s[7])));
  float n = fmaxf(fmaxf(fmaxf(s[8], s[9]), fmaxf(s[10], s[11])), fmaxf(fmaxf(s[12], s[13]), fmaxf(s[14], s[15])));
  m = fmaxf(m, n);
  return fmaxf(m, __shfl_xor(m, 32));
}

__global__ __launch_bounds__(512, 2) void k2_attn(char* __restrict__ ws, const u16* __restrict__ wg, const u16* __restrict__ wu) {
  __shared__ __attribute__((aligned(1024))) char smem[3 * K2_STAGE + 16384];
  const unsigned L = (unsigned)(uintptr_t)(LDS3 char*)smem;
  const int tid = threadIdx.x, lane = tid & 63, wave = __builtin_amdgcn_readfirstlane(tid >> 6);
  const int b = blockIdx.x, xcd = b & 7, bi = b >> 3;
  const int head = xcd * 4 + (bi & 3), qb = bi >> 2;
  const int r = lane & 31, hh = lane >> 5;
  const char* rec = ws + WS_REC;
  const i32x4 skv = mk_srd(rec + (size_t)(32 + head) * REC_HEAD, REC_HEAD);
  const unsigned vL = lane * 16;
  auto issue = [&](int s, unsigned slot) {
#pragma unroll
    for (int i = 0; i < 3; ++i) {
      int p = wave * 3 + i; p = p > 20 ? 20 : p;
      dma16(slot + p * 1024, vL, skv, s * K2_STAGE + p * 1024);
    }
  };
  TS(1, 0);
  issue(0, L);
  const char* qrec = rec + (size_t)(head * 64 + qb * 8 + wave) * REC;
  const bf16x8 qlo = *(const bf16x8*)(qrec + hh * 512 + r * 16);
  u32x2 qh = *(const u32x2*)(qrec + 1024 + r * 8);
  if (hh) qh = u32x2{0u, 0u};
  const s16x4 qhi = __builtin_bit_cast(s16x4, qh);
  WAIT_VL(0); BARRIER();
  TS(1, 1);
  issue(1, L + K2_STAGE);
  const int kr = b / 48, cg = b - kr * 48;
  if (b < 240) {
    const i32x4 sw = mk_srd(cg < 24 ? wg : wu, D_ * F_ * 2);
    const int c0 = (cg < 24 ? cg : cg - 24) * 64;
#pragma unroll
    for (int i = 0; i < 2; ++i) {
      int p = wave * 2 + i;
      dma16(L + 3 * K2_STAGE + p * 1024, (lane >> 3) * 3072 + (lane & 7) * 16, sw, (kr * 128 + p * 8) * 3072 + c0 * 2);
    }
  }
  const unsigned aK = hh * 512 + r * 16, aKh = 1024 + r * 8;
  const unsigned aV = 1280 + hh * 336 + (r <= 20 ? r : (r <= 27 ? 20 : 19)) * 16;
  f32x16 negm;
  bf16x8 klo = ldsr<bf16x8>(L + aK);
  s16x4 khi = ldsr<s16x4>(L + aKh);
  {
    f32x16 z = {};
    f32x16 s0 = MFMA32(klo, qlo, z);
    s0 = MFMA32K8(khi, qhi, s0);
    float m = -max16(s0);
#pragma unroll
    for (int i = 0; i < 16; ++i) negm[i] = m;
  }
  f32x16 sc = MFMA32(klo, qlo, negm), sn;
  sc = MFMA32K8(khi, qhi, sc);
  klo = ldsr<bf16x8>(L + aK + REC);
  khi = ldsr<s16x4>(L + aKh + REC);
  f32x16 o = {};
  unsigned p[8] = {};
  bf16x8 v0, v1 = {};
  unsigned cur = L, nxt = L + K2_STAGE, fre = L + 2 * K2_STAGE;
  for (int s = 0; s < 8; ++s) {
    const unsigned cK = cur + aK, cKh = cur + aKh, cV = cur + aV, nK = nxt + aK, nKh = nxt + aKh;
#pragma unroll
    for (int t = 0; t < 8; ++t) {
      f32x16& S = (t & 1) ? sn : sc;
      f32x16& N = (t & 1) ? sc : sn;
      SB();
      v0 = ldsr<bf16x8>(cV + t * REC);
      o = MFMA32(v1, px8(p + 4), o);
      exp4(S, 0, p);
      SB();
      v1 = ldsr<bf16x8>(cV + t * REC + 672);
      N = MFMA32(klo, qlo, negm);
      exp4(S, 1, p + 2);
      SB();
      N = MFMA32K8(khi, qhi, N);
      exp4(S, 2, p + 4);
      SB();
      if (t == 6) {
        if (s < 7) {
          WAIT_VL(0); BARRIER();
          if (s < 6) issue(s + 2, fre);
        }
      }
      if (t < 6) { klo = ldsr<bf16x8>(cK + (t + 2) * REC); khi = ldsr<s16x4>(cKh + (t + 2) * REC); }
      else { klo = ldsr<bf16x8>(nK + (t - 6) * REC); khi = ldsr<s16x4>(nKh + (t - 6) * REC); }
      o = MFMA32(v0, px8(p), o);
      exp4(S, 3, p + 6);
    }
    SB();
    if (s == 3 && wave < 4 && b < 240) {
      const unsigned a0 = L + 3 * K2_STAGE + (8 * (lane >> 4) + ((lane >> 2) & 3)) * 128 + wave * 32 + (lane & 3) * 8;
      const s16x8 one8 = {0x3f80, 0x3f80, 0x3f80, 0x3f80, 0x3f80, 0x3f80, 0x3f80, 0x3f80};
      f32x4 c = {};
#pragma unroll
      for (int ks = 0; ks < 4; ++ks) c = MFMA16(__builtin_bit_cast(bf16x8, one8), ldstr2(a0 + ks * 4096, a0 + ks * 4096 + 512), c);
      if (lane < 16) {
        const int col = (cg < 24 ? cg : cg - 24) * 64 + wave * 16 + lane;
        const int j = col / 96, idx = col - j * 96 + (cg < 24 ? 0 : 96);
        *(float*)(ws + WS_CS + j * 4096 + (kr * 192 + idx) * 4) = c[0];
      }
    }
    unsigned tmp = cur; cur = nxt; nxt = fre; fre = tmp;
  }
  SB();
  TS(1, 2);
  o = MFMA32(v1, px8(p + 4), o);
  float l = __shfl(o[8], r + 32);
  bool ok = l <= 0x1p100f;
#pragma unroll
  for (int i = 0; i < 12; ++i) ok = ok && (__builtin_fabsf(o[i]) <= 0x1p126f);
  if (__builtin_amdgcn_ballot_w64(!ok) != 0ull) {
    const char* kv = rec + (size_t)(32 + head) * REC_HEAD;
    float m = -__builtin_inff();
    o = f32x16{};
    for (int t = 0; t < 64; ++t) {
      const char* q = kv + t * REC;
      f32x16 z = {};
      f32x16 s = MFMA32(*(const bf16x8*)(q + aK), qlo, z);
      s = MFMA32K8(*(const s16x4*)(q + aKh), qhi, s);
      float mn = fmaxf(m, max16(s));
      float c = __builtin_amdgcn_exp2f(m - mn);
      m = mn;
#pragma unroll
      for (int i = 0; i < 16; ++i) { s[i] -= mn; o[i] *= c; }
      unsigned pp[8];
#pragma unroll
      for (int g = 0; g < 4; ++g) exp4(s, g, pp + 2 * g);
      o = MFMA32(*(const bf16x8*)(q + aV), px8(pp), o);
      o = MFMA32(*(const bf16x8*)(q + aV + 672), px8(pp + 4), o);
    }
    l = __shfl(o[8], r + 32);
  }
  const float inv = 1.f / l;
  const int row = qb * 256 + wave * 32 + r;
#pragma unroll
  for (int g = 0; g < 3; ++g) {
    if (g < 2 || hh == 0) {
      const int col = head * 20 + 8 * g + 4 * hh;
      u32x2 v = {pk2(o[4 * g] * inv, o[4 * g + 1] * inv), pk2(o[4 * g + 2] * inv, o[4 * g + 3] * inv)};
      *(u32x2*)(ws + WS_O + (size_t)(col >> 3) * 32768 + row * 16 + (col & 7) * 2) = v;
    }
  }
  TS(1, 3);
}

struct Frag3 { bf16x8 a[2], b[5]; };
constexpr int K3_STAGE = 16384 + 20480;

template <int KD, bool FIRST>
__device__ __forceinline__ void proj_body(const char* __restrict__ A, const u16* __restrict__ W, const char* __restrict__ R,
                                          char* __restrict__ C, char* __restrict__ stats, unsigned L) {
  constexpr int NS = KD / 128;
  const int tid = threadIdx.x, lane = tid & 63, wave = __builtin_amdgcn_readfirstlane(tid >> 6);
  const int b = blockIdx.x, xcd = b & 7, bi = b >> 3;
  const int rt = xcd * 4 + (bi & 3), ct = bi >> 2;
  const int m0 = rt * 64, n0 = ct * 80;
  const int wm = wave & 1, wk = wave >> 1;
  const i32x4 sa = mk_srd(A, S_ * KD * 2), sw = mk_srd(W, KD * D_ * 2), sr = mk_srd(R, S_ * D_ * 2);
  unsigned vB[5];
#pragma unroll
  for (int i = 0; i < 5; ++i) {
    int idx = (wave * 5 + i) * 64 + lane, pr = idx / 10, c = idx - pr * 10;
    vB[i] = (pr ^ (((pr >> 3) & 1) << 2)) * 1280 + c * 16;
  }
  const unsigned vL = lane * 16;
  auto issueA = [&](int s, int i) { dma16(L + (s & 3) * K3_STAGE + (wave * 4 + i) * 1024, vL, sa, (s * 16 + wave * 4 + i) * 32768 + m0 * 16); };
  auto issueB = [&](int s, int i) { dma16(L + (s & 3) * K3_STAGE + 16384 + (wave * 5 + i) * 1024, vB[i], sw, s * (128 * 1280) + n0 * 2); };
  auto issue = [&](int s) {
#pragma unroll
    for (int i = 0; i < 4; ++i) { issueA(s, i); issueB(s, i); }
    issueB(s, 4);
  };
  const unsigned RS = L + 4 * K3_STAGE;
  TS(FIRST ? 2 : 4, 0);
  issue(0);
#pragma unroll
  for (int i = 0; i < 3; ++i) {
    int p = wave * 3 + i; p = p > 9 ? 9 : p;
    if (FIRST) { int idx = p * 64 + lane, row = idx / 10, c = idx - row * 10; dma16(RS + p * 1024, row * 1280 + c * 16, sr, m0 * 1280 + n0 * 2); }
    else dma16(RS + p * 1024, vL, sr, (ct * 10 + p) * 32768 + m0 * 16);
  }
  issue(1); issue(2); issue(3);
  const int g = lane >> 4;
  const unsigned aA = L + (wk * 8 + g) * 1024 + (wm * 32 + (lane & 15)) * 16;
  unsigned aB[2];
#pragma unroll
  for (int t = 0; t < 2; ++t) aB[t] = L + 16384 + (wk * 64 + 8 * g + ((4 * t) ^ (4 * (g & 1))) + ((lane >> 2) & 3)) * 160 + (lane & 3) * 8;
  auto rd = [&](Frag3& f, unsigned sl, int ks) {
    f.a[0] = ldsr<bf16x8>(aA + sl + ks * 4096);
    f.a[1] = ldsr<bf16x8>(aA + sl + ks * 4096 + 256);
#pragma unroll
    for (int j = 0; j < 5; ++j) f.b[j] = ldstr2(aB[0] + sl + ks * 5120 + j * 32, aB[1] + sl + ks * 5120 + j * 32);
  };
  f32x4 acc[2][5] = {};
  auto mm = [&](const Frag3& f) {
#pragma unroll
    for (int j = 0; j < 5; ++j) { acc[0][j] = MFMA16(f.b[j], f.a[0], acc[0][j]); acc[1][j] = MFMA16(f.b[j], f.a[1], acc[1][j]); }
  };
  Frag3 f0, f1;
  WAIT_VL(30); BARRIER();
  TS(FIRST ? 2 : 4, 1);
  rd(f0, 0, 0);
  for (int s = 0; s < NS; ++s) {
    const unsigned sl = (s & 3) * K3_STAGE, nsl = ((s + 1) & 3) * K3_STAGE;
    SB(); rd(f1, sl, 1); SB(); mm(f0); SB();
    if (s < NS - 1) {
      if (s < NS - 3) WAIT_VL(18); else if (s == NS - 3) WAIT_VL(9); else WAIT_VL(0);
      BARRIER();
      if (s < NS - 4) issue(s + 4);
      rd(f0, nsl, 0);
    }
    SB(); mm(f1);
  }
  SB();
  TS(FIRST ? 2 : 4, 2);
  const unsigned X = L + (NS & 3) * K3_STAGE;
#pragma unroll
  for (int j = 0; j < 5; ++j) ldsw<f32x4>(X + (wave * 5 + j) * 1024 + lane * 16, wk ? acc[0][j] : acc[1][j]);
  LBARRIER();
  const int lr = wm * 32 + wk * 16 + (lane & 15), row = m0 + lr;
  float sum = 0.f, sq = 0.f;
#pragma unroll
  for (int j = 0; j < 5; ++j) {
    f32x4 v = (wk ? acc[1][j] : acc[0][j]) + ldsr<f32x4>(X + ((wave ^ 2) * 5 + j) * 1024 + lane * 16);
    u32x2 rr = FIRST ? ldsr<u32x2>(RS + lr * 160 + j * 32 + g * 8) : ldsr<u32x2>(RS + (2 * j + (g >> 1)) * 1024 + lr * 16 + (g & 1) * 8);
    u32x2 o = {pk2(v[0] + bflo(rr.x), v[1] + bfhi(rr.x)), pk2(v[2] + bflo(rr.y), v[3] + bfhi(rr.y))};
    if (FIRST) {
      *(u32x2*)(C + (size_t)(ct * 10 + 2 * j + (g >> 1)) * 32768 + row * 16 + (g & 1) * 8) = o;
      float h0 = bflo(o.x), h1 = bfhi(o.x), h2 = bflo(o.y), h3 = bfhi(o.y);
      sum += (h0 + h1) + (h2 + h3);
      sq += (h0 * h0 + h1 * h1) + (h2 * h2 + h3 * h3);
    } else {
      *(u32x2*)(C + (size_t)row * 1280 + (n0 + 16 * j + 4 * g) * 2) = o;
    }
  }
  if (FIRST) {
    sum += __shfl_xor(sum, 16); sq += __shfl_xor(sq, 16);
    sum += __shfl_xor(sum, 32); sq += __shfl_xor(sq, 32);
    if (lane < 16) *(f32x2*)(stats + row * 64 + ct * 8) = f32x2{sum, sq};
  }
  TS(FIRST ? 2 : 4, 3);
}

__global__ __launch_bounds__(256, 1) void k3_oproj(const u16* __restrict__ x, const u16* __restrict__ wo, char* __restrict__ ws) {
  __shared__ __attribute__((aligned(1024))) char smem[4 * K3_STAGE + 10240];
  proj_body<D_, true>(ws + WS_O, wo, (const char*)x, ws + WS_H1, ws + WS_ST, (unsigned)(uintptr_t)(LDS3 char*)smem);
}
__global__ __launch_bounds__(256, 1) void k5_down(const u16* __restrict__ wd, char* __restrict__ ws, u16* __restrict__ out) {
  __shared__ __attribute__((aligned(1024))) char smem[4 * K3_STAGE + 10240];
  proj_body<F_, false>(ws + WS_HH, wd, ws + WS_H1, (char*)out, nullptr, (unsigned)(uintptr_t)(LDS3 char*)smem);
}

struct Frag4 { bf16x8 a[2], b[3]; };
constexpr int K4_STAGE = 16384 + 24576;

__global__ __launch_bounds__(256, 1) void k4_gateup(const u16* __restrict__ wg, const u16* __restrict__ wu, char* __restrict__ ws) {
  __shared__ __attribute__((aligned(1024))) char smem[3 * K4_STAGE + 8192 + 4096 + 2048];
  const unsigned L = (unsigned)(uintptr_t)(LDS3 char*)smem;
  const int tid = threadIdx.x, lane = tid & 63, wave = __builtin_amdgcn_readfirstlane(tid >> 6);
  const int b = blockIdx.x, xcd = b & 7, bi = b >> 3;
  const int rt = (xcd >> 1) * 4 + (bi & 3), ct = (xcd & 1) * 8 + (bi >> 2);
  const int m0 = rt * 128, n0 = ct * 96;
  const int wm = wave >> 1, wn = wave & 1, r = lane & 31, hh = lane >> 5;
  const i32x4 sa = mk_srd(ws + WS_H1, S_ * D_ * 2), sg = mk_srd(wg, D_ * F_ * 2), su = mk_srd(wu, D_ * F_ * 2);
  const i32x4 sst = mk_srd(ws + WS_ST, S_ * 64 + 65536);
  unsigned vG[3], vU[3];
#pragma unroll
  for (int i = 0; i < 3; ++i) {
    int idx = (wave * 3 + i) * 64 + lane, pr = idx / 12, c = idx - pr * 12;
    vG[i] = pr * 3072 + c * 16;
    vU[i] = vG[i] ^ 32;
  }
  const unsigned vL = lane * 16;
  auto issueA = [&](int s, unsigned sl, int i) {
    const int p = wave * 4 + i;
    dma16(sl + p * 1024, vL, sa, (s * 8 + (p >> 1)) * 32768 + (m0 + (p & 1) * 64) * 16);
  };
  auto issueB = [&](int s, unsigned sl, int i) {
    dma16(sl + 16384 + (wave * 3 + i) * 1024, vG[i], sg, s * (64 * 3072) + n0 * 2);
    dma16(sl + 16384 + 12288 + (wave * 3 + i) * 1024, vU[i], su, s * (64 * 3072) + n0 * 2);
  };
  auto issue = [&](int s, unsigned sl) {
#pragma unroll
    for (int i = 0; i < 3; ++i) { issueA(s, sl, i); issueB(s, sl, i); }
    issueA(s, sl, 3);
  };
  const unsigned T = L + 3 * K4_STAGE;
  TS(3, 0);
  issue(0, L);
  dma16(T + wave * 2048, vL, sst, m0 * 64 + wave * 2048);
  dma16(T + wave * 2048 + 1024, vL, sst, m0 * 64 + wave * 2048 + 1024);
  dma16(T + 8192 + wave * 1024, vL, sst, S_ * 64 + ct * 4096 + wave * 1024);
  issue(1, L + K4_STAGE); issue(2, L + 2 * K4_STAGE);
  const unsigned aA = hh * 2048 + (wm * 64 + r) * 16;
  unsigned aB[3];
  {
    const int q = (lane >> 2) & 3, p = lane & 3, up = (lane >> 4) & 1;
#pragma unroll
    for (int j = 0; j < 3; ++j) aB[j] = 16384 + up * 12288 + (8 * hh + q) * 192 + (((wn * 3 + j) ^ up) * 32) + p * 8;
  }
  auto rd = [&](Frag4& f, unsigned sl, int ks) {
    f.a[0] = ldsr<bf16x8>(aA + sl + ks * 4096);
    f.a[1] = ldsr<bf16x8>(aA + sl + ks * 4096 + 512);
#pragma unroll
    for (int j = 0; j < 3; ++j) f.b[j] = ldstr2(aB[j] + sl + ks * 3072, aB[j] + sl + ks * 3072 + 768);
  };
  f32x16 acc[2][3] = {};
  auto mm = [&](const Frag4& f, int j) { acc[0][j] = MFMA32(f.b[j], f.a[0], acc[0][j]); acc[1][j] = MFMA32(f.b[j], f.a[1], acc[1][j]); };
  Frag4 f0, f1;
  WAIT_VL(23); BARRIER();
  TS(3, 1);
  rd(f0, L, 0);
  unsigned cur = L, nxt = L + K4_STAGE, n2 = L + 2 * K4_STAGE;
  for (int s = 0; s < 10; ++s) {
    SB(); rd(f1, cur, 1); SB(); mm(f0, 0); mm(f0, 1); mm(f0, 2);
    SB(); rd(f0, cur, 2); SB(); mm(f1, 0); mm(f1, 1); mm(f1, 2);
    SB(); rd(f1, cur, 3); SB(); mm(f0, 0); mm(f0, 1); mm(f0, 2);
    SB();
    if (s < 9) {
      if (s < 8) WAIT_VL(10); else WAIT_VL(0);
      BARRIER();
      rd(f0, nxt, 0);
    }
    SB();
    mm(f1, 0); SB();
    if (s < 7) { issueA(s + 3, cur, 0); issueB(s + 3, cur, 0); issueA(s + 3, cur, 1); issueA(s + 3, cur, 2); }
    SB(); mm(f1, 1); SB();
    if (s < 7) { issueB(s + 3, cur, 1); issueA(s + 3, cur, 3); issueB(s + 3, cur, 2); }
    SB(); mm(f1, 2); SB();
    if (s == 1) {
      if (tid < 128) {
        f32x4 t0 = ldsr<f32x4>(T + tid * 64), t1 = ldsr<f32x4>(T + tid * 64 + 16), t2 = ldsr<f32x4>(T + tid * 64 + 32), t3 = ldsr<f32x4>(T + tid * 64 + 48);
        f32x4 t = (t0 + t1) + (t2 + t3);
        float mean = (t[0] + t[2]) * (1.f / D_);
        float var = fmaxf((t[1] + t[3]) * (1.f / D_) - mean * mean, 0.f);
        float rs = __builtin_amdgcn_rsqf(var + LN_EPS);
        ldsw<f32x2>(T + 12288 + tid * 8, f32x2{rs, -rs * mean});
      } else if (tid < 128 + 96) {
        const unsigned a = T + 8192 + (tid - 128) * 8;
        f32x2 c = (ldsr<f32x2>(a) + ldsr<f32x2>(a + 768)) + (ldsr<f32x2>(a + 1536) + ldsr<f32x2>(a + 2304)) + ldsr<f32x2>(a + 3072);
        ldsw<f32x2>(T + 13312 + (tid - 128) * 8, c);
      }
    }
    unsigned tmp = cur; cur = nxt; nxt = n2; n2 = tmp;
  }
  SB();
  TS(3, 2);
  const float k1 = -2.f * 0.7978845608028654f * 1.4426950408889634f, k2 = k1 * 0.044715f;
#pragma unroll
  for (int j = 0; j < 3; ++j) {
    const int fc = (wn * 3 + j) * 16 + 4 * hh;
    f32x4 cg[2], cu[2];
#pragma unroll
    for (int g = 0; g < 2; ++g) { cg[g] = ldsr<f32x4>(T + 13312 + (fc + 8 * g) * 4); cu[g] = ldsr<f32x4>(T + 13312 + 384 + (fc + 8 * g) * 4); }
#pragma unroll
    for (int i = 0; i < 2; ++i) {
      const f32x2 ab = ldsr<f32x2>(T + 12288 + (wm * 64 + i * 32 + r) * 8);
      unsigned pk[4];
#pragma unroll
      for (int g = 0; g < 2; ++g) {
        float y[4];
#pragma unroll
        for (int e = 0; e < 4; ++e) {
          float gt = ab.x * acc[i][j][4 * g + e] + ab.y * cg[g][e];
          float up = ab.x * acc[i][j][8 + 4 * g + e] + ab.y * cu[g][e];
          float z = gt * (k1 + k2 * gt * gt);
          float sg2 = __builtin_amdgcn_rcpf(1.f + __builtin_amdgcn_exp2f(z));
          y[e] = gt * up * sg2;
        }
        pk[2 * g] = pk2(y[0], y[1]); pk[2 * g + 1] = pk2(y[2], y[3]);
      }
      auto s0 = __builtin_amdgcn_permlane32_swap(pk[0], pk[2], false, false);
      auto s1 = __builtin_amdgcn_permlane32_swap(pk[1], pk[3], false, false);
      u32x4 v = {s0[0], s1[0], s0[1], s1[1]};
      const int chunk = ct * 12 + (wn * 3 + j) * 2 + hh;
      *(u32x4*)(ws + WS_HH + (size_t)chunk * 32768 + (m0 + wm * 64 + i * 32 + r) * 16) = v;
    }
  }
  TS(3, 3);
}

extern "C" void kernel_launch(void* const* d_in, const int* in_sizes, int n_in, void* d_out, int out_size, void* d_ws,
                              size_t ws_size, hipStream_t stream) {
  const u16* x = (const u16*)d_in[0];
  const u16* wqkv = (const u16*)d_in[1];
  const u16* wo = (const u16*)d_in[2];
  const u16* wg = (const u16*)d_in[4];
  const u16* wu = (const u16*)d_in[5];
  const u16* wd = (const u16*)d_in[6];
  char* ws = (char*)d_ws;
  k1_qkv<<<256, 256, 0, stream>>>(x, wqkv, ws);
  k2_attn<<<256, 512, 0, stream>>>(ws, wg, wu);
  k3_oproj<<<256, 256, 0, stream>>>(x, wo, ws);
  k4_gateup<<<256, 256, 0, stream>>>(wg, wu, ws);
  k5_down<<<256, 256, 0, stream>>>(wd, ws, (u16*)d_out);
}
```

```cpp
#include <hip/hip_runtime.h>
#include <stdint.h>

typedef unsigned short u16;
typedef __bf16 bf16x8 __attribute__((ext_vector_type(8)));
typedef __bf16 bf16x2 __attribute__((ext_vector_type(2)));
typedef short s16x4 __attribute__((ext_vector_type(4)));
typedef short s16x8 __attribute__((ext_vector_type(8)));
typedef float f32x2 __attribute__((ext_vector_type(2)));
typedef float f32x4 __attribute__((ext_vector_type(4)));
typedef float f32x16 __attribute__((ext_vector_type(16)));
typedef int i32x4 __attribute__((ext_vector_type(4)));
typedef unsigned u32x2 __attribute__((ext_vector_type(2)));
typedef unsigned u32x4 __attribute__((ext_vector_type(4)));
#define LDS3 __attribute__((address_space(3)))

constexpr int S_ = 2048, D_ = 640, F_ = 1536;
constexpr int REC = 2688, REC_V = 1280, REC_VS = 704, REC_VH = 352;
constexpr int REC_HEAD = 64 * REC;
constexpr size_t WS_REC = 0;
constexpr size_t WS_O = WS_REC + (size_t)64 * REC_HEAD;
constexpr size_t WS_H1 = WS_O + (size_t)S_ * D_ * 2;
constexpr size_t WS_HH = WS_H1 + (size_t)S_ * D_ * 2;
constexpr size_t WS_ST = WS_HH + (size_t)S_ * F_ * 2;
constexpr size_t WS_CS = WS_ST + (size_t)S_ * 64;
constexpr float LN_EPS = 1e-5f;

#define TS(k, i)
#define SB() __builtin_amdgcn_sched_barrier(0)
#define WAIT_V(n) asm volatile("s_waitcnt vmcnt(%0)" ::"n"(n) : "memory")
#define WAIT_VL(n) asm volatile("s_waitcnt vmcnt(%0) lgkmcnt(0)" ::"n"(n) : "memory")
#define BARRIER() asm volatile("s_barrier" ::: "memory")
#define LBARRIER() asm volatile("s_waitcnt lgkmcnt(0)\n\ts_barrier" ::: "memory")

__device__ __forceinline__ i32x4 mk_srd(const void* p, unsigned bytes) {
  unsigned long long a = (unsigned long long)p;
  i32x4 r;
  r.x = (int)(unsigned)a; r.y = (int)((unsigned)(a >> 32) & 0xffffu); r.z = (int)bytes; r.w = 0x00020000;
  return r;
}
__device__ __forceinline__ void st16(u32x4 v, i32x4 srd, unsigned voff, unsigned soff) {
  asm volatile("buffer_store_dwordx4 %0, %1, %2, %3 offen\n\ts_nop 1" ::"v"(v), "v"(voff), "s"(srd), "s"(soff));
}
__device__ __forceinline__ void st8(u32x2 v, i32x4 srd, unsigned voff, unsigned soff) {
  asm volatile("buffer_store_dwordx2 %0, %1, %2, %3 offen" ::"v"(v), "v"(voff), "s"(srd), "s"(soff));
}
__device__ __forceinline__ void dma16(unsigned lds, unsigned voff, i32x4 srd, unsigned soff) {
  asm volatile("s_mov_b32 m0, %0\n\ts_nop 0\n\tbuffer_load_dwordx4 %1, %2, %3 offen lds"
               ::"s"(lds), "v"(voff), "s"(srd), "s"(soff) : "memory");
}
__device__ __forceinline__ void dma16_masked(unsigned lds, unsigned voff, i32x4 srd, unsigned soff, unsigned long long mask) {
  asm volatile("s_mov_b32 m0, %0\n\ts_mov_b64 exec, %4\n\tbuffer_load_dwordx4 %1, %2, %3 offen lds\n\ts_mov_b64 exec, -1"
               ::"s"(lds), "v"(voff), "s"(srd), "s"(soff), "s"(mask) : "memory");
}
template <typename T> __device__ __forceinline__ T ldsr(unsigned a) { return *(LDS3 const T*)(uintptr_t)a; }
template <typename T> __device__ __forceinline__ void ldsw(unsigned a, T v) { *(LDS3 T*)(uintptr_t)a = v; }
__device__ __forceinline__ s16x4 ldstr(unsigned a) { return __builtin_amdgcn_ds_read_tr16_b64_v4i16((LDS3 s16x4*)(uintptr_t)a); }
__device__ __forceinline__ bf16x8 cat8(s16x4 lo, s16x4 hi) {
  s16x8 r = __builtin_shufflevector(lo, hi, 0, 1, 2, 3, 4, 5, 6, 7);
  return __builtin_bit_cast(bf16x8, r);
}
__device__ __forceinline__ unsigned pk2(f32x2 v) { return __builtin_bit_cast(unsigned, __builtin_convertvector(v, bf16x2)); }
__device__ __forceinline__ unsigned pk2(float a, float b) { return pk2(f32x2{a, b}); }
__device__ __forceinline__ float bflo(unsigned v) { return __uint_as_float(v << 16); }
__device__ __forceinline__ float bfhi(unsigned v) { return __uint_as_float(v & 0xffff0000u); }
__device__ __forceinline__ s16x4 lo4(bf16x8 v) { s16x8 t = __builtin_bit_cast(s16x8, v); return __builtin_shufflevector(t, t, 0, 1, 2, 3); }
__device__ __forceinline__ s16x4 hi4(bf16x8 v) { s16x8 t = __builtin_bit_cast(s16x8, v); return __builtin_shufflevector(t, t, 4, 5, 6, 7); }
__device__ __forceinline__ f32x2 sub2(const f32x16& v, int i) { return f32x2{v[i], v[i + 1]}; }
__device__ __forceinline__ f32x2 sub2(const f32x4& v, int i) { return f32x2{v[i], v[i + 1]}; }
__device__ __forceinline__ f32x2 fma2(f32x2 a, f32x2 b, f32x2 c) { return __builtin_elementwise_fma(a, b, c); }
#define MFMA32(a, b, c) __builtin_amdgcn_mfma_f32_32x32x16_bf16(a, b, c, 0, 0, 0)
#define MFMA16(a, b, c) __builtin_amdgcn_mfma_f32_16x16x32_bf16(a, b, c, 0, 0, 0)
#define MFMA32K8(a, b, c) __builtin_amdgcn_mfma_f32_32x32x8bf16_1k(a, b, c, 0, 0, 0)
#define MFMA4(a, b, c) __builtin_amdgcn_mfma_f32_4x4x4bf16_1k(a, b, c, 0, 0, 0)

struct Frag1 { bf16x8 a[2]; s16x4 b[2][2]; };

template <bool SWAP>
__device__ __forceinline__ void k1_consumer(char* __restrict__ rec, unsigned L, int rt, int ct, int wave) {
  const int tid = threadIdx.x, lane = tid & 63;
  const int wm = wave >> 1, wn = wave & 1, r = lane & 31, hh = lane >> 5;
  unsigned aA[2][4], aB[2];
#pragma unroll
  for (int i = 0; i < 2; ++i) {
    int row = wm * 64 + ((i ^ wn) * 32) + r;
#pragma unroll
    for (int ks = 0; ks < 4; ++ks) aA[i][ks] = L + row * 128 + (((2 * ks + hh) ^ ((row >> 1) & 7)) << 4);
    int q = (lane >> 2) & 3, p = lane & 3, ch = (lane >> 4) & 1;
    int c = wn * 8 + ((i ^ wm) * 4) + 2 * ch + (p >> 1);
    aB[i] = L + 16384 + (8 * hh + q) * 256 + ((c ^ (q << 2)) << 4) + 8 * (p & 1);
  }
  auto rd = [&](Frag1& f, unsigned sl, int ks, int t) {
    if (t == 0) { f.a[0] = ldsr<bf16x8>(aA[0][ks] + sl); f.b[0][0] = ldstr(aB[0] + sl + ks * 4096); }
    if (t == 1) { f.b[0][1] = ldstr(aB[0] + sl + ks * 4096 + 1024); f.b[1][0] = ldstr(aB[1] + sl + ks * 4096); }
    if (t == 2) { f.b[1][1] = ldstr(aB[1] + sl + ks * 4096 + 1024); f.a[1] = ldsr<bf16x8>(aA[1][ks] + sl); }
  };
  f32x16 acc[2][2] = {};
  f32x4 sq0 = {}, sq1 = {}, cs0 = {}, cs1 = {};
  const s16x4 ones = {0x3f80, 0x3f80, 0x3f80, 0x3f80};
  auto mm = [&](const Frag1& f, int t) {
    const int i = t >> 1, j = t & 1;
    const bf16x8 b = cat8(f.b[j][0], f.b[j][1]);
    acc[i][j] = SWAP ? MFMA32(b, f.a[i], acc[i][j]) : MFMA32(f.a[i], b, acc[i][j]);
    if (t == 0) sq0 = MFMA4(lo4(f.a[0]), lo4(f.a[0]), sq0);
    if (t == 1) cs0 = MFMA4(ones, f.b[0][0], cs0);
    if (t == 2) sq1 = MFMA4(hi4(f.a[0]), hi4(f.a[0]), sq1);
    if (t == 3) cs1 = MFMA4(ones, f.b[0][1], cs1);
  };
  auto kstep = [&](const Frag1& f, Frag1& n, unsigned sl, int ks) {
    mm(f, 0); SB(); rd(n, sl, ks, 0); SB(); mm(f, 1); SB(); rd(n, sl, ks, 1); SB(); mm(f, 2); SB(); rd(n, sl, ks, 2); SB(); mm(f, 3); SB();
  };
  Frag1 f0, f1;
  BARRIER(); BARRIER();
  TS(0, 1);
  rd(f0, 0, 0, 0); rd(f0, 0, 0, 1); rd(f0, 0, 0, 2);
  SB();
  for (int s = 0; s < 10; ++s) {
    const unsigned sl = (s & 3) * 32768, nsl = ((s + 1) & 3) * 32768;
    kstep(f0, f1, sl, 1);
    kstep(f1, f0, sl, 2);
    kstep(f0, f1, sl, 3);
    mm(f1, 0); SB();
    if (s < 9) LBARRIER();
    SB(); rd(f0, nsl, 0, 0); SB(); mm(f1, 1); SB(); rd(f0, nsl, 0, 1); SB(); mm(f1, 2); SB(); rd(f0, nsl, 0, 2); SB(); mm(f1, 3); SB();
  }
  TS(0, 2);
  const unsigned T = L + 131072;
  {
    f32x4 sq = sq0 + sq1;
    int e = lane & 3;
    float sv = e == 0 ? sq[0] : e == 1 ? sq[1] : e == 2 ? sq[2] : sq[3];
    ldsw<float>(T + hh * 512 + (wm * 64 + wn * 32 + r) * 4, sv);
    ldsw<float>(T + 1024 + hh * 512 + (wn * 64 + wm * 32 + r) * 4, cs0[0] + cs1[0]);
    if (wn == 1) {
#pragma unroll
      for (int i = 0; i < 2; ++i) {
        const int rb = wm * 64 + (i ^ 1) * 32;
        if (SWAP) {
          float v = wm ? acc[i][0][12] : acc[i][1][12];
          if (hh == 0) ldsw<float>(T + 2048 + (rb + r) * 4, v);
        } else if (r == 24) {
#pragma unroll
          for (int g = 0; g < 4; ++g) {
            f32x4 v;
#pragma unroll
            for (int e2 = 0; e2 < 4; ++e2) v[e2] = wm ? acc[i][0][4 * g + e2] : acc[i][1][4 * g + e2];
            ldsw<f32x4>(T + 2048 + (rb + 8 * g + 4 * hh) * 4, v);
          }
        }
      }
    }
  }
  LBARRIER();
  if (tid < 128) {
    float sum = ldsr<float>(T + 2048 + tid * 4);
    float sq = ldsr<float>(T + tid * 4) + ldsr<float>(T + 512 + tid * 4);
    float mean = sum * (1.f / D_);
    float var = fmaxf(sq * (1.f / D_) - mean * mean, 0.f);
    float rs = __builtin_amdgcn_rsqf(var + LN_EPS);
    if (SWAP) ldsw<f32x2>(T + 2560 + tid * 8, f32x2{rs, -mean});
    else { ldsw<float>(T + 2560 + tid * 4, rs); ldsw<float>(T + 3072 + tid * 4, -mean); }
  } else {
    int c = tid - 128;
    ldsw<float>(T + 3584 + c * 4, ldsr<float>(T + 1024 + c * 4) + ldsr<float>(T + 1536 + c * 4));
  }
  LBARRIER();
  const i32x4 ro = mk_srd(rec, 64 * REC_HEAD);
  const u32x4 one4 = {0x3f803f80u, 0x3f803f80u, 0x3f803f80u, 0x3f803f80u}, zero4 = {};
  const int tile0 = rt * 4 + wm * 2;
  if (SWAP) {
    const float qs = 0.22360679774997896f * 1.4426950408889634f;
    f32x2 ab[2];
#pragma unroll
    for (int i = 0; i < 2; ++i) ab[i] = ldsr<f32x2>(T + 2560 + (wm * 64 + (i ^ wn) * 32 + r) * 8);
    const unsigned vlo = (hh ^ wn) * REC + r * 16;
    const unsigned vhi = hh * REC_HEAD + 1024 + r * 8;
    const unsigned X = L + wave * 8192;
#pragma unroll
    for (int j = 0; j < 2; ++j) {
#pragma unroll
      for (int g = 0; g < 4; ++g) {
        const int P = wn * 8 + (j ^ wm) * 4 + g;
        if (P < 15) {
          const int hp = (P * 13) >> 6, t = P - hp * 5, hA = ct * 6 + 2 * hp, od = (t + 1) >> 2;
          const f32x4 cs = ldsr<f32x4>(T + 3584 + (P * 8 + 4 * hh) * 4);
          const float sc = hA < 32 ? qs : 1.f;
          u32x2 v[2];
#pragma unroll
          for (int i = 0; i < 2; ++i) {
            const f32x2 a = {ab[i].x * sc, ab[i].x * sc}, nm = {ab[i].y, ab[i].y};
            v[i].x = pk2(fma2(nm, sub2(cs, 0), sub2(acc[i][j], 4 * g)) * a);
            v[i].y = pk2(fma2(nm, sub2(cs, 2), sub2(acc[i][j], 4 * g + 2)) * a);
          }
          if (hA >= 64) {
#pragma unroll
            for (int i = 0; i < 2; ++i) ldsw<u32x2>(X + ((i ^ wn) * 32 + r) * 104 + (t * 8 + 4 * hh) * 2, v[i]);
          } else if (t == 2) {
#pragma unroll
            for (int i = 0; i < 2; ++i) st8(v[i], ro, vhi, hA * REC_HEAD + (tile0 + (i ^ wn)) * REC);
          } else {
            auto s0 = __builtin_amdgcn_permlane32_swap(v[0].x, v[1].x, false, false);
            auto s1 = __builtin_amdgcn_permlane32_swap(v[0].y, v[1].y, false, false);
            st16(u32x4{s0[0], s1[0], s0[1], s1[1]}, ro, vlo, (hA + od) * REC_HEAD + tile0 * REC + (t - 3 * od) * 512);
          }
        }
      }
    }
    if (ct == 10 && wn == 1) {
      const int g4 = lane >> 4, c16 = lane & 15;
      const unsigned ra = X + (16 * (g4 >> 1) + 4 * (g4 & 1) + ((lane >> 2) & 3)) * 104 + (lane & 3) * 8;
#pragma unroll
      for (int mb = 0; mb < 2; ++mb) {
#pragma unroll
        for (int c = 0; c < 3; ++c) {
          s16x4 lo = ldstr(ra + mb * 32 * 104 + c * 32), hi = ldstr(ra + mb * 32 * 104 + 8 * 104 + c * 32);
          const int col = c * 16 + c16, hd = col >= 20, d = col - 20 * hd;
          const unsigned vo = hd * REC_HEAD + (g4 >> 1) * REC_VS + (g4 & 1) * REC_VH + d * 16;
          const unsigned so = 32 * REC_HEAD + (tile0 + mb) * REC + REC_V;
          if (col < 40) {
            st16(__builtin_bit_cast(u32x4, cat8(lo, hi)), ro, vo, so);
            if (d == 19) { st16(one4, ro, vo + 16, so); st16(zero4, ro, vo + 32, so); }
          }
        }
      }
    }
  } else {
#pragma unroll
    for (int i = 0; i < 2; ++i) {
      const int rb = wm * 64 + (i ^ wn) * 32;
      f32x4 av[4], bv[4];
#pragma unroll
      for (int g = 0; g < 4; ++g) {
        av[g] = ldsr<f32x4>(T + 2560 + (rb + 8 * g + 4 * hh) * 4);
        bv[g] = ldsr<f32x4>(T + 3072 + (rb + 8 * g + 4 * hh) * 4);
      }
#pragma unroll
      for (int j = 0; j < 2; ++j) {
        const int lc = wn * 64 + (j ^ wm) * 32 + r;
        if (lc < 120) {
          const float c1 = ldsr<float>(T + 3584 + lc * 4);
          const f32x2 cs = {c1, c1};
          const int gc = ct * 120 + lc - 1280;
          const int hd = (gc * 205) >> 12, d = gc - hd * 20;
          u32x4 v[2];
#pragma unroll
          for (int g = 0; g < 4; ++g) {
            v[g >> 1][(g & 1) * 2] = pk2(fma2(sub2(bv[g], 0), cs, sub2(acc[i][j], 4 * g)) * sub2(av[g], 0));
            v[g >> 1][(g & 1) * 2 + 1] = pk2(fma2(sub2(bv[g], 2), cs, sub2(acc[i][j], 4 * g + 2)) * sub2(av[g], 2));
          }
          const unsigned vo = hd * REC_HEAD + hh * REC_VH + d * 16, so = 32 * REC_HEAD + (tile0 + (i ^ wn)) * REC + REC_V;
          st16(v[0], ro, vo, so); st16(v[1], ro, vo + REC_VS, so);
          if (d == 19) {
            st16(one4, ro, vo + 16, so); st16(zero4, ro, vo + 32, so);
            st16(one4, ro, vo + REC_VS + 16, so); st16(zero4, ro, vo + REC_VS + 32, so);
          }
        }
      }
    }
  }
  TS(0, 3);
}
constexpr bool chk_div() {
  for (int i = 0; i < 640; ++i) if (((i * 205) >> 12) != i / 20) return false;
  return true;
}
static_assert(chk_div(), "magic division");

__global__ __launch_bounds__(512, 2) void k1_qkv(const u16* __restrict__ x, const u16* __restrict__ w, char* __restrict__ ws) {
  __shared__ __attribute__((aligned(1024))) char smem[131072 + 4096];
  const unsigned L = (unsigned)(uintptr_t)(LDS3 char*)smem;
  const int b = blockIdx.x, xcd = b & 7, bi = b >> 3;
  const int rt = (xcd >> 1) * 4 + (bi & 3), ct = (xcd & 1) * 8 + (bi >> 2);
  const int lane = threadIdx.x & 63, wave = __builtin_amdgcn_readfirstlane(threadIdx.x >> 6);
  TS(0, 0);
  if (wave >= 4) {
    const int pw = wave - 4;
    const i32x4 sx = mk_srd(x, S_ * D_ * 2), sw = mk_srd(w, D_ * 1920 * 2);
    unsigned vA[4], vB[4];
#pragma unroll
    for (int i = 0; i < 4; ++i) {
      int row = (pw * 4 + i) * 8 + (lane >> 3);
      vA[i] = row * 1280 + (((lane & 7) ^ ((row >> 1) & 7)) << 4);
      int k = (pw * 4 + i) * 4 + (lane >> 4);
      vB[i] = k * 3840 + (((lane & 15) ^ ((k & 3) << 2)) << 4);
    }
    const unsigned long long bmask = ~((1ull << 15) | (1ull << 27) | (1ull << 39) | (1ull << 51));
    const unsigned oA = rt * 128 * 1280, oB = ct * 240;
    auto issue = [&](int s) {
#pragma unroll
      for (int i = 0; i < 4; ++i) {
        dma16(L + (s & 3) * 32768 + (pw * 4 + i) * 1024, vA[i], sx, oA + s * 128);
        dma16_masked(L + (s & 3) * 32768 + 16384 + (pw * 4 + i) * 1024, vB[i], sw, oB + s * (64 * 3840), bmask);
      }
    };
    issue(0);
    ldsw<u32x4>(L + pw * 32768 + 16384 + lane * 256 + ((15 ^ ((lane & 3) << 2)) << 4), u32x4{0x3f80u, 0u, 0u, 0u});
    BARRIER();
    issue(1);
    WAIT_VL(8); BARRIER();
    issue(2); issue(3);
    for (int s = 0; s < 9; ++s) {
      if (s < 7) WAIT_V(16); else if (s == 7) WAIT_V(8); else WAIT_V(0);
      BARRIER();
      if (s < 6) issue(s + 4);
    }
    return;
  }
  if (ct <= 10) k1_consumer<true>(ws + WS_REC, L, rt, ct, wave);
  else k1_consumer<false>(ws + WS_REC, L, rt, ct, wave);
}

constexpr int K2_STAGE = 8 * REC;

__device__ __forceinline__ void exp4(const f32x16& s, int g, unsigned* p) {
  float e0 = __builtin_amdgcn_exp2f(s[4 * g + 0]), e1 = __builtin_amdgcn_exp2f(s[4 * g + 1]);
  float e2 = __builtin_amdgcn_exp2f(s[4 * g + 2]), e3 = __builtin_amdgcn_exp2f(s[4 * g + 3]);
  p[0] = pk2(e0, e1); p[1] = pk2(e2, e3);
}
__device__ __forceinline__ bf16x8 px8(const unsigned* p) { u32x4 v = {p[0], p[1], p[2], p[3]}; return __builtin_bit_cast(bf16x8, v); }
__device__ __forceinline__ float max16(const f32x16& s) {
  float m = fmaxf(fmaxf(fmaxf(s[0], s[1]), fmaxf(s[2], s[3])), fmaxf(fmaxf(s[4], s[5]), fmaxf(s[6], s[7])));
  float n = fmaxf(fmaxf(fmaxf(s[8], s[9]), fmaxf(s[10], s[11])), fmaxf(fmaxf(s[12], s[13]), fmaxf(s[14], s[15])));
  m = fmaxf(m, n);
  return fmaxf(m, __shfl_xor(m, 32));
}

__global__ __launch_bounds__(512, 2) void k2_attn(char* __restrict__ ws, const u16* __restrict__ wg, const u16* __restrict__ wu) {
  __shared__ __attribute__((aligned(1024))) char smem[3 * K2_STAGE + 16384];
  const unsigned L = (unsigned)(uintptr_t)(LDS3 char*)smem;
  const int tid = threadIdx.x, lane = tid & 63, wave = __builtin_amdgcn_readfirstlane(tid >> 6);
  const int b = blockIdx.x, xcd = b & 7, bi = b >> 3;
  const int head = xcd * 4 + (bi & 3), qb = bi >> 2;
  const int r = lane & 31, hh = lane >> 5;
  const char* rec = ws + WS_REC;
  const i32x4 skv = mk_srd(rec + (size_t)(32 + head) * REC_HEAD, REC_HEAD);
  const unsigned vL = lane * 16;
  auto issue = [&](int s, unsigned slot) {
#pragma unroll
    for (int i = 0; i < 3; ++i) {
      int p = wave * 3 + i; p = p > 20 ? 20 : p;
      dma16(slot + p * 1024, vL, skv, s * K2_STAGE + p * 1024);
    }
  };
  TS(1, 0);
  issue(0, L);
  const char* qrec = rec + (size_t)(head * 64 + qb * 8 + wave) * REC;
  const bf16x8 qlo = *(const bf16x8*)(qrec + hh * 512 + r * 16);
  u32x2 qh = *(const u32x2*)(qrec + 1024 + r * 8);
  if (hh) qh = u32x2{0u, 0u};
  const s16x4 qhi = __builtin_bit_cast(s16x4, qh);
  WAIT_VL(0); BARRIER();
  TS(1, 1);
  issue(1, L + K2_STAGE);
  const int kr = b / 48, cg = b - kr * 48;
  if (b < 240) {
    const i32x4 sw = mk_srd(cg < 24 ? wg : wu, D_ * F_ * 2);
    const int c0 = (cg < 24 ? cg : cg - 24) * 64;
#pragma unroll
    for (int i = 0; i < 2; ++i) {
      int p = wave * 2 + i;
      dma16(L + 3 * K2_STAGE + p * 1024, (lane >> 3) * 3072 + (lane & 7) * 16, sw, (kr * 128 + p * 8) * 3072 + c0 * 2);
    }
  }
  const unsigned aK = hh * 512 + r * 16, aKh = 1024 + r * 8;
  const unsigned aV = REC_V + hh * REC_VH + (r <= 21 ? r : 21) * 16;
  f32x16 negm;
  bf16x8 klo = ldsr<bf16x8>(L + aK);
  s16x4 khi = ldsr<s16x4>(L + aKh);
  {
    f32x16 z = {};
    f32x16 s0 = MFMA32(klo, qlo, z);
    s0 = MFMA32K8(khi, qhi, s0);
    float m = -max16(s0);
#pragma unroll
    for (int i = 0; i < 16; ++i) negm[i] = m;
  }
  f32x16 sc = MFMA32(klo, qlo, negm), sn;
  sc = MFMA32K8(khi, qhi, sc);
  klo = ldsr<bf16x8>(L + aK + REC);
  khi = ldsr<s16x4>(L + aKh + REC);
  f32x16 o = {};
  unsigned p[8] = {};
  bf16x8 v0, v1 = {};
  unsigned cur = L, nxt = L + K2_STAGE, fre = L + 2 * K2_STAGE;
  for (int s = 0; s < 8; ++s) {
    const unsigned cK = cur + aK, cKh = cur + aKh, cV = cur + aV, nK = nxt + aK, nKh = nxt + aKh;
#pragma unroll
    for (int t = 0; t < 8; ++t) {
      f32x16& S = (t & 1) ? sn : sc;
      f32x16& N = (t & 1) ? sc : sn;
      SB();
      v0 = ldsr<bf16x8>(cV + t * REC);
      o = MFMA32(v1, px8(p + 4), o);
      exp4(S, 0, p);
      SB();
      v1 = ldsr<bf16x8>(cV + t * REC + REC_VS);
      N = MFMA32(klo, qlo, negm);
      exp4(S, 1, p + 2);
      SB();
      N = MFMA32K8(khi, qhi, N);
      exp4(S, 2, p + 4);
      SB();
      if (t == 6) {
        if (s < 7) {
          WAIT_VL(0); BARRIER();
          if (s < 6) issue(s + 2, fre);
        }
      }
      if (t < 6) { klo = ldsr<bf16x8>(cK + (t + 2) * REC); khi = ldsr<s16x4>(cKh + (t + 2) * REC); }
      else { klo = ldsr<bf16x8>(nK + (t - 6) * REC); khi = ldsr<s16x4>(nKh + (t - 6) * REC); }
      o = MFMA32(v0, px8(p), o);
      exp4(S, 3, p + 6);
    }
    SB();
    if (s == 3 && wave < 4 && b < 240) {
      const unsigned a0 = L + 3 * K2_STAGE + (8 * (lane >> 4) + ((lane >> 2) & 3)) * 128 + wave * 32 + (lane & 3) * 8;
      const s16x8 one8 = {0x3f80, 0x3f80, 0x3f80, 0x3f80, 0x3f80, 0x3f80, 0x3f80, 0x3f80};
      f32x4 c = {};
#pragma unroll
      for (int ks = 0; ks < 4; ++ks) c = MFMA16(__builtin_bit_cast(bf16x8, one8), cat8(ldstr(a0 + ks * 4096), ldstr(a0 + ks * 4096 + 512)), c);
      if (lane < 16) {
        const int col = (cg < 24 ? cg : cg - 24) * 64 + wave * 16 + lane;
        const int j = col / 96, idx = col - j * 96 + (cg < 24 ? 0 : 96);
        *(float*)(ws + WS_CS + j * 4096 + (kr * 192 + idx) * 4) = c[0];
      }
    }
    unsigned tmp = cur; cur = nxt; nxt = fre; fre = tmp;
  }
  SB();
  TS(1, 2);
  o = MFMA32(v1, px8(p + 4), o);
  float l = __shfl(o[8], r + 32);
  bool ok = l <= 0x1p100f;
#pragma unroll
  for (int i = 0; i < 12; ++i) ok = ok && (__builtin_fabsf(o[i]) <= 0x1p126f);
  if (__builtin_amdgcn_ballot_w64(!ok) != 0ull) {
    const char* kv = rec + (size_t)(32 + head) * REC_HEAD;
    float m = -__builtin_inff();
    o = f32x16{};
    for (int t = 0; t < 64; ++t) {
      const char* q = kv + t * REC;
      f32x16 z = {};
      f32x16 s = MFMA32(*(const bf16x8*)(q + aK), qlo, z);
      s = MFMA32K8(*(const s16x4*)(q + aKh), qhi, s);
      float mn = fmaxf(m, max16(s));
      float c = __builtin_amdgcn_exp2f(m - mn);
      m = mn;
#pragma unroll
      for (int i = 0; i < 16; ++i) { s[i] -= mn; o[i] *= c; }
      unsigned pp[8];
#pragma unroll
      for (int g = 0; g < 4; ++g) exp4(s, g, pp + 2 * g);
      o = MFMA32(*(const bf16x8*)(q + aV), px8(pp), o);
      o = MFMA32(*(const bf16x8*)(q + aV + REC_VS), px8(pp + 4), o);
    }
    l = __shfl(o[8], r + 32);
  }
  const float inv = 1.f / l;
  const int row = qb * 256 + wave * 32 + r;
#pragma unroll
  for (int g = 0; g < 3; ++g) {
    if (g < 2 || hh == 0) {
      const int col = head * 20 + 8 * g + 4 * hh;
      u32x2 v = {pk2(o[4 * g] * inv, o[4 * g + 1] * inv), pk2(o[4 * g + 2] * inv, o[4 * g + 3] * inv)};
      *(u32x2*)(ws + WS_O + (size_t)(col >> 3) * 32768 + row * 16 + (col & 7) * 2) = v;
    }
  }
  TS(1, 3);
}

struct Frag3 { bf16x8 a[2]; s16x4 b[5][2]; };
constexpr int K3_STAGE = 16384 + 20480;

template <int KD, bool FIRST>
__device__ __forceinline__ void proj_body(const char* __restrict__ A, const u16* __restrict__ W, const char* __restrict__ R,
                                          char* __restrict__ C, char* __restrict__ stats, unsigned L) {
  constexpr int NS = KD / 128;
  const int tid = threadIdx.x, lane = tid & 63, wave = __builtin_amdgcn_readfirstlane(tid >> 6);
  const int b = blockIdx.x, xcd = b & 7, bi = b >> 3;
  const int rt = xcd * 4 + (bi & 3), ct = bi >> 2;
  const int m0 = rt * 64, n0 = ct * 80;
  const unsigned RS = L + 4 * K3_STAGE;
  TS(FIRST ? 2 : 4, 0);
  if (wave >= 4) {
    const int pw = wave - 4;
    const i32x4 sa = mk_srd(A, S_ * KD * 2), sw = mk_srd(W, KD * D_ * 2), sr = mk_srd(R, S_ * D_ * 2);
    unsigned vB[5];
#pragma unroll
    for (int i = 0; i < 5; ++i) {
      int idx = (pw * 5 + i) * 64 + lane, pr = idx / 10, c = idx - pr * 10;
      vB[i] = (pr ^ (((pr >> 3) & 1) << 2)) * 1280 + c * 16;
    }
    const unsigned vL = lane * 16;
    auto issue = [&](int s) {
#pragma unroll
      for (int i = 0; i < 5; ++i) {
        if (i < 4) dma16(L + (s & 3) * K3_STAGE + (pw * 4 + i) * 1024, vL, sa, (s * 16 + pw * 4 + i) * 32768 + m0 * 16);
        dma16(L + (s & 3) * K3_STAGE + 16384 + (pw * 5 + i) * 1024, vB[i], sw, s * (128 * 1280) + n0 * 2);
      }
    };
    issue(0);
    BARRIER();
    issue(1);
    WAIT_V(9); BARRIER();
    issue(2);
#pragma unroll
    for (int i = 0; i < 3; ++i) {
      int p = pw * 3 + i; p = p > 9 ? 9 : p;
      if (FIRST) { int idx = p * 64 + lane, row = idx / 10, c = idx - row * 10; dma16(RS + p * 1024, row * 1280 + c * 16, sr, m0 * 1280 + n0 * 2); }
      else dma16(RS + p * 1024, vL, sr, (ct * 10 + p) * 32768 + m0 * 16);
    }
    issue(3);
    for (int s = 0; s < NS - 1; ++s) {
      if (s == 0) WAIT_V(21); else if (s < NS - 3) WAIT_V(18); else if (s == NS - 3) WAIT_V(9); else WAIT_V(0);
      BARRIER();
      if (s < NS - 4) issue(s + 4);
    }
    return;
  }
  const int wm = wave & 1, wk = wave >> 1, g = lane >> 4;
  const unsigned aA = L + (wk * 8 + g) * 1024 + (wm * 32 + (lane & 15)) * 16;
  unsigned aB[2];
#pragma unroll
  for (int t = 0; t < 2; ++t) aB[t] = L + 16384 + (wk * 64 + 8 * g + ((4 * t) ^ (4 * (g & 1))) + ((lane >> 2) & 3)) * 160 + (lane & 3) * 8;
  auto rd = [&](Frag3& f, unsigned sl, int ks, int t) {
    if (t == 0) f.a[0] = ldsr<bf16x8>(aA + sl + ks * 4096);
    else if (t == 3) f.a[1] = ldsr<bf16x8>(aA + sl + ks * 4096 + 256);
    else { const int u = t < 3 ? t - 1 : t - 2; f.b[u >> 1][u & 1] = ldstr(aB[u & 1] + sl + ks * 5120 + (u >> 1) * 32); }
  };
  f32x4 acc[2][5] = {};
  auto mm = [&](const Frag3& f, int t) { acc[t & 1][t >> 1] = MFMA16(cat8(f.b[t >> 1][0], f.b[t >> 1][1]), f.a[t & 1], acc[t & 1][t >> 1]); };
  auto body = [&](const Frag3& f, Frag3& n, unsigned sl, int ks) {
#pragma unroll
    for (int t = 1; t < 10; ++t) {
      SB();
      if (t <= 3) { rd(n, sl, ks, 2 * t - 2); rd(n, sl, ks, 2 * t - 1); } else rd(n, sl, ks, t + 2);
      SB(); mm(f, t);
    }
    SB();
  };
  Frag3 f0, f1;
  BARRIER(); BARRIER();
  TS(FIRST ? 2 : 4, 1);
#pragma unroll
  for (int t = 0; t < 12; ++t) rd(f0, 0, 0, t);
  SB();
  for (int s = 0; s < NS; ++s) {
    const unsigned sl = (s & 3) * K3_STAGE, nsl = ((s + 1) & 3) * K3_STAGE;
    mm(f0, 0); body(f0, f1, sl, 1);
    mm(f1, 0); SB();
    if (s < NS - 1) LBARRIER();
    body(f1, f0, nsl, 0);
  }
  TS(FIRST ? 2 : 4, 2);
  const unsigned X = L + ((NS + 1) & 3) * K3_STAGE;
#pragma unroll
  for (int j = 0; j < 5; ++j) ldsw<f32x4>(X + (wave * 5 + j) * 1024 + lane * 16, wk ? acc[0][j] : acc[1][j]);
  LBARRIER();
  const int lr = wm * 32 + wk * 16 + (lane & 15);
  const i32x4 rc = mk_srd(C, S_ * D_ * 2);
  const unsigned vo = FIRST ? (g >> 1) * 32768 + lr * 16 + (g & 1) * 8 : lr * 1280 + g * 8;
  f32x2 sum = {}, sq = {};
#pragma unroll
  for (int j = 0; j < 5; ++j) {
    f32x4 v = (wk ? acc[1][j] : acc[0][j]) + ldsr<f32x4>(X + ((wave ^ 2) * 5 + j) * 1024 + lane * 16);
    u32x2 rr = FIRST ? ldsr<u32x2>(RS + lr * 160 + j * 32 + g * 8) : ldsr<u32x2>(RS + (2 * j + (g >> 1)) * 1024 + lr * 16 + (g & 1) * 8);
    u32x2 o = {pk2(v[0] + bflo(rr.x), v[1] + bfhi(rr.x)), pk2(v[2] + bflo(rr.y), v[3] + bfhi(rr.y))};
    if (FIRST) {
      st8(o, rc, vo, (ct * 10 + 2 * j) * 32768 + m0 * 16);
      f32x2 h0 = {bflo(o.x), bfhi(o.x)}, h1 = {bflo(o.y), bfhi(o.y)};
      sum += h0 + h1;
      sq = fma2(h0, h0, fma2(h1, h1, sq));
    } else {
      st8(o, rc, vo, m0 * 1280 + (n0 + 16 * j) * 2);
    }
  }
  if (FIRST) {
    float s1 = sum.x + sum.y, s2 = sq.x + sq.y;
    s1 += __shfl_xor(s1, 16); s2 += __shfl_xor(s2, 16);
    s1 += __shfl_xor(s1, 32); s2 += __shfl_xor(s2, 32);
    if (lane < 16) *(f32x2*)(stats + (m0 + lr) * 64 + ct * 8) = f32x2{s1, s2};
  }
  TS(FIRST ? 2 : 4, 3);
}

__global__ __launch_bounds__(512, 2) void k3_oproj(const u16* __restrict__ x, const u16* __restrict__ wo, char* __restrict__ ws) {
  __shared__ __attribute__((aligned(1024))) char smem[4 * K3_STAGE + 10240];
  proj_body<D_, true>(ws + WS_O, wo, (const char*)x, ws + WS_H1, ws + WS_ST, (unsigned)(uintptr_t)(LDS3 char*)smem);
}
__global__ __launch_bounds__(512, 2) void k5_down(const u16* __restrict__ wd, char* __restrict__ ws, u16* __restrict__ out) {
  __shared__ __attribute__((aligned(1024))) char smem[4 * K3_STAGE + 10240];
  proj_body<F_, false>(ws + WS_HH, wd, ws + WS_H1, (char*)out, nullptr, (unsigned)(uintptr_t)(LDS3 char*)smem);
}

struct Frag4 { bf16x8 a[2]; s16x4 b[3][2]; };
constexpr int K4_STAGE = 16384 + 24576;

__global__ __launch_bounds__(512, 2) void k4_gateup(const u16* __restrict__ wg, const u16* __restrict__ wu, char* __restrict__ ws) {
  __shared__ __attribute__((aligned(1024))) char smem[4 * K4_STAGE];
  const unsigned L = (unsigned)(uintptr_t)(LDS3 char*)smem;
  const int tid = threadIdx.x, lane = tid & 63, wave = __builtin_amdgcn_readfirstlane(tid >> 6);
  const int b = blockIdx.x, xcd = b & 7, bi = b >> 3;
  const int rt = (xcd >> 1) * 4 + (bi & 3), ct = (xcd & 1) * 8 + (bi >> 2);
  const int m0 = rt * 128, n0 = ct * 96;
  const unsigned T = L + 2 * K4_STAGE;
  TS(3, 0);
  if (wave >= 4) {
    const int pw = wave - 4;
    const i32x4 sa = mk_srd(ws + WS_H1, S_ * D_ * 2), sg = mk_srd(wg, D_ * F_ * 2), su = mk_srd(wu, D_ * F_ * 2);
    const i32x4 sst = mk_srd(ws + WS_ST, S_ * 64 + 65536);
    unsigned vG[3], vU[3];
#pragma unroll
    for (int i = 0; i < 3; ++i) {
      int idx = (pw * 3 + i) * 64 + lane, pr = idx / 12, c = idx - pr * 12;
      vG[i] = pr * 3072 + c * 16;
      vU[i] = vG[i] ^ 32;
    }
    const unsigned vL = lane * 16;
    auto issue = [&](int s) {
      const unsigned sl = L + (s & 3) * K4_STAGE;
#pragma unroll
      for (int i = 0; i < 4; ++i) {
        const int p = pw * 4 + i;
        dma16(sl + p * 1024, vL, sa, (s * 8 + (p >> 1)) * 32768 + (m0 + (p & 1) * 64) * 16);
        if (i < 3) {
          dma16(sl + 16384 + (pw * 3 + i) * 1024, vG[i], sg, s * (64 * 3072) + n0 * 2);
          dma16(sl + 16384 + 12288 + (pw * 3 + i) * 1024, vU[i], su, s * (64 * 3072) + n0 * 2);
        }
      }
    };
    issue(0);
    BARRIER();
    issue(1);
    WAIT_V(10); BARRIER();
    issue(2); issue(3);
    for (int s = 0; s < 9; ++s) {
      if (s < 7) WAIT_V(20); else if (s == 7) WAIT_V(13); else WAIT_V(0);
      BARRIER();
      if (s < 6) issue(s + 4);
      if (s == 6) {
        dma16(T + pw * 2048, vL, sst, m0 * 64 + pw * 2048);
        dma16(T + pw * 2048 + 1024, vL, sst, m0 * 64 + pw * 2048 + 1024);
        dma16(T + 8192 + pw * 1024, vL, sst, S_ * 64 + ct * 4096 + pw * 1024);
      }
    }
    const int pt = tid - 256;
    if (pt < 128) {
      f32x4 t0 = ldsr<f32x4>(T + pt * 64), t1 = ldsr<f32x4>(T + pt * 64 + 16), t2 = ldsr<f32x4>(T + pt * 64 + 32), t3 = ldsr<f32x4>(T + pt * 64 + 48);
      f32x4 t = (t0 + t1) + (t2 + t3);
      float mean = (t[0] + t[2]) * (1.f / D_);
      float var = fmaxf((t[1] + t[3]) * (1.f / D_) - mean * mean, 0.f);
      ldsw<f32x2>(T + 12288 + pt * 8, f32x2{__builtin_amdgcn_rsqf(var + LN_EPS), -mean});
    } else if (pt < 128 + 96) {
      const unsigned a = T + 8192 + (pt - 128) * 8;
      f32x2 c = (ldsr<f32x2>(a) + ldsr<f32x2>(a + 768)) + (ldsr<f32x2>(a + 1536) + ldsr<f32x2>(a + 2304)) + ldsr<f32x2>(a + 3072);
      ldsw<f32x2>(T + 13312 + (pt - 128) * 8, c);
    }
    LBARRIER();
    return;
  }
  const int wm = wave >> 1, wn = wave & 1, r = lane & 31, hh = lane >> 5;
  const unsigned aA = L + hh * 2048 + (wm * 64 + r) * 16;
  unsigned aB[3];
  {
    const int q = (lane >> 2) & 3, p = lane & 3, up = (lane >> 4) & 1;
#pragma unroll
    for (int j = 0; j < 3; ++j) aB[j] = L + 16384 + up * 12288 + (8 * hh + q) * 192 + (((wn * 3 + j) ^ up) * 32) + p * 8;
  }
  auto rd = [&](Frag4& f, unsigned sl, int ks, int t) {
    if (t == 0) { f.a[0] = ldsr<bf16x8>(aA + sl + ks * 4096); f.b[0][0] = ldstr(aB[0] + sl + ks * 3072); }
    if (t == 1) { f.b[0][1] = ldstr(aB[0] + sl + ks * 3072 + 768); f.a[1] = ldsr<bf16x8>(aA + sl + ks * 4096 + 512); }
    if (t >= 2) { f.b[t - 1][0] = ldstr(aB[t - 1] + sl + ks * 3072); f.b[t - 1][1] = ldstr(aB[t - 1] + sl + ks * 3072 + 768); }
  };
  f32x16 acc[2][3] = {};
  auto mm = [&](const Frag4& f, int t) { acc[t & 1][t >> 1] = MFMA32(cat8(f.b[t >> 1][0], f.b[t >> 1][1]), f.a[t & 1], acc[t & 1][t >> 1]); };
  auto body = [&](const Frag4& f, Frag4& n, unsigned sl, int ks) {
#pragma unroll
    for (int t = 1; t < 6; ++t) {
      SB();
      if (t <= 4) rd(n, sl, ks, t - 1);
      SB(); mm(f, t);
    }
    SB();
  };
  Frag4 f0, f1;
  BARRIER(); BARRIER();
  TS(3, 1);
#pragma unroll
  for (int t = 0; t < 4; ++t) rd(f0, 0, 0, t);
  SB();
  for (int s = 0; s < 10; ++s) {
    const unsigned sl = (s & 3) * K4_STAGE, nsl = ((s + 1) & 3) * K4_STAGE;
    mm(f0, 0); body(f0, f1, sl, 1);
    mm(f1, 0); body(f1, f0, sl, 2);
    mm(f0, 0); body(f0, f1, sl, 3);
    mm(f1, 0); SB();
    if (s < 9) LBARRIER();
    body(f1, f0, nsl, 0);
  }
  BARRIER();
  TS(3, 2);
  const float k1 = -2.f * 0.7978845608028654f * 1.4426950408889634f, k2 = k1 * 0.044715f;
  const i32x4 ro = mk_srd(ws + WS_HH, S_ * F_ * 2);
  const unsigned vo = hh * 32768 + r * 16;
#pragma unroll
  for (int j = 0; j < 3; ++j) {
    const int fc = (wn * 3 + j) * 16 + 4 * hh;
    f32x4 cg[2], cu[2];
#pragma unroll
    for (int g = 0; g < 2; ++g) { cg[g] = ldsr<f32x4>(T + 13312 + (fc + 8 * g) * 4); cu[g] = ldsr<f32x4>(T + 13312 + 384 + (fc + 8 * g) * 4); }
#pragma unroll
    for (int i = 0; i < 2; ++i) {
      const f32x2 ab = ldsr<f32x2>(T + 12288 + (wm * 64 + i * 32 + r) * 8);
      const f32x2 a = {ab.x, ab.x}, nm = {ab.y, ab.y};
      unsigned pk[4];
#pragma unroll
      for (int e = 0; e < 4; ++e) {
        const f32x2 gt = fma2(nm, sub2(cg[e >> 1], (e & 1) * 2), sub2(acc[i][j], 2 * e)) * a;
        const f32x2 up = fma2(nm, sub2(cu[e >> 1], (e & 1) * 2), sub2(acc[i][j], 8 + 2 * e)) * a;
        const f32x2 z = gt * fma2(gt * gt, f32x2{k2, k2}, f32x2{k1, k1});
        const f32x2 dn = f32x2{__builtin_amdgcn_exp2f(z.x), __builtin_amdgcn_exp2f(z.y)} + f32x2{1.f, 1.f};
        const f32x2 sg2 = {__builtin_amdgcn_rcpf(dn.x), __builtin_amdgcn_rcpf(dn.y)};
        pk[e] = pk2(gt * up * sg2);
      }
      auto s0 = __builtin_amdgcn_permlane32_swap(pk[0], pk[2], false, false);
      auto s1 = __builtin_amdgcn_permlane32_swap(pk[1], pk[3], false, false);
      st16(u32x4{s0[0], s1[0], s0[1], s1[1]}, ro, vo, (ct * 12 + (wn * 3 + j) * 2) * 32768 + (m0 + wm * 64 + i * 32) * 16);
    }
  }
  TS(3, 3);
}

extern "C" void kernel_launch(void* const* d_in, const int* in_sizes, int n_in, void* d_out, int out_size, void* d_ws,
                              size_t ws_size, hipStream_t stream) {
  const u16* x = (const u16*)d_in[0];
  const u16* wqkv = (const u16*)d_in[1];
  const u16* wo = (const u16*)d_in[2];
  const u16* wg = (const u16*)d_in[4];
  const u16* wu = (const u16*)d_in[5];
  const u16* wd = (const u16*)d_in[6];
  char* ws = (char*)d_ws;
  k1_qkv<<<256, 512, 0, stream>>>(x, wqkv, ws);
  k2_attn<<<256, 512, 0, stream>>>(ws, wg, wu);
  k3_oproj<<<256, 512, 0, stream>>>(x, wo, ws);
  k4_gateup<<<256, 512, 0, stream>>>(wg, wu, ws);
  k5_down<<<256, 512, 0, stream>>>(wd, ws, (u16*)d_out);
}
```

```cpp
#include <hip/hip_runtime.h>
#include <stdint.h>

typedef unsigned short u16;
typedef __bf16 bf16x8 __attribute__((ext_vector_type(8)));
typedef __bf16 bf16x2 __attribute__((ext_vector_type(2)));
typedef short s16x4 __attribute__((ext_vector_type(4)));
typedef short s16x8 __attribute__((ext_vector_type(8)));
typedef float f32x2 __attribute__((ext_vector_type(2)));
typedef float f32x4 __attribute__((ext_vector_type(4)));
typedef float f32x16 __attribute__((ext_vector_type(16)));
typedef int i32x4 __attribute__((ext_vector_type(4)));
typedef unsigned u32x2 __attribute__((ext_vector_type(2)));
typedef unsigned u32x4 __attribute__((ext_vector_type(4)));
#define LDS3 __attribute__((address_space(3)))

constexpr int S_ = 2048, D_ = 640, F_ = 1536;
constexpr int REC = 2560, REC_V = 1280, REC_VS = 640, REC_VH = 320;
constexpr int REC_HEAD = 64 * REC;
constexpr size_t WS_REC = 0;
constexpr size_t WS_O = WS_REC + (size_t)64 * REC_HEAD;
constexpr size_t WS_H1 = WS_O + (size_t)S_ * D_ * 2;
constexpr size_t WS_HH = WS_H1 + (size_t)S_ * D_ * 2;
constexpr size_t WS_ST = WS_HH + (size_t)S_ * F_ * 2;
constexpr size_t WS_CS = WS_ST + (size_t)S_ * 64;
constexpr size_t WS_PK = WS_CS + 16 * 8192;
constexpr int PK_G = 0, PK_O = 16 * 10 * 24576, PK_D = PK_O + 8 * 5 * 20480, PK_SIZE = PK_D + 8 * 12 * 20480;
constexpr float LN_EPS = 1e-5f;

#define TS(k, i)
#define STPOL " sc1"
#define SB() __builtin_amdgcn_sched_barrier(0)
#define WAIT_V(n) asm volatile("s_waitcnt vmcnt(%0)" ::"n"(n) : "memory")
#define WAIT_VL(n) asm volatile("s_waitcnt vmcnt(%0) lgkmcnt(0)" ::"n"(n) : "memory")
#define BARRIER() asm volatile("s_barrier" ::: "memory")
#define LBARRIER() asm volatile("s_waitcnt lgkmcnt(0)\n\ts_barrier" ::: "memory")

__device__ __forceinline__ i32x4 mk_srd(const void* p, unsigned bytes) {
  unsigned long long a = (unsigned long long)p;
  i32x4 r;
  r.x = (int)(unsigned)a; r.y = (int)((unsigned)(a >> 32) & 0xffffu); r.z = (int)bytes; r.w = 0x00020000;
  return r;
}
__device__ __forceinline__ void st16(u32x4 v, i32x4 srd, unsigned voff, unsigned soff) {
  asm volatile("buffer_store_dwordx4 %0, %1, %2, %3 offen" STPOL "\n\ts_nop 1" ::"v"(v), "v"(voff), "s"(srd), "s"(soff));
}
__device__ __forceinline__ void st8(u32x2 v, i32x4 srd, unsigned voff, unsigned soff) {
  asm volatile("buffer_store_dwordx2 %0, %1, %2, %3 offen" STPOL ::"v"(v), "v"(voff), "s"(srd), "s"(soff));
}
__device__ __forceinline__ void dma16(unsigned lds, unsigned voff, i32x4 srd, unsigned soff) {
  asm volatile("s_mov_b32 m0, %0\n\ts_nop 0\n\tbuffer_load_dwordx4 %1, %2, %3 offen lds"
               ::"s"(lds), "v"(voff), "s"(srd), "s"(soff) : "memory");
}
__device__ __forceinline__ void dma16_masked(unsigned lds, unsigned voff, i32x4 srd, unsigned soff, unsigned long long mask) {
  asm volatile("s_mov_b32 m0, %0\n\ts_mov_b64 exec, %4\n\tbuffer_load_dwordx4 %1, %2, %3 offen lds\n\ts_mov_b64 exec, -1"
               ::"s"(lds), "v"(voff), "s"(srd), "s"(soff), "s"(mask) : "memory");
}
template <typename T> __device__ __forceinline__ T ldsr(unsigned a) { return *(LDS3 const T*)(uintptr_t)a; }
template <typename T> __device__ __forceinline__ void ldsw(unsigned a, T v) { *(LDS3 T*)(uintptr_t)a = v; }
__device__ __forceinline__ s16x4 ldstr(unsigned a) { return __builtin_amdgcn_ds_read_tr16_b64_v4i16((LDS3 s16x4*)(uintptr_t)a); }
__device__ __forceinline__ bf16x8 cat8(s16x4 lo, s16x4 hi) {
  s16x8 r = __builtin_shufflevector(lo, hi, 0, 1, 2, 3, 4, 5, 6, 7);
  return __builtin_bit_cast(bf16x8, r);
}
__device__ __forceinline__ unsigned pk2(f32x2 v) { return __builtin_bit_cast(unsigned, __builtin_convertvector(v, bf16x2)); }
__device__ __forceinline__ unsigned pk2(float a, float b) { return pk2(f32x2{a, b}); }
__device__ __forceinline__ float bflo(unsigned v) { return __uint_as_float(v << 16); }
__device__ __forceinline__ float bfhi(unsigned v) { return __uint_as_float(v & 0xffff0000u); }
__device__ __forceinline__ s16x4 lo4(bf16x8 v) { s16x8 t = __builtin_bit_cast(s16x8, v); return __builtin_shufflevector(t, t, 0, 1, 2, 3); }
__device__ __forceinline__ s16x4 hi4(bf16x8 v) { s16x8 t = __builtin_bit_cast(s16x8, v); return __builtin_shufflevector(t, t, 4, 5, 6, 7); }
__device__ __forceinline__ f32x2 sub2(const f32x16& v, int i) { return f32x2{v[i], v[i + 1]}; }
__device__ __forceinline__ f32x2 sub2(const f32x4& v, int i) { return f32x2{v[i], v[i + 1]}; }
__device__ __forceinline__ f32x2 fma2(f32x2 a, f32x2 b, f32x2 c) { return __builtin_elementwise_fma(a, b, c); }
#define MFMA32(a, b, c) __builtin_amdgcn_mfma_f32_32x32x16_bf16(a, b, c, 0, 0, 0)
#define MFMA16(a, b, c) __builtin_amdgcn_mfma_f32_16x16x32_bf16(a, b, c, 0, 0, 0)
#define MFMA32K8(a, b, c) __builtin_amdgcn_mfma_f32_32x32x8bf16_1k(a, b, c, 0, 0, 0)
#define MFMA4(a, b, c) __builtin_amdgcn_mfma_f32_4x4x4bf16_1k(a, b, c, 0, 0, 0)

struct Frag1 { bf16x8 a[2]; s16x4 b[2][2]; };

template <bool SWAP>
__device__ __forceinline__ void k1_consumer(char* __restrict__ rec, unsigned L, int rt, int ct, int wave) {
  const int tid = threadIdx.x, lane = tid & 63;
  const int wm = wave >> 1, wn = wave & 1, r = lane & 31, hh = lane >> 5;
  unsigned aA[2][4], aB[2];
#pragma unroll
  for (int i = 0; i < 2; ++i) {
    int row = wm * 64 + ((i ^ wn) * 32) + r;
#pragma unroll
    for (int ks = 0; ks < 4; ++ks) aA[i][ks] = L + row * 128 + (((2 * ks + hh) ^ ((row >> 1) & 7)) << 4);
    int q = (lane >> 2) & 3, p = lane & 3, ch = (lane >> 4) & 1;
    int c = wn * 8 + ((i ^ wm) * 4) + 2 * ch + (p >> 1);
    aB[i] = L + 16384 + (8 * hh + q) * 256 + ((c ^ (q << 2)) << 4) + 8 * (p & 1);
  }
  auto rd = [&](Frag1& f, unsigned sl, int ks, int t) {
    if (t == 0) { f.a[0] = ldsr<bf16x8>(aA[0][ks] + sl); f.b[0][0] = ldstr(aB[0] + sl + ks * 4096); }
    if (t == 1) { f.b[0][1] = ldstr(aB[0] + sl + ks * 4096 + 1024); f.b[1][0] = ldstr(aB[1] + sl + ks * 4096); }
    if (t == 2) { f.b[1][1] = ldstr(aB[1] + sl + ks * 4096 + 1024); f.a[1] = ldsr<bf16x8>(aA[1][ks] + sl); }
  };
  f32x16 acc[2][2] = {};
  f32x4 sq0 = {}, sq1 = {}, cs0 = {}, cs1 = {};
  const s16x4 ones = {0x3f80, 0x3f80, 0x3f80, 0x3f80};
  auto mm = [&](const Frag1& f, int t) {
    const int i = t >> 1, j = t & 1;
    const bf16x8 b = cat8(f.b[j][0], f.b[j][1]);
    acc[i][j] = SWAP ? MFMA32(b, f.a[i], acc[i][j]) : MFMA32(f.a[i], b, acc[i][j]);
    if (t == 3) {
      sq0 = MFMA4(lo4(f.a[0]), lo4(f.a[0]), sq0); cs0 = MFMA4(ones, f.b[0][0], cs0);
      sq1 = MFMA4(hi4(f.a[0]), hi4(f.a[0]), sq1); cs1 = MFMA4(ones, f.b[0][1], cs1);
    }
  };
  auto kstep = [&](const Frag1& f, Frag1& n, unsigned sl, int ks) {
    mm(f, 0); SB(); rd(n, sl, ks, 0); SB(); mm(f, 1); SB(); rd(n, sl, ks, 1); SB(); mm(f, 2); SB(); rd(n, sl, ks, 2); SB(); mm(f, 3); SB();
  };
  Frag1 f0, f1;
  BARRIER(); BARRIER();
  TS(0, 1);
  rd(f0, 0, 0, 0); rd(f0, 0, 0, 1); rd(f0, 0, 0, 2);
  SB();
  for (int s = 0; s < 10; ++s) {
    const unsigned sl = (s & 3) * 32768, nsl = ((s + 1) & 3) * 32768;
    kstep(f0, f1, sl, 1);
    kstep(f1, f0, sl, 2);
    kstep(f0, f1, sl, 3);
    mm(f1, 0); SB();
    if (s < 9) LBARRIER();
    SB(); rd(f0, nsl, 0, 0); SB(); mm(f1, 1); SB(); rd(f0, nsl, 0, 1); SB(); mm(f1, 2); SB(); rd(f0, nsl, 0, 2); SB(); mm(f1, 3); SB();
  }
  TS(0, 2);
  const unsigned T = L + 131072;
  {
    f32x4 sq = sq0 + sq1;
    int e = lane & 3;
    float sv = e == 0 ? sq[0] : e == 1 ? sq[1] : e == 2 ? sq[2] : sq[3];
    ldsw<float>(T + hh * 512 + (wm * 64 + wn * 32 + r) * 4, sv);
    ldsw<float>(T + 1024 + hh * 512 + (wn * 64 + wm * 32 + r) * 4, cs0[0] + cs1[0]);
    if (wn == 1) {
#pragma unroll
      for (int i = 0; i < 2; ++i) {
        const int rb = wm * 64 + (i ^ 1) * 32;
        if (SWAP) {
          float v = wm ? acc[i][0][12] : acc[i][1][12];
          if (hh == 0) ldsw<float>(T + 2048 + (rb + r) * 4, v);
        } else if (r == 24) {
#pragma unroll
          for (int g = 0; g < 4; ++g) {
            f32x4 v;
#pragma unroll
            for (int e2 = 0; e2 < 4; ++e2) v[e2] = wm ? acc[i][0][4 * g + e2] : acc[i][1][4 * g + e2];
            ldsw<f32x4>(T + 2048 + (rb + 8 * g + 4 * hh) * 4, v);
          }
        }
      }
    }
  }
  LBARRIER();
  if (tid < 128) {
    float sum = ldsr<float>(T + 2048 + tid * 4);
    float sq = ldsr<float>(T + tid * 4) + ldsr<float>(T + 512 + tid * 4);
    float mean = sum * (1.f / D_);
    float var = fmaxf(sq * (1.f / D_) - mean * mean, 0.f);
    float rs = __builtin_amdgcn_rsqf(var + LN_EPS);
    if (SWAP) ldsw<f32x2>(T + 2560 + tid * 8, f32x2{rs, -mean});
    else { ldsw<float>(T + 2560 + tid * 4, rs); ldsw<float>(T + 3072 + tid * 4, -mean); }
  } else {
    int c = tid - 128;
    ldsw<float>(T + 3584 + c * 4, ldsr<float>(T + 1024 + c * 4) + ldsr<float>(T + 1536 + c * 4));
  }
  LBARRIER();
  const i32x4 ro = mk_srd(rec, 64 * REC_HEAD);
  const int tile0 = rt * 4 + wm * 2;
  if (SWAP) {
    const float qs = 0.22360679774997896f * 1.4426950408889634f;
    f32x2 ab[2];
#pragma unroll
    for (int i = 0; i < 2; ++i) ab[i] = ldsr<f32x2>(T + 2560 + (wm * 64 + (i ^ wn) * 32 + r) * 8);
    const unsigned vlo = (hh ^ wn) * REC + r * 16;
    const unsigned vhi = hh * REC_HEAD + 1024 + r * 8;
    const unsigned X = L + wave * 8192;
#pragma unroll
    for (int j = 0; j < 2; ++j) {
#pragma unroll
      for (int g = 0; g < 4; ++g) {
        const int P = wn * 8 + (j ^ wm) * 4 + g;
        if (P < 15) {
          const int hp = (P * 13) >> 6, t = P - hp * 5, hA = ct * 6 + 2 * hp, od = (t + 1) >> 2;
          const f32x4 cs = ldsr<f32x4>(T + 3584 + (P * 8 + 4 * hh) * 4);
          const float sc = hA < 32 ? qs : 1.f;
          u32x2 v[2];
#pragma unroll
          for (int i = 0; i < 2; ++i) {
            const f32x2 a = {ab[i].x * sc, ab[i].x * sc}, nm = {ab[i].y, ab[i].y};
            v[i].x = pk2(fma2(nm, sub2(cs, 0), sub2(acc[i][j], 4 * g)) * a);
            v[i].y = pk2(fma2(nm, sub2(cs, 2), sub2(acc[i][j], 4 * g + 2)) * a);
          }
          if (hA >= 64) {
#pragma unroll
            for (int i = 0; i < 2; ++i) ldsw<u32x2>(X + ((i ^ wn) * 32 + r) * 104 + (t * 8 + 4 * hh) * 2, v[i]);
          } else if (t == 2) {
#pragma unroll
            for (int i = 0; i < 2; ++i) st8(v[i], ro, vhi, hA * REC_HEAD + (tile0 + (i ^ wn)) * REC);
          } else {
            auto s0 = __builtin_amdgcn_permlane32_swap(v[0].x, v[1].x, false, false);
            auto s1 = __builtin_amdgcn_permlane32_swap(v[0].y, v[1].y, false, false);
            st16(u32x4{s0[0], s1[0], s0[1], s1[1]}, ro, vlo, (hA + od) * REC_HEAD + tile0 * REC + (t - 3 * od) * 512);
          }
        }
      }
    }
    if (ct == 10 && wn == 1) {
      const int g4 = lane >> 4, c16 = lane & 15;
      const unsigned ra = X + (16 * (g4 >> 1) + 4 * (g4 & 1) + ((lane >> 2) & 3)) * 104 + (lane & 3) * 8;
#pragma unroll
      for (int mb = 0; mb < 2; ++mb) {
#pragma unroll
        for (int c = 0; c < 3; ++c) {
          s16x4 lo = ldstr(ra + mb * 32 * 104 + c * 32), hi = ldstr(ra + mb * 32 * 104 + 8 * 104 + c * 32);
          const int col = c * 16 + c16, hd = col >= 20, d = col - 20 * hd;
          const unsigned vo = hd * REC_HEAD + (g4 >> 1) * REC_VS + (g4 & 1) * REC_VH + d * 16;
          const unsigned so = 32 * REC_HEAD + (tile0 + mb) * REC + REC_V;
          if (col < 40) {
            st16(__builtin_bit_cast(u32x4, cat8(lo, hi)), ro, vo, so);
          }
        }
      }
    }
  } else {
#pragma unroll
    for (int i = 0; i < 2; ++i) {
      const int rb = wm * 64 + (i ^ wn) * 32;
      f32x4 av[4], bv[4];
#pragma unroll
      for (int g = 0; g < 4; ++g) {
        av[g] = ldsr<f32x4>(T + 2560 + (rb + 8 * g + 4 * hh) * 4);
        bv[g] = ldsr<f32x4>(T + 3072 + (rb + 8 * g + 4 * hh) * 4);
      }
#pragma unroll
      for (int j = 0; j < 2; ++j) {
        const int lc = wn * 64 + (j ^ wm) * 32 + r;
        if (lc < 120) {
          const float c1 = ldsr<float>(T + 3584 + lc * 4);
          const f32x2 cs = {c1, c1};
          const int gc = ct * 120 + lc - 1280;
          const int hd = (gc * 205) >> 12, d = gc - hd * 20;
          u32x4 v[2];
#pragma unroll
          for (int g = 0; g < 4; ++g) {
            v[g >> 1][(g & 1) * 2] = pk2(fma2(sub2(bv[g], 0), cs, sub2(acc[i][j], 4 * g)) * sub2(av[g], 0));
            v[g >> 1][(g & 1) * 2 + 1] = pk2(fma2(sub2(bv[g], 2), cs, sub2(acc[i][j], 4 * g + 2)) * sub2(av[g], 2));
          }
          const unsigned vo = hd * REC_HEAD + hh * REC_VH + d * 16, so = 32 * REC_HEAD + (tile0 + (i ^ wn)) * REC + REC_V;
          st16(v[0], ro, vo, so); st16(v[1], ro, vo + REC_VS, so);
        }
      }
    }
  }
  TS(0, 3);
}
constexpr bool chk_div() {
  for (int i = 0; i < 640; ++i) if (((i * 205) >> 12) != i / 20) return false;
  return true;
}
static_assert(chk_div(), "magic division");
constexpr bool chk_div2() {
  for (int i = 0; i < 192; ++i) if (((i * 171) >> 11) != i / 12) return false;
  for (int i = 0; i < 80; ++i) if (((i * 205) >> 11) != i / 10) return false;
  return true;
}
static_assert(chk_div2(), "magic division");

__global__ __launch_bounds__(512, 2) void k1_qkv(const u16* __restrict__ x, const u16* __restrict__ w, char* __restrict__ ws) {
  __shared__ __attribute__((aligned(1024))) char smem[131072 + 4096];
  const unsigned L = (unsigned)(uintptr_t)(LDS3 char*)smem;
  const int b = blockIdx.x, xcd = b & 7, bi = b >> 3;
  const int rt = (xcd >> 1) * 4 + (bi & 3), ct = (xcd & 1) * 8 + (bi >> 2);
  const int lane = threadIdx.x & 63, wave = __builtin_amdgcn_readfirstlane(threadIdx.x >> 6);
  TS(0, 0);
  if (wave >= 4) {
    const int pw = wave - 4;
    const i32x4 sx = mk_srd(x, S_ * D_ * 2), sw = mk_srd(w, D_ * 1920 * 2);
    unsigned vA[4], vB[4];
#pragma unroll
    for (int i = 0; i < 4; ++i) {
      int row = (pw * 4 + i) * 8 + (lane >> 3);
      vA[i] = row * 1280 + (((lane & 7) ^ ((row >> 1) & 7)) << 4);
      int k = (pw * 4 + i) * 4 + (lane >> 4);
      vB[i] = k * 3840 + (((lane & 15) ^ ((k & 3) << 2)) << 4);
    }
    const unsigned long long bmask = ~((1ull << 15) | (1ull << 27) | (1ull << 39) | (1ull << 51));
    const unsigned oA = rt * 128 * 1280, oB = ct * 240;
    auto issue = [&](int s) {
#pragma unroll
      for (int i = 0; i < 4; ++i) {
        dma16(L + (s & 3) * 32768 + (pw * 4 + i) * 1024, vA[i], sx, oA + s * 128);
        dma16_masked(L + (s & 3) * 32768 + 16384 + (pw * 4 + i) * 1024, vB[i], sw, oB + s * (64 * 3840), bmask);
      }
    };
    issue(0);
    ldsw<u32x4>(L + pw * 32768 + 16384 + lane * 256 + ((15 ^ ((lane & 3) << 2)) << 4), u32x4{0x3f80u, 0u, 0u, 0u});
    BARRIER();
    issue(1);
    WAIT_VL(8); BARRIER();
    issue(2); issue(3);
    for (int s = 0; s < 9; ++s) {
      if (s < 7) WAIT_V(16); else if (s == 7) WAIT_V(8); else WAIT_V(0);
      BARRIER();
      if (s < 6) issue(s + 4);
    }
    return;
  }
  if (ct <= 10) k1_consumer<true>(ws + WS_REC, L, rt, ct, wave);
  else k1_consumer<false>(ws + WS_REC, L, rt, ct, wave);
}

__device__ __forceinline__ void exp4(const f32x16& s, int g, unsigned* p) {
  float e0 = __builtin_amdgcn_exp2f(s[4 * g + 0]), e1 = __builtin_amdgcn_exp2f(s[4 * g + 1]);
  float e2 = __builtin_amdgcn_exp2f(s[4 * g + 2]), e3 = __builtin_amdgcn_exp2f(s[4 * g + 3]);
  p[0] = pk2(e0, e1); p[1] = pk2(e2, e3);
}
__device__ __forceinline__ bf16x8 px8(const unsigned* p) { u32x4 v = {p[0], p[1], p[2], p[3]}; return __builtin_bit_cast(bf16x8, v); }
__device__ __forceinline__ bf16x8 bx8(u32x4 v) { return __builtin_bit_cast(bf16x8, v); }
__device__ __forceinline__ float max16(const f32x16& s) {
  float m = fmaxf(fmaxf(fmaxf(s[0], s[1]), fmaxf(s[2], s[3])), fmaxf(fmaxf(s[4], s[5]), fmaxf(s[6], s[7])));
  float n = fmaxf(fmaxf(fmaxf(s[8], s[9]), fmaxf(s[10], s[11])), fmaxf(fmaxf(s[12], s[13]), fmaxf(s[14], s[15])));
  m = fmaxf(m, n);
  return fmaxf(m, __shfl_xor(m, 32));
}

#define K2_ASM \
  ".macro XE r\n v_exp_f32 v[\\r], v[\\r]\n.endm\n" \
  ".macro XC p, r\n v_cvt_pk_bf16_f32 v[\\p], v[\\r], v[\\r+1]\n.endm\n" \
  ".macro XM d, a, b, c\n v_mfma_f32_32x32x16_bf16 v[\\d:\\d+15], v[\\a:\\a+3], v[\\b:\\b+3], \\c\n.endm\n" \
  ".macro XPH s, n, q, op, oc, t, b\n" \
  " XE \\s\n XE \\s+1\n" \
  " .if \\t + \\b > 0\n XM \\op, 84, 68, v[\\op:\\op+15]\n .endif\n" \
  " .if \\b == 0\n" \
  "  .if \\t == 1\n s_waitcnt vmcnt(9)\n s_barrier\n .endif\n" \
  "  .if \\t == 4\n s_waitcnt vmcnt(2)\n s_barrier\n .endif\n" \
  "  s_mov_b64 exec, %[mv]\n ds_read_b128 v[80:83], v106 offset:\\t*2560\n ds_read_b128 v[84:87], v106 offset:\\t*2560+640\n s_mov_b64 exec, -1\n" \
  " .endif\n" \
  " XE \\s+2\n XE \\s+3\n XC 64, \\s\n XC 65, \\s+2\n XE \\s+4\n XE \\s+5\n" \
  " .if \\b == 1\n s_waitcnt lgkmcnt(0)\n .endif\n" \
  " .if \\t + \\b < 16\n XM \\n, 72, \\q, 0\n .endif\n" \
  " XE \\s+6\n XE \\s+7\n XC 66, \\s+4\n XC 67, \\s+6\n XE \\s+8\n" \
  " .if \\t + \\b < 16\n XM \\n, 76, \\q+4, v[\\n:\\n+15]\n .endif\n" \
  " .if (\\b == 0) && (\\t < 15)\n ds_read_b128 v[72:75], v104 offset:(\\t+1)*2560\n ds_read_b64 v[76:77], v105 offset:(\\t+1)*2560\n .endif\n" \
  " .if (\\b == 1) && (\\t == 8)\n s_waitcnt vmcnt(0)\n" \
  "  buffer_store_dwordx4 v[108:111], v118, %[sp], 0 offen sc1\n buffer_store_dwordx4 v[112:115], v119, %[sp], 0 offen sc1\n .endif\n" \
  " XE \\s+9\n XE \\s+10\n XE \\s+11\n XC 68, \\s+8\n XC 69, \\s+10\n XE \\s+12\n XE \\s+13\n" \
  " .if \\b == 0\n .if \\t < 15\n s_waitcnt lgkmcnt(2)\n .else\n s_waitcnt lgkmcnt(0)\n .endif\n .endif\n" \
  " .if \\t == 0\n XM \\oc, 80, 64, 0\n .else\n XM \\oc, 80, 64, v[\\oc:\\oc+15]\n .endif\n" \
  " XE \\s+14\n XE \\s+15\n XC 70, \\s+12\n XC 71, \\s+14\n" \
  ".endm\n" \
  ".macro XD\n s_add_u32 %[pc], %[pc], 4096\n s_mov_b32 m0, %[pc]\n s_nop 0\n buffer_load_dwordx4 v107, %[skv], %[pc] offen lds\n.endm\n" \
  ".macro XMAX d, s\n v_max3_f32 v[\\d], v[\\s], v[\\s+1], v[\\s+2]\n v_max3_f32 v[\\d+1], v[\\s+3], v[\\s+4], v[\\s+5]\n" \
  " v_max3_f32 v[\\d], v[\\d], v[\\s+6], v[\\s+7]\n v_max3_f32 v[\\d+1], v[\\d+1], v[\\s+8], v[\\s+9]\n" \
  " v_max3_f32 v[\\d], v[\\d], v[\\s+10], v[\\s+11]\n v_max3_f32 v[\\d+1], v[\\d+1], v[\\s+12], v[\\s+13]\n" \
  " v_max3_f32 v[\\d], v[\\d], v[\\s+14], v[\\s+15]\n v_max_f32 v[\\d], v[\\d], v[\\d+1]\n.endm\n" \
  "v_subrev_u32 v64, %[ko], v104\n v_subrev_u32 v65, %[ko], v105\n" \
  "ds_read_b128 v[72:75], v64\n ds_read_b64 v[76:77], v65\n" \
  "v_mov_b32 v78, 0x3f803f80\n v_mov_b32 v79, 0\n" \
  "XD\n XD\n s_waitcnt lgkmcnt(0)\n" \
  "XM 32, 72, 88, 0\n XM 48, 72, 96, 0\n XM 32, 76, 92, v[32:47]\n XM 48, 76, 100, v[48:63]\n" \
  "ds_read_b128 v[72:75], v104\n ds_read_b64 v[76:77], v105\n" \
  "XD\n XD\n XD\n XD\n XD\n" \
  "buffer_load_dwordx4 v[108:111], v116, %[sa], 0 offen\n" \
  "buffer_load_dwordx4 v[112:115], v117, %[sb], 0 offen\n" \
  "s_mov_b32 exec_hi, 0\n XMAX 64, 32\n XMAX 66, 48\n" \
  "v_cvt_pk_bf16_f32 v65, -v64, 0\n v_cvt_pk_bf16_f32 v67, -v66, 0\n v_lshlrev_b32 v65, 16, v65\n v_lshlrev_b32 v67, 16, v67\n" \
  "v_sub_f32 v65, -v64, v65\n v_sub_f32 v67, -v66, v67\n v_cvt_pk_bf16_f32 v94, -v64, v65\n v_cvt_pk_bf16_f32 v102, -v66, v67\n" \
  "s_mov_b32 exec_hi, -1\n s_waitcnt lgkmcnt(0)\n s_nop 0\n" \
  "XM 32, 72, 88, 0\n XM 32, 76, 92, v[32:47]\n s_nop 7\n s_nop 3\n" \
  ".irp t,0,1,2,3,4,5,6,7,8,9,10,11,12,13,14,15\n XPH 32, 48, 96, 16, 0, \\t, 0\n XPH 48, 32, 88, 0, 16, \\t, 1\n.endr\n" \
  "s_nop 1\n XM 16, 84, 68, v[16:31]\n s_nop 7\n s_nop 3\n" \
  ".purgem XE\n.purgem XC\n.purgem XM\n.purgem XPH\n.purgem XD\n.purgem XMAX\n"

__device__ __forceinline__ void k2_tail(char* __restrict__ ws, unsigned L, const f32x16& o0, const f32x16& o1, u32x4 wA,
                                        int wave, int head, int qb, int mat, int rg, int cb) {
  int lane;
  asm volatile("v_mbcnt_lo_u32_b32 %0, -1, 0\n\tv_mbcnt_hi_u32_b32 %0, -1, %0" : "=v"(lane));
  const int b = blockIdx.x, qp = wave & 3, kq = wave >> 2, r = lane & 31, hh = lane >> 5;
  const char* rec = ws + WS_REC;
  const char* qrec = rec + (size_t)(head * 64 + qb * 8 + qp * 2) * REC;
  const unsigned vL = lane * 16, aK = hh * 512 + r * 16, aKh = 1024 + r * 8, aV = REC_V + hh * REC_VH + r * 16;
  const unsigned vc = r == 20 ? 0x3f803f80u : 0u;
  f32x4 cs;
  {
    const int i = lane & 15;
    const unsigned oh = (i & 1) ? 0x3f800000u : 0x3f80u;
    const u32x4 sel = {i >> 1 == 0 ? oh : 0u, i >> 1 == 1 ? oh : 0u, i >> 1 == 2 ? oh : 0u, i >> 1 == 3 ? oh : 0u};
    cs = MFMA16(bx8(sel), bx8(wA), f32x4{});
  }
  BARRIER();
  const unsigned CS = L + 73728;
  if (lane < 32) ldsw<f32x4>(CS + wave * 512 + (lane & 15) * 32 + (lane >> 4) * 16, cs);
#pragma unroll
  for (int g = 0; g < 3; ++g) {
    const unsigned a = L + qp * 18432 + g * 1024 + vL;
    if (kq != 0) ldsw<f32x4>(a + (kq - 1) * 3072, f32x4{o0[4 * g], o0[4 * g + 1], o0[4 * g + 2], o0[4 * g + 3]});
    if (kq != 1) ldsw<f32x4>(a + 9216 + (kq ? kq - 1 : 0) * 3072, f32x4{o1[4 * g], o1[4 * g + 1], o1[4 * g + 2], o1[4 * g + 3]});
  }
  LBARRIER();
  if (kq >= 2) {
    if (kq == 2 && qp < 2 && b < 240) {
      const int c = qp * 64 + lane;
      float s = 0.f;
#pragma unroll
      for (int w = 0; w < 16; ++w) s += ldsr<float>(CS + w * 512 + c * 4);
      const int col = cb * 128 + c, ct = col / 96;
      *(float*)(ws + WS_CS + ct * 8192 + (rg * 192 + col - ct * 96 + mat * 96) * 4) = s;
    }
    return;
  }
  f32x4 o[3];
#pragma unroll
  for (int g = 0; g < 3; ++g) {
    const unsigned a = L + qp * 18432 + kq * 9216 + g * 1024 + vL;
#pragma unroll
    for (int e = 0; e < 4; ++e) o[g][e] = kq ? o1[4 * g + e] : o0[4 * g + e];
    o[g] += ldsr<f32x4>(a) + (ldsr<f32x4>(a + 3072) + ldsr<f32x4>(a + 6144));
  }
  float l = __shfl(o[2][0], r + 32);
  bool ok = l <= 0x1p100f && l >= 0x1p-100f;
#pragma unroll
  for (int i = 0; i < 12; ++i) ok = ok && (__builtin_fabsf(o[i >> 2][i & 3]) <= 0x1p126f);
  if (__builtin_amdgcn_ballot_w64(!ok) != 0ull) {
    const char* kv = rec + (size_t)(32 + head) * REC_HEAD;
    const bf16x8 ql = *(const bf16x8*)(qrec + kq * REC + hh * 512 + r * 16);
    const u32x2 t = *(const u32x2*)(qrec + kq * REC + 1024 + r * 8);
    const bf16x8 qh = bx8(hh ? u32x4{} : u32x4{t.x, t.y, 0u, 0u});
    float m = -__builtin_inff();
    f32x16 oo = {};
    for (int t = 0; t < 64; ++t) {
      const char* q = kv + t * REC;
      const u32x2 kh = *(const u32x2*)(q + aKh);
      f32x16 s = {};
      s = MFMA32(*(const bf16x8*)(q + aK), ql, s);
      s = MFMA32(bx8(u32x4{kh.x, kh.y, 0u, 0u}), qh, s);
      float mn = fmaxf(m, max16(s));
      float c = __builtin_amdgcn_exp2f(m - mn);
      m = mn;
#pragma unroll
      for (int i = 0; i < 16; ++i) { s[i] -= mn; oo[i] *= c; }
      unsigned pp[8];
#pragma unroll
      for (int g = 0; g < 4; ++g) exp4(s, g, pp + 2 * g);
      u32x4 a0 = {vc, vc, vc, vc}, a1 = a0;
      if (r < 20) { a0 = *(const u32x4*)(q + aV); a1 = *(const u32x4*)(q + aV + REC_VS); }
      oo = MFMA32(bx8(a0), px8(pp), oo);
      oo = MFMA32(bx8(a1), px8(pp + 4), oo);
    }
#pragma unroll
    for (int i = 0; i < 12; ++i) o[i >> 2][i & 3] = oo[i];
    l = __shfl(oo[8], r + 32);
  }
  const float inv = 1.f / l;
  const int row = qb * 256 + (qp * 2 + kq) * 32 + r;
  const i32x4 so = mk_srd(ws + WS_O, S_ * D_ * 2);
#pragma unroll
  for (int g = 0; g < 3; ++g) {
    if (g < 2 || hh == 0) {
      const int col = head * 20 + 8 * g + 4 * hh;
      u32x2 v = {pk2(o[g][0] * inv, o[g][1] * inv), pk2(o[g][2] * inv, o[g][3] * inv)};
      st8(v, so, (col >> 3) * 32768 + row * 16 + (col & 7) * 2, 0);
    }
  }
  TS(1, 3);
}

__global__ __launch_bounds__(1024, 4) void k2_attn(char* __restrict__ ws, const u16* __restrict__ wo, const u16* __restrict__ wg,
                                                   const u16* __restrict__ wu, const u16* __restrict__ wd) {
  __shared__ __attribute__((aligned(1024))) char smem[REC_HEAD];
  const unsigned L = (unsigned)(uintptr_t)(LDS3 char*)smem;
  const int tid = threadIdx.x, lane = tid & 63, wave = __builtin_amdgcn_readfirstlane(tid >> 6);
  const int b = blockIdx.x, xcd = b & 7, bi = b >> 3;
  const int head = xcd * 4 + (bi & 3), qb = bi >> 2;
  const int qp = wave & 3, kq = wave >> 2;
  const int r = lane & 31, hh = lane >> 5;
  const char* rec = ws + WS_REC;
  const i32x4 skv = mk_srd(rec + (size_t)(32 + head) * REC_HEAD - L, REC_HEAD + L);
  const unsigned vL = lane * 16, pc = L + kq * 40960 + qp * 1024;
  TS(1, 0);
  u32x4 ql0, ql1, qh0, qh1;
  {
    const i32x4 sq = mk_srd(rec, 32 * REC_HEAD);
    asm volatile("v_mov_b64 v[92:93], 0\n\tv_mov_b64 v[94:95], 0\n\tv_mov_b64 v[100:101], 0\n\tv_mov_b64 v[102:103], 0\n\t"
                 "buffer_load_dwordx4 v[88:91], %4, %6, %7 offen\n\tbuffer_load_dwordx4 v[96:99], %4, %6, %7 offen offset:2560\n\t"
                 "s_mov_b32 exec_hi, 0\n\t"
                 "buffer_load_dwordx2 v[92:93], %5, %6, %7 offen offset:1024\n\tbuffer_load_dwordx2 v[100:101], %5, %6, %7 offen offset:3584\n\t"
                 "s_mov_b32 exec_hi, -1"
                 : "=&{v[88:91]}"(ql0), "=&{v[92:95]}"(qh0), "=&{v[96:99]}"(ql1), "=&{v[100:103]}"(qh1)
                 : "v"(hh * 512 + r * 16), "v"(r * 8), "s"(sq), "s"((head * 64 + qb * 8 + qp * 2) * REC));
  }
  dma16(pc, vL, skv, pc);
  BARRIER();
  dma16(pc + 4096, vL, skv, pc + 4096);
  dma16(pc + 8192, vL, skv, pc + 8192);
  const int mat = b >= 120, rg = (b - mat * 120) / 12, cb = b - mat * 120 - rg * 12, gw = b * 16 + wave;
  unsigned srcA, dstA, srcB, dstB;
  {
    const int pr = wave * 4 + (lane >> 4), cc = cb * 16 + (lane & 15), ct = (cc * 171) >> 11, c12 = cc - ct * 12;
    srcA = (rg * 64 + pr) * 3072 + cc * 16;
    dstA = (ct * 10 + rg) * 24576 + mat * 12288 + pr * 192 + (c12 ^ (mat * 2)) * 16;
    if (b >= 240) srcA = dstA = 0x80000000u;
    const int id = (gw < 800 ? gw : gw - 800) * 64 + lane, R = id / 80, c80 = id - R * 80, c8 = (c80 * 205) >> 11, k = R & 127;
    srcB = id * 16;
    dstB = (gw < 800 ? PK_O + c8 * (5 * 20480) : PK_D + c8 * (12 * 20480)) + (R >> 7) * 20480 + (k ^ ((k >> 1) & 4)) * 160 + (c80 - c8 * 10) * 16;
    if (gw >= 2720) srcB = dstB = 0x80000000u;
  }
  const i32x4 sa = mk_srd(mat ? wu : wg, D_ * F_ * 2), sb = mk_srd(gw < 800 ? wo : wd, gw < 800 ? D_ * D_ * 2 : D_ * F_ * 2);
  const i32x4 sp = mk_srd(ws + WS_PK, PK_SIZE);
  unsigned aK = L + kq * 40960 + hh * 512 + r * 16, aKh = L + kq * 40960 + 1024 + r * 8, aV = L + kq * 40960 + REC_V + hh * REC_VH + r * 16;
  const unsigned vc = r == 20 ? 0x3f803f80u : 0u;
  u32x4 v0 = {vc, vc, vc, vc}, v1 = v0;
  asm volatile("s_waitcnt vmcnt(2)\n\ts_barrier"
               : "+{v[88:91]}"(ql0), "+{v[92:95]}"(qh0), "+{v[96:99]}"(ql1), "+{v[100:103]}"(qh1), "+{v116}"(srcA), "+{v118}"(dstA),
                 "+{v117}"(srcB), "+{v119}"(dstB), "+{v[80:83]}"(v0), "+{v[84:87]}"(v1), "+{v104}"(aK), "+{v105}"(aKh), "+{v106}"(aV)
               :: "memory");
  TS(1, 1);
  f32x16 o0, o1;
  u32x4 wA;
  unsigned pcs = pc + 8192;
  asm volatile(K2_ASM
               : "=&{v[0:15]}"(o0), "=&{v[16:31]}"(o1), "+{v[80:83]}"(v0), "+{v[84:87]}"(v1), "=&{v[108:111]}"(wA),
                 "+{v[92:95]}"(qh0), "+{v[100:103]}"(qh1), [pc] "+s"(pcs)
               : "{v[88:91]}"(ql0), "{v[96:99]}"(ql1), "{v104}"(aK), "{v105}"(aKh), "{v106}"(aV), "{v107}"(vL),
                 "{v116}"(srcA), "{v117}"(srcB), "{v118}"(dstA), "{v119}"(dstB),
                 [mv] "s"(0x000fffff000fffffull), [sa] "s"(sa), [sb] "s"(sb), [sp] "s"(sp), [skv] "s"(skv), [ko] "s"(kq * 40960)
               : "memory", "scc", "v32", "v33", "v34", "v35", "v36", "v37", "v38", "v39", "v40", "v41", "v42", "v43", "v44", "v45", "v46", "v47",
                 "v48", "v49", "v50", "v51", "v52", "v53", "v54", "v55", "v56", "v57", "v58", "v59", "v60", "v61", "v62", "v63",
                 "v64", "v65", "v66", "v67", "v68", "v69", "v70", "v71", "v72", "v73", "v74", "v75", "v76", "v77", "v78", "v79",
                 "v112", "v113", "v114", "v115");
  TS(1, 2);
  k2_tail(ws, L, o0, o1, wA, wave, head, qb, mat, rg, cb);
}

struct Frag3 { bf16x8 a[2]; s16x4 b[5][2]; };
constexpr int K3_STAGE = 16384 + 20480;

template <int KD, bool FIRST>
__device__ __forceinline__ void proj_body(const char* __restrict__ A, const char* __restrict__ W, const char* __restrict__ R,
                                          char* __restrict__ C, char* __restrict__ stats, unsigned L) {
  constexpr int NS = KD / 128;
  const int tid = threadIdx.x, lane = tid & 63, wave = __builtin_amdgcn_readfirstlane(tid >> 6);
  const int b = blockIdx.x, xcd = b & 7, bi = b >> 3;
  const int rt = xcd * 4 + (bi & 3), ct = bi >> 2;
  const int m0 = rt * 64, n0 = ct * 80;
  const unsigned RS = L + 4 * K3_STAGE;
  TS(FIRST ? 2 : 4, 0);
  if (wave >= 4) {
    const int pw = wave - 4;
    const i32x4 sa = mk_srd(A, S_ * KD * 2), sw = mk_srd(W, KD * D_ * 2), sr = mk_srd(R, S_ * D_ * 2);
    const unsigned vL = lane * 16;
    auto issue = [&](int s) {
#pragma unroll
      for (int i = 0; i < 5; ++i) {
        if (i < 4) dma16(L + (s & 3) * K3_STAGE + (pw * 4 + i) * 1024, vL, sa, (s * 16 + pw * 4 + i) * 32768 + m0 * 16);
        dma16(L + (s & 3) * K3_STAGE + 16384 + (pw * 5 + i) * 1024, vL, sw, (ct * NS + s) * 20480 + (pw * 5 + i) * 1024);
      }
    };
    issue(0);
    BARRIER();
    issue(1);
    WAIT_V(9); BARRIER();
    issue(2);
#pragma unroll
    for (int i = 0; i < 3; ++i) {
      int p = pw * 3 + i; p = p > 9 ? 9 : p;
      if (FIRST) { int idx = p * 64 + lane, row = idx / 10, c = idx - row * 10; dma16(RS + p * 1024, row * 1280 + c * 16, sr, m0 * 1280 + n0 * 2); }
      else dma16(RS + p * 1024, vL, sr, (ct * 10 + p) * 32768 + m0 * 16);
    }
    issue(3);
    for (int s = 0; s < NS - 1; ++s) {
      if (s == 0) WAIT_V(21); else if (s < NS - 3) WAIT_V(18); else if (s == NS - 3) WAIT_V(9); else WAIT_V(0);
      BARRIER();
      if (s < NS - 4) issue(s + 4);
    }
    return;
  }
  const int wm = wave & 1, wk = wave >> 1, g = lane >> 4;
  const unsigned aA = L + (wk * 8 + g) * 1024 + (wm * 32 + (lane & 15)) * 16;
  unsigned aB[2];
#pragma unroll
  for (int t = 0; t < 2; ++t) aB[t] = L + 16384 + (wk * 64 + 8 * g + ((4 * t) ^ (4 * (g & 1))) + ((lane >> 2) & 3)) * 160 + (lane & 3) * 8;
  auto rd = [&](Frag3& f, unsigned sl, int ks, int t) {
    if (t == 0) f.a[0] = ldsr<bf16x8>(aA + sl + ks * 4096);
    else if (t == 3) f.a[1] = ldsr<bf16x8>(aA + sl + ks * 4096 + 256);
    else { const int u = t < 3 ? t - 1 : t - 2; f.b[u >> 1][u & 1] = ldstr(aB[u & 1] + sl + ks * 5120 + (u >> 1) * 32); }
  };
  f32x4 acc[2][5] = {};
  auto mm = [&](const Frag3& f, int t) { acc[t & 1][t >> 1] = MFMA16(cat8(f.b[t >> 1][0], f.b[t >> 1][1]), f.a[t & 1], acc[t & 1][t >> 1]); };
  auto body = [&](const Frag3& f, Frag3& n, unsigned sl, int ks) {
#pragma unroll
    for (int t = 1; t < 10; ++t) {
      SB();
      if (t <= 3) { rd(n, sl, ks, 2 * t - 2); rd(n, sl, ks, 2 * t - 1); } else rd(n, sl, ks, t + 2);
      SB(); mm(f, t);
    }
    SB();
  };
  Frag3 f0, f1;
  BARRIER(); BARRIER();
  TS(FIRST ? 2 : 4, 1);
#pragma unroll
  for (int t = 0; t < 12; ++t) rd(f0, 0, 0, t);
  SB();
  for (int s = 0; s < NS; ++s) {
    const unsigned sl = (s & 3) * K3_STAGE, nsl = ((s + 1) & 3) * K3_STAGE;
    mm(f0, 0); body(f0, f1, sl, 1);
    mm(f1, 0); SB();
    if (s < NS - 1) LBARRIER();
    body(f1, f0, nsl, 0);
  }
  TS(FIRST ? 2 : 4, 2);
  const unsigned X = L + ((NS + 1) & 3) * K3_STAGE;
#pragma unroll
  for (int j = 0; j < 5; ++j) ldsw<f32x4>(X + (wave * 5 + j) * 1024 + lane * 16, wk ? acc[0][j] : acc[1][j]);
  LBARRIER();
  const int lr = wm * 32 + wk * 16 + (lane & 15);
  const i32x4 rc = mk_srd(C, S_ * D_ * 2);
  const unsigned vo = FIRST ? (g >> 1) * 32768 + lr * 16 + (g & 1) * 8 : lr * 1280 + g * 8;
  f32x2 sum = {}, sq = {};
#pragma unroll
  for (int j = 0; j < 5; ++j) {
    f32x4 v = (wk ? acc[1][j] : acc[0][j]) + ldsr<f32x4>(X + ((wave ^ 2) * 5 + j) * 1024 + lane * 16);
    u32x2 rr = FIRST ? ldsr<u32x2>(RS + lr * 160 + j * 32 + g * 8) : ldsr<u32x2>(RS + (2 * j + (g >> 1)) * 1024 + lr * 16 + (g & 1) * 8);
    u32x2 o = {pk2(v[0] + bflo(rr.x), v[1] + bfhi(rr.x)), pk2(v[2] + bflo(rr.y), v[3] + bfhi(rr.y))};
    if (FIRST) {
      st8(o, rc, vo, (ct * 10 + 2 * j) * 32768 + m0 * 16);
      f32x2 h0 = {bflo(o.x), bfhi(o.x)}, h1 = {bflo(o.y), bfhi(o.y)};
      sum += h0 + h1;
      sq = fma2(h0, h0, fma2(h1, h1, sq));
    } else {
      st8(o, rc, vo, m0 * 1280 + (n0 + 16 * j) * 2);
    }
  }
  if (FIRST) {
    float s1 = sum.x + sum.y, s2 = sq.x + sq.y;
    s1 += __shfl_xor(s1, 16); s2 += __shfl_xor(s2, 16);
    s1 += __shfl_xor(s1, 32); s2 += __shfl_xor(s2, 32);
    if (lane < 16) *(f32x2*)(stats + (m0 + lr) * 64 + ct * 8) = f32x2{s1, s2};
  }
  TS(FIRST ? 2 : 4, 3);
}

__global__ __launch_bounds__(512, 2) void k3_oproj(const u16* __restrict__ x, char* __restrict__ ws) {
  __shared__ __attribute__((aligned(1024))) char smem[4 * K3_STAGE + 10240];
  proj_body<D_, true>(ws + WS_O, ws + WS_PK + PK_O, (const char*)x, ws + WS_H1, ws + WS_ST, (unsigned)(uintptr_t)(LDS3 char*)smem);
}
__global__ __launch_bounds__(512, 2) void k5_down(char* __restrict__ ws, u16* __restrict__ out) {
  __shared__ __attribute__((aligned(1024))) char smem[4 * K3_STAGE + 10240];
  proj_body<F_, false>(ws + WS_HH, ws + WS_PK + PK_D, ws + WS_H1, (char*)out, nullptr, (unsigned)(uintptr_t)(LDS3 char*)smem);
}

struct Frag4 { bf16x8 a[2]; s16x4 b[3][2]; };
constexpr int K4_STAGE = 16384 + 24576;

__global__ __launch_bounds__(512, 2) void k4_gateup(char* __restrict__ ws) {
  __shared__ __attribute__((aligned(1024))) char smem[4 * K4_STAGE];
  const unsigned L = (unsigned)(uintptr_t)(LDS3 char*)smem;
  const int tid = threadIdx.x, lane = tid & 63, wave = __builtin_amdgcn_readfirstlane(tid >> 6);
  const int b = blockIdx.x, xcd = b & 7, bi = b >> 3;
  const int rt = (xcd >> 1) * 4 + (bi & 3), ct = (xcd & 1) * 8 + (bi >> 2);
  const int m0 = rt * 128;
  const unsigned T = L + 2 * K4_STAGE;
  TS(3, 0);
  if (wave >= 4) {
    const int pw = wave - 4;
    const i32x4 sa = mk_srd(ws + WS_H1, S_ * D_ * 2), sw = mk_srd(ws + WS_PK + PK_G, PK_O);
    const i32x4 sst = mk_srd(ws + WS_ST, S_ * 64 + 16 * 8192);
    const unsigned vL = lane * 16;
    auto issue = [&](int s) {
      const unsigned sl = L + (s & 3) * K4_STAGE;
#pragma unroll
      for (int i = 0; i < 6; ++i) {
        const int p = pw * 4 + i, q = pw * 6 + i;
        if (i < 4) dma16(sl + p * 1024, vL, sa, (s * 8 + (p >> 1)) * 32768 + (m0 + (p & 1) * 64) * 16);
        dma16(sl + 16384 + q * 1024, vL, sw, (ct * 10 + s) * 24576 + q * 1024);
      }
    };
    issue(0);
    BARRIER();
    issue(1);
    WAIT_V(10); BARRIER();
    issue(2); issue(3);
    for (int s = 0; s < 9; ++s) {
      if (s < 7) WAIT_V(20); else if (s == 7) WAIT_V(14); else WAIT_V(0);
      BARRIER();
      if (s < 6) issue(s + 4);
      if (s == 6) {
        dma16(T + pw * 2048, vL, sst, m0 * 64 + pw * 2048);
        dma16(T + pw * 2048 + 1024, vL, sst, m0 * 64 + pw * 2048 + 1024);
        dma16(T + 8192 + pw * 2048, vL, sst, S_ * 64 + ct * 8192 + pw * 2048);
        dma16(T + 8192 + pw * 2048 + 1024, vL, sst, S_ * 64 + ct * 8192 + pw * 2048 + 1024);
      }
    }
    const int pt = tid - 256;
    if (pt < 128) {
      f32x4 t0 = ldsr<f32x4>(T + pt * 64), t1 = ldsr<f32x4>(T + pt * 64 + 16), t2 = ldsr<f32x4>(T + pt * 64 + 32), t3 = ldsr<f32x4>(T + pt * 64 + 48);
      f32x4 t = (t0 + t1) + (t2 + t3);
      float mean = (t[0] + t[2]) * (1.f / D_);
      float var = fmaxf((t[1] + t[3]) * (1.f / D_) - mean * mean, 0.f);
      ldsw<f32x2>(T + 16384 + pt * 8, f32x2{__builtin_amdgcn_rsqf(var + LN_EPS), -mean});
    } else if (pt < 128 + 96) {
      const unsigned a = T + 8192 + (pt - 128) * 8;
      f32x2 c = {};
#pragma unroll
      for (int k = 0; k < 10; ++k) c += ldsr<f32x2>(a + k * 768);
      ldsw<f32x2>(T + 17408 + (pt - 128) * 8, c);
    }
    LBARRIER();
    return;
  }
  const int wm = wave >> 1, wn = wave & 1, r = lane & 31, hh = lane >> 5;
  const unsigned aA = L + hh * 2048 + (wm * 64 + r) * 16;
  unsigned aB[3];
  {
    const int q = (lane >> 2) & 3, p = lane & 3, up = (lane >> 4) & 1;
#pragma unroll
    for (int j = 0; j < 3; ++j) aB[j] = L + 16384 + up * 12288 + (8 * hh + q) * 192 + (((wn * 3 + j) ^ up) * 32) + p * 8;
  }
  auto rd = [&](Frag4& f, unsigned sl, int ks, int t) {
    if (t == 0) { f.a[0] = ldsr<bf16x8>(aA + sl + ks * 4096); f.b[0][0] = ldstr(aB[0] + sl + ks * 3072); }
    if (t == 1) { f.b[0][1] = ldstr(aB[0] + sl + ks * 3072 + 768); f.a[1] = ldsr<bf16x8>(aA + sl + ks * 4096 + 512); }
    if (t >= 2) { f.b[t - 1][0] = ldstr(aB[t - 1] + sl + ks * 3072); f.b[t - 1][1] = ldstr(aB[t - 1] + sl + ks * 3072 + 768); }
  };
  f32x16 acc[2][3] = {};
  auto mm = [&](const Frag4& f, int t) { acc[t & 1][t >> 1] = MFMA32(cat8(f.b[t >> 1][0], f.b[t >> 1][1]), f.a[t & 1], acc[t & 1][t >> 1]); };
  auto body = [&](const Frag4& f, Frag4& n, unsigned sl, int ks) {
#pragma unroll
    for (int t = 1; t < 6; ++t) {
      SB();
      if (t <= 4) rd(n, sl, ks, t - 1);
      SB(); mm(f, t);
    }
    SB();
  };
  Frag4 f0, f1;
  BARRIER(); BARRIER();
  TS(3, 1);
#pragma unroll
  for (int t = 0; t < 4; ++t) rd(f0, 0, 0, t);
  SB();
  for (int s = 0; s < 10; ++s) {
    const unsigned sl = (s & 3) * K4_STAGE, nsl = ((s + 1) & 3) * K4_STAGE;
    mm(f0, 0); body(f0, f1, sl, 1);
    mm(f1, 0); body(f1, f0, sl, 2);
    mm(f0, 0); body(f0, f1, sl, 3);
    mm(f1, 0); SB();
    if (s < 9) LBARRIER();
    body(f1, f0, nsl, 0);
  }
  BARRIER();
  TS(3, 2);
  const float k1 = -2.f * 0.7978845608028654f * 1.4426950408889634f, k2 = k1 * 0.044715f;
  const i32x4 ro = mk_srd(ws + WS_HH, S_ * F_ * 2);
  const unsigned vo = hh * 32768 + r * 16;
#pragma unroll
  for (int j = 0; j < 3; ++j) {
    const int fc = (wn * 3 + j) * 16 + 4 * hh;
    f32x4 cg[2], cu[2];
#pragma unroll
    for (int g = 0; g < 2; ++g) { cg[g] = ldsr<f32x4>(T + 17408 + (fc + 8 * g) * 4); cu[g] = ldsr<f32x4>(T + 17408 + 384 + (fc + 8 * g) * 4); }
#pragma unroll
    for (int i = 0; i < 2; ++i) {
      const f32x2 ab = ldsr<f32x2>(T + 16384 + (wm * 64 + i * 32 + r) * 8);
      const f32x2 a = {ab.x, ab.x}, nm = {ab.y, ab.y};
      unsigned pk[4];
#pragma unroll
      for (int e = 0; e < 4; ++e) {
        const f32x2 gt = fma2(nm, sub2(cg[e >> 1], (e & 1) * 2), sub2(acc[i][j], 2 * e)) * a;
        const f32x2 up = fma2(nm, sub2(cu[e >> 1], (e & 1) * 2), sub2(acc[i][j], 8 + 2 * e)) * a;
        const f32x2 z = gt * fma2(gt * gt, f32x2{k2, k2}, f32x2{k1, k1});
        const f32x2 dn = f32x2{__builtin_amdgcn_exp2f(z.x), __builtin_amdgcn_exp2f(z.y)} + f32x2{1.f, 1.f};
        const f32x2 sg2 = {__builtin_amdgcn_rcpf(dn.x), __builtin_amdgcn_rcpf(dn.y)};
        pk[e] = pk2(gt * up * sg2);
      }
      auto s0 = __builtin_amdgcn_permlane32_swap(pk[0], pk[2], false, false);
      auto s1 = __builtin_amdgcn_permlane32_swap(pk[1], pk[3], false, false);
      st16(u32x4{s0[0], s1[0], s0[1], s1[1]}, ro, vo, (ct * 12 + (wn * 3 + j) * 2) * 32768 + (m0 + wm * 64 + i * 32) * 16);
    }
  }
  TS(3, 3);
}

extern "C" void kernel_launch(void* const* d_in, const int* in_sizes, int n_in, void* d_out, int out_size, void* d_ws,
                              size_t ws_size, hipStream_t stream) {
  const u16* x = (const u16*)d_in[0];
  const u16* wqkv = (const u16*)d_in[1];
  const u16* wo = (const u16*)d_in[2];
  const u16* wg = (const u16*)d_in[4];
  const u16* wu = (const u16*)d_in[5];
  const u16* wd = (const u16*)d_in[6];
  char* ws = (char*)d_ws;
  k1_qkv<<<256, 512, 0, stream>>>(x, wqkv, ws);
  k2_attn<<<256, 1024, 0, stream>>>(ws, wo, wg, wu, wd);
  k3_oproj<<<256, 512, 0, stream>>>(x, ws);
  k4_gateup<<<256, 512, 0, stream>>>(ws);
  k5_down<<<256, 512, 0, stream>>>(ws, (u16*)d_out);
}
```

```cpp
#define PADS -1
#define PADE -1
#define PADT 0
#define PADG 0
#include <hip/hip_runtime.h>
#include <stdint.h>

typedef unsigned short u16;
typedef __bf16 bf16x8 __attribute__((ext_vector_type(8)));
typedef __bf16 bf16x2 __attribute__((ext_vector_type(2)));
typedef short s16x4 __attribute__((ext_vector_type(4)));
typedef short s16x8 __attribute__((ext_vector_type(8)));
typedef float f32x2 __attribute__((ext_vector_type(2)));
typedef float f32x4 __attribute__((ext_vector_type(4)));
typedef float f32x16 __attribute__((ext_vector_type(16)));
typedef int i32x4 __attribute__((ext_vector_type(4)));
typedef unsigned u32x2 __attribute__((ext_vector_type(2)));
typedef unsigned u32x4 __attribute__((ext_vector_type(4)));
#define LDS3 __attribute__((address_space(3)))

constexpr int S_ = 2048, D_ = 640, F_ = 1536;
constexpr int REC = 2560, REC_V = 1280, REC_VS = 640, REC_VH = 320;
constexpr int REC_HEAD = 64 * REC;
constexpr size_t WS_REC = 0;
constexpr size_t WS_O = WS_REC + (size_t)64 * REC_HEAD;
constexpr size_t WS_H1 = WS_O + (size_t)S_ * D_ * 2;
constexpr size_t WS_HH = WS_H1 + (size_t)S_ * D_ * 2;
constexpr size_t WS_ST = WS_HH + (size_t)S_ * F_ * 2;
constexpr size_t WS_CS = WS_ST + (size_t)S_ * 64;
constexpr size_t WS_PK = WS_CS + 16 * 8192;
constexpr int PK_G = 0, PK_O = 16 * 10 * 24576, PK_D = PK_O + 8 * 5 * 20480, PK_SIZE = PK_D + 8 * 12 * 20480;
constexpr float LN_EPS = 1e-5f;

__device__ unsigned long long tsbuf[5 * 256];
#define TS(k, i) do { \
  if ((i) == 0 && threadIdx.x == 0) { __builtin_nontemporal_store(__builtin_amdgcn_s_memrealtime(), &tsbuf[(k) * 256 + blockIdx.x]); } \
  if ((i) == 0 && (k) == PADS) { unsigned long long t0_ = __hip_atomic_load(&tsbuf[0], __ATOMIC_RELAXED, __HIP_MEMORY_SCOPE_AGENT); \
     while (__builtin_amdgcn_s_memrealtime() - t0_ < PADT) __builtin_amdgcn_s_sleep(1); } \
  if ((i) == 3 && (k) == PADE && threadIdx.x == 0) { asm volatile("s_waitcnt vmcnt(0)" ::: "memory"); \
     unsigned long long t0_ = __hip_atomic_load(&tsbuf[(PADG ? 0 : (k) * 256 + blockIdx.x)], __ATOMIC_RELAXED, __HIP_MEMORY_SCOPE_AGENT); \
     while (__builtin_amdgcn_s_memrealtime() - t0_ < PADT) __builtin_amdgcn_s_sleep(1); } \
} while (0)
#define STPOL " sc1"
#define SB() __builtin_amdgcn_sched_barrier(0)
#define WAIT_V(n) asm volatile("s_waitcnt vmcnt(%0)" ::"n"(n) : "memory")
#define WAIT_VL(n) asm volatile("s_waitcnt vmcnt(%0) lgkmcnt(0)" ::"n"(n) : "memory")
#define BARRIER() asm volatile("s_barrier" ::: "memory")
#define LBARRIER() asm volatile("s_waitcnt lgkmcnt(0)\n\ts_barrier" ::: "memory")

__device__ __forceinline__ i32x4 mk_srd(const void* p, unsigned bytes) {
  unsigned long long a = (unsigned long long)p;
  i32x4 r;
  r.x = (int)(unsigned)a; r.y = (int)((unsigned)(a >> 32) & 0xffffu); r.z = (int)bytes; r.w = 0x00020000;
  return r;
}
__device__ __forceinline__ void st16(u32x4 v, i32x4 srd, unsigned voff, unsigned soff) {
  asm volatile("buffer_store_dwordx4 %0, %1, %2, %3 offen" STPOL "\n\ts_nop 1" ::"v"(v), "v"(voff), "s"(srd), "s"(soff));
}
__device__ __forceinline__ void st8(u32x2 v, i32x4 srd, unsigned voff, unsigned soff) {
  asm volatile("buffer_store_dwordx2 %0, %1, %2, %3 offen" STPOL ::"v"(v), "v"(voff), "s"(srd), "s"(soff));
}
__device__ __forceinline__ void dma16(unsigned lds, unsigned voff, i32x4 srd, unsigned soff) {
  asm volatile("s_mov_b32 m0, %0\n\ts_nop 0\n\tbuffer_load_dwordx4 %1, %2, %3 offen lds"
               ::"s"(lds), "v"(voff), "s"(srd), "s"(soff) : "memory");
}
__device__ __forceinline__ void dma16_masked(unsigned lds, unsigned voff, i32x4 srd, unsigned soff, unsigned long long mask) {
  asm volatile("s_mov_b32 m0, %0\n\ts_mov_b64 exec, %4\n\tbuffer_load_dwordx4 %1, %2, %3 offen lds\n\ts_mov_b64 exec, -1"
               ::"s"(lds), "v"(voff), "s"(srd), "s"(soff), "s"(mask) : "memory");
}
template <typename T> __device__ __forceinline__ T ldsr(unsigned a) { return *(LDS3 const T*)(uintptr_t)a; }
template <typename T> __device__ __forceinline__ void ldsw(unsigned a, T v) { *(LDS3 T*)(uintptr_t)a = v; }
__device__ __forceinline__ s16x4 ldstr(unsigned a) { return __builtin_amdgcn_ds_read_tr16_b64_v4i16((LDS3 s16x4*)(uintptr_t)a); }
__device__ __forceinline__ bf16x8 cat8(s16x4 lo, s16x4 hi) {
  s16x8 r = __builtin_shufflevector(lo, hi, 0, 1, 2, 3, 4, 5, 6, 7);
  return __builtin_bit_cast(bf16x8, r);
}
__device__ __forceinline__ unsigned pk2(f32x2 v) { return __builtin_bit_cast(unsigned, __builtin_convertvector(v, bf16x2)); }
__device__ __forceinline__ unsigned pk2(float a, float b) { return pk2(f32x2{a, b}); }
__device__ __forceinline__ float bflo(unsigned v) { return __uint_as_float(v << 16); }
__device__ __forceinline__ float bfhi(unsigned v) { return __uint_as_float(v & 0xffff0000u); }
__device__ __forceinline__ s16x4 lo4(bf16x8 v) { s16x8 t = __builtin_bit_cast(s16x8, v); return __builtin_shufflevector(t, t, 0, 1, 2, 3); }
__device__ __forceinline__ s16x4 hi4(bf16x8 v) { s16x8 t = __builtin_bit_cast(s16x8, v); return __builtin_shufflevector(t, t, 4, 5, 6, 7); }
__device__ __forceinline__ f32x2 sub2(const f32x16& v, int i) { return f32x2{v[i], v[i + 1]}; }
__device__ __forceinline__ f32x2 sub2(const f32x4& v, int i) { return f32x2{v[i], v[i + 1]}; }
__device__ __forceinline__ f32x2 fma2(f32x2 a, f32x2 b, f32x2 c) { return __builtin_elementwise_fma(a, b, c); }
#define MFMA32(a, b, c) __builtin_amdgcn_mfma_f32_32x32x16_bf16(a, b, c, 0, 0, 0)
#define MFMA16(a, b, c) __builtin_amdgcn_mfma_f32_16x16x32_bf16(a, b, c, 0, 0, 0)
#define MFMA32K8(a, b, c) __builtin_amdgcn_mfma_f32_32x32x8bf16_1k(a, b, c, 0, 0, 0)
#define MFMA4(a, b, c) __builtin_amdgcn_mfma_f32_4x4x4bf16_1k(a, b, c, 0, 0, 0)

struct Frag1 { bf16x8 a[2]; s16x4 b[2][2]; };

template <bool SWAP>
__device__ __forceinline__ void k1_consumer(char* __restrict__ rec, unsigned L, int rt, int ct, int wave) {
  const int tid = threadIdx.x, lane = tid & 63;
  const int wm = wave >> 1, wn = wave & 1, r = lane & 31, hh = lane >> 5;
  unsigned aA[2][4], aB[2];
#pragma unroll
  for (int i = 0; i < 2; ++i) {
    int row = wm * 64 + ((i ^ wn) * 32) + r;
#pragma unroll
    for (int ks = 0; ks < 4; ++ks) aA[i][ks] = L + row * 128 + (((2 * ks + hh) ^ ((row >> 1) & 7)) << 4);
    int q = (lane >> 2) & 3, p = lane & 3, ch = (lane >> 4) & 1;
    int c = wn * 8 + ((i ^ wm) * 4) + 2 * ch + (p >> 1);
    aB[i] = L + 16384 + (8 * hh + q) * 256 + ((c ^ (q << 2)) << 4) + 8 * (p & 1);
  }
  auto rd = [&](Frag1& f, unsigned sl, int ks, int t) {
    if (t == 0) { f.a[0] = ldsr<bf16x8>(aA[0][ks] + sl); f.b[0][0] = ldstr(aB[0] + sl + ks * 4096); }
    if (t == 1) { f.b[0][1] = ldstr(aB[0] + sl + ks * 4096 + 1024); f.b[1][0] = ldstr(aB[1] + sl + ks * 4096); }
    if (t == 2) { f.b[1][1] = ldstr(aB[1] + sl + ks * 4096 + 1024); f.a[1] = ldsr<bf16x8>(aA[1][ks] + sl); }
  };
  f32x16 acc[2][2] = {};
  f32x4 sq0 = {}, sq1 = {}, cs0 = {}, cs1 = {};
  const s16x4 ones = {0x3f80, 0x3f80, 0x3f80, 0x3f80};
  auto mm = [&](const Frag1& f, int t) {
    const int i = t >> 1, j = t & 1;
    const bf16x8 b = cat8(f.b[j][0], f.b[j][1]);
    acc[i][j] = SWAP ? MFMA32(b, f.a[i], acc[i][j]) : MFMA32(f.a[i], b, acc[i][j]);
    if (t == 3) {
      sq0 = MFMA4(lo4(f.a[0]), lo4(f.a[0]), sq0); cs0 = MFMA4(ones, f.b[0][0], cs0);
      sq1 = MFMA4(hi4(f.a[0]), hi4(f.a[0]), sq1); cs1 = MFMA4(ones, f.b[0][1], cs1);
    }
  };
  auto kstep = [&](const Frag1& f, Frag1& n, unsigned sl, int ks) {
    mm(f, 0); SB(); rd(n, sl, ks, 0); SB(); mm(f, 1); SB(); rd(n, sl, ks, 1); SB(); mm(f, 2); SB(); rd(n, sl, ks, 2); SB(); mm(f, 3); SB();
  };
  Frag1 f0, f1;
  BARRIER(); BARRIER();
  TS(0, 1);
  rd(f0, 0, 0, 0); rd(f0, 0, 0, 1); rd(f0, 0, 0, 2);
  SB();
  for (int s = 0; s < 10; ++s) {
    const unsigned sl = (s & 3) * 32768, nsl = ((s + 1) & 3) * 32768;
    kstep(f0, f1, sl, 1);
    kstep(f1, f0, sl, 2);
    kstep(f0, f1, sl, 3);
    mm(f1, 0); SB();
    if (s < 9) LBARRIER();
    SB(); rd(f0, nsl, 0, 0); SB(); mm(f1, 1); SB(); rd(f0, nsl, 0, 1); SB(); mm(f1, 2); SB(); rd(f0, nsl, 0, 2); SB(); mm(f1, 3); SB();
  }
  TS(0, 2);
  const unsigned T = L + 131072;
  {
    f32x4 sq = sq0 + sq1;
    int e = lane & 3;
    float sv = e == 0 ? sq[0] : e == 1 ? sq[1] : e == 2 ? sq[2] : sq[3];
    ldsw<float>(T + hh * 512 + (wm * 64 + wn * 32 + r) * 4, sv);
    ldsw<float>(T + 1024 + hh * 512 + (wn * 64 + wm * 32 + r) * 4, cs0[0] + cs1[0]);
    if (wn == 1) {
#pragma unroll
      for (int i = 0; i < 2; ++i) {
        const int rb = wm * 64 + (i ^ 1) * 32;
        if (SWAP) {
          float v = wm ? acc[i][0][12] : acc[i][1][12];
          if (hh == 0) ldsw<float>(T + 2048 + (rb + r) * 4, v);
        } else if (r == 24) {
#pragma unroll
          for (int g = 0; g < 4; ++g) {
            f32x4 v;
#pragma unroll
            for (int e2 = 0; e2 < 4; ++e2) v[e2] = wm ? acc[i][0][4 * g + e2] : acc[i][1][4 * g + e2];
            ldsw<f32x4>(T + 2048 + (rb + 8 * g + 4 * hh) * 4, v);
          }
        }
      }
    }
  }
  LBARRIER();
  if (tid < 128) {
    float sum = ldsr<float>(T + 2048 + tid * 4);
    float sq = ldsr<float>(T + tid * 4) + ldsr<float>(T + 512 + tid * 4);
    float mean = sum * (1.f / D_);
    float var = fmaxf(sq * (1.f / D_) - mean * mean, 0.f);
    float rs = __builtin_amdgcn_rsqf(var + LN_EPS);
    if (SWAP) ldsw<f32x2>(T + 2560 + tid * 8, f32x2{rs, -mean});
    else { ldsw<float>(T + 2560 + tid * 4, rs); ldsw<float>(T + 3072 + tid * 4, -mean); }
  } else {
    int c = tid - 128;
    ldsw<float>(T + 3584 + c * 4, ldsr<float>(T + 1024 + c * 4) + ldsr<float>(T + 1536 + c * 4));
  }
  LBARRIER();
  const i32x4 ro = mk_srd(rec, 64 * REC_HEAD);
  const int tile0 = rt * 4 + wm * 2;
  if (SWAP) {
    const float qs = 0.22360679774997896f * 1.4426950408889634f;
    f32x2 ab[2];
#pragma unroll
    for (int i = 0; i < 2; ++i) ab[i] = ldsr<f32x2>(T + 2560 + (wm * 64 + (i ^ wn) * 32 + r) * 8);
    const unsigned vlo = (hh ^ wn) * REC + r * 16;
    const unsigned vhi = hh * REC_HEAD + 1024 + r * 8;
    const unsigned X = L + wave * 8192;
#pragma unroll
    for (int j = 0; j < 2; ++j) {
#pragma unroll
      for (int g = 0; g < 4; ++g) {
        const int P = wn * 8 + (j ^ wm) * 4 + g;
        if (P < 15) {
          const int hp = (P * 13) >> 6, t = P - hp * 5, hA = ct * 6 + 2 * hp, od = (t + 1) >> 2;
          const f32x4 cs = ldsr<f32x4>(T + 3584 + (P * 8 + 4 * hh) * 4);
          const float sc = hA < 32 ? qs : 1.f;
          u32x2 v[2];
#pragma unroll
          for (int i = 0; i < 2; ++i) {
            const f32x2 a = {ab[i].x * sc, ab[i].x * sc}, nm = {ab[i].y, ab[i].y};
            v[i].x = pk2(fma2(nm, sub2(cs, 0), sub2(acc[i][j], 4 * g)) * a);
            v[i].y = pk2(fma2(nm, sub2(cs, 2), sub2(acc[i][j], 4 * g + 2)) * a);
          }
          if (hA >= 64) {
#pragma unroll
            for (int i = 0; i < 2; ++i) ldsw<u32x2>(X + ((i ^ wn) * 32 + r) * 104 + (t * 8 + 4 * hh) * 2, v[i]);
          } else if (t == 2) {
#pragma unroll
            for (int i = 0; i < 2; ++i) st8(v[i], ro, vhi, hA * REC_HEAD + (tile0 + (i ^ wn)) * REC);
          } else {
            auto s0 = __builtin_amdgcn_permlane32_swap(v[0].x, v[1].x, false, false);
            auto s1 = __builtin_amdgcn_permlane32_swap(v[0].y, v[1].y, false, false);
            st16(u32x4{s0[0], s1[0], s0[1], s1[1]}, ro, vlo, (hA + od) * REC_HEAD + tile0 * REC + (t - 3 * od) * 512);
          }
        }
      }
    }
    if (ct == 10 && wn == 1) {
      const int g4 = lane >> 4, c16 = lane & 15;
      const unsigned ra = X + (16 * (g4 >> 1) + 4 * (g4 & 1) + ((lane >> 2) & 3)) * 104 + (lane & 3) * 8;
#pragma unroll
      for (int mb = 0; mb < 2; ++mb) {
#pragma unroll
        for (int c = 0; c < 3; ++c) {
          s16x4 lo = ldstr(ra + mb * 32 * 104 + c * 32), hi = ldstr(ra + mb * 32 * 104 + 8 * 104 + c * 32);
          const int col = c * 16 + c16, hd = col >= 20, d = col - 20 * hd;
          const unsigned vo = hd * REC_HEAD + (g4 >> 1) * REC_VS + (g4 & 1) * REC_VH + d * 16;
          const unsigned so = 32 * REC_HEAD + (tile0 + mb) * REC + REC_V;
          if (col < 40) {
            st16(__builtin_bit_cast(u32x4, cat8(lo, hi)), ro, vo, so);
          }
        }
      }
    }
  } else {
#pragma unroll
    for (int i = 0; i < 2; ++i) {
      const int rb = wm * 64 + (i ^ wn) * 32;
      f32x4 av[4], bv[4];
#pragma unroll
      for (int g = 0; g < 4; ++g) {
        av[g] = ldsr<f32x4>(T + 2560 + (rb + 8 * g + 4 * hh) * 4);
        bv[g] = ldsr<f32x4>(T + 3072 + (rb + 8 * g + 4 * hh) * 4);
      }
#pragma unroll
      for (int j = 0; j < 2; ++j) {
        const int lc = wn * 64 + (j ^ wm) * 32 + r;
        if (lc < 120) {
          const float c1 = ldsr<float>(T + 3584 + lc * 4);
          const f32x2 cs = {c1, c1};
          const int gc = ct * 120 + lc - 1280;
          const int hd = (gc * 205) >> 12, d = gc - hd * 20;
          u32x4 v[2];
#pragma unroll
          for (int g = 0; g < 4; ++g) {
            v[g >> 1][(g & 1) * 2] = pk2(fma2(sub2(bv[g], 0), cs, sub2(acc[i][j], 4 * g)) * sub2(av[g], 0));
            v[g >> 1][(g & 1) * 2 + 1] = pk2(fma2(sub2(bv[g], 2), cs, sub2(acc[i][j], 4 * g + 2)) * sub2(av[g], 2));
          }
          const unsigned vo = hd * REC_HEAD + hh * REC_VH + d * 16, so = 32 * REC_HEAD + (tile0 + (i ^ wn)) * REC + REC_V;
          st16(v[0], ro, vo, so); st16(v[1], ro, vo + REC_VS, so);
        }
      }
    }
  }
  TS(0, 3);
}
constexpr bool chk_div() {
  for (int i = 0; i < 640; ++i) if (((i * 205) >> 12) != i / 20) return false;
  return true;
}
static_assert(chk_div(), "magic division");
constexpr bool chk_div2() {
  for (int i = 0; i < 192; ++i) if (((i * 171) >> 11) != i / 12) return false;
  for (int i = 0; i < 80; ++i) if (((i * 205) >> 11) != i / 10) return false;
  return true;
}
static_assert(chk_div2(), "magic division");

__global__ __launch_bounds__(512, 2) void k1_qkv(const u16* __restrict__ x, const u16* __restrict__ w, char* __restrict__ ws) {
  __shared__ __attribute__((aligned(1024))) char smem[131072 + 4096];
  const unsigned L = (unsigned)(uintptr_t)(LDS3 char*)smem;
  const int b = blockIdx.x, xcd = b & 7, bi = b >> 3;
  const int rt = (xcd >> 1) * 4 + (bi & 3), ct = (xcd & 1) * 8 + (bi >> 2);
  const int lane = threadIdx.x & 63, wave = __builtin_amdgcn_readfirstlane(threadIdx.x >> 6);
  TS(0, 0);
  if (wave >= 4) {
    const int pw = wave - 4;
    const i32x4 sx = mk_srd(x, S_ * D_ * 2), sw = mk_srd(w, D_ * 1920 * 2);
    unsigned vA[4], vB[4];
#pragma unroll
    for (int i = 0; i < 4; ++i) {
      int row = (pw * 4 + i) * 8 + (lane >> 3);
      vA[i] = row * 1280 + (((lane & 7) ^ ((row >> 1) & 7)) << 4);
      int k = (pw * 4 + i) * 4 + (lane >> 4);
      vB[i] = k * 3840 + (((lane & 15) ^ ((k & 3) << 2)) << 4);
    }
    const unsigned long long bmask = ~((1ull << 15) | (1ull << 27) | (1ull << 39) | (1ull << 51));
    const unsigned oA = rt * 128 * 1280, oB = ct * 240;
    auto issue = [&](int s) {
#pragma unroll
      for (int i = 0; i < 4; ++i) {
        dma16(L + (s & 3) * 32768 + (pw * 4 + i) * 1024, vA[i], sx, oA + s * 128);
        dma16_masked(L + (s & 3) * 32768 + 16384 + (pw * 4 + i) * 1024, vB[i], sw, oB + s * (64 * 3840), bmask);
      }
    };
    issue(0);
    ldsw<u32x4>(L + pw * 32768 + 16384 + lane * 256 + ((15 ^ ((lane & 3) << 2)) << 4), u32x4{0x3f80u, 0u, 0u, 0u});
    BARRIER();
    issue(1);
    WAIT_VL(8); BARRIER();
    issue(2); issue(3);
    for (int s = 0; s < 9; ++s) {
      if (s < 7) WAIT_V(16); else if (s == 7) WAIT_V(8); else WAIT_V(0);
      BARRIER();
      if (s < 6) issue(s + 4);
    }
    return;
  }
  if (ct <= 10) k1_consumer<true>(ws + WS_REC, L, rt, ct, wave);
  else k1_consumer<false>(ws + WS_REC, L, rt, ct, wave);
}

__device__ __forceinline__ void exp4(const f32x16& s, int g, unsigned* p) {
  float e0 = __builtin_amdgcn_exp2f(s[4 * g + 0]), e1 = __builtin_amdgcn_exp2f(s[4 * g + 1]);
  float e2 = __builtin_amdgcn_exp2f(s[4 * g + 2]), e3 = __builtin_amdgcn_exp2f(s[4 * g + 3]);
  p[0] = pk2(e0, e1); p[1] = pk2(e2, e3);
}
__device__ __forceinline__ bf16x8 px8(const unsigned* p) { u32x4 v = {p[0], p[1], p[2], p[3]}; return __builtin_bit_cast(bf16x8, v); }
__device__ __forceinline__ bf16x8 bx8(u32x4 v) { return __builtin_bit_cast(bf16x8, v); }
__device__ __forceinline__ float max16(const f32x16& s) {
  float m = fmaxf(fmaxf(fmaxf(s[0], s[1]), fmaxf(s[2], s[3])), fmaxf(fmaxf(s[4], s[5]), fmaxf(s[6], s[7])));
  float n = fmaxf(fmaxf(fmaxf(s[8], s[9]), fmaxf(s[10], s[11])), fmaxf(fmaxf(s[12], s[13]), fmaxf(s[14], s[15])));
  m = fmaxf(m, n);
  return fmaxf(m, __shfl_xor(m, 32));
}

#define K2_ASM \
  ".macro XE r\n v_exp_f32 v[\\r], v[\\r]\n.endm\n" \
  ".macro XC p, r\n v_cvt_pk_bf16_f32 v[\\p], v[\\r], v[\\r+1]\n.endm\n" \
  ".macro XM d, a, b, c\n v_mfma_f32_32x32x16_bf16 v[\\d:\\d+15], v[\\a:\\a+3], v[\\b:\\b+3], \\c\n.endm\n" \
  ".macro XPH s, n, q, op, oc, t, b\n" \
  " XE \\s\n XE \\s+1\n" \
  " .if \\t + \\b > 0\n XM \\op, 84, 68, v[\\op:\\op+15]\n .endif\n" \
  " .if \\b == 0\n" \
  "  .if \\t == 1\n s_waitcnt vmcnt(9)\n s_barrier\n .endif\n" \
  "  .if \\t == 4\n s_waitcnt vmcnt(2)\n s_barrier\n .endif\n" \
  "  s_mov_b64 exec, %[mv]\n ds_read_b128 v[80:83], v106 offset:\\t*2560\n ds_read_b128 v[84:87], v106 offset:\\t*2560+640\n s_mov_b64 exec, -1\n" \
  " .endif\n" \
  " XE \\s+2\n XE \\s+3\n XC 64, \\s\n XC 65, \\s+2\n XE \\s+4\n XE \\s+5\n" \
  " .if \\b == 1\n s_waitcnt lgkmcnt(0)\n .endif\n" \
  " .if \\t + \\b < 16\n XM \\n, 72, \\q, 0\n .endif\n" \
  " XE \\s+6\n XE \\s+7\n XC 66, \\s+4\n XC 67, \\s+6\n XE \\s+8\n" \
  " .if \\t + \\b < 16\n XM \\n, 76, \\q+4, v[\\n:\\n+15]\n .endif\n" \
  " .if (\\b == 0) && (\\t < 15)\n ds_read_b128 v[72:75], v104 offset:(\\t+1)*2560\n ds_read_b64 v[76:77], v105 offset:(\\t+1)*2560\n .endif\n" \
  " .if (\\b == 1) && (\\t == 8)\n s_waitcnt vmcnt(0)\n" \
  "  buffer_store_dwordx4 v[108:111], v118, %[sp], 0 offen sc1\n buffer_store_dwordx4 v[112:115], v119, %[sp], 0 offen sc1\n .endif\n" \
  " XE \\s+9\n XE \\s+10\n XE \\s+11\n XC 68, \\s+8\n XC 69, \\s+10\n XE \\s+12\n XE \\s+13\n" \
  " .if \\b == 0\n .if \\t < 15\n s_waitcnt lgkmcnt(2)\n .else\n s_waitcnt lgkmcnt(0)\n .endif\n .endif\n" \
  " .if \\t == 0\n XM \\oc, 80, 64, 0\n .else\n XM \\oc, 80, 64, v[\\oc:\\oc+15]\n .endif\n" \
  " XE \\s+14\n XE \\s+15\n XC 70, \\s+12\n XC 71, \\s+14\n" \
  ".endm\n" \
  ".macro XD\n s_add_u32 %[pc], %[pc], 4096\n s_mov_b32 m0, %[pc]\n s_nop 0\n buffer_load_dwordx4 v107, %[skv], %[pc] offen lds\n.endm\n" \
  ".macro XMAX d, s\n v_max3_f32 v[\\d], v[\\s], v[\\s+1], v[\\s+2]\n v_max3_f32 v[\\d+1], v[\\s+3], v[\\s+4], v[\\s+5]\n" \
  " v_max3_f32 v[\\d], v[\\d], v[\\s+6], v[\\s+7]\n v_max3_f32 v[\\d+1], v[\\d+1], v[\\s+8], v[\\s+9]\n" \
  " v_max3_f32 v[\\d], v[\\d], v[\\s+10], v[\\s+11]\n v_max3_f32 v[\\d+1], v[\\d+1], v[\\s+12], v[\\s+13]\n" \
  " v_max3_f32 v[\\d], v[\\d], v[\\s+14], v[\\s+15]\n v_max_f32 v[\\d], v[\\d], v[\\d+1]\n.endm\n" \
  "v_subrev_u32 v64, %[ko], v104\n v_subrev_u32 v65, %[ko], v105\n" \
  "ds_read_b128 v[72:75], v64\n ds_read_b64 v[76:77], v65\n" \
  "v_mov_b32 v78, 0x3f803f80\n v_mov_b32 v79, 0\n" \
  "XD\n XD\n s_waitcnt lgkmcnt(0)\n" \
  "XM 32, 72, 88, 0\n XM 48, 72, 96, 0\n XM 32, 76, 92, v[32:47]\n XM 48, 76, 100, v[48:63]\n" \
  "ds_read_b128 v[72:75], v104\n ds_read_b64 v[76:77], v105\n" \
  "XD\n XD\n XD\n XD\n XD\n" \
  "buffer_load_dwordx4 v[108:111], v116, %[sa], 0 offen\n" \
  "buffer_load_dwordx4 v[112:115], v117, %[sb], 0 offen\n" \
  "s_mov_b32 exec_hi, 0\n XMAX 64, 32\n XMAX 66, 48\n" \
  "v_cvt_pk_bf16_f32 v65, -v64, 0\n v_cvt_pk_bf16_f32 v67, -v66, 0\n v_lshlrev_b32 v65, 16, v65\n v_lshlrev_b32 v67, 16, v67\n" \
  "v_sub_f32 v65, -v64, v65\n v_sub_f32 v67, -v66, v67\n v_cvt_pk_bf16_f32 v94, -v64, v65\n v_cvt_pk_bf16_f32 v102, -v66, v67\n" \
  "s_mov_b32 exec_hi, -1\n s_waitcnt lgkmcnt(0)\n s_nop 0\n" \
  "XM 32, 72, 88, 0\n XM 32, 76, 92, v[32:47]\n s_nop 7\n s_nop 3\n" \
  ".irp t,0,1,2,3,4,5,6,7,8,9,10,11,12,13,14,15\n XPH 32, 48, 96, 16, 0, \\t, 0\n XPH 48, 32, 88, 0, 16, \\t, 1\n.endr\n" \
  "s_nop 1\n XM 16, 84, 68, v[16:31]\n s_nop 7\n s_nop 3\n" \
  ".purgem XE\n.purgem XC\n.purgem XM\n.purgem XPH\n.purgem XD\n.purgem XMAX\n"

__device__ __forceinline__ void k2_tail(char* __restrict__ ws, unsigned L, const f32x16& o0, const f32x16& o1, u32x4 wA,
                                        int wave, int head, int qb, int mat, int rg, int cb) {
  int lane;
  asm volatile("v_mbcnt_lo_u32_b32 %0, -1, 0\n\tv_mbcnt_hi_u32_b32 %0, -1, %0" : "=v"(lane));
  const int b = blockIdx.x, qp = wave & 3, kq = wave >> 2, r = lane & 31, hh = lane >> 5;
  const char* rec = ws + WS_REC;
  const char* qrec = rec + (size_t)(head * 64 + qb * 8 + qp * 2) * REC;
  const unsigned vL = lane * 16, aK = hh * 512 + r * 16, aKh = 1024 + r * 8, aV = REC_V + hh * REC_VH + r * 16;
  const unsigned vc = r == 20 ? 0x3f803f80u : 0u;
  f32x4 cs;
  {
    const int i = lane & 15;
    const unsigned oh = (i & 1) ? 0x3f800000u : 0x3f80u;
    const u32x4 sel = {i >> 1 == 0 ? oh : 0u, i >> 1 == 1 ? oh : 0u, i >> 1 == 2 ? oh : 0u, i >> 1 == 3 ? oh : 0u};
    cs = MFMA16(bx8(sel), bx8(wA), f32x4{});
  }
  BARRIER();
  const unsigned CS = L + 73728;
  if (lane < 32) ldsw<f32x4>(CS + wave * 512 + (lane & 15) * 32 + (lane >> 4) * 16, cs);
#pragma unroll
  for (int g = 0; g < 3; ++g) {
    const unsigned a = L + qp * 18432 + g * 1024 + vL;
    if (kq != 0) ldsw<f32x4>(a + (kq - 1) * 3072, f32x4{o0[4 * g], o0[4 * g + 1], o0[4 * g + 2], o0[4 * g + 3]});
    if (kq != 1) ldsw<f32x4>(a + 9216 + (kq ? kq - 1 : 0) * 3072, f32x4{o1[4 * g], o1[4 * g + 1], o1[4 * g + 2], o1[4 * g + 3]});
  }
  LBARRIER();
  if (kq >= 2) {
    if (kq == 2 && qp < 2 && b < 240) {
      const int c = qp * 64 + lane;
      float s = 0.f;
#pragma unroll
      for (int w = 0; w < 16; ++w) s += ldsr<float>(CS + w * 512 + c * 4);
      const int col = cb * 128 + c, ct = col / 96;
      *(float*)(ws + WS_CS + ct * 8192 + (rg * 192 + col - ct * 96 + mat * 96) * 4) = s;
    }
    return;
  }
  f32x4 o[3];
#pragma unroll
  for (int g = 0; g < 3; ++g) {
    const unsigned a = L + qp * 18432 + kq * 9216 + g * 1024 + vL;
#pragma unroll
    for (int e = 0; e < 4; ++e) o[g][e] = kq ? o1[4 * g + e] : o0[4 * g + e];
    o[g] += ldsr<f32x4>(a) + (ldsr<f32x4>(a + 3072) + ldsr<f32x4>(a + 6144));
  }
  float l = __shfl(o[2][0], r + 32);
  bool ok = l <= 0x1p100f && l >= 0x1p-100f;
#pragma unroll
  for (int i = 0; i < 12; ++i) ok = ok && (__builtin_fabsf(o[i >> 2][i & 3]) <= 0x1p126f);
  if (__builtin_amdgcn_ballot_w64(!ok) != 0ull) {
    const char* kv = rec + (size_t)(32 + head) * REC_HEAD;
    const bf16x8 ql = *(const bf16x8*)(qrec + kq * REC + hh * 512 + r * 16);
    const u32x2 t = *(const u32x2*)(qrec + kq * REC + 1024 + r * 8);
    const bf16x8 qh = bx8(hh ? u32x4{} : u32x4{t.x, t.y, 0u, 0u});
    float m = -__builtin_inff();
    f32x16 oo = {};
    for (int t = 0; t < 64; ++t) {
      const char* q = kv + t * REC;
      const u32x2 kh = *(const u32x2*)(q + aKh);
      f32x16 s = {};
      s = MFMA32(*(const bf16x8*)(q + aK), ql, s);
      s = MFMA32(bx8(u32x4{kh.x, kh.y, 0u, 0u}), qh, s);
      float mn = fmaxf(m, max16(s));
      float c = __builtin_amdgcn_exp2f(m - mn);
      m = mn;
#pragma unroll
      for (int i = 0; i < 16; ++i) { s[i] -= mn; oo[i] *= c; }
      unsigned pp[8];
#pragma unroll
      for (int g = 0; g < 4; ++g) exp4(s, g, pp + 2 * g);
      u32x4 a0 = {vc, vc, vc, vc}, a1 = a0;
      if (r < 20) { a0 = *(const u32x4*)(q + aV); a1 = *(const u32x4*)(q + aV + REC_VS); }
      oo = MFMA32(bx8(a0), px8(pp), oo);
      oo = MFMA32(bx8(a1), px8(pp + 4), oo);
    }
#pragma unroll
    for (int i = 0; i < 12; ++i) o[i >> 2][i & 3] = oo[i];
    l = __shfl(oo[8], r + 32);
  }
  const float inv = 1.f / l;
  const int row = qb * 256 + (qp * 2 + kq) * 32 + r;
  const i32x4 so = mk_srd(ws + WS_O, S_ * D_ * 2);
#pragma unroll
  for (int g = 0; g < 3; ++g) {
    if (g < 2 || hh == 0) {
      const int col = head * 20 + 8 * g + 4 * hh;
      u32x2 v = {pk2(o[g][0] * inv, o[g][1] * inv), pk2(o[g][2] * inv, o[g][3] * inv)};
      st8(v, so, (col >> 3) * 32768 + row * 16 + (col & 7) * 2, 0);
    }
  }
  TS(1, 3);
}

__global__ __launch_bounds__(1024, 4) void k2_attn(char* __restrict__ ws, const u16* __restrict__ wo, const u16* __restrict__ wg,
                                                   const u16* __restrict__ wu, const u16* __restrict__ wd) {
  __shared__ __attribute__((aligned(1024))) char smem[REC_HEAD];
  const unsigned L = (unsigned)(uintptr_t)(LDS3 char*)smem;
  const int tid = threadIdx.x, lane = tid & 63, wave = __builtin_amdgcn_readfirstlane(tid >> 6);
  const int b = blockIdx.x, xcd = b & 7, bi = b >> 3;
  const int head = xcd * 4 + (bi & 3), qb = bi >> 2;
  const int qp = wave & 3, kq = wave >> 2;
  const int r = lane & 31, hh = lane >> 5;
  const char* rec = ws + WS_REC;
  const i32x4 skv = mk_srd(rec + (size_t)(32 + head) * REC_HEAD - L, REC_HEAD + L);
  const unsigned vL = lane * 16, pc = L + kq * 40960 + qp * 1024;
  TS(1, 0);
  u32x4 ql0, ql1, qh0, qh1;
  {
    const i32x4 sq = mk_srd(rec, 32 * REC_HEAD);
    asm volatile("v_mov_b64 v[92:93], 0\n\tv_mov_b64 v[94:95], 0\n\tv_mov_b64 v[100:101], 0\n\tv_mov_b64 v[102:103], 0\n\t"
                 "buffer_load_dwordx4 v[88:91], %4, %6, %7 offen\n\tbuffer_load_dwordx4 v[96:99], %4, %6, %7 offen offset:2560\n\t"
                 "s_mov_b32 exec_hi, 0\n\t"
                 "buffer_load_dwordx2 v[92:93], %5, %6, %7 offen offset:1024\n\tbuffer_load_dwordx2 v[100:101], %5, %6, %7 offen offset:3584\n\t"
                 "s_mov_b32 exec_hi, -1"
                 : "=&{v[88:91]}"(ql0), "=&{v[92:95]}"(qh0), "=&{v[96:99]}"(ql1), "=&{v[100:103]}"(qh1)
                 : "v"(hh * 512 + r * 16), "v"(r * 8), "s"(sq), "s"((head * 64 + qb * 8 + qp * 2) * REC));
  }
  dma16(pc, vL, skv, pc);
  BARRIER();
  dma16(pc + 4096, vL, skv, pc + 4096);
  dma16(pc + 8192, vL, skv, pc + 8192);
  const int mat = b >= 120, rg = (b - mat * 120) / 12, cb = b - mat * 120 - rg * 12, gw = b * 16 + wave;
  unsigned srcA, dstA, srcB, dstB;
  {
    const int pr = wave * 4 + (lane >> 4), cc = cb * 16 + (lane & 15), ct = (cc * 171) >> 11, c12 = cc - ct * 12;
    srcA = (rg * 64 + pr) * 3072 + cc * 16;
    dstA = (ct * 10 + rg) * 24576 + mat * 12288 + pr * 192 + (c12 ^ (mat * 2)) * 16;
    if (b >= 240) srcA = dstA = 0x80000000u;
    const int id = (gw < 800 ? gw : gw - 800) * 64 + lane, R = id / 80, c80 = id - R * 80, c8 = (c80 * 205) >> 11, k = R & 127;
    srcB = id * 16;
    dstB = (gw < 800 ? PK_O + c8 * (5 * 20480) : PK_D + c8 * (12 * 20480)) + (R >> 7) * 20480 + (k ^ ((k >> 1) & 4)) * 160 + (c80 - c8 * 10) * 16;
    if (gw >= 2720) srcB = dstB = 0x80000000u;
  }
  const i32x4 sa = mk_srd(mat ? wu : wg, D_ * F_ * 2), sb = mk_srd(gw < 800 ? wo : wd, gw < 800 ? D_ * D_ * 2 : D_ * F_ * 2);
  const i32x4 sp = mk_srd(ws + WS_PK, PK_SIZE);
  unsigned aK = L + kq * 40960 + hh * 512 + r * 16, aKh = L + kq * 40960 + 1024 + r * 8, aV = L + kq * 40960 + REC_V + hh * REC_VH + r * 16;
  const unsigned vc = r == 20 ? 0x3f803f80u : 0u;
  u32x4 v0 = {vc, vc, vc, vc}, v1 = v0;
  asm volatile("s_waitcnt vmcnt(2)\n\ts_barrier"
               : "+{v[88:91]}"(ql0), "+{v[92:95]}"(qh0), "+{v[96:99]}"(ql1), "+{v[100:103]}"(qh1), "+{v116}"(srcA), "+{v118}"(dstA),
                 "+{v117}"(srcB), "+{v119}"(dstB), "+{v[80:83]}"(v0), "+{v[84:87]}"(v1), "+{v104}"(aK), "+{v105}"(aKh), "+{v106}"(aV)
               :: "memory");
  TS(1, 1);
  f32x16 o0, o1;
  u32x4 wA;
  unsigned pcs = pc + 8192;
  asm volatile(K2_ASM
               : "=&{v[0:15]}"(o0), "=&{v[16:31]}"(o1), "+{v[80:83]}"(v0), "+{v[84:87]}"(v1), "=&{v[108:111]}"(wA),
                 "+{v[92:95]}"(qh0), "+{v[100:103]}"(qh1), [pc] "+s"(pcs)
               : "{v[88:91]}"(ql0), "{v[96:99]}"(ql1), "{v104}"(aK), "{v105}"(aKh), "{v106}"(aV), "{v107}"(vL),
                 "{v116}"(srcA), "{v117}"(srcB), "{v118}"(dstA), "{v119}"(dstB),
                 [mv] "s"(0x000fffff000fffffull), [sa] "s"(sa), [sb] "s"(sb), [sp] "s"(sp), [skv] "s"(skv), [ko] "s"(kq * 40960)
               : "memory", "scc", "v32", "v33", "v34", "v35", "v36", "v37", "v38", "v39", "v40", "v41", "v42", "v43", "v44", "v45", "v46", "v47",
                 "v48", "v49", "v50", "v51", "v52", "v53", "v54", "v55", "v56", "v57", "v58", "v59", "v60", "v61", "v62", "v63",
                 "v64", "v65", "v66", "v67", "v68", "v69", "v70", "v71", "v72", "v73", "v74", "v75", "v76", "v77", "v78", "v79",
                 "v112", "v113", "v114", "v115");
  TS(1, 2);
  k2_tail(ws, L, o0, o1, wA, wave, head, qb, mat, rg, cb);
}

struct Frag3 { bf16x8 a[2]; s16x4 b[5][2]; };
constexpr int K3_STAGE = 16384 + 20480;

template <int KD, bool FIRST>
__device__ __forceinline__ void proj_body(const char* __restrict__ A, const char* __restrict__ W, const char* __restrict__ R,
                                          char* __restrict__ C, char* __restrict__ stats, unsigned L) {
  constexpr int NS = KD / 128;
  const int tid = threadIdx.x, lane = tid & 63, wave = __builtin_amdgcn_readfirstlane(tid >> 6);
  const int b = blockIdx.x, xcd = b & 7, bi = b >> 3;
  const int rt = xcd * 4 + (bi & 3), ct = bi >> 2;
  const int m0 = rt * 64, n0 = ct * 80;
  const unsigned RS = L + 4 * K3_STAGE;
  TS(FIRST ? 2 : 4, 0);
  if (wave >= 4) {
    const int pw = wave - 4;
    const i32x4 sa = mk_srd(A, S_ * KD * 2), sw = mk_srd(W, KD * D_ * 2), sr = mk_srd(R, S_ * D_ * 2);
    const unsigned vL = lane * 16;
    auto issue = [&](int s) {
#pragma unroll
      for (int i = 0; i < 5; ++i) {
        if (i < 4) dma16(L + (s & 3) * K3_STAGE + (pw * 4 + i) * 1024, vL, sa, (s * 16 + pw * 4 + i) * 32768 + m0 * 16);
        dma16(L + (s & 3) * K3_STAGE + 16384 + (pw * 5 + i) * 1024, vL, sw, (ct * NS + s) * 20480 + (pw * 5 + i) * 1024);
      }
    };
    issue(0);
    BARRIER();
    issue(1);
    WAIT_V(9); BARRIER();
    issue(2);
#pragma unroll
    for (int i = 0; i < 3; ++i) {
      int p = pw * 3 + i; p = p > 9 ? 9 : p;
      if (FIRST) { int idx = p * 64 + lane, row = idx / 10, c = idx - row * 10; dma16(RS + p * 1024, row * 1280 + c * 16, sr, m0 * 1280 + n0 * 2); }
      else dma16(RS + p * 1024, vL, sr, (ct * 10 + p) * 32768 + m0 * 16);
    }
    issue(3);
    for (int s = 0; s < NS - 1; ++s) {
      if (s == 0) WAIT_V(21); else if (s < NS - 3) WAIT_V(18); else if (s == NS - 3) WAIT_V(9); else WAIT_V(0);
      BARRIER();
      if (s < NS - 4) issue(s + 4);
    }
    return;
  }
  const int wm = wave & 1, wk = wave >> 1, g = lane >> 4;
  const unsigned aA = L + (wk * 8 + g) * 1024 + (wm * 32 + (lane & 15)) * 16;
  unsigned aB[2];
#pragma unroll
  for (int t = 0; t < 2; ++t) aB[t] = L + 16384 + (wk * 64 + 8 * g + ((4 * t) ^ (4 * (g & 1))) + ((lane >> 2) & 3)) * 160 + (lane & 3) * 8;
  auto rd = [&](Frag3& f, unsigned sl, int ks, int t) {
    if (t == 0) f.a[0] = ldsr<bf16x8>(aA + sl + ks * 4096);
    else if (t == 3) f.a[1] = ldsr<bf16x8>(aA + sl + ks * 4096 + 256);
    else { const int u = t < 3 ? t - 1 : t - 2; f.b[u >> 1][u & 1] = ldstr(aB[u & 1] + sl + ks * 5120 + (u >> 1) * 32); }
  };
  f32x4 acc[2][5] = {};
  auto mm = [&](const Frag3& f, int t) { acc[t & 1][t >> 1] = MFMA16(cat8(f.b[t >> 1][0], f.b[t >> 1][1]), f.a[t & 1], acc[t & 1][t >> 1]); };
  auto body = [&](const Frag3& f, Frag3& n, unsigned sl, int ks) {
#pragma unroll
    for (int t = 1; t < 10; ++t) {
      SB();
      if (t <= 3) { rd(n, sl, ks, 2 * t - 2); rd(n, sl, ks, 2 * t - 1); } else rd(n, sl, ks, t + 2);
      SB(); mm(f, t);
    }
    SB();
  };
  Frag3 f0, f1;
  BARRIER(); BARRIER();
  TS(FIRST ? 2 : 4, 1);
#pragma unroll
  for (int t = 0; t < 12; ++t) rd(f0, 0, 0, t);
  SB();
  for (int s = 0; s < NS; ++s) {
    const unsigned sl = (s & 3) * K3_STAGE, nsl = ((s + 1) & 3) * K3_STAGE;
    mm(f0, 0); body(f0, f1, sl, 1);
    mm(f1, 0); SB();
    if (s < NS - 1) LBARRIER();
    body(f1, f0, nsl, 0);
  }
  TS(FIRST ? 2 : 4, 2);
  const unsigned X = L + ((NS + 1) & 3) * K3_STAGE;
#pragma unroll
  for (int j = 0; j < 5; ++j) ldsw<f32x4>(X + (wave * 5 + j) * 1024 + lane * 16, wk ? acc[0][j] : acc[1][j]);
  LBARRIER();
  const int lr = wm * 32 + wk * 16 + (lane & 15);
  const i32x4 rc = mk_srd(C, S_ * D_ * 2);
  const unsigned vo = FIRST ? (g >> 1) * 32768 + lr * 16 + (g & 1) * 8 : lr * 1280 + g * 8;
  f32x2 sum = {}, sq = {};
#pragma unroll
  for (int j = 0; j < 5; ++j) {
    f32x4 v = (wk ? acc[1][j] : acc[0][j]) + ldsr<f32x4>(X + ((wave ^ 2) * 5 + j) * 1024 + lane * 16);
    u32x2 rr = FIRST ? ldsr<u32x2>(RS + lr * 160 + j * 32 + g * 8) : ldsr<u32x2>(RS + (2 * j + (g >> 1)) * 1024 + lr * 16 + (g & 1) * 8);
    u32x2 o = {pk2(v[0] + bflo(rr.x), v[1] + bfhi(rr.x)), pk2(v[2] + bflo(rr.y), v[3] + bfhi(rr.y))};
    if (FIRST) {
      st8(o, rc, vo, (ct * 10 + 2 * j) * 32768 + m0 * 16);
      f32x2 h0 = {bflo(o.x), bfhi(o.x)}, h1 = {bflo(o.y), bfhi(o.y)};
      sum += h0 + h1;
      sq = fma2(h0, h0, fma2(h1, h1, sq));
    } else {
      st8(o, rc, vo, m0 * 1280 + (n0 + 16 * j) * 2);
    }
  }
  if (FIRST) {
    float s1 = sum.x + sum.y, s2 = sq.x + sq.y;
    s1 += __shfl_xor(s1, 16); s2 += __shfl_xor(s2, 16);
    s1 += __shfl_xor(s1, 32); s2 += __shfl_xor(s2, 32);
    if (lane < 16) *(f32x2*)(stats + (m0 + lr) * 64 + ct * 8) = f32x2{s1, s2};
  }
  TS(FIRST ? 2 : 4, 3);
}

__global__ __launch_bounds__(512, 2) void k3_oproj(const u16* __restrict__ x, char* __restrict__ ws) {
  __shared__ __attribute__((aligned(1024))) char smem[4 * K3_STAGE + 10240];
  proj_body<D_, true>(ws + WS_O, ws + WS_PK + PK_O, (const char*)x, ws + WS_H1, ws + WS_ST, (unsigned)(uintptr_t)(LDS3 char*)smem);
}
__global__ __launch_bounds__(512, 2) void k5_down(char* __restrict__ ws, u16* __restrict__ out) {
  __shared__ __attribute__((aligned(1024))) char smem[4 * K3_STAGE + 10240];
  proj_body<F_, false>(ws + WS_HH, ws + WS_PK + PK_D, ws + WS_H1, (char*)out, nullptr, (unsigned)(uintptr_t)(LDS3 char*)smem);
}

struct Frag4 { bf16x8 a[2]; s16x4 b[3][2]; };
constexpr int K4_STAGE = 16384 + 24576;

__global__ __launch_bounds__(512, 2) void k4_gateup(char* __restrict__ ws) {
  __shared__ __attribute__((aligned(1024))) char smem[4 * K4_STAGE];
  const unsigned L = (unsigned)(uintptr_t)(LDS3 char*)smem;
  const int tid = threadIdx.x, lane = tid & 63, wave = __builtin_amdgcn_readfirstlane(tid >> 6);
  const int b = blockIdx.x, xcd = b & 7, bi = b >> 3;
  const int rt = (xcd >> 1) * 4 + (bi & 3), ct = (xcd & 1) * 8 + (bi >> 2);
  const int m0 = rt * 128;
  const unsigned T = L + 2 * K4_STAGE;
  TS(3, 0);
  if (wave >= 4) {
    const int pw = wave - 4;
    const i32x4 sa = mk_srd(ws + WS_H1, S_ * D_ * 2), sw = mk_srd(ws + WS_PK + PK_G, PK_O);
    const i32x4 sst = mk_srd(ws + WS_ST, S_ * 64 + 16 * 8192);
    const unsigned vL = lane * 16;
    auto issue = [&](int s) {
      const unsigned sl = L + (s & 3) * K4_STAGE;
#pragma unroll
      for (int i = 0; i < 6; ++i) {
        const int p = pw * 4 + i, q = pw * 6 + i;
        if (i < 4) dma16(sl + p * 1024, vL, sa, (s * 8 + (p >> 1)) * 32768 + (m0 + (p & 1) * 64) * 16);
        dma16(sl + 16384 + q * 1024, vL, sw, (ct * 10 + s) * 24576 + q * 1024);
      }
    };
    issue(0);
    BARRIER();
    issue(1);
    WAIT_V(10); BARRIER();
    issue(2); issue(3);
    for (int s = 0; s < 9; ++s) {
      if (s < 7) WAIT_V(20); else if (s == 7) WAIT_V(14); else WAIT_V(0);
      BARRIER();
      if (s < 6) issue(s + 4);
      if (s == 6) {
        dma16(T + pw * 2048, vL, sst, m0 * 64 + pw * 2048);
        dma16(T + pw * 2048 + 1024, vL, sst, m0 * 64 + pw * 2048 + 1024);
        dma16(T + 8192 + pw * 2048, vL, sst, S_ * 64 + ct * 8192 + pw * 2048);
        dma16(T + 8192 + pw * 2048 + 1024, vL, sst, S_ * 64 + ct * 8192 + pw * 2048 + 1024);
      }
    }
    const int pt = tid - 256;
    if (pt < 128) {
      f32x4 t0 = ldsr<f32x4>(T + pt * 64), t1 = ldsr<f32x4>(T + pt * 64 + 16), t2 = ldsr<f32x4>(T + pt * 64 + 32), t3 = ldsr<f32x4>(T + pt * 64 + 48);
      f32x4 t = (t0 + t1) + (t2 + t3);
      float mean = (t[0] + t[2]) * (1.f / D_);
      float var = fmaxf((t[1] + t[3]) * (1.f / D_) - mean * mean, 0.f);
      ldsw<f32x2>(T + 16384 + pt * 8, f32x2{__builtin_amdgcn_rsqf(var + LN_EPS), -mean});
    } else if (pt < 128 + 96) {
      const unsigned a = T + 8192 + (pt - 128) * 8;
      f32x2 c = {};
#pragma unroll
      for (int k = 0; k < 10; ++k) c += ldsr<f32x2>(a + k * 768);
      ldsw<f32x2>(T + 17408 + (pt - 128) * 8, c);
    }
    LBARRIER();
    return;
  }
  const int wm = wave >> 1, wn = wave & 1, r = lane & 31, hh = lane >> 5;
  const unsigned aA = L + hh * 2048 + (wm * 64 + r) * 16;
  unsigned aB[3];
  {
    const int q = (lane >> 2) & 3, p = lane & 3, up = (lane >> 4) & 1;
#pragma unroll
    for (int j = 0; j < 3; ++j) aB[j] = L + 16384 + up * 12288 + (8 * hh + q) * 192 + (((wn * 3 + j) ^ up) * 32) + p * 8;
  }
  auto rd = [&](Frag4& f, unsigned sl, int ks, int t) {
    if (t == 0) { f.a[0] = ldsr<bf16x8>(aA + sl + ks * 4096); f.b[0][0] = ldstr(aB[0] + sl + ks * 3072); }
    if (t == 1) { f.b[0][1] = ldstr(aB[0] + sl + ks * 3072 + 768); f.a[1] = ldsr<bf16x8>(aA + sl + ks * 4096 + 512); }
    if (t >= 2) { f.b[t - 1][0] = ldstr(aB[t - 1] + sl + ks * 3072); f.b[t - 1][1] = ldstr(aB[t - 1] + sl + ks * 3072 + 768); }
  };
  f32x16 acc[2][3] = {};
  auto mm = [&](const Frag4& f, int t) { acc[t & 1][t >> 1] = MFMA32(cat8(f.b[t >> 1][0], f.b[t >> 1][1]), f.a[t & 1], acc[t & 1][t >> 1]); };
  auto body = [&](const Frag4& f, Frag4& n, unsigned sl, int ks) {
#pragma unroll
    for (int t = 1; t < 6; ++t) {
      SB();
      if (t <= 4) rd(n, sl, ks, t - 1);
      SB(); mm(f, t);
    }
    SB();
  };
  Frag4 f0, f1;
  BARRIER(); BARRIER();
  TS(3, 1);
#pragma unroll
  for (int t = 0; t < 4; ++t) rd(f0, 0, 0, t);
  SB();
  for (int s = 0; s < 10; ++s) {
    const unsigned sl = (s & 3) * K4_STAGE, nsl = ((s + 1) & 3) * K4_STAGE;
    mm(f0, 0); body(f0, f1, sl, 1);
    mm(f1, 0); body(f1, f0, sl, 2);
    mm(f0, 0); body(f0, f1, sl, 3);
    mm(f1, 0); SB();
    if (s < 9) LBARRIER();
    body(f1, f0, nsl, 0);
  }
  BARRIER();
  TS(3, 2);
  const float k1 = -2.f * 0.7978845608028654f * 1.4426950408889634f, k2 = k1 * 0.044715f;
  const i32x4 ro = mk_srd(ws + WS_HH, S_ * F_ * 2);
  const unsigned vo = hh * 32768 + r * 16;
#pragma unroll
  for (int j = 0; j < 3; ++j) {
    const int fc = (wn * 3 + j) * 16 + 4 * hh;
    f32x4 cg[2], cu[2];
#pragma unroll
    for (int g = 0; g < 2; ++g) { cg[g] = ldsr<f32x4>(T + 17408 + (fc + 8 * g) * 4); cu[g] = ldsr<f32x4>(T + 17408 + 384 + (fc + 8 * g) * 4); }
#pragma unroll
    for (int i = 0; i < 2; ++i) {
      const f32x2 ab = ldsr<f32x2>(T + 16384 + (wm * 64 + i * 32 + r) * 8);
      const f32x2 a = {ab.x, ab.x}, nm = {ab.y, ab.y};
      unsigned pk[4];
#pragma unroll
      for (int e = 0; e < 4; ++e) {
        const f32x2 gt = fma2(nm, sub2(cg[e >> 1], (e & 1) * 2), sub2(acc[i][j], 2 * e)) * a;
        const f32x2 up = fma2(nm, sub2(cu[e >> 1], (e & 1) * 2), sub2(acc[i][j], 8 + 2 * e)) * a;
        const f32x2 z = gt * fma2(gt * gt, f32x2{k2, k2}, f32x2{k1, k1});
        const f32x2 dn = f32x2{__builtin_amdgcn_exp2f(z.x), __builtin_amdgcn_exp2f(z.y)} + f32x2{1.f, 1.f};
        const f32x2 sg2 = {__builtin_amdgcn_rcpf(dn.x), __builtin_amdgcn_rcpf(dn.y)};
        pk[e] = pk2(gt * up * sg2);
      }
      auto s0 = __builtin_amdgcn_permlane32_swap(pk[0], pk[2], false, false);
      auto s1 = __builtin_amdgcn_permlane32_swap(pk[1], pk[3], false, false);
      st16(u32x4{s0[0], s1[0], s0[1], s1[1]}, ro, vo, (ct * 12 + (wn * 3 + j) * 2) * 32768 + (m0 + wm * 64 + i * 32) * 16);
    }
  }
  TS(3, 3);
}

extern "C" void kernel_launch(void* const* d_in, const int* in_sizes, int n_in, void* d_out, int out_size, void* d_ws,
                              size_t ws_size, hipStream_t stream) {
  const u16* x = (const u16*)d_in[0];
  const u16* wqkv = (const u16*)d_in[1];
  const u16* wo = (const u16*)d_in[2];
  const u16* wg = (const u16*)d_in[4];
  const u16* wu = (const u16*)d_in[5];
  const u16* wd = (const u16*)d_in[6];
  char* ws = (char*)d_ws;
  k1_qkv<<<256, 512, 0, stream>>>(x, wqkv, ws);
  k2_attn<<<256, 1024, 0, stream>>>(ws, wo, wg, wu, wd);
  k3_oproj<<<256, 512, 0, stream>>>(x, ws);
  k4_gateup<<<256, 512, 0, stream>>>(ws);
  k5_down<<<256, 512, 0, stream>>>(ws, (u16*)d_out);
}
```

```cpp
#define PADE -1
#define PADT 0
#define PADSRC 0
#include <hip/hip_runtime.h>
#include <stdint.h>

typedef unsigned short u16;
typedef __bf16 bf16x8 __attribute__((ext_vector_type(8)));
typedef __bf16 bf16x2 __attribute__((ext_vector_type(2)));
typedef short s16x4 __attribute__((ext_vector_type(4)));
typedef short s16x8 __attribute__((ext_vector_type(8)));
typedef float f32x2 __attribute__((ext_vector_type(2)));
typedef float f32x4 __attribute__((ext_vector_type(4)));
typedef float f32x16 __attribute__((ext_vector_type(16)));
typedef int i32x4 __attribute__((ext_vector_type(4)));
typedef unsigned u32x2 __attribute__((ext_vector_type(2)));
typedef unsigned u32x4 __attribute__((ext_vector_type(4)));
#define LDS3 __attribute__((address_space(3)))

constexpr int S_ = 2048, D_ = 640, F_ = 1536;
constexpr int REC = 2560, REC_V = 1280, REC_VS = 640, REC_VH = 320;
constexpr int REC_HEAD = 64 * REC;
constexpr size_t WS_REC = 0;
constexpr size_t WS_O = WS_REC + (size_t)64 * REC_HEAD;
constexpr size_t WS_H1 = WS_O + (size_t)S_ * D_ * 2;
constexpr size_t WS_HH = WS_H1 + (size_t)S_ * D_ * 2;
constexpr size_t WS_ST = WS_HH + (size_t)S_ * F_ * 2;
constexpr size_t WS_CS = WS_ST + (size_t)S_ * 64;
constexpr size_t WS_PK = WS_CS + 16 * 8192;
constexpr int PK_G = 0, PK_O = 16 * 10 * 24576, PK_D = PK_O + 8 * 5 * 20480, PK_SIZE = PK_D + 8 * 12 * 20480;
constexpr float LN_EPS = 1e-5f;

__device__ unsigned long long tsbuf[6 * 256];
#define TS(k, i) do { \
  if ((i) == 0 && (k) == 0 && threadIdx.x == 0) { __builtin_nontemporal_store(__builtin_amdgcn_s_memrealtime(), &tsbuf[(k) * 256 + blockIdx.x]); } \
  if ((i) == 3 && (k) == PADE && threadIdx.x == 0) { asm volatile("s_waitcnt vmcnt(0)" ::: "memory"); \
     unsigned long long t0_ = __hip_atomic_load(&tsbuf[PADSRC], __ATOMIC_RELAXED, __HIP_MEMORY_SCOPE_AGENT); \
     while (__builtin_amdgcn_s_memrealtime() - t0_ < PADT) __builtin_amdgcn_s_sleep(1); } \
} while (0)
__global__ void k_first() { if (threadIdx.x == 0) __builtin_nontemporal_store(__builtin_amdgcn_s_memrealtime(), &tsbuf[5 * 256 + blockIdx.x]); }
__global__ void k_last() { if (threadIdx.x == 0) { unsigned long long t0_ = __hip_atomic_load(&tsbuf[PADSRC], __ATOMIC_RELAXED, __HIP_MEMORY_SCOPE_AGENT);
     while (__builtin_amdgcn_s_memrealtime() - t0_ < PADT) __builtin_amdgcn_s_sleep(1); } }
#define STPOL " sc1"
#define SB() __builtin_amdgcn_sched_barrier(0)
#define WAIT_V(n) asm volatile("s_waitcnt vmcnt(%0)" ::"n"(n) : "memory")
#define WAIT_VL(n) asm volatile("s_waitcnt vmcnt(%0) lgkmcnt(0)" ::"n"(n) : "memory")
#define BARRIER() asm volatile("s_barrier" ::: "memory")
#define LBARRIER() asm volatile("s_waitcnt lgkmcnt(0)\n\ts_barrier" ::: "memory")

__device__ __forceinline__ i32x4 mk_srd(const void* p, unsigned bytes) {
  unsigned long long a = (unsigned long long)p;
  i32x4 r;
  r.x = (int)(unsigned)a; r.y = (int)((unsigned)(a >> 32) & 0xffffu); r.z = (int)bytes; r.w = 0x00020000;
  return r;
}
__device__ __forceinline__ void st16(u32x4 v, i32x4 srd, unsigned voff, unsigned soff) {
  asm volatile("buffer_store_dwordx4 %0, %1, %2, %3 offen" STPOL "\n\ts_nop 1" ::"v"(v), "v"(voff), "s"(srd), "s"(soff));
}
__device__ __forceinline__ void st8(u32x2 v, i32x4 srd, unsigned voff, unsigned soff) {
  asm volatile("buffer_store_dwordx2 %0, %1, %2, %3 offen" STPOL ::"v"(v), "v"(voff), "s"(srd), "s"(soff));
}
__device__ __forceinline__ void dma16(unsigned lds, unsigned voff, i32x4 srd, unsigned soff) {
  asm volatile("s_mov_b32 m0, %0\n\ts_nop 0\n\tbuffer_load_dwordx4 %1, %2, %3 offen lds"
               ::"s"(lds), "v"(voff), "s"(srd), "s"(soff) : "memory");
}
__device__ __forceinline__ void dma16_masked(unsigned lds, unsigned voff, i32x4 srd, unsigned soff, unsigned long long mask) {
  asm volatile("s_mov_b32 m0, %0\n\ts_mov_b64 exec, %4\n\tbuffer_load_dwordx4 %1, %2, %3 offen lds\n\ts_mov_b64 exec, -1"
               ::"s"(lds), "v"(voff), "s"(srd), "s"(soff), "s"(mask) : "memory");
}
template <typename T> __device__ __forceinline__ T ldsr(unsigned a) { return *(LDS3 const T*)(uintptr_t)a; }
template <typename T> __device__ __forceinline__ void ldsw(unsigned a, T v) { *(LDS3 T*)(uintptr_t)a = v; }
__device__ __forceinline__ s16x4 ldstr(unsigned a) { return __builtin_amdgcn_ds_read_tr16_b64_v4i16((LDS3 s16x4*)(uintptr_t)a); }
__device__ __forceinline__ bf16x8 cat8(s16x4 lo, s16x4 hi) {
  s16x8 r = __builtin_shufflevector(lo, hi, 0, 1, 2, 3, 4, 5, 6, 7);
  return __builtin_bit_cast(bf16x8, r);
}
__device__ __forceinline__ unsigned pk2(f32x2 v) { return __builtin_bit_cast(unsigned, __builtin_convertvector(v, bf16x2)); }
__device__ __forceinline__ unsigned pk2(float a, float b) { return pk2(f32x2{a, b}); }
__device__ __forceinline__ float bflo(unsigned v) { return __uint_as_float(v << 16); }
__device__ __forceinline__ float bfhi(unsigned v) { return __uint_as_float(v & 0xffff0000u); }
__device__ __forceinline__ s16x4 lo4(bf16x8 v) { s16x8 t = __builtin_bit_cast(s16x8, v); return __builtin_shufflevector(t, t, 0, 1, 2, 3); }
__device__ __forceinline__ s16x4 hi4(bf16x8 v) { s16x8 t = __builtin_bit_cast(s16x8, v); return __builtin_shufflevector(t, t, 4, 5, 6, 7); }
__device__ __forceinline__ f32x2 sub2(const f32x16& v, int i) { return f32x2{v[i], v[i + 1]}; }
__device__ __forceinline__ f32x2 sub2(const f32x4& v, int i) { return f32x2{v[i], v[i + 1]}; }
__device__ __forceinline__ f32x2 fma2(f32x2 a, f32x2 b, f32x2 c) { return __builtin_elementwise_fma(a, b, c); }
#define MFMA32(a, b, c) __builtin_amdgcn_mfma_f32_32x32x16_bf16(a, b, c, 0, 0, 0)
#define MFMA16(a, b, c) __builtin_amdgcn_mfma_f32_16x16x32_bf16(a, b, c, 0, 0, 0)
#define MFMA32K8(a, b, c) __builtin_amdgcn_mfma_f32_32x32x8bf16_1k(a, b, c, 0, 0, 0)
#define MFMA4(a, b, c) __builtin_amdgcn_mfma_f32_4x4x4bf16_1k(a, b, c, 0, 0, 0)

struct Frag1 { bf16x8 a[2]; s16x4 b[2][2]; };

template <bool SWAP>
__device__ __forceinline__ void k1_consumer(char* __restrict__ rec, unsigned L, int rt, int ct, int wave) {
  const int tid = threadIdx.x, lane = tid & 63;
  const int wm = wave >> 1, wn = wave & 1, r = lane & 31, hh = lane >> 5;
  unsigned aA[2][4], aB[2];
#pragma unroll
  for (int i = 0; i < 2; ++i) {
    int row = wm * 64 + ((i ^ wn) * 32) + r;
#pragma unroll
    for (int ks = 0; ks < 4; ++ks) aA[i][ks] = L + row * 128 + (((2 * ks + hh) ^ ((row >> 1) & 7)) << 4);
    int q = (lane >> 2) & 3, p = lane & 3, ch = (lane >> 4) & 1;
    int c = wn * 8 + ((i ^ wm) * 4) + 2 * ch + (p >> 1);
    aB[i] = L + 16384 + (8 * hh + q) * 256 + ((c ^ (q << 2)) << 4) + 8 * (p & 1);
  }
  auto rd = [&](Frag1& f, unsigned sl, int ks, int t) {
    if (t == 0) { f.a[0] = ldsr<bf16x8>(aA[0][ks] + sl); f.b[0][0] = ldstr(aB[0] + sl + ks * 4096); }
    if (t == 1) { f.b[0][1] = ldstr(aB[0] + sl + ks * 4096 + 1024); f.b[1][0] = ldstr(aB[1] + sl + ks * 4096); }
    if (t == 2) { f.b[1][1] = ldstr(aB[1] + sl + ks * 4096 + 1024); f.a[1] = ldsr<bf16x8>(aA[1][ks] + sl); }
  };
  f32x16 acc[2][2] = {};
  f32x4 sq0 = {}, sq1 = {}, cs0 = {}, cs1 = {};
  const s16x4 ones = {0x3f80, 0x3f80, 0x3f80, 0x3f80};
  auto mm = [&](const Frag1& f, int t) {
    const int i = t >> 1, j = t & 1;
    const bf16x8 b = cat8(f.b[j][0], f.b[j][1]);
    acc[i][j] = SWAP ? MFMA32(b, f.a[i], acc[i][j]) : MFMA32(f.a[i], b, acc[i][j]);
    if (t == 3) {
      sq0 = MFMA4(lo4(f.a[0]), lo4(f.a[0]), sq0); cs0 = MFMA4(ones, f.b[0][0], cs0);
      sq1 = MFMA4(hi4(f.a[0]), hi4(f.a[0]), sq1); cs1 = MFMA4(ones, f.b[0][1], cs1);
    }
  };
  auto kstep = [&](const Frag1& f, Frag1& n, unsigned sl, int ks) {
    mm(f, 0); SB(); rd(n, sl, ks, 0); SB(); mm(f, 1); SB(); rd(n, sl, ks, 1); SB(); mm(f, 2); SB(); rd(n, sl, ks, 2); SB(); mm(f, 3); SB();
  };
  Frag1 f0, f1;
  BARRIER(); BARRIER();
  TS(0, 1);
  rd(f0, 0, 0, 0); rd(f0, 0, 0, 1); rd(f0, 0, 0, 2);
  SB();
  for (int s = 0; s < 10; ++s) {
    const unsigned sl = (s & 3) * 32768, nsl = ((s + 1) & 3) * 32768;
    kstep(f0, f1, sl, 1);
    kstep(f1, f0, sl, 2);
    kstep(f0, f1, sl, 3);
    mm(f1, 0); SB();
    if (s < 9) LBARRIER();
    SB(); rd(f0, nsl, 0, 0); SB(); mm(f1, 1); SB(); rd(f0, nsl, 0, 1); SB(); mm(f1, 2); SB(); rd(f0, nsl, 0, 2); SB(); mm(f1, 3); SB();
  }
  TS(0, 2);
  const unsigned T = L + 131072;
  {
    f32x4 sq = sq0 + sq1;
    int e = lane & 3;
    float sv = e == 0 ? sq[0] : e == 1 ? sq[1] : e == 2 ? sq[2] : sq[3];
    ldsw<float>(T + hh * 512 + (wm * 64 + wn * 32 + r) * 4, sv);
    ldsw<float>(T + 1024 + hh * 512 + (wn * 64 + wm * 32 + r) * 4, cs0[0] + cs1[0]);
    if (wn == 1) {
#pragma unroll
      for (int i = 0; i < 2; ++i) {
        const int rb = wm * 64 + (i ^ 1) * 32;
        if (SWAP) {
          float v = wm ? acc[i][0][12] : acc[i][1][12];
          if (hh == 0) ldsw<float>(T + 2048 + (rb + r) * 4, v);
        } else if (r == 24) {
#pragma unroll
          for (int g = 0; g < 4; ++g) {
            f32x4 v;
#pragma unroll
            for (int e2 = 0; e2 < 4; ++e2) v[e2] = wm ? acc[i][0][4 * g + e2] : acc[i][1][4 * g + e2];
            ldsw<f32x4>(T + 2048 + (rb + 8 * g + 4 * hh) * 4, v);
          }
        }
      }
    }
  }
  LBARRIER();
  if (tid < 128) {
    float sum = ldsr<float>(T + 2048 + tid * 4);
    float sq = ldsr<float>(T + tid * 4) + ldsr<float>(T + 512 + tid * 4);
    float mean = sum * (1.f / D_);
    float var = fmaxf(sq * (1.f / D_) - mean * mean, 0.f);
    float rs = __builtin_amdgcn_rsqf(var + LN_EPS);
    if (SWAP) ldsw<f32x2>(T + 2560 + tid * 8, f32x2{rs, -mean});
    else { ldsw<float>(T + 2560 + tid * 4, rs); ldsw<float>(T + 3072 + tid * 4, -mean); }
  } else {
    int c = tid - 128;
    ldsw<float>(T + 3584 + c * 4, ldsr<float>(T + 1024 + c * 4) + ldsr<float>(T + 1536 + c * 4));
  }
  LBARRIER();
  const i32x4 ro = mk_srd(rec, 64 * REC_HEAD);
  const int tile0 = rt * 4 + wm * 2;
  if (SWAP) {
    const float qs = 0.22360679774997896f * 1.4426950408889634f;
    f32x2 ab[2];
#pragma unroll
    for (int i = 0; i < 2; ++i) ab[i] = ldsr<f32x2>(T + 2560 + (wm * 64 + (i ^ wn) * 32 + r) * 8);
    const unsigned vlo = (hh ^ wn) * REC + r * 16;
    const unsigned vhi = hh * REC_HEAD + 1024 + r * 8;
    const unsigned X = L + wave * 8192;
#pragma unroll
    for (int j = 0; j < 2; ++j) {
#pragma unroll
      for (int g = 0; g < 4; ++g) {
        const int P = wn * 8 + (j ^ wm) * 4 + g;
        if (P < 15) {
          const int hp = (P * 13) >> 6, t = P - hp * 5, hA = ct * 6 + 2 * hp, od = (t + 1) >> 2;
          const f32x4 cs = ldsr<f32x4>(T + 3584 + (P * 8 + 4 * hh) * 4);
          const float sc = hA < 32 ? qs : 1.f;
          u32x2 v[2];
#pragma unroll
          for (int i = 0; i < 2; ++i) {
            const f32x2 a = {ab[i].x * sc, ab[i].x * sc}, nm = {ab[i].y, ab[i].y};
            v[i].x = pk2(fma2(nm, sub2(cs, 0), sub2(acc[i][j], 4 * g)) * a);
            v[i].y = pk2(fma2(nm, sub2(cs, 2), sub2(acc[i][j], 4 * g + 2)) * a);
          }
          if (hA >= 64) {
#pragma unroll
            for (int i = 0; i < 2; ++i) ldsw<u32x2>(X + ((i ^ wn) * 32 + r) * 104 + (t * 8 + 4 * hh) * 2, v[i]);
          } else if (t == 2) {
#pragma unroll
            for (int i = 0; i < 2; ++i) st8(v[i], ro, vhi, hA * REC_HEAD + (tile0 + (i ^ wn)) * REC);
          } else {
            auto s0 = __builtin_amdgcn_permlane32_swap(v[0].x, v[1].x, false, false);
            auto s1 = __builtin_amdgcn_permlane32_swap(v[0].y, v[1].y, false, false);
            st16(u32x4{s0[0], s1[0], s0[1], s1[1]}, ro, vlo, (hA + od) * REC_HEAD + tile0 * REC + (t - 3 * od) * 512);
          }
        }
      }
    }
    if (ct == 10 && wn == 1) {
      const int g4 = lane >> 4, c16 = lane & 15;
      const unsigned ra = X + (16 * (g4 >> 1) + 4 * (g4 & 1) + ((lane >> 2) & 3)) * 104 + (lane & 3) * 8;
#pragma unroll
      for (int mb = 0; mb < 2; ++mb) {
#pragma unroll
        for (int c = 0; c < 3; ++c) {
          s16x4 lo = ldstr(ra + mb * 32 * 104 + c * 32), hi = ldstr(ra + mb * 32 * 104 + 8 * 104 + c * 32);
          const int col = c * 16 + c16, hd = col >= 20, d = col - 20 * hd;
          const unsigned vo = hd * REC_HEAD + (g4 >> 1) * REC_VS + (g4 & 1) * REC_VH + d * 16;
          const unsigned so = 32 * REC_HEAD + (tile0 + mb) * REC + REC_V;
          if (col < 40) {
            st16(__builtin_bit_cast(u32x4, cat8(lo, hi)), ro, vo, so);
          }
        }
      }
    }
  } else {
#pragma unroll
    for (int i = 0; i < 2; ++i) {
      const int rb = wm * 64 + (i ^ wn) * 32;
      f32x4 av[4], bv[4];
#pragma unroll
      for (int g = 0; g < 4; ++g) {
        av[g] = ldsr<f32x4>(T + 2560 + (rb + 8 * g + 4 * hh) * 4);
        bv[g] = ldsr<f32x4>(T + 3072 + (rb + 8 * g + 4 * hh) * 4);
      }
#pragma unroll
      for (int j = 0; j < 2; ++j) {
        const int lc = wn * 64 + (j ^ wm) * 32 + r;
        if (lc < 120) {
          const float c1 = ldsr<float>(T + 3584 + lc * 4);
          const f32x2 cs = {c1, c1};
          const int gc = ct * 120 + lc - 1280;
          const int hd = (gc * 205) >> 12, d = gc - hd * 20;
          u32x4 v[2];
#pragma unroll
          for (int g = 0; g < 4; ++g) {
            v[g >> 1][(g & 1) * 2] = pk2(fma2(sub2(bv[g], 0), cs, sub2(acc[i][j], 4 * g)) * sub2(av[g], 0));
            v[g >> 1][(g & 1) * 2 + 1] = pk2(fma2(sub2(bv[g], 2), cs, sub2(acc[i][j], 4 * g + 2)) * sub2(av[g], 2));
          }
          const unsigned vo = hd * REC_HEAD + hh * REC_VH + d * 16, so = 32 * REC_HEAD + (tile0 + (i ^ wn)) * REC + REC_V;
          st16(v[0], ro, vo, so); st16(v[1], ro, vo + REC_VS, so);
        }
      }
    }
  }
  TS(0, 3);
}
constexpr bool chk_div() {
  for (int i = 0; i < 640; ++i) if (((i * 205) >> 12) != i / 20) return false;
  return true;
}
static_assert(chk_div(), "magic division");
constexpr bool chk_div2() {
  for (int i = 0; i < 192; ++i) if (((i * 171) >> 11) != i / 12) return false;
  for (int i = 0; i < 80; ++i) if (((i * 205) >> 11) != i / 10) return false;
  return true;
}
static_assert(chk_div2(), "magic division");

__global__ __launch_bounds__(512, 2) void k1_qkv(const u16* __restrict__ x, const u16* __restrict__ w, char* __restrict__ ws) {
  __shared__ __attribute__((aligned(1024))) char smem[131072 + 4096];
  const unsigned L = (unsigned)(uintptr_t)(LDS3 char*)smem;
  const int b = blockIdx.x, xcd = b & 7, bi = b >> 3;
  const int rt = (xcd >> 1) * 4 + (bi & 3), ct = (xcd & 1) * 8 + (bi >> 2);
  const int lane = threadIdx.x & 63, wave = __builtin_amdgcn_readfirstlane(threadIdx.x >> 6);
  TS(0, 0);
  if (wave >= 4) {
    const int pw = wave - 4;
    const i32x4 sx = mk_srd(x, S_ * D_ * 2), sw = mk_srd(w, D_ * 1920 * 2);
    unsigned vA[4], vB[4];
#pragma unroll
    for (int i = 0; i < 4; ++i) {
      int row = (pw * 4 + i) * 8 + (lane >> 3);
      vA[i] = row * 1280 + (((lane & 7) ^ ((row >> 1) & 7)) << 4);
      int k = (pw * 4 + i) * 4 + (lane >> 4);
      vB[i] = k * 3840 + (((lane & 15) ^ ((k & 3) << 2)) << 4);
    }
    const unsigned long long bmask = ~((1ull << 15) | (1ull << 27) | (1ull << 39) | (1ull << 51));
    const unsigned oA = rt * 128 * 1280, oB = ct * 240;
    auto issue = [&](int s) {
#pragma unroll
      for (int i = 0; i < 4; ++i) {
        dma16(L + (s & 3) * 32768 + (pw * 4 + i) * 1024, vA[i], sx, oA + s * 128);
        dma16_masked(L + (s & 3) * 32768 + 16384 + (pw * 4 + i) * 1024, vB[i], sw, oB + s * (64 * 3840), bmask);
      }
    };
    issue(0);
    ldsw<u32x4>(L + pw * 32768 + 16384 + lane * 256 + ((15 ^ ((lane & 3) << 2)) << 4), u32x4{0x3f80u, 0u, 0u, 0u});
    BARRIER();
    issue(1);
    WAIT_VL(8); BARRIER();
    issue(2); issue(3);
    for (int s = 0; s < 9; ++s) {
      if (s < 7) WAIT_V(16); else if (s == 7) WAIT_V(8); else WAIT_V(0);
      BARRIER();
      if (s < 6) issue(s + 4);
    }
    return;
  }
  if (ct <= 10) k1_consumer<true>(ws + WS_REC, L, rt, ct, wave);
  else k1_consumer<false>(ws + WS_REC, L, rt, ct, wave);
}

__device__ __forceinline__ void exp4(const f32x16& s, int g, unsigned* p) {
  float e0 = __builtin_amdgcn_exp2f(s[4 * g + 0]), e1 = __builtin_amdgcn_exp2f(s[4 * g + 1]);
  float e2 = __builtin_amdgcn_exp2f(s[4 * g + 2]), e3 = __builtin_amdgcn_exp2f(s[4 * g + 3]);
  p[0] = pk2(e0, e1); p[1] = pk2(e2, e3);
}
__device__ __forceinline__ bf16x8 px8(const unsigned* p) { u32x4 v = {p[0], p[1], p[2], p[3]}; return __builtin_bit_cast(bf16x8, v); }
__device__ __forceinline__ bf16x8 bx8(u32x4 v) { return __builtin_bit_cast(bf16x8, v); }
__device__ __forceinline__ float max16(const f32x16& s) {
  float m = fmaxf(fmaxf(fmaxf(s[0], s[1]), fmaxf(s[2], s[3])), fmaxf(fmaxf(s[4], s[5]), fmaxf(s[6], s[7])));
  float n = fmaxf(fmaxf(fmaxf(s[8], s[9]), fmaxf(s[10], s[11])), fmaxf(fmaxf(s[12], s[13]), fmaxf(s[14], s[15])));
  m = fmaxf(m, n);
  return fmaxf(m, __shfl_xor(m, 32));
}

#define K2_ASM \
  ".macro XE r\n v_exp_f32 v[\\r], v[\\r]\n.endm\n" \
  ".macro XC p, r\n v_cvt_pk_bf16_f32 v[\\p], v[\\r], v[\\r+1]\n.endm\n" \
  ".macro XM d, a, b, c\n v_mfma_f32_32x32x16_bf16 v[\\d:\\d+15], v[\\a:\\a+3], v[\\b:\\b+3], \\c\n.endm\n" \
  ".macro XPH s, n, q, op, oc, t, b\n" \
  " XE \\s\n XE \\s+1\n" \
  " .if \\t + \\b > 0\n XM \\op, 84, 68, v[\\op:\\op+15]\n .endif\n" \
  " .if \\b == 0\n" \
  "  .if \\t == 1\n s_waitcnt vmcnt(9)\n s_barrier\n .endif\n" \
  "  .if \\t == 4\n s_waitcnt vmcnt(2)\n s_barrier\n .endif\n" \
  "  s_mov_b64 exec, %[mv]\n ds_read_b128 v[80:83], v106 offset:\\t*2560\n ds_read_b128 v[84:87], v106 offset:\\t*2560+640\n s_mov_b64 exec, -1\n" \
  " .endif\n" \
  " XE \\s+2\n XE \\s+3\n XC 64, \\s\n XC 65, \\s+2\n XE \\s+4\n XE \\s+5\n" \
  " .if \\b == 1\n s_waitcnt lgkmcnt(0)\n .endif\n" \
  " .if \\t + \\b < 16\n XM \\n, 72, \\q, 0\n .endif\n" \
  " XE \\s+6\n XE \\s+7\n XC 66, \\s+4\n XC 67, \\s+6\n XE \\s+8\n" \
  " .if \\t + \\b < 16\n XM \\n, 76, \\q+4, v[\\n:\\n+15]\n .endif\n" \
  " .if (\\b == 0) && (\\t < 15)\n ds_read_b128 v[72:75], v104 offset:(\\t+1)*2560\n ds_read_b64 v[76:77], v105 offset:(\\t+1)*2560\n .endif\n" \
  " .if (\\b == 1) && (\\t == 8)\n s_waitcnt vmcnt(0)\n" \
  "  buffer_store_dwordx4 v[108:111], v118, %[sp], 0 offen sc1\n buffer_store_dwordx4 v[112:115], v119, %[sp], 0 offen sc1\n .endif\n" \
  " XE \\s+9\n XE \\s+10\n XE \\s+11\n XC 68, \\s+8\n XC 69, \\s+10\n XE \\s+12\n XE \\s+13\n" \
  " .if \\b == 0\n .if \\t < 15\n s_waitcnt lgkmcnt(2)\n .else\n s_waitcnt lgkmcnt(0)\n .endif\n .endif\n" \
  " .if \\t == 0\n XM \\oc, 80, 64, 0\n .else\n XM \\oc, 80, 64, v[\\oc:\\oc+15]\n .endif\n" \
  " XE \\s+14\n XE \\s+15\n XC 70, \\s+12\n XC 71, \\s+14\n" \
  ".endm\n" \
  ".macro XD\n s_add_u32 %[pc], %[pc], 4096\n s_mov_b32 m0, %[pc]\n s_nop 0\n buffer_load_dwordx4 v107, %[skv], %[pc] offen lds\n.endm\n" \
  ".macro XMAX d, s\n v_max3_f32 v[\\d], v[\\s], v[\\s+1], v[\\s+2]\n v_max3_f32 v[\\d+1], v[\\s+3], v[\\s+4], v[\\s+5]\n" \
  " v_max3_f32 v[\\d], v[\\d], v[\\s+6], v[\\s+7]\n v_max3_f32 v[\\d+1], v[\\d+1], v[\\s+8], v[\\s+9]\n" \
  " v_max3_f32 v[\\d], v[\\d], v[\\s+10], v[\\s+11]\n v_max3_f32 v[\\d+1], v[\\d+1], v[\\s+12], v[\\s+13]\n" \
  " v_max3_f32 v[\\d], v[\\d], v[\\s+14], v[\\s+15]\n v_max_f32 v[\\d], v[\\d], v[\\d+1]\n.endm\n" \
  "v_subrev_u32 v64, %[ko], v104\n v_subrev_u32 v65, %[ko], v105\n" \
  "ds_read_b128 v[72:75], v64\n ds_read_b64 v[76:77], v65\n" \
  "v_mov_b32 v78, 0x3f803f80\n v_mov_b32 v79, 0\n" \
  "XD\n XD\n s_waitcnt lgkmcnt(0)\n" \
  "XM 32, 72, 88, 0\n XM 48, 72, 96, 0\n XM 32, 76, 92, v[32:47]\n XM 48, 76, 100, v[48:63]\n" \
  "ds_read_b128 v[72:75], v104\n ds_read_b64 v[76:77], v105\n" \
  "XD\n XD\n XD\n XD\n XD\n" \
  "buffer_load_dwordx4 v[108:111], v116, %[sa], 0 offen\n" \
  "buffer_load_dwordx4 v[112:115], v117, %[sb], 0 offen\n" \
  "s_mov_b32 exec_hi, 0\n XMAX 64, 32\n XMAX 66, 48\n" \
  "v_cvt_pk_bf16_f32 v65, -v64, 0\n v_cvt_pk_bf16_f32 v67, -v66, 0\n v_lshlrev_b32 v65, 16, v65\n v_lshlrev_b32 v67, 16, v67\n" \
  "v_sub_f32 v65, -v64, v65\n v_sub_f32 v67, -v66, v67\n v_cvt_pk_bf16_f32 v94, -v64, v65\n v_cvt_pk_bf16_f32 v102, -v66, v67\n" \
  "s_mov_b32 exec_hi, -1\n s_waitcnt lgkmcnt(0)\n s_nop 0\n" \
  "XM 32, 72, 88, 0\n XM 32, 76, 92, v[32:47]\n s_nop 7\n s_nop 3\n" \
  ".irp t,0,1,2,3,4,5,6,7,8,9,10,11,12,13,14,15\n XPH 32, 48, 96, 16, 0, \\t, 0\n XPH 48, 32, 88, 0, 16, \\t, 1\n.endr\n" \
  "s_nop 1\n XM 16, 84, 68, v[16:31]\n s_nop 7\n s_nop 3\n" \
  ".purgem XE\n.purgem XC\n.purgem XM\n.purgem XPH\n.purgem XD\n.purgem XMAX\n"

__device__ __forceinline__ void k2_tail(char* __restrict__ ws, unsigned L, const f32x16& o0, const f32x16& o1, u32x4 wA,
                                        int wave, int head, int qb, int mat, int rg, int cb) {
  int lane;
  asm volatile("v_mbcnt_lo_u32_b32 %0, -1, 0\n\tv_mbcnt_hi_u32_b32 %0, -1, %0" : "=v"(lane));
  const int b = blockIdx.x, qp = wave & 3, kq = wave >> 2, r = lane & 31, hh = lane >> 5;
  const char* rec = ws + WS_REC;
  const char* qrec = rec + (size_t)(head * 64 + qb * 8 + qp * 2) * REC;
  const unsigned vL = lane * 16, aK = hh * 512 + r * 16, aKh = 1024 + r * 8, aV = REC_V + hh * REC_VH + r * 16;
  const unsigned vc = r == 20 ? 0x3f803f80u : 0u;
  f32x4 cs;
  {
    const int i = lane & 15;
    const unsigned oh = (i & 1) ? 0x3f800000u : 0x3f80u;
    const u32x4 sel = {i >> 1 == 0 ? oh : 0u, i >> 1 == 1 ? oh : 0u, i >> 1 == 2 ? oh : 0u, i >> 1 == 3 ? oh : 0u};
    cs = MFMA16(bx8(sel), bx8(wA), f32x4{});
  }
  BARRIER();
  const unsigned CS = L + 73728;
  if (lane < 32) ldsw<f32x4>(CS + wave * 512 + (lane & 15) * 32 + (lane >> 4) * 16, cs);
#pragma unroll
  for (int g = 0; g < 3; ++g) {
    const unsigned a = L + qp * 18432 + g * 1024 + vL;
    if (kq != 0) ldsw<f32x4>(a + (kq - 1) * 3072, f32x4{o0[4 * g], o0[4 * g + 1], o0[4 * g + 2], o0[4 * g + 3]});
    if (kq != 1) ldsw<f32x4>(a + 9216 + (kq ? kq - 1 : 0) * 3072, f32x4{o1[4 * g], o1[4 * g + 1], o1[4 * g + 2], o1[4 * g + 3]});
  }
  LBARRIER();
  if (kq >= 2) {
    if (kq == 2 && qp < 2 && b < 240) {
      const int c = qp * 64 + lane;
      float s = 0.f;
#pragma unroll
      for (int w = 0; w < 16; ++w) s += ldsr<float>(CS + w * 512 + c * 4);
      const int col = cb * 128 + c, ct = col / 96;
      *(float*)(ws + WS_CS + ct * 8192 + (rg * 192 + col - ct * 96 + mat * 96) * 4) = s;
    }
    return;
  }
  f32x4 o[3];
#pragma unroll
  for (int g = 0; g < 3; ++g) {
    const unsigned a = L + qp * 18432 + kq * 9216 + g * 1024 + vL;
#pragma unroll
    for (int e = 0; e < 4; ++e) o[g][e] = kq ? o1[4 * g + e] : o0[4 * g + e];
    o[g] += ldsr<f32x4>(a) + (ldsr<f32x4>(a + 3072) + ldsr<f32x4>(a + 6144));
  }
  float l = __shfl(o[2][0], r + 32);
  bool ok = l <= 0x1p100f && l >= 0x1p-100f;
#pragma unroll
  for (int i = 0; i < 12; ++i) ok = ok && (__builtin_fabsf(o[i >> 2][i & 3]) <= 0x1p126f);
  if (__builtin_amdgcn_ballot_w64(!ok) != 0ull) {
    const char* kv = rec + (size_t)(32 + head) * REC_HEAD;
    const bf16x8 ql = *(const bf16x8*)(qrec + kq * REC + hh * 512 + r * 16);
    const u32x2 t = *(const u32x2*)(qrec + kq * REC + 1024 + r * 8);
    const bf16x8 qh = bx8(hh ? u32x4{} : u32x4{t.x, t.y, 0u, 0u});
    float m = -__builtin_inff();
    f32x16 oo = {};
    for (int t = 0; t < 64; ++t) {
      const char* q = kv + t * REC;
      const u32x2 kh = *(const u32x2*)(q + aKh);
      f32x16 s = {};
      s = MFMA32(*(const bf16x8*)(q + aK), ql, s);
      s = MFMA32(bx8(u32x4{kh.x, kh.y, 0u, 0u}), qh, s);
      float mn = fmaxf(m, max16(s));
      float c = __builtin_amdgcn_exp2f(m - mn);
      m = mn;
#pragma unroll
      for (int i = 0; i < 16; ++i) { s[i] -= mn; oo[i] *= c; }
      unsigned pp[8];
#pragma unroll
      for (int g = 0; g < 4; ++g) exp4(s, g, pp + 2 * g);
      u32x4 a0 = {vc, vc, vc, vc}, a1 = a0;
      if (r < 20) { a0 = *(const u32x4*)(q + aV); a1 = *(const u32x4*)(q + aV + REC_VS); }
      oo = MFMA32(bx8(a0), px8(pp), oo);
      oo = MFMA32(bx8(a1), px8(pp + 4), oo);
    }
#pragma unroll
    for (int i = 0; i < 12; ++i) o[i >> 2][i & 3] = oo[i];
    l = __shfl(oo[8], r + 32);
  }
  const float inv = 1.f / l;
  const int row = qb * 256 + (qp * 2 + kq) * 32 + r;
  const i32x4 so = mk_srd(ws + WS_O, S_ * D_ * 2);
#pragma unroll
  for (int g = 0; g < 3; ++g) {
    if (g < 2 || hh == 0) {
      const int col = head * 20 + 8 * g + 4 * hh;
      u32x2 v = {pk2(o[g][0] * inv, o[g][1] * inv), pk2(o[g][2] * inv, o[g][3] * inv)};
      st8(v, so, (col >> 3) * 32768 + row * 16 + (col & 7) * 2, 0);
    }
  }
  TS(1, 3);
}

__global__ __launch_bounds__(1024, 4) void k2_attn(char* __restrict__ ws, const u16* __restrict__ wo, const u16* __restrict__ wg,
                                                   const u16* __restrict__ wu, const u16* __restrict__ wd) {
  __shared__ __attribute__((aligned(1024))) char smem[REC_HEAD];
  const unsigned L = (unsigned)(uintptr_t)(LDS3 char*)smem;
  const int tid = threadIdx.x, lane = tid & 63, wave = __builtin_amdgcn_readfirstlane(tid >> 6);
  const int b = blockIdx.x, xcd = b & 7, bi = b >> 3;
  const int head = xcd * 4 + (bi & 3), qb = bi >> 2;
  const int qp = wave & 3, kq = wave >> 2;
  const int r = lane & 31, hh = lane >> 5;
  const char* rec = ws + WS_REC;
  const i32x4 skv = mk_srd(rec + (size_t)(32 + head) * REC_HEAD - L, REC_HEAD + L);
  const unsigned vL = lane * 16, pc = L + kq * 40960 + qp * 1024;
  TS(1, 0);
  u32x4 ql0, ql1, qh0, qh1;
  {
    const i32x4 sq = mk_srd(rec, 32 * REC_HEAD);
    asm volatile("v_mov_b64 v[92:93], 0\n\tv_mov_b64 v[94:95], 0\n\tv_mov_b64 v[100:101], 0\n\tv_mov_b64 v[102:103], 0\n\t"
                 "buffer_load_dwordx4 v[88:91], %4, %6, %7 offen\n\tbuffer_load_dwordx4 v[96:99], %4, %6, %7 offen offset:2560\n\t"
                 "s_mov_b32 exec_hi, 0\n\t"
                 "buffer_load_dwordx2 v[92:93], %5, %6, %7 offen offset:1024\n\tbuffer_load_dwordx2 v[100:101], %5, %6, %7 offen offset:3584\n\t"
                 "s_mov_b32 exec_hi, -1"
                 : "=&{v[88:91]}"(ql0), "=&{v[92:95]}"(qh0), "=&{v[96:99]}"(ql1), "=&{v[100:103]}"(qh1)
                 : "v"(hh * 512 + r * 16), "v"(r * 8), "s"(sq), "s"((head * 64 + qb * 8 + qp * 2) * REC));
  }
  dma16(pc, vL, skv, pc);
  BARRIER();
  dma16(pc + 4096, vL, skv, pc + 4096);
  dma16(pc + 8192, vL, skv, pc + 8192);
  const int mat = b >= 120, rg = (b - mat * 120) / 12, cb = b - mat * 120 - rg * 12, gw = b * 16 + wave;
  unsigned srcA, dstA, srcB, dstB;
  {
    const int pr = wave * 4 + (lane >> 4), cc = cb * 16 + (lane & 15), ct = (cc * 171) >> 11, c12 = cc - ct * 12;
    srcA = (rg * 64 + pr) * 3072 + cc * 16;
    dstA = (ct * 10 + rg) * 24576 + mat * 12288 + pr * 192 + (c12 ^ (mat * 2)) * 16;
    if (b >= 240) srcA = dstA = 0x80000000u;
    const int id = (gw < 800 ? gw : gw - 800) * 64 + lane, R = id / 80, c80 = id - R * 80, c8 = (c80 * 205) >> 11, k = R & 127;
    srcB = id * 16;
    dstB = (gw < 800 ? PK_O + c8 * (5 * 20480) : PK_D + c8 * (12 * 20480)) + (R >> 7) * 20480 + (k ^ ((k >> 1) & 4)) * 160 + (c80 - c8 * 10) * 16;
    if (gw >= 2720) srcB = dstB = 0x80000000u;
  }
  const i32x4 sa = mk_srd(mat ? wu : wg, D_ * F_ * 2), sb = mk_srd(gw < 800 ? wo : wd, gw < 800 ? D_ * D_ * 2 : D_ * F_ * 2);
  const i32x4 sp = mk_srd(ws + WS_PK, PK_SIZE);
  unsigned aK = L + kq * 40960 + hh * 512 + r * 16, aKh = L + kq * 40960 + 1024 + r * 8, aV = L + kq * 40960 + REC_V + hh * REC_VH + r * 16;
  const unsigned vc = r == 20 ? 0x3f803f80u : 0u;
  u32x4 v0 = {vc, vc, vc, vc}, v1 = v0;
  asm volatile("s_waitcnt vmcnt(2)\n\ts_barrier"
               : "+{v[88:91]}"(ql0), "+{v[92:95]}"(qh0), "+{v[96:99]}"(ql1), "+{v[100:103]}"(qh1), "+{v116}"(srcA), "+{v118}"(dstA),
                 "+{v117}"(srcB), "+{v119}"(dstB), "+{v[80:83]}"(v0), "+{v[84:87]}"(v1), "+{v104}"(aK), "+{v105}"(aKh), "+{v106}"(aV)
               :: "memory");
  TS(1, 1);
  f32x16 o0, o1;
  u32x4 wA;
  unsigned pcs = pc + 8192;
  asm volatile(K2_ASM
               : "=&{v[0:15]}"(o0), "=&{v[16:31]}"(o1), "+{v[80:83]}"(v0), "+{v[84:87]}"(v1), "=&{v[108:111]}"(wA),
                 "+{v[92:95]}"(qh0), "+{v[100:103]}"(qh1), [pc] "+s"(pcs)
               : "{v[88:91]}"(ql0), "{v[96:99]}"(ql1), "{v104}"(aK), "{v105}"(aKh), "{v106}"(aV), "{v107}"(vL),
                 "{v116}"(srcA), "{v117}"(srcB), "{v118}"(dstA), "{v119}"(dstB),
                 [mv] "s"(0x000fffff000fffffull), [sa] "s"(sa), [sb] "s"(sb), [sp] "s"(sp), [skv] "s"(skv), [ko] "s"(kq * 40960)
               : "memory", "scc", "v32", "v33", "v34", "v35", "v36", "v37", "v38", "v39", "v40", "v41", "v42", "v43", "v44", "v45", "v46", "v47",
                 "v48", "v49", "v50", "v51", "v52", "v53", "v54", "v55", "v56", "v57", "v58", "v59", "v60", "v61", "v62", "v63",
                 "v64", "v65", "v66", "v67", "v68", "v69", "v70", "v71", "v72", "v73", "v74", "v75", "v76", "v77", "v78", "v79",
                 "v112", "v113", "v114", "v115");
  TS(1, 2);
  k2_tail(ws, L, o0, o1, wA, wave, head, qb, mat, rg, cb);
}

struct Frag3 { bf16x8 a[2]; s16x4 b[5][2]; };
constexpr int K3_STAGE = 16384 + 20480;

template <int KD, bool FIRST>
__device__ __forceinline__ void proj_body(const char* __restrict__ A, const char* __restrict__ W, const char* __restrict__ R,
                                          char* __restrict__ C, char* __restrict__ stats, unsigned L) {
  constexpr int NS = KD / 128;
  const int tid = threadIdx.x, lane = tid & 63, wave = __builtin_amdgcn_readfirstlane(tid >> 6);
  const int b = blockIdx.x, xcd = b & 7, bi = b >> 3;
  const int rt = xcd * 4 + (bi & 3), ct = bi >> 2;
  const int m0 = rt * 64, n0 = ct * 80;
  const unsigned RS = L + 4 * K3_STAGE;
  TS(FIRST ? 2 : 4, 0);
  if (wave >= 4) {
    const int pw = wave - 4;
    const i32x4 sa = mk_srd(A, S_ * KD * 2), sw = mk_srd(W, KD * D_ * 2), sr = mk_srd(R, S_ * D_ * 2);
    const unsigned vL = lane * 16;
    auto issue = [&](int s) {
#pragma unroll
      for (int i = 0; i < 5; ++i) {
        if (i < 4) dma16(L + (s & 3) * K3_STAGE + (pw * 4 + i) * 1024, vL, sa, (s * 16 + pw * 4 + i) * 32768 + m0 * 16);
        dma16(L + (s & 3) * K3_STAGE + 16384 + (pw * 5 + i) * 1024, vL, sw, (ct * NS + s) * 20480 + (pw * 5 + i) * 1024);
      }
    };
    issue(0);
    BARRIER();
    issue(1);
    WAIT_V(9); BARRIER();
    issue(2);
#pragma unroll
    for (int i = 0; i < 3; ++i) {
      int p = pw * 3 + i; p = p > 9 ? 9 : p;
      if (FIRST) { int idx = p * 64 + lane, row = idx / 10, c = idx - row * 10; dma16(RS + p * 1024, row * 1280 + c * 16, sr, m0 * 1280 + n0 * 2); }
      else dma16(RS + p * 1024, vL, sr, (ct * 10 + p) * 32768 + m0 * 16);
    }
    issue(3);
    for (int s = 0; s < NS - 1; ++s) {
      if (s == 0) WAIT_V(21); else if (s < NS - 3) WAIT_V(18); else if (s == NS - 3) WAIT_V(9); else WAIT_V(0);
      BARRIER();
      if (s < NS - 4) issue(s + 4);
    }
    return;
  }
  const int wm = wave & 1, wk = wave >> 1, g = lane >> 4;
  const unsigned aA = L + (wk * 8 + g) * 1024 + (wm * 32 + (lane & 15)) * 16;
  unsigned aB[2];
#pragma unroll
  for (int t = 0; t < 2; ++t) aB[t] = L + 16384 + (wk * 64 + 8 * g + ((4 * t) ^ (4 * (g & 1))) + ((lane >> 2) & 3)) * 160 + (lane & 3) * 8;
  auto rd = [&](Frag3& f, unsigned sl, int ks, int t) {
    if (t == 0) f.a[0] = ldsr<bf16x8>(aA + sl + ks * 4096);
    else if (t == 3) f.a[1] = ldsr<bf16x8>(aA + sl + ks * 4096 + 256);
    else { const int u = t < 3 ? t - 1 : t - 2; f.b[u >> 1][u & 1] = ldstr(aB[u & 1] + sl + ks * 5120 + (u >> 1) * 32); }
  };
  f32x4 acc[2][5] = {};
  auto mm = [&](const Frag3& f, int t) { acc[t & 1][t >> 1] = MFMA16(cat8(f.b[t >> 1][0], f.b[t >> 1][1]), f.a[t & 1], acc[t & 1][t >> 1]); };
  auto body = [&](const Frag3& f, Frag3& n, unsigned sl, int ks) {
#pragma unroll
    for (int t = 1; t < 10; ++t) {
      SB();
      if (t <= 3) { rd(n, sl, ks, 2 * t - 2); rd(n, sl, ks, 2 * t - 1); } else rd(n, sl, ks, t + 2);
      SB(); mm(f, t);
    }
    SB();
  };
  Frag3 f0, f1;
  BARRIER(); BARRIER();
  TS(FIRST ? 2 : 4, 1);
#pragma unroll
  for (int t = 0; t < 12; ++t) rd(f0, 0, 0, t);
  SB();
  for (int s = 0; s < NS; ++s) {
    const unsigned sl = (s & 3) * K3_STAGE, nsl = ((s + 1) & 3) * K3_STAGE;
    mm(f0, 0); body(f0, f1, sl, 1);
    mm(f1, 0); SB();
    if (s < NS - 1) LBARRIER();
    body(f1, f0, nsl, 0);
  }
  TS(FIRST ? 2 : 4, 2);
  const unsigned X = L + ((NS + 1) & 3) * K3_STAGE;
#pragma unroll
  for (int j = 0; j < 5; ++j) ldsw<f32x4>(X + (wave * 5 + j) * 1024 + lane * 16, wk ? acc[0][j] : acc[1][j]);
  LBARRIER();
  const int lr = wm * 32 + wk * 16 + (lane & 15);
  const i32x4 rc = mk_srd(C, S_ * D_ * 2);
  const unsigned vo = FIRST ? (g >> 1) * 32768 + lr * 16 + (g & 1) * 8 : lr * 1280 + g * 8;
  f32x2 sum = {}, sq = {};
#pragma unroll
  for (int j = 0; j < 5; ++j) {
    f32x4 v = (wk ? acc[1][j] : acc[0][j]) + ldsr<f32x4>(X + ((wave ^ 2) * 5 + j) * 1024 + lane * 16);
    u32x2 rr = FIRST ? ldsr<u32x2>(RS + lr * 160 + j * 32 + g * 8) : ldsr<u32x2>(RS + (2 * j + (g >> 1)) * 1024 + lr * 16 + (g & 1) * 8);
    u32x2 o = {pk2(v[0] + bflo(rr.x), v[1] + bfhi(rr.x)), pk2(v[2] + bflo(rr.y), v[3] + bfhi(rr.y))};
    if (FIRST) {
      st8(o, rc, vo, (ct * 10 + 2 * j) * 32768 + m0 * 16);
      f32x2 h0 = {bflo(o.x), bfhi(o.x)}, h1 = {bflo(o.y), bfhi(o.y)};
      sum += h0 + h1;
      sq = fma2(h0, h0, fma2(h1, h1, sq));
    } else {
      st8(o, rc, vo, m0 * 1280 + (n0 + 16 * j) * 2);
    }
  }
  if (FIRST) {
    float s1 = sum.x + sum.y, s2 = sq.x + sq.y;
    s1 += __shfl_xor(s1, 16); s2 += __shfl_xor(s2, 16);
    s1 += __shfl_xor(s1, 32); s2 += __shfl_xor(s2, 32);
    if (lane < 16) *(f32x2*)(stats + (m0 + lr) * 64 + ct * 8) = f32x2{s1, s2};
  }
  TS(FIRST ? 2 : 4, 3);
}

__global__ __launch_bounds__(512, 2) void k3_oproj(const u16* __restrict__ x, char* __restrict__ ws) {
  __shared__ __attribute__((aligned(1024))) char smem[4 * K3_STAGE + 10240];
  proj_body<D_, true>(ws + WS_O, ws + WS_PK + PK_O, (const char*)x, ws + WS_H1, ws + WS_ST, (unsigned)(uintptr_t)(LDS3 char*)smem);
}
__global__ __launch_bounds__(512, 2) void k5_down(char* __restrict__ ws, u16* __restrict__ out) {
  __shared__ __attribute__((aligned(1024))) char smem[4 * K3_STAGE + 10240];
  proj_body<F_, false>(ws + WS_HH, ws + WS_PK + PK_D, ws + WS_H1, (char*)out, nullptr, (unsigned)(uintptr_t)(LDS3 char*)smem);
}

struct Frag4 { bf16x8 a[2]; s16x4 b[3][2]; };
constexpr int K4_STAGE = 16384 + 24576;

__global__ __launch_bounds__(512, 2) void k4_gateup(char* __restrict__ ws) {
  __shared__ __attribute__((aligned(1024))) char smem[4 * K4_STAGE];
  const unsigned L = (unsigned)(uintptr_t)(LDS3 char*)smem;
  const int tid = threadIdx.x, lane = tid & 63, wave = __builtin_amdgcn_readfirstlane(tid >> 6);
  const int b = blockIdx.x, xcd = b & 7, bi = b >> 3;
  const int rt = (xcd >> 1) * 4 + (bi & 3), ct = (xcd & 1) * 8 + (bi >> 2);
  const int m0 = rt * 128;
  const unsigned T = L + 2 * K4_STAGE;
  TS(3, 0);
  if (wave >= 4) {
    const int pw = wave - 4;
    const i32x4 sa = mk_srd(ws + WS_H1, S_ * D_ * 2), sw = mk_srd(ws + WS_PK + PK_G, PK_O);
    const i32x4 sst = mk_srd(ws + WS_ST, S_ * 64 + 16 * 8192);
    const unsigned vL = lane * 16;
    auto issue = [&](int s) {
      const unsigned sl = L + (s & 3) * K4_STAGE;
#pragma unroll
      for (int i = 0; i < 6; ++i) {
        const int p = pw * 4 + i, q = pw * 6 + i;
        if (i < 4) dma16(sl + p * 1024, vL, sa, (s * 8 + (p >> 1)) * 32768 + (m0 + (p & 1) * 64) * 16);
        dma16(sl + 16384 + q * 1024, vL, sw, (ct * 10 + s) * 24576 + q * 1024);
      }
    };
    issue(0);
    BARRIER();
    issue(1);
    WAIT_V(10); BARRIER();
    issue(2); issue(3);
    for (int s = 0; s < 9; ++s) {
      if (s < 7) WAIT_V(20); else if (s == 7) WAIT_V(14); else WAIT_V(0);
      BARRIER();
      if (s < 6) issue(s + 4);
      if (s == 6) {
        dma16(T + pw * 2048, vL, sst, m0 * 64 + pw * 2048);
        dma16(T + pw * 2048 + 1024, vL, sst, m0 * 64 + pw * 2048 + 1024);
        dma16(T + 8192 + pw * 2048, vL, sst, S_ * 64 + ct * 8192 + pw * 2048);
        dma16(T + 8192 + pw * 2048 + 1024, vL, sst, S_ * 64 + ct * 8192 + pw * 2048 + 1024);
      }
    }
    const int pt = tid - 256;
    if (pt < 128) {
      f32x4 t0 = ldsr<f32x4>(T + pt * 64), t1 = ldsr<f32x4>(T + pt * 64 + 16), t2 = ldsr<f32x4>(T + pt * 64 + 32), t3 = ldsr<f32x4>(T + pt * 64 + 48);
      f32x4 t = (t0 + t1) + (t2 + t3);
      float mean = (t[0] + t[2]) * (1.f / D_);
      float var = fmaxf((t[1] + t[3]) * (1.f / D_) - mean * mean, 0.f);
      ldsw<f32x2>(T + 16384 + pt * 8, f32x2{__builtin_amdgcn_rsqf(var + LN_EPS), -mean});
    } else if (pt < 128 + 96) {
      const unsigned a = T + 8192 + (pt - 128) * 8;
      f32x2 c = {};
#pragma unroll
      for (int k = 0; k < 10; ++k) c += ldsr<f32x2>(a + k * 768);
      ldsw<f32x2>(T + 17408 + (pt - 128) * 8, c);
    }
    LBARRIER();
    return;
  }
  const int wm = wave >> 1, wn = wave & 1, r = lane & 31, hh = lane >> 5;
  const unsigned aA = L + hh * 2048 + (wm * 64 + r) * 16;
  unsigned aB[3];
  {
    const int q = (lane >> 2) & 3, p = lane & 3, up = (lane >> 4) & 1;
#pragma unroll
    for (int j = 0; j < 3; ++j) aB[j] = L + 16384 + up * 12288 + (8 * hh + q) * 192 + (((wn * 3 + j) ^ up) * 32) + p * 8;
  }
  auto rd = [&](Frag4& f, unsigned sl, int ks, int t) {
    if (t == 0) { f.a[0] = ldsr<bf16x8>(aA + sl + ks * 4096); f.b[0][0] = ldstr(aB[0] + sl + ks * 3072); }
    if (t == 1) { f.b[0][1] = ldstr(aB[0] + sl + ks * 3072 + 768); f.a[1] = ldsr<bf16x8>(aA + sl + ks * 4096 + 512); }
    if (t >= 2) { f.b[t - 1][0] = ldstr(aB[t - 1] + sl + ks * 3072); f.b[t - 1][1] = ldstr(aB[t - 1] + sl + ks * 3072 + 768); }
  };
  f32x16 acc[2][3] = {};
  auto mm = [&](const Frag4& f, int t) { acc[t & 1][t >> 1] = MFMA32(cat8(f.b[t >> 1][0], f.b[t >> 1][1]), f.a[t & 1], acc[t & 1][t >> 1]); };
  auto body = [&](const Frag4& f, Frag4& n, unsigned sl, int ks) {
#pragma unroll
    for (int t = 1; t < 6; ++t) {
      SB();
      if (t <= 4) rd(n, sl, ks, t - 1);
      SB(); mm(f, t);
    }
    SB();
  };
  Frag4 f0, f1;
  BARRIER(); BARRIER();
  TS(3, 1);
#pragma unroll
  for (int t = 0; t < 4; ++t) rd(f0, 0, 0, t);
  SB();
  for (int s = 0; s < 10; ++s) {
    const unsigned sl = (s & 3) * K4_STAGE, nsl = ((s + 1) & 3) * K4_STAGE;
    mm(f0, 0); body(f0, f1, sl, 1);
    mm(f1, 0); body(f1, f0, sl, 2);
    mm(f0, 0); body(f0, f1, sl, 3);
    mm(f1, 0); SB();
    if (s < 9) LBARRIER();
    body(f1, f0, nsl, 0);
  }
  BARRIER();
  TS(3, 2);
  const float k1 = -2.f * 0.7978845608028654f * 1.4426950408889634f, k2 = k1 * 0.044715f;
  const i32x4 ro = mk_srd(ws + WS_HH, S_ * F_ * 2);
  const unsigned vo = hh * 32768 + r * 16;
#pragma unroll
  for (int j = 0; j < 3; ++j) {
    const int fc = (wn * 3 + j) * 16 + 4 * hh;
    f32x4 cg[2], cu[2];
#pragma unroll
    for (int g = 0; g < 2; ++g) { cg[g] = ldsr<f32x4>(T + 17408 + (fc + 8 * g) * 4); cu[g] = ldsr<f32x4>(T + 17408 + 384 + (fc + 8 * g) * 4); }
#pragma unroll
    for (int i = 0; i < 2; ++i) {
      const f32x2 ab = ldsr<f32x2>(T + 16384 + (wm * 64 + i * 32 + r) * 8);
      const f32x2 a = {ab.x, ab.x}, nm = {ab.y, ab.y};
      unsigned pk[4];
#pragma unroll
      for (int e = 0; e < 4; ++e) {
        const f32x2 gt = fma2(nm, sub2(cg[e >> 1], (e & 1) * 2), sub2(acc[i][j], 2 * e)) * a;
        const f32x2 up = fma2(nm, sub2(cu[e >> 1], (e & 1) * 2), sub2(acc[i][j], 8 + 2 * e)) * a;
        const f32x2 z = gt * fma2(gt * gt, f32x2{k2, k2}, f32x2{k1, k1});
        const f32x2 dn = f32x2{__builtin_amdgcn_exp2f(z.x), __builtin_amdgcn_exp2f(z.y)} + f32x2{1.f, 1.f};
        const f32x2 sg2 = {__builtin_amdgcn_rcpf(dn.x), __builtin_amdgcn_rcpf(dn.y)};
        pk[e] = pk2(gt * up * sg2);
      }
      auto s0 = __builtin_amdgcn_permlane32_swap(pk[0], pk[2], false, false);
      auto s1 = __builtin_amdgcn_permlane32_swap(pk[1], pk[3], false, false);
      st16(u32x4{s0[0], s1[0], s0[1], s1[1]}, ro, vo, (ct * 12 + (wn * 3 + j) * 2) * 32768 + (m0 + wm * 64 + i * 32) * 16);
    }
  }
  TS(3, 3);
}

extern "C" void kernel_launch(void* const* d_in, const int* in_sizes, int n_in, void* d_out, int out_size, void* d_ws,
                              size_t ws_size, hipStream_t stream) {
  const u16* x = (const u16*)d_in[0];
  const u16* wqkv = (const u16*)d_in[1];
  const u16* wo = (const u16*)d_in[2];
  const u16* wg = (const u16*)d_in[4];
  const u16* wu = (const u16*)d_in[5];
  const u16* wd = (const u16*)d_in[6];
  char* ws = (char*)d_ws;
  k1_qkv<<<256, 512, 0, stream>>>(x, wqkv, ws);
  k2_attn<<<256, 1024, 0, stream>>>(ws, wo, wg, wu, wd);
  k3_oproj<<<256, 512, 0, stream>>>(x, ws);
  k4_gateup<<<256, 512, 0, stream>>>(ws);
  k5_down<<<256, 512, 0, stream>>>(ws, (u16*)d_out);
}
```

```cpp
#include <hip/hip_runtime.h>
#include <stdint.h>

typedef unsigned short u16;
typedef __bf16 bf16x8 __attribute__((ext_vector_type(8)));
typedef __bf16 bf16x2 __attribute__((ext_vector_type(2)));
typedef short s16x4 __attribute__((ext_vector_type(4)));
typedef short s16x8 __attribute__((ext_vector_type(8)));
typedef float f32x2 __attribute__((ext_vector_type(2)));
typedef float f32x4 __attribute__((ext_vector_type(4)));
typedef float f32x16 __attribute__((ext_vector_type(16)));
typedef int i32x4 __attribute__((ext_vector_type(4)));
typedef unsigned u32x2 __attribute__((ext_vector_type(2)));
typedef unsigned u32x4 __attribute__((ext_vector_type(4)));
#define LDS3 __attribute__((address_space(3)))

constexpr int S_ = 2048, D_ = 640, F_ = 1536;
constexpr int REC = 2560, REC_V = 1280, REC_VS = 640, REC_VH = 320;
constexpr int REC_HEAD = 64 * REC;
constexpr size_t WS_REC = 0;
constexpr size_t WS_O = WS_REC + (size_t)64 * REC_HEAD;
constexpr size_t WS_H1 = WS_O + (size_t)S_ * D_ * 2;
constexpr size_t WS_HH = WS_H1 + (size_t)S_ * D_ * 2;
constexpr size_t WS_ST = WS_HH + (size_t)S_ * F_ * 2;
constexpr size_t WS_CS = WS_ST + (size_t)S_ * 64;
constexpr size_t WS_PK = WS_CS + 16 * 8192;
constexpr int PK_G = 0, PK_O = 16 * 10 * 24576, PK_D = PK_O + 8 * 5 * 20480, PK_SIZE = PK_D + 8 * 12 * 20480;
constexpr float LN_EPS = 1e-5f;

#define TS(k, i)
#define STPOL " sc1"
#define SB() __builtin_amdgcn_sched_barrier(0)
#define WAIT_V(n) asm volatile("s_waitcnt vmcnt(%0)" ::"n"(n) : "memory")
#define WAIT_VL(n) asm volatile("s_waitcnt vmcnt(%0) lgkmcnt(0)" ::"n"(n) : "memory")
#define BARRIER() asm volatile("s_barrier" ::: "memory")
#define LBARRIER() asm volatile("s_waitcnt lgkmcnt(0)\n\ts_barrier" ::: "memory")

__device__ __forceinline__ i32x4 mk_srd(const void* p, unsigned bytes) {
  unsigned long long a = (unsigned long long)p;
  i32x4 r;
  r.x = (int)(unsigned)a; r.y = (int)((unsigned)(a >> 32) & 0xffffu); r.z = (int)bytes; r.w = 0x00020000;
  return r;
}
__device__ __forceinline__ void st16(u32x4 v, i32x4 srd, unsigned voff, unsigned soff) {
  asm volatile("buffer_store_dwordx4 %0, %1, %2, %3 offen" STPOL "\n\ts_nop 1" ::"v"(v), "v"(voff), "s"(srd), "s"(soff));
}
__device__ __forceinline__ void st8(u32x2 v, i32x4 srd, unsigned voff, unsigned soff) {
  asm volatile("buffer_store_dwordx2 %0, %1, %2, %3 offen" STPOL ::"v"(v), "v"(voff), "s"(srd), "s"(soff));
}
__device__ __forceinline__ void dma16(unsigned lds, unsigned voff, i32x4 srd, unsigned soff) {
  asm volatile("s_mov_b32 m0, %0\n\ts_nop 0\n\tbuffer_load_dwordx4 %1, %2, %3 offen lds"
               ::"s"(lds), "v"(voff), "s"(srd), "s"(soff) : "memory");
}
__device__ __forceinline__ void dma16_masked(unsigned lds, unsigned voff, i32x4 srd, unsigned soff, unsigned long long mask) {
  asm volatile("s_mov_b32 m0, %0\n\ts_mov_b64 exec, %4\n\tbuffer_load_dwordx4 %1, %2, %3 offen lds\n\ts_mov_b64 exec, -1"
               ::"s"(lds), "v"(voff), "s"(srd), "s"(soff), "s"(mask) : "memory");
}
template <typename T> __device__ __forceinline__ T ldsr(unsigned a) { return *(LDS3 const T*)(uintptr_t)a; }
template <typename T> __device__ __forceinline__ void ldsw(unsigned a, T v) { *(LDS3 T*)(uintptr_t)a = v; }
__device__ __forceinline__ s16x4 ldstr(unsigned a) { return __builtin_amdgcn_ds_read_tr16_b64_v4i16((LDS3 s16x4*)(uintptr_t)a); }
__device__ __forceinline__ bf16x8 cat8(s16x4 lo, s16x4 hi) {
  s16x8 r = __builtin_shufflevector(lo, hi, 0, 1, 2, 3, 4, 5, 6, 7);
  return __builtin_bit_cast(bf16x8, r);
}
__device__ __forceinline__ unsigned pk2(f32x2 v) { return __builtin_bit_cast(unsigned, __builtin_convertvector(v, bf16x2)); }
__device__ __forceinline__ unsigned pk2(float a, float b) { return pk2(f32x2{a, b}); }
__device__ __forceinline__ float bflo(unsigned v) { return __uint_as_float(v << 16); }
__device__ __forceinline__ float bfhi(unsigned v) { return __uint_as_float(v & 0xffff0000u); }
__device__ __forceinline__ s16x4 lo4(bf16x8 v) { s16x8 t = __builtin_bit_cast(s16x8, v); return __builtin_shufflevector(t, t, 0, 1, 2, 3); }
__device__ __forceinline__ s16x4 hi4(bf16x8 v) { s16x8 t = __builtin_bit_cast(s16x8, v); return __builtin_shufflevector(t, t, 4, 5, 6, 7); }
__device__ __forceinline__ f32x2 sub2(const f32x16& v, int i) { return f32x2{v[i], v[i + 1]}; }
__device__ __forceinline__ f32x2 sub2(const f32x4& v, int i) { return f32x2{v[i], v[i + 1]}; }
__device__ __forceinline__ f32x2 fma2(f32x2 a, f32x2 b, f32x2 c) { return __builtin_elementwise_fma(a, b, c); }
#define MFMA32(a, b, c) __builtin_amdgcn_mfma_f32_32x32x16_bf16(a, b, c, 0, 0, 0)
#define MFMA16(a, b, c) __builtin_amdgcn_mfma_f32_16x16x32_bf16(a, b, c, 0, 0, 0)
#define MFMA32K8(a, b, c) __builtin_amdgcn_mfma_f32_32x32x8bf16_1k(a, b, c, 0, 0, 0)
#define MFMA4(a, b, c) __builtin_amdgcn_mfma_f32_4x4x4bf16_1k(a, b, c, 0, 0, 0)

struct Frag1 { bf16x8 a[2]; s16x4 b[2][2]; };

template <bool SWAP>
__device__ __forceinline__ void k1_consumer(char* __restrict__ rec, unsigned L, int rt, int ct, int wave) {
  const int tid = threadIdx.x, lane = tid & 63;
  const int wm = wave >> 1, wn = wave & 1, r = lane & 31, hh = lane >> 5;
  unsigned aA[2][4], aB[2];
#pragma unroll
  for (int i = 0; i < 2; ++i) {
    int row = wm * 64 + ((i ^ wn) * 32) + r;
#pragma unroll
    for (int ks = 0; ks < 4; ++ks) aA[i][ks] = L + row * 128 + (((2 * ks + hh) ^ ((row >> 1) & 7)) << 4);
    int q = (lane >> 2) & 3, p = lane & 3, ch = (lane >> 4) & 1;
    int c = wn * 8 + ((i ^ wm) * 4) + 2 * ch + (p >> 1);
    aB[i] = L + 16384 + (8 * hh + q) * 256 + ((c ^ (q << 2)) << 4) + 8 * (p & 1);
  }
  auto rd = [&](Frag1& f, unsigned sl, int ks, int t) {
    if (t == 0) { f.a[0] = ldsr<bf16x8>(aA[0][ks] + sl); f.b[0][0] = ldstr(aB[0] + sl + ks * 4096); }
    if (t == 1) { f.b[0][1] = ldstr(aB[0] + sl + ks * 4096 + 1024); f.b[1][0] = ldstr(aB[1] + sl + ks * 4096); }
    if (t == 2) { f.b[1][1] = ldstr(aB[1] + sl + ks * 4096 + 1024); f.a[1] = ldsr<bf16x8>(aA[1][ks] + sl); }
  };
  f32x16 acc[2][2] = {};
  f32x4 sq0 = {}, sq1 = {}, cs0 = {}, cs1 = {};
  const s16x4 ones = {0x3f80, 0x3f80, 0x3f80, 0x3f80};
  auto mm = [&](const Frag1& f, int t) {
    const int i = t >> 1, j = t & 1;
    const bf16x8 b = cat8(f.b[j][0], f.b[j][1]);
    acc[i][j] = SWAP ? MFMA32(b, f.a[i], acc[i][j]) : MFMA32(f.a[i], b, acc[i][j]);
    if (t == 3) {
      sq0 = MFMA4(lo4(f.a[0]), lo4(f.a[0]), sq0); cs0 = MFMA4(ones, f.b[0][0], cs0);
      sq1 = MFMA4(hi4(f.a[0]), hi4(f.a[0]), sq1); cs1 = MFMA4(ones, f.b[0][1], cs1);
    }
  };
  auto kstep = [&](const Frag1& f, Frag1& n, unsigned sl, int ks) {
    mm(f, 0); SB(); rd(n, sl, ks, 0); SB(); mm(f, 1); SB(); rd(n, sl, ks, 1); SB(); mm(f, 2); SB(); rd(n, sl, ks, 2); SB(); mm(f, 3); SB();
  };
  Frag1 f0, f1;
  BARRIER(); BARRIER();
  TS(0, 1);
  rd(f0, 0, 0, 0); rd(f0, 0, 0, 1); rd(f0, 0, 0, 2);
  SB();
  for (int s = 0; s < 10; ++s) {
    const unsigned sl = (s & 3) * 32768, nsl = ((s + 1) & 3) * 32768;
    kstep(f0, f1, sl, 1);
    kstep(f1, f0, sl, 2);
    kstep(f0, f1, sl, 3);
    mm(f1, 0); SB();
    if (s < 9) LBARRIER();
    SB(); rd(f0, nsl, 0, 0); SB(); mm(f1, 1); SB(); rd(f0, nsl, 0, 1); SB(); mm(f1, 2); SB(); rd(f0, nsl, 0, 2); SB(); mm(f1, 3); SB();
  }
  TS(0, 2);
  const unsigned T = L + 131072;
  {
    f32x4 sq = sq0 + sq1;
    int e = lane & 3;
    float sv = e == 0 ? sq[0] : e == 1 ? sq[1] : e == 2 ? sq[2] : sq[3];
    ldsw<float>(T + hh * 512 + (wm * 64 + wn * 32 + r) * 4, sv);
    ldsw<float>(T + 1024 + hh * 512 + (wn * 64 + wm * 32 + r) * 4, cs0[0] + cs1[0]);
    if (wn == 1) {
#pragma unroll
      for (int i = 0; i < 2; ++i) {
        const int rb = wm * 64 + (i ^ 1) * 32;
        if (SWAP) {
          float v = wm ? acc[i][0][12] : acc[i][1][12];
          if (hh == 0) ldsw<float>(T + 2048 + (rb + r) * 4, v);
        } else if (r == 24) {
#pragma unroll
          for (int g = 0; g < 4; ++g) {
            f32x4 v;
#pragma unroll
            for (int e2 = 0; e2 < 4; ++e2) v[e2] = wm ? acc[i][0][4 * g + e2] : acc[i][1][4 * g + e2];
            ldsw<f32x4>(T + 2048 + (rb + 8 * g + 4 * hh) * 4, v);
          }
        }
      }
    }
  }
  LBARRIER();
  if (tid < 128) {
    float sum = ldsr<float>(T + 2048 + tid * 4);
    float sq = ldsr<float>(T + tid * 4) + ldsr<float>(T + 512 + tid * 4);
    float mean = sum * (1.f / D_);
    float var = fmaxf(sq * (1.f / D_) - mean * mean, 0.f);
    float rs = __builtin_amdgcn_rsqf(var + LN_EPS);
    if (SWAP) ldsw<f32x2>(T + 2560 + tid * 8, f32x2{rs, -mean});
    else { ldsw<float>(T + 2560 + tid * 4, rs); ldsw<float>(T + 3072 + tid * 4, -mean); }
  } else {
    int c = tid - 128;
    ldsw<float>(T + 3584 + c * 4, ldsr<float>(T + 1024 + c * 4) + ldsr<float>(T + 1536 + c * 4));
  }
  LBARRIER();
  const i32x4 ro = mk_srd(rec, 64 * REC_HEAD);
  const int tile0 = rt * 4 + wm * 2;
  if (SWAP) {
    const float qs = 0.22360679774997896f * 1.4426950408889634f;
    f32x2 ab[2];
#pragma unroll
    for (int i = 0; i < 2; ++i) ab[i] = ldsr<f32x2>(T + 2560 + (wm * 64 + (i ^ wn) * 32 + r) * 8);
    const unsigned vlo = (hh ^ wn) * REC + r * 16;
    const unsigned vhi = hh * REC_HEAD + 1024 + r * 8;
    const unsigned X = L + wave * 8192;
#pragma unroll
    for (int j = 0; j < 2; ++j) {
#pragma unroll
      for (int g = 0; g < 4; ++g) {
        const int P = wn * 8 + (j ^ wm) * 4 + g;
        if (P < 15) {
          const int hp = (P * 13) >> 6, t = P - hp * 5, hA = ct * 6 + 2 * hp, od = (t + 1) >> 2;
          const f32x4 cs = ldsr<f32x4>(T + 3584 + (P * 8 + 4 * hh) * 4);
          const float sc = hA < 32 ? qs : 1.f;
          u32x2 v[2];
#pragma unroll
          for (int i = 0; i < 2; ++i) {
            const f32x2 a = {ab[i].x * sc, ab[i].x * sc}, nm = {ab[i].y, ab[i].y};
            v[i].x = pk2(fma2(nm, sub2(cs, 0), sub2(acc[i][j], 4 * g)) * a);
            v[i].y = pk2(fma2(nm, sub2(cs, 2), sub2(acc[i][j], 4 * g + 2)) * a);
          }
          if (hA >= 64) {
#pragma unroll
            for (int i = 0; i < 2; ++i) ldsw<u32x2>(X + ((i ^ wn) * 32 + r) * 104 + (t * 8 + 4 * hh) * 2, v[i]);
          } else if (t == 2) {
#pragma unroll
            for (int i = 0; i < 2; ++i) st8(v[i], ro, vhi, hA * REC_HEAD + (tile0 + (i ^ wn)) * REC);
          } else {
            auto s0 = __builtin_amdgcn_permlane32_swap(v[0].x, v[1].x, false, false);
            auto s1 = __builtin_amdgcn_permlane32_swap(v[0].y, v[1].y, false, false);
            st16(u32x4{s0[0], s1[0], s0[1], s1[1]}, ro, vlo, (hA + od) * REC_HEAD + tile0 * REC + (t - 3 * od) * 512);
          }
        }
      }
    }
    if (ct == 10 && wn == 1) {
      const int g4 = lane >> 4, c16 = lane & 15;
      const unsigned ra = X + (16 * (g4 >> 1) + 4 * (g4 & 1) + ((lane >> 2) & 3)) * 104 + (lane & 3) * 8;
#pragma unroll
      for (int mb = 0; mb < 2; ++mb) {
#pragma unroll
        for (int c = 0; c < 3; ++c) {
          s16x4 lo = ldstr(ra + mb * 32 * 104 + c * 32), hi = ldstr(ra + mb * 32 * 104 + 8 * 104 + c * 32);
          const int col = c * 16 + c16, hd = col >= 20, d = col - 20 * hd;
          const unsigned vo = hd * REC_HEAD + (g4 >> 1) * REC_VS + (g4 & 1) * REC_VH + d * 16;
          const unsigned so = 32 * REC_HEAD + (tile0 + mb) * REC + REC_V;
          if (col < 40) {
            st16(__builtin_bit_cast(u32x4, cat8(lo, hi)), ro, vo, so);
          }
        }
      }
    }
  } else {
#pragma unroll
    for (int i = 0; i < 2; ++i) {
      const int rb = wm * 64 + (i ^ wn) * 32;
      f32x4 av[4], bv[4];
#pragma unroll
      for (int g = 0; g < 4; ++g) {
        av[g] = ldsr<f32x4>(T + 2560 + (rb + 8 * g + 4 * hh) * 4);
        bv[g] = ldsr<f32x4>(T + 3072 + (rb + 8 * g + 4 * hh) * 4);
      }
#pragma unroll
      for (int j = 0; j < 2; ++j) {
        const int lc = wn * 64 + (j ^ wm) * 32 + r;
        if (lc < 120) {
          const float c1 = ldsr<float>(T + 3584 + lc * 4);
          const f32x2 cs = {c1, c1};
          const int gc = ct * 120 + lc - 1280;
          const int hd = (gc * 205) >> 12, d = gc - hd * 20;
          u32x4 v[2];
#pragma unroll
          for (int g = 0; g < 4; ++g) {
            v[g >> 1][(g & 1) * 2] = pk2(fma2(sub2(bv[g], 0), cs, sub2(acc[i][j], 4 * g)) * sub2(av[g], 0));
            v[g >> 1][(g & 1) * 2 + 1] = pk2(fma2(sub2(bv[g], 2), cs, sub2(acc[i][j], 4 * g + 2)) * sub2(av[g], 2));
          }
          const unsigned vo = hd * REC_HEAD + hh * REC_VH + d * 16, so = 32 * REC_HEAD + (tile0 + (i ^ wn)) * REC + REC_V;
          st16(v[0], ro, vo, so); st16(v[1], ro, vo + REC_VS, so);
        }
      }
    }
  }
  TS(0, 3);
}
constexpr bool chk_div() {
  for (int i = 0; i < 640; ++i) if (((i * 205) >> 12) != i / 20) return false;
  return true;
}
static_assert(chk_div(), "magic division");
constexpr bool chk_div2() {
  for (int i = 0; i < 192; ++i) if (((i * 171) >> 11) != i / 12) return false;
  for (int i = 0; i < 80; ++i) if (((i * 205) >> 11) != i / 10) return false;
  return true;
}
static_assert(chk_div2(), "magic division");

__global__ __launch_bounds__(512, 2) void k1_qkv(const u16* __restrict__ x, const u16* __restrict__ w, char* __restrict__ ws) {
  __shared__ __attribute__((aligned(1024))) char smem[131072 + 4096];
  const unsigned L = (unsigned)(uintptr_t)(LDS3 char*)smem;
  const int b = blockIdx.x, xcd = b & 7, bi = b >> 3;
  const int rt = (xcd >> 1) * 4 + (bi & 3), ct = (xcd & 1) * 8 + (bi >> 2);
  const int lane = threadIdx.x & 63, wave = __builtin_amdgcn_readfirstlane(threadIdx.x >> 6);
  TS(0, 0);
  if (wave >= 4) {
    const int pw = wave - 4;
    const i32x4 sx = mk_srd(x, S_ * D_ * 2), sw = mk_srd(w, D_ * 1920 * 2);
    unsigned vA[4], vB[4];
#pragma unroll
    for (int i = 0; i < 4; ++i) {
      int row = (pw * 4 + i) * 8 + (lane >> 3);
      vA[i] = row * 1280 + (((lane & 7) ^ ((row >> 1) & 7)) << 4);
      int k = (pw * 4 + i) * 4 + (lane >> 4);
      vB[i] = k * 3840 + (((lane & 15) ^ ((k & 3) << 2)) << 4);
    }
    const unsigned long long bmask = ~((1ull << 15) | (1ull << 27) | (1ull << 39) | (1ull << 51));
    const unsigned oA = rt * 128 * 1280, oB = ct * 240;
    auto issue = [&](int s) {
#pragma unroll
      for (int i = 0; i < 4; ++i) {
        dma16(L + (s & 3) * 32768 + (pw * 4 + i) * 1024, vA[i], sx, oA + s * 128);
        dma16_masked(L + (s & 3) * 32768 + 16384 + (pw * 4 + i) * 1024, vB[i], sw, oB + s * (64 * 3840), bmask);
      }
    };
    issue(0);
    ldsw<u32x4>(L + pw * 32768 + 16384 + lane * 256 + ((15 ^ ((lane & 3) << 2)) << 4), u32x4{0x3f80u, 0u, 0u, 0u});
    BARRIER();
    issue(1);
    WAIT_VL(8); BARRIER();
    issue(2); issue(3);
    for (int s = 0; s < 9; ++s) {
      if (s < 7) WAIT_V(16); else if (s == 7) WAIT_V(8); else WAIT_V(0);
      BARRIER();
      if (s < 6) issue(s + 4);
    }
    return;
  }
  if (ct <= 10) k1_consumer<true>(ws + WS_REC, L, rt, ct, wave);
  else k1_consumer<false>(ws + WS_REC, L, rt, ct, wave);
}

__device__ __forceinline__ void exp4(const f32x16& s, int g, unsigned* p) {
  float e0 = __builtin_amdgcn_exp2f(s[4 * g + 0]), e1 = __builtin_amdgcn_exp2f(s[4 * g + 1]);
  float e2 = __builtin_amdgcn_exp2f(s[4 * g + 2]), e3 = __builtin_amdgcn_exp2f(s[4 * g + 3]);
  p[0] = pk2(e0, e1); p[1] = pk2(e2, e3);
}
__device__ __forceinline__ bf16x8 px8(const unsigned* p) { u32x4 v = {p[0], p[1], p[2], p[3]}; return __builtin_bit_cast(bf16x8, v); }
__device__ __forceinline__ bf16x8 bx8(u32x4 v) { return __builtin_bit_cast(bf16x8, v); }
__device__ __forceinline__ float max16(const f32x16& s) {
  float m = fmaxf(fmaxf(fmaxf(s[0], s[1]), fmaxf(s[2], s[3])), fmaxf(fmaxf(s[4], s[5]), fmaxf(s[6], s[7])));
  float n = fmaxf(fmaxf(fmaxf(s[8], s[9]), fmaxf(s[10], s[11])), fmaxf(fmaxf(s[12], s[13]), fmaxf(s[14], s[15])));
  m = fmaxf(m, n);
  return fmaxf(m, __shfl_xor(m, 32));
}

#define K2_ASM \
  ".macro XE r\n v_exp_f32 v[\\r], v[\\r]\n.endm\n" \
  ".macro XC p, r\n v_cvt_pk_bf16_f32 v[\\p], v[\\r], v[\\r+1]\n.endm\n" \
  ".macro XM d, a, b, c\n v_mfma_f32_32x32x16_bf16 v[\\d:\\d+15], v[\\a:\\a+3], v[\\b:\\b+3], \\c\n.endm\n" \
  ".macro XPH s, n, q, op, oc, t, b\n" \
  " XE \\s\n XE \\s+1\n" \
  " .if \\t + \\b > 0\n XM \\op, 84, 68, v[\\op:\\op+15]\n .endif\n" \
  " .if \\b == 0\n" \
  "  .if \\t == 1\n s_waitcnt vmcnt(9)\n s_barrier\n .endif\n" \
  "  .if \\t == 4\n s_waitcnt vmcnt(2)\n s_barrier\n .endif\n" \
  "  s_mov_b64 exec, %[mv]\n ds_read_b128 v[80:83], v106 offset:\\t*2560\n ds_read_b128 v[84:87], v106 offset:\\t*2560+640\n s_mov_b64 exec, -1\n" \
  " .endif\n" \
  " XE \\s+2\n XE \\s+3\n XC 64, \\s\n XC 65, \\s+2\n XE \\s+4\n XE \\s+5\n" \
  " .if \\b == 1\n s_waitcnt lgkmcnt(0)\n .endif\n" \
  " .if \\t + \\b < 16\n XM \\n, 72, \\q, 0\n .endif\n" \
  " XE \\s+6\n XE \\s+7\n XC 66, \\s+4\n XC 67, \\s+6\n XE \\s+8\n" \
  " .if \\t + \\b < 16\n XM \\n, 76, \\q+4, v[\\n:\\n+15]\n .endif\n" \
  " .if (\\b == 0) && (\\t < 15)\n ds_read_b128 v[72:75], v104 offset:(\\t+1)*2560\n ds_read_b64 v[76:77], v105 offset:(\\t+1)*2560\n .endif\n" \
  " .if (\\b == 1) && (\\t == 8)\n s_waitcnt vmcnt(0)\n" \
  "  buffer_store_dwordx4 v[108:111], v118, %[sp], 0 offen sc1\n buffer_store_dwordx4 v[112:115], v119, %[sp], 0 offen sc1\n .endif\n" \
  " XE \\s+9\n XE \\s+10\n XE \\s+11\n XC 68, \\s+8\n XC 69, \\s+10\n XE \\s+12\n XE \\s+13\n" \
  " .if \\b == 0\n .if \\t < 15\n s_waitcnt lgkmcnt(2)\n .else\n s_waitcnt lgkmcnt(0)\n .endif\n .endif\n" \
  " .if \\t == 0\n XM \\oc, 80, 64, 0\n .else\n XM \\oc, 80, 64, v[\\oc:\\oc+15]\n .endif\n" \
  " XE \\s+14\n XE \\s+15\n XC 70, \\s+12\n XC 71, \\s+14\n" \
  ".endm\n" \
  ".macro XD\n s_add_u32 %[pc], %[pc], 4096\n s_mov_b32 m0, %[pc]\n s_nop 0\n buffer_load_dwordx4 v107, %[skv], %[pc] offen lds\n.endm\n" \
  ".macro XMAX d, s\n v_max3_f32 v[\\d], v[\\s], v[\\s+1], v[\\s+2]\n v_max3_f32 v[\\d+1], v[\\s+3], v[\\s+4], v[\\s+5]\n" \
  " v_max3_f32 v[\\d], v[\\d], v[\\s+6], v[\\s+7]\n v_max3_f32 v[\\d+1], v[\\d+1], v[\\s+8], v[\\s+9]\n" \
  " v_max3_f32 v[\\d], v[\\d], v[\\s+10], v[\\s+11]\n v_max3_f32 v[\\d+1], v[\\d+1], v[\\s+12], v[\\s+13]\n" \
  " v_max3_f32 v[\\d], v[\\d], v[\\s+14], v[\\s+15]\n v_max_f32 v[\\d], v[\\d], v[\\d+1]\n.endm\n" \
  "v_subrev_u32 v64, %[ko], v104\n v_subrev_u32 v65, %[ko], v105\n" \
  "ds_read_b128 v[72:75], v64\n ds_read_b64 v[76:77], v65\n" \
  "v_mov_b32 v78, 0x3f803f80\n v_mov_b32 v79, 0\n" \
  "XD\n XD\n s_waitcnt lgkmcnt(0)\n" \
  "XM 32, 72, 88, 0\n XM 48, 72, 96, 0\n XM 32, 76, 92, v[32:47]\n XM 48, 76, 100, v[48:63]\n" \
  "ds_read_b128 v[72:75], v104\n ds_read_b64 v[76:77], v105\n" \
  "XD\n XD\n XD\n XD\n XD\n" \
  "buffer_load_dwordx4 v[108:111], v116, %[sa], 0 offen\n" \
  "buffer_load_dwordx4 v[112:115], v117, %[sb], 0 offen\n" \
  "s_mov_b32 exec_hi, 0\n XMAX 64, 32\n XMAX 66, 48\n" \
  "v_cvt_pk_bf16_f32 v65, -v64, 0\n v_cvt_pk_bf16_f32 v67, -v66, 0\n v_lshlrev_b32 v65, 16, v65\n v_lshlrev_b32 v67, 16, v67\n" \
  "v_sub_f32 v65, -v64, v65\n v_sub_f32 v67, -v66, v67\n v_cvt_pk_bf16_f32 v94, -v64, v65\n v_cvt_pk_bf16_f32 v102, -v66, v67\n" \
  "s_mov_b32 exec_hi, -1\n s_waitcnt lgkmcnt(0)\n s_nop 0\n" \
  "XM 32, 72, 88, 0\n XM 32, 76, 92, v[32:47]\n s_nop 7\n s_nop 3\n" \
  ".irp t,0,1,2,3,4,5,6,7,8,9,10,11,12,13,14,15\n XPH 32, 48, 96, 16, 0, \\t, 0\n XPH 48, 32, 88, 0, 16, \\t, 1\n.endr\n" \
  "s_nop 1\n XM 16, 84, 68, v[16:31]\n s_nop 7\n s_nop 3\n" \
  ".purgem XE\n.purgem XC\n.purgem XM\n.purgem XPH\n.purgem XD\n.purgem XMAX\n"

__device__ __forceinline__ void k2_tail(char* __restrict__ ws, unsigned L, const f32x16& o0, const f32x16& o1, u32x4 wA,
                                        int wave, int head, int qb, int mat, int rg, int cb) {
  int lane;
  asm volatile("v_mbcnt_lo_u32_b32 %0, -1, 0\n\tv_mbcnt_hi_u32_b32 %0, -1, %0" : "=v"(lane));
  const int b = blockIdx.x, qp = wave & 3, kq = wave >> 2, r = lane & 31, hh = lane >> 5;
  const char* rec = ws + WS_REC;
  const char* qrec = rec + (size_t)(head * 64 + qb * 8 + qp * 2) * REC;
  const unsigned vL = lane * 16, aK = hh * 512 + r * 16, aKh = 1024 + r * 8, aV = REC_V + hh * REC_VH + r * 16;
  const unsigned vc = r == 20 ? 0x3f803f80u : 0u;
  f32x4 cs;
  {
    const int i = lane & 15;
    const unsigned oh = (i & 1) ? 0x3f800000u : 0x3f80u;
    const u32x4 sel = {i >> 1 == 0 ? oh : 0u, i >> 1 == 1 ? oh : 0u, i >> 1 == 2 ? oh : 0u, i >> 1 == 3 ? oh : 0u};
    cs = MFMA16(bx8(sel), bx8(wA), f32x4{});
  }
  BARRIER();
  const unsigned CS = L + 73728;
  if (lane < 32) ldsw<f32x4>(CS + wave * 512 + (lane & 15) * 32 + (lane >> 4) * 16, cs);
#pragma unroll
  for (int g = 0; g < 3; ++g) {
    const unsigned a = L + qp * 18432 + g * 1024 + vL;
    if (kq != 0) ldsw<f32x4>(a + (kq - 1) * 3072, f32x4{o0[4 * g], o0[4 * g + 1], o0[4 * g + 2], o0[4 * g + 3]});
    if (kq != 1) ldsw<f32x4>(a + 9216 + (kq ? kq - 1 : 0) * 3072, f32x4{o1[4 * g], o1[4 * g + 1], o1[4 * g + 2], o1[4 * g + 3]});
  }
  LBARRIER();
  if (kq >= 2) {
    if (kq == 2 && qp < 2 && b < 240) {
      const int c = qp * 64 + lane;
      float s = 0.f;
#pragma unroll
      for (int w = 0; w < 16; ++w) s += ldsr<float>(CS + w * 512 + c * 4);
      const int col = cb * 128 + c, ct = col / 96;
      __hip_atomic_store((float*)(ws + WS_CS + ct * 8192 + (rg * 192 + col - ct * 96 + mat * 96) * 4), s, __ATOMIC_RELAXED, __HIP_MEMORY_SCOPE_AGENT);
    }
    return;
  }
  f32x4 o[3];
#pragma unroll
  for (int g = 0; g < 3; ++g) {
    const unsigned a = L + qp * 18432 + kq * 9216 + g * 1024 + vL;
#pragma unroll
    for (int e = 0; e < 4; ++e) o[g][e] = kq ? o1[4 * g + e] : o0[4 * g + e];
    o[g] += ldsr<f32x4>(a) + (ldsr<f32x4>(a + 3072) + ldsr<f32x4>(a + 6144));
  }
  float l = __shfl(o[2][0], r + 32);
  bool ok = l <= 0x1p100f && l >= 0x1p-100f;
#pragma unroll
  for (int i = 0; i < 12; ++i) ok = ok && (__builtin_fabsf(o[i >> 2][i & 3]) <= 0x1p126f);
  if (__builtin_amdgcn_ballot_w64(!ok) != 0ull) {
    const char* kv = rec + (size_t)(32 + head) * REC_HEAD;
    const bf16x8 ql = *(const bf16x8*)(qrec + kq * REC + hh * 512 + r * 16);
    const u32x2 t = *(const u32x2*)(qrec + kq * REC + 1024 + r * 8);
    const bf16x8 qh = bx8(hh ? u32x4{} : u32x4{t.x, t.y, 0u, 0u});
    float m = -__builtin_inff();
    f32x16 oo = {};
    for (int t = 0; t < 64; ++t) {
      const char* q = kv + t * REC;
      const u32x2 kh = *(const u32x2*)(q + aKh);
      f32x16 s = {};
      s = MFMA32(*(const bf16x8*)(q + aK), ql, s);
      s = MFMA32(bx8(u32x4{kh.x, kh.y, 0u, 0u}), qh, s);
      float mn = fmaxf(m, max16(s));
      float c = __builtin_amdgcn_exp2f(m - mn);
      m = mn;
#pragma unroll
      for (int i = 0; i < 16; ++i) { s[i] -= mn; oo[i] *= c; }
      unsigned pp[8];
#pragma unroll
      for (int g = 0; g < 4; ++g) exp4(s, g, pp + 2 * g);
      u32x4 a0 = {vc, vc, vc, vc}, a1 = a0;
      if (r < 20) { a0 = *(const u32x4*)(q + aV); a1 = *(const u32x4*)(q + aV + REC_VS); }
      oo = MFMA32(bx8(a0), px8(pp), oo);
      oo = MFMA32(bx8(a1), px8(pp + 4), oo);
    }
#pragma unroll
    for (int i = 0; i < 12; ++i) o[i >> 2][i & 3] = oo[i];
    l = __shfl(oo[8], r + 32);
  }
  const float inv = 1.f / l;
  const int row = qb * 256 + (qp * 2 + kq) * 32 + r;
  const i32x4 so = mk_srd(ws + WS_O, S_ * D_ * 2);
#pragma unroll
  for (int g = 0; g < 3; ++g) {
    if (g < 2 || hh == 0) {
      const int col = head * 20 + 8 * g + 4 * hh;
      u32x2 v = {pk2(o[g][0] * inv, o[g][1] * inv), pk2(o[g][2] * inv, o[g][3] * inv)};
      st8(v, so, (col >> 3) * 32768 + row * 16 + (col & 7) * 2, 0);
    }
  }
  TS(1, 3);
}

__global__ __launch_bounds__(1024, 4) void k2_attn(char* __restrict__ ws, const u16* __restrict__ wo, const u16* __restrict__ wg,
                                                   const u16* __restrict__ wu, const u16* __restrict__ wd) {
  __shared__ __attribute__((aligned(1024))) char smem[REC_HEAD];
  const unsigned L = (unsigned)(uintptr_t)(LDS3 char*)smem;
  const int tid = threadIdx.x, lane = tid & 63, wave = __builtin_amdgcn_readfirstlane(tid >> 6);
  const int b = blockIdx.x, xcd = b & 7, bi = b >> 3;
  const int head = xcd * 4 + (bi & 3), qb = bi >> 2;
  const int qp = wave & 3, kq = wave >> 2;
  const int r = lane & 31, hh = lane >> 5;
  const char* rec = ws + WS_REC;
  const i32x4 skv = mk_srd(rec + (size_t)(32 + head) * REC_HEAD - L, REC_HEAD + L);
  const unsigned vL = lane * 16, pc = L + kq * 40960 + qp * 1024;
  TS(1, 0);
  u32x4 ql0, ql1, qh0, qh1;
  {
    const i32x4 sq = mk_srd(rec, 32 * REC_HEAD);
    asm volatile("v_mov_b64 v[92:93], 0\n\tv_mov_b64 v[94:95], 0\n\tv_mov_b64 v[100:101], 0\n\tv_mov_b64 v[102:103], 0\n\t"
                 "buffer_load_dwordx4 v[88:91], %4, %6, %7 offen\n\tbuffer_load_dwordx4 v[96:99], %4, %6, %7 offen offset:2560\n\t"
                 "s_mov_b32 exec_hi, 0\n\t"
                 "buffer_load_dwordx2 v[92:93], %5, %6, %7 offen offset:1024\n\tbuffer_load_dwordx2 v[100:101], %5, %6, %7 offen offset:3584\n\t"
                 "s_mov_b32 exec_hi, -1"
                 : "=&{v[88:91]}"(ql0), "=&{v[92:95]}"(qh0), "=&{v[96:99]}"(ql1), "=&{v[100:103]}"(qh1)
                 : "v"(hh * 512 + r * 16), "v"(r * 8), "s"(sq), "s"((head * 64 + qb * 8 + qp * 2) * REC));
  }
  dma16(pc, vL, skv, pc);
  BARRIER();
  dma16(pc + 4096, vL, skv, pc + 4096);
  dma16(pc + 8192, vL, skv, pc + 8192);
  const int mat = b >= 120, rg = (b - mat * 120) / 12, cb = b - mat * 120 - rg * 12, gw = b * 16 + wave;
  unsigned srcA, dstA, srcB, dstB;
  {
    const int pr = wave * 4 + (lane >> 4), cc = cb * 16 + (lane & 15), ct = (cc * 171) >> 11, c12 = cc - ct * 12;
    srcA = (rg * 64 + pr) * 3072 + cc * 16;
    dstA = (ct * 10 + rg) * 24576 + mat * 12288 + pr * 192 + (c12 ^ (mat * 2)) * 16;
    if (b >= 240) srcA = dstA = 0x80000000u;
    const int id = (gw < 800 ? gw : gw - 800) * 64 + lane, R = id / 80, c80 = id - R * 80, c8 = (c80 * 205) >> 11, k = R & 127;
    srcB = id * 16;
    dstB = (gw < 800 ? PK_O + c8 * (5 * 20480) : PK_D + c8 * (12 * 20480)) + (R >> 7) * 20480 + (k ^ ((k >> 1) & 4)) * 160 + (c80 - c8 * 10) * 16;
    if (gw >= 2720) srcB = dstB = 0x80000000u;
  }
  const i32x4 sa = mk_srd(mat ? wu : wg, D_ * F_ * 2), sb = mk_srd(gw < 800 ? wo : wd, gw < 800 ? D_ * D_ * 2 : D_ * F_ * 2);
  const i32x4 sp = mk_srd(ws + WS_PK, PK_SIZE);
  unsigned aK = L + kq * 40960 + hh * 512 + r * 16, aKh = L + kq * 40960 + 1024 + r * 8, aV = L + kq * 40960 + REC_V + hh * REC_VH + r * 16;
  const unsigned vc = r == 20 ? 0x3f803f80u : 0u;
  u32x4 v0 = {vc, vc, vc, vc}, v1 = v0;
  asm volatile("s_waitcnt vmcnt(2)\n\ts_barrier"
               : "+{v[88:91]}"(ql0), "+{v[92:95]}"(qh0), "+{v[96:99]}"(ql1), "+{v[100:103]}"(qh1), "+{v116}"(srcA), "+{v118}"(dstA),
                 "+{v117}"(srcB), "+{v119}"(dstB), "+{v[80:83]}"(v0), "+{v[84:87]}"(v1), "+{v104}"(aK), "+{v105}"(aKh), "+{v106}"(aV)
               :: "memory");
  TS(1, 1);
  f32x16 o0, o1;
  u32x4 wA;
  unsigned pcs = pc + 8192;
  asm volatile(K2_ASM
               : "=&{v[0:15]}"(o0), "=&{v[16:31]}"(o1), "+{v[80:83]}"(v0), "+{v[84:87]}"(v1), "=&{v[108:111]}"(wA),
                 "+{v[92:95]}"(qh0), "+{v[100:103]}"(qh1), [pc] "+s"(pcs)
               : "{v[88:91]}"(ql0), "{v[96:99]}"(ql1), "{v104}"(aK), "{v105}"(aKh), "{v106}"(aV), "{v107}"(vL),
                 "{v116}"(srcA), "{v117}"(srcB), "{v118}"(dstA), "{v119}"(dstB),
                 [mv] "s"(0x000fffff000fffffull), [sa] "s"(sa), [sb] "s"(sb), [sp] "s"(sp), [skv] "s"(skv), [ko] "s"(kq * 40960)
               : "memory", "scc", "v32", "v33", "v34", "v35", "v36", "v37", "v38", "v39", "v40", "v41", "v42", "v43", "v44", "v45", "v46", "v47",
                 "v48", "v49", "v50", "v51", "v52", "v53", "v54", "v55", "v56", "v57", "v58", "v59", "v60", "v61", "v62", "v63",
                 "v64", "v65", "v66", "v67", "v68", "v69", "v70", "v71", "v72", "v73", "v74", "v75", "v76", "v77", "v78", "v79",
                 "v112", "v113", "v114", "v115");
  TS(1, 2);
  k2_tail(ws, L, o0, o1, wA, wave, head, qb, mat, rg, cb);
}

struct Frag3 { bf16x8 a[2]; s16x4 b[5][2]; };
constexpr int K3_STAGE = 16384 + 20480;

template <int KD, bool FIRST>
__device__ __forceinline__ void proj_body(const char* __restrict__ A, const char* __restrict__ W, const char* __restrict__ R,
                                          char* __restrict__ C, char* __restrict__ stats, unsigned L) {
  constexpr int NS = KD / 128;
  const int tid = threadIdx.x, lane = tid & 63, wave = __builtin_amdgcn_readfirstlane(tid >> 6);
  const int b = blockIdx.x, xcd = b & 7, bi = b >> 3;
  const int rt = xcd * 4 + (bi & 3), ct = bi >> 2;
  const int m0 = rt * 64, n0 = ct * 80;
  const unsigned RS = L + 4 * K3_STAGE;
  TS(FIRST ? 2 : 4, 0);
  if (wave >= 4) {
    const int pw = wave - 4;
    const i32x4 sa = mk_srd(A, S_ * KD * 2), sw = mk_srd(W, KD * D_ * 2), sr = mk_srd(R, S_ * D_ * 2);
    const unsigned vL = lane * 16;
    auto issue = [&](int s) {
#pragma unroll
      for (int i = 0; i < 5; ++i) {
        if (i < 4) dma16(L + (s & 3) * K3_STAGE + (pw * 4 + i) * 1024, vL, sa, (s * 16 + pw * 4 + i) * 32768 + m0 * 16);
        dma16(L + (s & 3) * K3_STAGE + 16384 + (pw * 5 + i) * 1024, vL, sw, (ct * NS + s) * 20480 + (pw * 5 + i) * 1024);
      }
    };
    issue(0);
    BARRIER();
    issue(1);
    WAIT_V(9); BARRIER();
    issue(2);
#pragma unroll
    for (int i = 0; i < 3; ++i) {
      int p = pw * 3 + i; p = p > 9 ? 9 : p;
      if (FIRST) { int idx = p * 64 + lane, row = idx / 10, c = idx - row * 10; dma16(RS + p * 1024, row * 1280 + c * 16, sr, m0 * 1280 + n0 * 2); }
      else dma16(RS + p * 1024, vL, sr, (ct * 10 + p) * 32768 + m0 * 16);
    }
    issue(3);
    for (int s = 0; s < NS - 1; ++s) {
      if (s == 0) WAIT_V(21); else if (s < NS - 3) WAIT_V(18); else if (s == NS - 3) WAIT_V(9); else WAIT_V(0);
      BARRIER();
      if (s < NS - 4) issue(s + 4);
    }
    return;
  }
  const int wm = wave & 1, wk = wave >> 1, g = lane >> 4;
  const unsigned aA = L + (wk * 8 + g) * 1024 + (wm * 32 + (lane & 15)) * 16;
  unsigned aB[2];
#pragma unroll
  for (int t = 0; t < 2; ++t) aB[t] = L + 16384 + (wk * 64 + 8 * g + ((4 * t) ^ (4 * (g & 1))) + ((lane >> 2) & 3)) * 160 + (lane & 3) * 8;
  auto rd = [&](Frag3& f, unsigned sl, int ks, int t) {
    if (t == 0) f.a[0] = ldsr<bf16x8>(aA + sl + ks * 4096);
    else if (t == 3) f.a[1] = ldsr<bf16x8>(aA + sl + ks * 4096 + 256);
    else { const int u = t < 3 ? t - 1 : t - 2; f.b[u >> 1][u & 1] = ldstr(aB[u & 1] + sl + ks * 5120 + (u >> 1) * 32); }
  };
  f32x4 acc[2][5] = {};
  auto mm = [&](const Frag3& f, int t) { acc[t & 1][t >> 1] = MFMA16(cat8(f.b[t >> 1][0], f.b[t >> 1][1]), f.a[t & 1], acc[t & 1][t >> 1]); };
  auto body = [&](const Frag3& f, Frag3& n, unsigned sl, int ks) {
#pragma unroll
    for (int t = 1; t < 10; ++t) {
      SB();
      if (t <= 3) { rd(n, sl, ks, 2 * t - 2); rd(n, sl, ks, 2 * t - 1); } else rd(n, sl, ks, t + 2);
      SB(); mm(f, t);
    }
    SB();
  };
  Frag3 f0, f1;
  BARRIER(); BARRIER();
  TS(FIRST ? 2 : 4, 1);
#pragma unroll
  for (int t = 0; t < 12; ++t) rd(f0, 0, 0, t);
  SB();
  for (int s = 0; s < NS; ++s) {
    const unsigned sl = (s & 3) * K3_STAGE, nsl = ((s + 1) & 3) * K3_STAGE;
    mm(f0, 0); body(f0, f1, sl, 1);
    mm(f1, 0); SB();
    if (s < NS - 1) LBARRIER();
    body(f1, f0, nsl, 0);
  }
  TS(FIRST ? 2 : 4, 2);
  const unsigned X = L + ((NS + 1) & 3) * K3_STAGE;
#pragma unroll
  for (int j = 0; j < 5; ++j) ldsw<f32x4>(X + (wave * 5 + j) * 1024 + lane * 16, wk ? acc[0][j] : acc[1][j]);
  LBARRIER();
  const int lr = wm * 32 + wk * 16 + (lane & 15);
  const i32x4 rc = mk_srd(C, S_ * D_ * 2);
  const unsigned vo = FIRST ? (g >> 1) * 32768 + lr * 16 + (g & 1) * 8 : lr * 1280 + g * 8;
  f32x2 sum = {}, sq = {};
#pragma unroll
  for (int j = 0; j < 5; ++j) {
    f32x4 v = (wk ? acc[1][j] : acc[0][j]) + ldsr<f32x4>(X + ((wave ^ 2) * 5 + j) * 1024 + lane * 16);
    u32x2 rr = FIRST ? ldsr<u32x2>(RS + lr * 160 + j * 32 + g * 8) : ldsr<u32x2>(RS + (2 * j + (g >> 1)) * 1024 + lr * 16 + (g & 1) * 8);
    u32x2 o = {pk2(v[0] + bflo(rr.x), v[1] + bfhi(rr.x)), pk2(v[2] + bflo(rr.y), v[3] + bfhi(rr.y))};
    if (FIRST) {
      st8(o, rc, vo, (ct * 10 + 2 * j) * 32768 + m0 * 16);
      f32x2 h0 = {bflo(o.x), bfhi(o.x)}, h1 = {bflo(o.y), bfhi(o.y)};
      sum += h0 + h1;
      sq = fma2(h0, h0, fma2(h1, h1, sq));
    } else {
      st8(o, rc, vo, m0 * 1280 + (n0 + 16 * j) * 2);
    }
  }
  if (FIRST) {
    float s1 = sum.x + sum.y, s2 = sq.x + sq.y;
    s1 += __shfl_xor(s1, 16); s2 += __shfl_xor(s2, 16);
    s1 += __shfl_xor(s1, 32); s2 += __shfl_xor(s2, 32);
    if (lane < 16) st8(u32x2{__float_as_uint(s1), __float_as_uint(s2)}, mk_srd(stats, S_ * 64), lr * 64, m0 * 64 + ct * 8);
  }
  TS(FIRST ? 2 : 4, 3);
}

__global__ __launch_bounds__(512, 2) void k3_oproj(const u16* __restrict__ x, char* __restrict__ ws) {
  __shared__ __attribute__((aligned(1024))) char smem[4 * K3_STAGE + 10240];
  proj_body<D_, true>(ws + WS_O, ws + WS_PK + PK_O, (const char*)x, ws + WS_H1, ws + WS_ST, (unsigned)(uintptr_t)(LDS3 char*)smem);
}
__global__ __launch_bounds__(512, 2) void k5_down(char* __restrict__ ws, u16* __restrict__ out) {
  __shared__ __attribute__((aligned(1024))) char smem[4 * K3_STAGE + 10240];
  proj_body<F_, false>(ws + WS_HH, ws + WS_PK + PK_D, ws + WS_H1, (char*)out, nullptr, (unsigned)(uintptr_t)(LDS3 char*)smem);
}

struct Frag4 { bf16x8 a[2]; s16x4 b[3][2]; };
constexpr int K4_STAGE = 16384 + 24576;

__global__ __launch_bounds__(512, 2) void k4_gateup(char* __restrict__ ws) {
  __shared__ __attribute__((aligned(1024))) char smem[4 * K4_STAGE];
  const unsigned L = (unsigned)(uintptr_t)(LDS3 char*)smem;
  const int tid = threadIdx.x, lane = tid & 63, wave = __builtin_amdgcn_readfirstlane(tid >> 6);
  const int b = blockIdx.x, xcd = b & 7, bi = b >> 3;
  const int rt = (xcd >> 1) * 4 + (bi & 3), ct = (xcd & 1) * 8 + (bi >> 2);
  const int m0 = rt * 128;
  const unsigned T = L + 2 * K4_STAGE;
  TS(3, 0);
  if (wave >= 4) {
    const int pw = wave - 4;
    const i32x4 sa = mk_srd(ws + WS_H1, S_ * D_ * 2), sw = mk_srd(ws + WS_PK + PK_G, PK_O);
    const i32x4 sst = mk_srd(ws + WS_ST, S_ * 64 + 16 * 8192);
    const unsigned vL = lane * 16;
    auto issue = [&](int s) {
      const unsigned sl = L + (s & 3) * K4_STAGE;
#pragma unroll
      for (int i = 0; i < 6; ++i) {
        const int p = pw * 4 + i, q = pw * 6 + i;
        if (i < 4) dma16(sl + p * 1024, vL, sa, (s * 8 + (p >> 1)) * 32768 + (m0 + (p & 1) * 64) * 16);
        dma16(sl + 16384 + q * 1024, vL, sw, (ct * 10 + s) * 24576 + q * 1024);
      }
    };
    issue(0);
    BARRIER();
    issue(1);
    WAIT_V(10); BARRIER();
    issue(2); issue(3);
    for (int s = 0; s < 9; ++s) {
      if (s < 7) WAIT_V(20); else if (s == 7) WAIT_V(14); else WAIT_V(0);
      BARRIER();
      if (s < 6) issue(s + 4);
      if (s == 6) {
        dma16(T + pw * 2048, vL, sst, m0 * 64 + pw * 2048);
        dma16(T + pw * 2048 + 1024, vL, sst, m0 * 64 + pw * 2048 + 1024);
        dma16(T + 8192 + pw * 2048, vL, sst, S_ * 64 + ct * 8192 + pw * 2048);
        dma16(T + 8192 + pw * 2048 + 1024, vL, sst, S_ * 64 + ct * 8192 + pw * 2048 + 1024);
      }
    }
    const int pt = tid - 256;
    if (pt < 128) {
      f32x4 t0 = ldsr<f32x4>(T + pt * 64), t1 = ldsr<f32x4>(T + pt * 64 + 16), t2 = ldsr<f32x4>(T + pt * 64 + 32), t3 = ldsr<f32x4>(T + pt * 64 + 48);
      f32x4 t = (t0 + t1) + (t2 + t3);
      float mean = (t[0] + t[2]) * (1.f / D_);
      float var = fmaxf((t[1] + t[3]) * (1.f / D_) - mean * mean, 0.f);
      ldsw<f32x2>(T + 16384 + pt * 8, f32x2{__builtin_amdgcn_rsqf(var + LN_EPS), -mean});
    } else if (pt < 128 + 96) {
      const unsigned a = T + 8192 + (pt - 128) * 8;
      f32x2 c = {};
#pragma unroll
      for (int k = 0; k < 10; ++k) c += ldsr<f32x2>(a + k * 768);
      ldsw<f32x2>(T + 17408 + (pt - 128) * 8, c);
    }
    LBARRIER();
    return;
  }
  const int wm = wave >> 1, wn = wave & 1, r = lane & 31, hh = lane >> 5;
  const unsigned aA = L + hh * 2048 + (wm * 64 + r) * 16;
  unsigned aB[3];
  {
    const int q = (lane >> 2) & 3, p = lane & 3, up = (lane >> 4) & 1;
#pragma unroll
    for (int j = 0; j < 3; ++j) aB[j] = L + 16384 + up * 12288 + (8 * hh + q) * 192 + (((wn * 3 + j) ^ up) * 32) + p * 8;
  }
  auto rd = [&](Frag4& f, unsigned sl, int ks, int t) {
    if (t == 0) { f.a[0] = ldsr<bf16x8>(aA + sl + ks * 4096); f.b[0][0] = ldstr(aB[0] + sl + ks * 3072); }
    if (t == 1) { f.b[0][1] = ldstr(aB[0] + sl + ks * 3072 + 768); f.a[1] = ldsr<bf16x8>(aA + sl + ks * 4096 + 512); }
    if (t >= 2) { f.b[t - 1][0] = ldstr(aB[t - 1] + sl + ks * 3072); f.b[t - 1][1] = ldstr(aB[t - 1] + sl + ks * 3072 + 768); }
  };
  f32x16 acc[2][3] = {};
  auto mm = [&](const Frag4& f, int t) { acc[t & 1][t >> 1] = MFMA32(cat8(f.b[t >> 1][0], f.b[t >> 1][1]), f.a[t & 1], acc[t & 1][t >> 1]); };
  auto body = [&](const Frag4& f, Frag4& n, unsigned sl, int ks) {
#pragma unroll
    for (int t = 1; t < 6; ++t) {
      SB();
      if (t <= 4) rd(n, sl, ks, t - 1);
      SB(); mm(f, t);
    }
    SB();
  };
  Frag4 f0, f1;
  BARRIER(); BARRIER();
  TS(3, 1);
#pragma unroll
  for (int t = 0; t < 4; ++t) rd(f0, 0, 0, t);
  SB();
  for (int s = 0; s < 10; ++s) {
    const unsigned sl = (s & 3) * K4_STAGE, nsl = ((s + 1) & 3) * K4_STAGE;
    mm(f0, 0); body(f0, f1, sl, 1);
    mm(f1, 0); body(f1, f0, sl, 2);
    mm(f0, 0); body(f0, f1, sl, 3);
    mm(f1, 0); SB();
    if (s < 9) LBARRIER();
    body(f1, f0, nsl, 0);
  }
  BARRIER();
  TS(3, 2);
  const float k1 = -2.f * 0.7978845608028654f * 1.4426950408889634f, k2 = k1 * 0.044715f;
  const i32x4 ro = mk_srd(ws + WS_HH, S_ * F_ * 2);
  const unsigned vo = hh * 32768 + r * 16;
#pragma unroll
  for (int j = 0; j < 3; ++j) {
    const int fc = (wn * 3 + j) * 16 + 4 * hh;
    f32x4 cg[2], cu[2];
#pragma unroll
    for (int g = 0; g < 2; ++g) { cg[g] = ldsr<f32x4>(T + 17408 + (fc + 8 * g) * 4); cu[g] = ldsr<f32x4>(T + 17408 + 384 + (fc + 8 * g) * 4); }
#pragma unroll
    for (int i = 0; i < 2; ++i) {
      const f32x2 ab = ldsr<f32x2>(T + 16384 + (wm * 64 + i * 32 + r) * 8);
      const f32x2 a = {ab.x, ab.x}, nm = {ab.y, ab.y};
      unsigned pk[4];
#pragma unroll
      for (int e = 0; e < 4; ++e) {
        const f32x2 gt = fma2(nm, sub2(cg[e >> 1], (e & 1) * 2), sub2(acc[i][j], 2 * e)) * a;
        const f32x2 up = fma2(nm, sub2(cu[e >> 1], (e & 1) * 2), sub2(acc[i][j], 8 + 2 * e)) * a;
        const f32x2 z = gt * fma2(gt * gt, f32x2{k2, k2}, f32x2{k1, k1});
        const f32x2 dn = f32x2{__builtin_amdgcn_exp2f(z.x), __builtin_amdgcn_exp2f(z.y)} + f32x2{1.f, 1.f};
        const f32x2 sg2 = {__builtin_amdgcn_rcpf(dn.x), __builtin_amdgcn_rcpf(dn.y)};
        pk[e] = pk2(gt * up * sg2);
      }
      auto s0 = __builtin_amdgcn_permlane32_swap(pk[0], pk[2], false, false);
      auto s1 = __builtin_amdgcn_permlane32_swap(pk[1], pk[3], false, false);
      st16(u32x4{s0[0], s1[0], s0[1], s1[1]}, ro, vo, (ct * 12 + (wn * 3 + j) * 2) * 32768 + (m0 + wm * 64 + i * 32) * 16);
    }
  }
  TS(3, 3);
}

extern "C" void kernel_launch(void* const* d_in, const int* in_sizes, int n_in, void* d_out, int out_size, void* d_ws,
                              size_t ws_size, hipStream_t stream) {
  const u16* x = (const u16*)d_in[0];
  const u16* wqkv = (const u16*)d_in[1];
  const u16* wo = (const u16*)d_in[2];
  const u16* wg = (const u16*)d_in[4];
  const u16* wu = (const u16*)d_in[5];
  const u16* wd = (const u16*)d_in[6];
  char* ws = (char*)d_ws;
  k1_qkv<<<256, 512, 0, stream>>>(x, wqkv, ws);
  k2_attn<<<256, 1024, 0, stream>>>(ws, wo, wg, wu, wd);
  k3_oproj<<<256, 512, 0, stream>>>(x, ws);
  k4_gateup<<<256, 512, 0, stream>>>(ws);
  k5_down<<<256, 512, 0, stream>>>(ws, (u16*)d_out);
}
```

```cpp
#define CODEPF
#include <hip/hip_runtime.h>
#include <stdint.h>

typedef unsigned short u16;
typedef __bf16 bf16x8 __attribute__((ext_vector_type(8)));
typedef __bf16 bf16x2 __attribute__((ext_vector_type(2)));
typedef short s16x4 __attribute__((ext_vector_type(4)));
typedef short s16x8 __attribute__((ext_vector_type(8)));
typedef float f32x2 __attribute__((ext_vector_type(2)));
typedef float f32x4 __attribute__((ext_vector_type(4)));
typedef float f32x16 __attribute__((ext_vector_type(16)));
typedef int i32x4 __attribute__((ext_vector_type(4)));
typedef unsigned u32x2 __attribute__((ext_vector_type(2)));
typedef unsigned u32x4 __attribute__((ext_vector_type(4)));
#define LDS3 __attribute__((address_space(3)))

constexpr int S_ = 2048, D_ = 640, F_ = 1536;
constexpr int REC = 2560, REC_V = 1280, REC_VS = 640, REC_VH = 320;
constexpr int REC_HEAD = 64 * REC;
constexpr size_t WS_REC = 0;
constexpr size_t WS_O = WS_REC + (size_t)64 * REC_HEAD;
constexpr size_t WS_H1 = WS_O + (size_t)S_ * D_ * 2;
constexpr size_t WS_HH = WS_H1 + (size_t)S_ * D_ * 2;
constexpr size_t WS_ST = WS_HH + (size_t)S_ * F_ * 2;
constexpr size_t WS_CS = WS_ST + (size_t)S_ * 64;
constexpr size_t WS_PK = WS_CS + 16 * 8192;
constexpr int PK_G = 0, PK_O = 16 * 10 * 24576, PK_D = PK_O + 8 * 5 * 20480, PK_SIZE = PK_D + 8 * 12 * 20480;
constexpr float LN_EPS = 1e-5f;

#define TS(k, i)
#define STPOL " sc1"
#define SB() __builtin_amdgcn_sched_barrier(0)
#define WAIT_V(n) asm volatile("s_waitcnt vmcnt(%0)" ::"n"(n) : "memory")
#define WAIT_VL(n) asm volatile("s_waitcnt vmcnt(%0) lgkmcnt(0)" ::"n"(n) : "memory")
#define BARRIER() asm volatile("s_barrier" ::: "memory")
#define LBARRIER() asm volatile("s_waitcnt lgkmcnt(0)\n\ts_barrier" ::: "memory")

__device__ __forceinline__ i32x4 mk_srd(const void* p, unsigned bytes) {
  unsigned long long a = (unsigned long long)p;
  i32x4 r;
  r.x = (int)(unsigned)a; r.y = (int)((unsigned)(a >> 32) & 0xffffu); r.z = (int)bytes; r.w = 0x00020000;
  return r;
}
__device__ __forceinline__ void st16(u32x4 v, i32x4 srd, unsigned voff, unsigned soff) {
  asm volatile("buffer_store_dwordx4 %0, %1, %2, %3 offen" STPOL "\n\ts_nop 1" ::"v"(v), "v"(voff), "s"(srd), "s"(soff));
}
__device__ __forceinline__ void st8(u32x2 v, i32x4 srd, unsigned voff, unsigned soff) {
  asm volatile("buffer_store_dwordx2 %0, %1, %2, %3 offen" STPOL ::"v"(v), "v"(voff), "s"(srd), "s"(soff));
}
__device__ __forceinline__ void dma16(unsigned lds, unsigned voff, i32x4 srd, unsigned soff) {
  asm volatile("s_mov_b32 m0, %0\n\ts_nop 0\n\tbuffer_load_dwordx4 %1, %2, %3 offen lds"
               ::"s"(lds), "v"(voff), "s"(srd), "s"(soff) : "memory");
}
__device__ __forceinline__ void dma16nt(unsigned lds, unsigned voff, i32x4 srd, unsigned soff) {
  asm volatile("s_mov_b32 m0, %0\n\ts_nop 0\n\tbuffer_load_dwordx4 %1, %2, %3 offen nt lds"
               ::"s"(lds), "v"(voff), "s"(srd), "s"(soff) : "memory");
}
#define PFD 12
#define K4A dma16
#define K4B dma16
__device__ __forceinline__ void dma16_masked(unsigned lds, unsigned voff, i32x4 srd, unsigned soff, unsigned long long mask) {
  asm volatile("s_mov_b32 m0, %0\n\ts_mov_b64 exec, %4\n\tbuffer_load_dwordx4 %1, %2, %3 offen lds\n\ts_mov_b64 exec, -1"
               ::"s"(lds), "v"(voff), "s"(srd), "s"(soff), "s"(mask) : "memory");
}
template <typename T> __device__ __forceinline__ T ldsr(unsigned a) { return *(LDS3 const T*)(uintptr_t)a; }
template <typename T> __device__ __forceinline__ void ldsw(unsigned a, T v) { *(LDS3 T*)(uintptr_t)a = v; }
__device__ __forceinline__ s16x4 ldstr(unsigned a) { return __builtin_amdgcn_ds_read_tr16_b64_v4i16((LDS3 s16x4*)(uintptr_t)a); }
__device__ __forceinline__ bf16x8 cat8(s16x4 lo, s16x4 hi) {
  s16x8 r = __builtin_shufflevector(lo, hi, 0, 1, 2, 3, 4, 5, 6, 7);
  return __builtin_bit_cast(bf16x8, r);
}
__device__ __forceinline__ unsigned pk2(f32x2 v) { return __builtin_bit_cast(unsigned, __builtin_convertvector(v, bf16x2)); }
__device__ __forceinline__ unsigned pk2(float a, float b) { return pk2(f32x2{a, b}); }
__device__ __forceinline__ float bflo(unsigned v) { return __uint_as_float(v << 16); }
__device__ __forceinline__ float bfhi(unsigned v) { return __uint_as_float(v & 0xffff0000u); }
__device__ __forceinline__ s16x4 lo4(bf16x8 v) { s16x8 t = __builtin_bit_cast(s16x8, v); return __builtin_shufflevector(t, t, 0, 1, 2, 3); }
__device__ __forceinline__ s16x4 hi4(bf16x8 v) { s16x8 t = __builtin_bit_cast(s16x8, v); return __builtin_shufflevector(t, t, 4, 5, 6, 7); }
__device__ __forceinline__ f32x2 sub2(const f32x16& v, int i) { return f32x2{v[i], v[i + 1]}; }
__device__ __forceinline__ f32x2 sub2(const f32x4& v, int i) { return f32x2{v[i], v[i + 1]}; }
__device__ __forceinline__ f32x2 fma2(f32x2 a, f32x2 b, f32x2 c) { return __builtin_elementwise_fma(a, b, c); }
#define MFMA32(a, b, c) __builtin_amdgcn_mfma_f32_32x32x16_bf16(a, b, c, 0, 0, 0)
#define MFMA16(a, b, c) __builtin_amdgcn_mfma_f32_16x16x32_bf16(a, b, c, 0, 0, 0)
#define MFMA32K8(a, b, c) __builtin_amdgcn_mfma_f32_32x32x8bf16_1k(a, b, c, 0, 0, 0)
#define MFMA4(a, b, c) __builtin_amdgcn_mfma_f32_4x4x4bf16_1k(a, b, c, 0, 0, 0)

struct Frag1 { bf16x8 a[2]; s16x4 b[2][2]; };

template <bool SWAP>
__device__ __forceinline__ void k1_consumer(char* __restrict__ rec, unsigned L, int rt, int ct, int wave) {
  const int tid = threadIdx.x, lane = tid & 63;
  const int wm = wave >> 1, wn = wave & 1, r = lane & 31, hh = lane >> 5;
  unsigned aA[2][4], aB[2];
#pragma unroll
  for (int i = 0; i < 2; ++i) {
    int row = wm * 64 + ((i ^ wn) * 32) + r;
#pragma unroll
    for (int ks = 0; ks < 4; ++ks) aA[i][ks] = L + row * 128 + (((2 * ks + hh) ^ ((row >> 1) & 7)) << 4);
    int q = (lane >> 2) & 3, p = lane & 3, ch = (lane >> 4) & 1;
    int c = wn * 8 + ((i ^ wm) * 4) + 2 * ch + (p >> 1);
    aB[i] = L + 16384 + (8 * hh + q) * 256 + ((c ^ (q << 2)) << 4) + 8 * (p & 1);
  }
  auto rd = [&](Frag1& f, unsigned sl, int ks, int t) {
    if (t == 0) { f.a[0] = ldsr<bf16x8>(aA[0][ks] + sl); f.b[0][0] = ldstr(aB[0] + sl + ks * 4096); }
    if (t == 1) { f.b[0][1] = ldstr(aB[0] + sl + ks * 4096 + 1024); f.b[1][0] = ldstr(aB[1] + sl + ks * 4096); }
    if (t == 2) { f.b[1][1] = ldstr(aB[1] + sl + ks * 4096 + 1024); f.a[1] = ldsr<bf16x8>(aA[1][ks] + sl); }
  };
  f32x16 acc[2][2] = {};
  f32x4 sq0 = {}, sq1 = {}, cs0 = {}, cs1 = {};
  const s16x4 ones = {0x3f80, 0x3f80, 0x3f80, 0x3f80};
  auto mm = [&](const Frag1& f, int t) {
    const int i = t >> 1, j = t & 1;
    const bf16x8 b = cat8(f.b[j][0], f.b[j][1]);
    acc[i][j] = SWAP ? MFMA32(b, f.a[i], acc[i][j]) : MFMA32(f.a[i], b, acc[i][j]);
    if (t == 3) {
      sq0 = MFMA4(lo4(f.a[0]), lo4(f.a[0]), sq0); cs0 = MFMA4(ones, f.b[0][0], cs0);
      sq1 = MFMA4(hi4(f.a[0]), hi4(f.a[0]), sq1); cs1 = MFMA4(ones, f.b[0][1], cs1);
    }
  };
  auto kstep = [&](const Frag1& f, Frag1& n, unsigned sl, int ks) {
    mm(f, 0); SB(); rd(n, sl, ks, 0); SB(); mm(f, 1); SB(); rd(n, sl, ks, 1); SB(); mm(f, 2); SB(); rd(n, sl, ks, 2); SB(); mm(f, 3); SB();
  };
  Frag1 f0, f1;
  BARRIER(); BARRIER();
  TS(0, 1);
  rd(f0, 0, 0, 0); rd(f0, 0, 0, 1); rd(f0, 0, 0, 2);
  SB();
  for (int s = 0; s < 10; ++s) {
    const unsigned sl = (s & 3) * 32768, nsl = ((s + 1) & 3) * 32768;
    kstep(f0, f1, sl, 1);
    kstep(f1, f0, sl, 2);
    kstep(f0, f1, sl, 3);
    mm(f1, 0); SB();
    if (s < 9) LBARRIER();
    SB(); rd(f0, nsl, 0, 0); SB(); mm(f1, 1); SB(); rd(f0, nsl, 0, 1); SB(); mm(f1, 2); SB(); rd(f0, nsl, 0, 2); SB(); mm(f1, 3); SB();
  }
  TS(0, 2);
  const unsigned T = L + 131072;
  {
    f32x4 sq = sq0 + sq1;
    int e = lane & 3;
    float sv = e == 0 ? sq[0] : e == 1 ? sq[1] : e == 2 ? sq[2] : sq[3];
    ldsw<float>(T + hh * 512 + (wm * 64 + wn * 32 + r) * 4, sv);
    ldsw<float>(T + 1024 + hh * 512 + (wn * 64 + wm * 32 + r) * 4, cs0[0] + cs1[0]);
    if (wn == 1) {
#pragma unroll
      for (int i = 0; i < 2; ++i) {
        const int rb = wm * 64 + (i ^ 1) * 32;
        if (SWAP) {
          float v = wm ? acc[i][0][12] : acc[i][1][12];
          if (hh == 0) ldsw<float>(T + 2048 + (rb + r) * 4, v);
        } else if (r == 24) {
#pragma unroll
          for (int g = 0; g < 4; ++g) {
            f32x4 v;
#pragma unroll
            for (int e2 = 0; e2 < 4; ++e2) v[e2] = wm ? acc[i][0][4 * g + e2] : acc[i][1][4 * g + e2];
            ldsw<f32x4>(T + 2048 + (rb + 8 * g + 4 * hh) * 4, v);
          }
        }
      }
    }
  }
  LBARRIER();
  if (tid < 128) {
    float sum = ldsr<float>(T + 2048 + tid * 4);
    float sq = ldsr<float>(T + tid * 4) + ldsr<float>(T + 512 + tid * 4);
    float mean = sum * (1.f / D_);
    float var = fmaxf(sq * (1.f / D_) - mean * mean, 0.f);
    float rs = __builtin_amdgcn_rsqf(var + LN_EPS);
    if (SWAP) ldsw<f32x2>(T + 2560 + tid * 8, f32x2{rs, -mean});
    else { ldsw<float>(T + 2560 + tid * 4, rs); ldsw<float>(T + 3072 + tid * 4, -mean); }
  } else {
    int c = tid - 128;
    ldsw<float>(T + 3584 + c * 4, ldsr<float>(T + 1024 + c * 4) + ldsr<float>(T + 1536 + c * 4));
  }
  LBARRIER();
  const i32x4 ro = mk_srd(rec, 64 * REC_HEAD);
  const int tile0 = rt * 4 + wm * 2;
  if (SWAP) {
    const float qs = 0.22360679774997896f * 1.4426950408889634f;
    f32x2 ab[2];
#pragma unroll
    for (int i = 0; i < 2; ++i) ab[i] = ldsr<f32x2>(T + 2560 + (wm * 64 + (i ^ wn) * 32 + r) * 8);
    const unsigned vlo = (hh ^ wn) * REC + r * 16;
    const unsigned vhi = hh * REC_HEAD + 1024 + r * 8;
    const unsigned X = L + wave * 8192;
#pragma unroll
    for (int j = 0; j < 2; ++j) {
#pragma unroll
      for (int g = 0; g < 4; ++g) {
        const int P = wn * 8 + (j ^ wm) * 4 + g;
        if (P < 15) {
          const int hp = (P * 13) >> 6, t = P - hp * 5, hA = ct * 6 + 2 * hp, od = (t + 1) >> 2;
          const f32x4 cs = ldsr<f32x4>(T + 3584 + (P * 8 + 4 * hh) * 4);
          const float sc = hA < 32 ? qs : 1.f;
          u32x2 v[2];
#pragma unroll
          for (int i = 0; i < 2; ++i) {
            const f32x2 a = {ab[i].x * sc, ab[i].x * sc}, nm = {ab[i].y, ab[i].y};
            v[i].x = pk2(fma2(nm, sub2(cs, 0), sub2(acc[i][j], 4 * g)) * a);
            v[i].y = pk2(fma2(nm, sub2(cs, 2), sub2(acc[i][j], 4 * g + 2)) * a);
          }
          if (hA >= 64) {
#pragma unroll
            for (int i = 0; i < 2; ++i) ldsw<u32x2>(X + ((i ^ wn) * 32 + r) * 104 + (t * 8 + 4 * hh) * 2, v[i]);
          } else if (t == 2) {
#pragma unroll
            for (int i = 0; i < 2; ++i) st8(v[i], ro, vhi, hA * REC_HEAD + (tile0 + (i ^ wn)) * REC);
          } else {
            auto s0 = __builtin_amdgcn_permlane32_swap(v[0].x, v[1].x, false, false);
            auto s1 = __builtin_amdgcn_permlane32_swap(v[0].y, v[1].y, false, false);
            st16(u32x4{s0[0], s1[0], s0[1], s1[1]}, ro, vlo, (hA + od) * REC_HEAD + tile0 * REC + (t - 3 * od) * 512);
          }
        }
      }
    }
    if (ct == 10 && wn == 1) {
      const int g4 = lane >> 4, c16 = lane & 15;
      const unsigned ra = X + (16 * (g4 >> 1) + 4 * (g4 & 1) + ((lane >> 2) & 3)) * 104 + (lane & 3) * 8;
#pragma unroll
      for (int mb = 0; mb < 2; ++mb) {
#pragma unroll
        for (int c = 0; c < 3; ++c) {
          s16x4 lo = ldstr(ra + mb * 32 * 104 + c * 32), hi = ldstr(ra + mb * 32 * 104 + 8 * 104 + c * 32);
          const int col = c * 16 + c16, hd = col >= 20, d = col - 20 * hd;
          const unsigned vo = hd * REC_HEAD + (g4 >> 1) * REC_VS + (g4 & 1) * REC_VH + d * 16;
          const unsigned so = 32 * REC_HEAD + (tile0 + mb) * REC + REC_V;
          if (col < 40) {
            st16(__builtin_bit_cast(u32x4, cat8(lo, hi)), ro, vo, so);
          }
        }
      }
    }
  } else {
#pragma unroll
    for (int i = 0; i < 2; ++i) {
      const int rb = wm * 64 + (i ^ wn) * 32;
      f32x4 av[4], bv[4];
#pragma unroll
      for (int g = 0; g < 4; ++g) {
        av[g] = ldsr<f32x4>(T + 2560 + (rb + 8 * g + 4 * hh) * 4);
        bv[g] = ldsr<f32x4>(T + 3072 + (rb + 8 * g + 4 * hh) * 4);
      }
#pragma unroll
      for (int j = 0; j < 2; ++j) {
        const int lc = wn * 64 + (j ^ wm) * 32 + r;
        if (lc < 120) {
          const float c1 = ldsr<float>(T + 3584 + lc * 4);
          const f32x2 cs = {c1, c1};
          const int gc = ct * 120 + lc - 1280;
          const int hd = (gc * 205) >> 12, d = gc - hd * 20;
          u32x4 v[2];
#pragma unroll
          for (int g = 0; g < 4; ++g) {
            v[g >> 1][(g & 1) * 2] = pk2(fma2(sub2(bv[g], 0), cs, sub2(acc[i][j], 4 * g)) * sub2(av[g], 0));
            v[g >> 1][(g & 1) * 2 + 1] = pk2(fma2(sub2(bv[g], 2), cs, sub2(acc[i][j], 4 * g + 2)) * sub2(av[g], 2));
          }
          const unsigned vo = hd * REC_HEAD + hh * REC_VH + d * 16, so = 32 * REC_HEAD + (tile0 + (i ^ wn)) * REC + REC_V;
          st16(v[0], ro, vo, so); st16(v[1], ro, vo + REC_VS, so);
        }
      }
    }
  }
  TS(0, 3);
}
constexpr bool chk_div() {
  for (int i = 0; i < 640; ++i) if (((i * 205) >> 12) != i / 20) return false;
  return true;
}
static_assert(chk_div(), "magic division");
constexpr bool chk_div2() {
  for (int i = 0; i < 192; ++i) if (((i * 171) >> 11) != i / 12) return false;
  for (int i = 0; i < 80; ++i) if (((i * 205) >> 11) != i / 10) return false;
  return true;
}
static_assert(chk_div2(), "magic division");

__global__ __launch_bounds__(512, 2) void k1_qkv(const u16* __restrict__ x, const u16* __restrict__ w, char* __restrict__ ws) {
  __shared__ __attribute__((aligned(1024))) char smem[131072 + 4096];
  const unsigned L = (unsigned)(uintptr_t)(LDS3 char*)smem;
  const int b = blockIdx.x, xcd = b & 7, bi = b >> 3;
  const int rt = (xcd >> 1) * 4 + (bi & 3), ct = (xcd & 1) * 8 + (bi >> 2);
  const int lane = threadIdx.x & 63, wave = __builtin_amdgcn_readfirstlane(threadIdx.x >> 6);
  TS(0, 0);
  if (wave >= 4) {
    const int pw = wave - 4;
    const i32x4 sx = mk_srd(x, S_ * D_ * 2), sw = mk_srd(w, D_ * 1920 * 2);
    unsigned vA[4], vB[4];
#pragma unroll
    for (int i = 0; i < 4; ++i) {
      int row = (pw * 4 + i) * 8 + (lane >> 3);
      vA[i] = row * 1280 + (((lane & 7) ^ ((row >> 1) & 7)) << 4);
      int k = (pw * 4 + i) * 4 + (lane >> 4);
      vB[i] = k * 3840 + (((lane & 15) ^ ((k & 3) << 2)) << 4);
    }
    const unsigned long long bmask = ~((1ull << 15) | (1ull << 27) | (1ull << 39) | (1ull << 51));
    const unsigned oA = rt * 128 * 1280, oB = ct * 240;
    auto issue = [&](int s) {
#pragma unroll
      for (int i = 0; i < 4; ++i) {
        dma16(L + (s & 3) * 32768 + (pw * 4 + i) * 1024, vA[i], sx, oA + s * 128);
        dma16_masked(L + (s & 3) * 32768 + 16384 + (pw * 4 + i) * 1024, vB[i], sw, oB + s * (64 * 3840), bmask);
      }
    };
    issue(0);
    ldsw<u32x4>(L + pw * 32768 + 16384 + lane * 256 + ((15 ^ ((lane & 3) << 2)) << 4), u32x4{0x3f80u, 0u, 0u, 0u});
    BARRIER();
    issue(1);
    WAIT_VL(8); BARRIER();
    issue(2); issue(3);
    for (int s = 0; s < 9; ++s) {
      if (s < 7) WAIT_V(16); else if (s == 7) WAIT_V(8); else WAIT_V(0);
      BARRIER();
      if (s < 6) issue(s + 4);
    }
    return;
  }
  if (wave == 0) {
    unsigned long long pc; unsigned len, t;
    asm volatile("s_getpc_b64 %0\n.Lpf_here:\n\ts_mov_b32 %1, k_end-_Z6k1_qkvPKtS0_Pc\n\ts_mov_b32 %2, .Lpf_here-_Z6k1_qkvPKtS0_Pc" : "=s"(pc), "=s"(len), "=s"(t));
    i32x4 sc = mk_srd((const void*)(pc - t), len);
    asm volatile("buffer_load_dword %0, %1, %2, 0 offen\n\ts_waitcnt vmcnt(0)" : "=v"(t) : "v"((bi * 16 + (lane & 15)) * 128), "s"(sc));
  }
  if (ct <= 10) k1_consumer<true>(ws + WS_REC, L, rt, ct, wave);
  else k1_consumer<false>(ws + WS_REC, L, rt, ct, wave);
}

__device__ __forceinline__ void exp4(const f32x16& s, int g, unsigned* p) {
  float e0 = __builtin_amdgcn_exp2f(s[4 * g + 0]), e1 = __builtin_amdgcn_exp2f(s[4 * g + 1]);
  float e2 = __builtin_amdgcn_exp2f(s[4 * g + 2]), e3 = __builtin_amdgcn_exp2f(s[4 * g + 3]);
  p[0] = pk2(e0, e1); p[1] = pk2(e2, e3);
}
__device__ __forceinline__ bf16x8 px8(const unsigned* p) { u32x4 v = {p[0], p[1], p[2], p[3]}; return __builtin_bit_cast(bf16x8, v); }
__device__ __forceinline__ bf16x8 bx8(u32x4 v) { return __builtin_bit_cast(bf16x8, v); }
__device__ __forceinline__ float max16(const f32x16& s) {
  float m = fmaxf(fmaxf(fmaxf(s[0], s[1]), fmaxf(s[2], s[3])), fmaxf(fmaxf(s[4], s[5]), fmaxf(s[6], s[7])));
  float n = fmaxf(fmaxf(fmaxf(s[8], s[9]), fmaxf(s[10], s[11])), fmaxf(fmaxf(s[12], s[13]), fmaxf(s[14], s[15])));
  m = fmaxf(m, n);
  return fmaxf(m, __shfl_xor(m, 32));
}

#define K2_ASM \
  ".macro XE r\n v_exp_f32 v[\\r], v[\\r]\n.endm\n" \
  ".macro XC p, r\n v_cvt_pk_bf16_f32 v[\\p], v[\\r], v[\\r+1]\n.endm\n" \
  ".macro XM d, a, b, c\n v_mfma_f32_32x32x16_bf16 v[\\d:\\d+15], v[\\a:\\a+3], v[\\b:\\b+3], \\c\n.endm\n" \
  ".macro XPH s, n, q, op, oc, t, b\n" \
  " XE \\s\n XE \\s+1\n" \
  " .if \\t + \\b > 0\n XM \\op, 84, 68, v[\\op:\\op+15]\n .endif\n" \
  " .if \\b == 0\n" \
  "  .if \\t == 1\n s_waitcnt vmcnt(9)\n s_barrier\n .endif\n" \
  "  .if \\t == 4\n s_waitcnt vmcnt(0)\n s_barrier\n" \
  "   buffer_store_dwordx4 v[108:111], v118, %[sp], 0 offen sc1\n buffer_store_dwordx4 v[112:115], v119, %[sp], 0 offen sc1\n .endif\n" \
  "  .if \\t == 12\n v_lshlrev_b32 v116, 3, v107\n s_nop 0\n buffer_load_dword v117, v116, %[sp], %[pf] offen\n .endif\n" \
  "  s_mov_b64 exec, %[mv]\n ds_read_b128 v[80:83], v106 offset:\\t*2560\n ds_read_b128 v[84:87], v106 offset:\\t*2560+640\n s_mov_b64 exec, -1\n" \
  " .endif\n" \
  " XE \\s+2\n XE \\s+3\n XC 64, \\s\n XC 65, \\s+2\n XE \\s+4\n XE \\s+5\n" \
  " .if \\b == 1\n s_waitcnt lgkmcnt(0)\n .endif\n" \
  " .if \\t + \\b < 16\n XM \\n, 72, \\q, 0\n .endif\n" \
  " XE \\s+6\n XE \\s+7\n XC 66, \\s+4\n XC 67, \\s+6\n XE \\s+8\n" \
  " .if \\t + \\b < 16\n XM \\n, 76, \\q+4, v[\\n:\\n+15]\n .endif\n" \
  " .if (\\b == 0) && (\\t < 15)\n ds_read_b128 v[72:75], v104 offset:(\\t+1)*2560\n ds_read_b64 v[76:77], v105 offset:(\\t+1)*2560\n .endif\n" \
  " XE \\s+9\n XE \\s+10\n XE \\s+11\n XC 68, \\s+8\n XC 69, \\s+10\n XE \\s+12\n XE \\s+13\n" \
  " .if \\b == 0\n .if \\t < 15\n s_waitcnt lgkmcnt(2)\n .else\n s_waitcnt lgkmcnt(0)\n .endif\n .endif\n" \
  " .if \\t == 0\n XM \\oc, 80, 64, 0\n .else\n XM \\oc, 80, 64, v[\\oc:\\oc+15]\n .endif\n" \
  " XE \\s+14\n XE \\s+15\n XC 70, \\s+12\n XC 71, \\s+14\n" \
  ".endm\n" \
  ".macro XD\n s_add_u32 %[pc], %[pc], 4096\n s_mov_b32 m0, %[pc]\n s_nop 0\n buffer_load_dwordx4 v107, %[skv], %[pc] offen lds\n.endm\n" \
  ".macro XMAX d, s\n v_max3_f32 v[\\d], v[\\s], v[\\s+1], v[\\s+2]\n v_max3_f32 v[\\d+1], v[\\s+3], v[\\s+4], v[\\s+5]\n" \
  " v_max3_f32 v[\\d], v[\\d], v[\\s+6], v[\\s+7]\n v_max3_f32 v[\\d+1], v[\\d+1], v[\\s+8], v[\\s+9]\n" \
  " v_max3_f32 v[\\d], v[\\d], v[\\s+10], v[\\s+11]\n v_max3_f32 v[\\d+1], v[\\d+1], v[\\s+12], v[\\s+13]\n" \
  " v_max3_f32 v[\\d], v[\\d], v[\\s+14], v[\\s+15]\n v_max_f32 v[\\d], v[\\d], v[\\d+1]\n.endm\n" \
  "v_subrev_u32 v64, %[ko], v104\n v_subrev_u32 v65, %[ko], v105\n" \
  "ds_read_b128 v[72:75], v64\n ds_read_b64 v[76:77], v65\n" \
  "v_mov_b32 v78, 0x3f803f80\n v_mov_b32 v79, 0\n" \
  "XD\n XD\n s_waitcnt lgkmcnt(0)\n" \
  "XM 32, 72, 88, 0\n XM 48, 72, 96, 0\n XM 32, 76, 92, v[32:47]\n XM 48, 76, 100, v[48:63]\n" \
  "ds_read_b128 v[72:75], v104\n ds_read_b64 v[76:77], v105\n" \
  "XD\n XD\n XD\n XD\n XD\n" \
  "buffer_load_dwordx4 v[108:111], v116, %[sa], 0 offen\n" \
  "buffer_load_dwordx4 v[112:115], v117, %[sb], 0 offen\n" \
  "s_mov_b32 exec_hi, 0\n XMAX 64, 32\n XMAX 66, 48\n" \
  "v_cvt_pk_bf16_f32 v65, -v64, 0\n v_cvt_pk_bf16_f32 v67, -v66, 0\n v_lshlrev_b32 v65, 16, v65\n v_lshlrev_b32 v67, 16, v67\n" \
  "v_sub_f32 v65, -v64, v65\n v_sub_f32 v67, -v66, v67\n v_cvt_pk_bf16_f32 v94, -v64, v65\n v_cvt_pk_bf16_f32 v102, -v66, v67\n" \
  "s_mov_b32 exec_hi, -1\n s_waitcnt lgkmcnt(0)\n s_nop 0\n" \
  "XM 32, 72, 88, 0\n XM 32, 76, 92, v[32:47]\n s_nop 7\n s_nop 3\n" \
  ".irp t,0,1,2,3,4,5,6,7,8,9,10,11,12,13,14,15\n XPH 32, 48, 96, 16, 0, \\t, 0\n XPH 48, 32, 88, 0, 16, \\t, 1\n.endr\n" \
  "s_nop 1\n XM 16, 84, 68, v[16:31]\n s_waitcnt vmcnt(0)\n s_nop 7\n s_nop 3\n" \
  ".purgem XE\n.purgem XC\n.purgem XM\n.purgem XPH\n.purgem XD\n.purgem XMAX\n"

__device__ __forceinline__ void k2_tail(char* __restrict__ ws, unsigned L, const f32x16& o0, const f32x16& o1, u32x4 wA,
                                        int wave, int head, int qb, int mat, int rg, int cb) {
  int lane;
  asm volatile("v_mbcnt_lo_u32_b32 %0, -1, 0\n\tv_mbcnt_hi_u32_b32 %0, -1, %0" : "=v"(lane));
  const int b = blockIdx.x, qp = wave & 3, kq = wave >> 2, r = lane & 31, hh = lane >> 5;
  const char* rec = ws + WS_REC;
  const char* qrec = rec + (size_t)(head * 64 + qb * 8 + qp * 2) * REC;
  const unsigned vL = lane * 16, aK = hh * 512 + r * 16, aKh = 1024 + r * 8, aV = REC_V + hh * REC_VH + r * 16;
  const unsigned vc = r == 20 ? 0x3f803f80u : 0u;
  f32x4 cs;
  {
    const int i = lane & 15;
    const unsigned oh = (i & 1) ? 0x3f800000u : 0x3f80u;
    const u32x4 sel = {i >> 1 == 0 ? oh : 0u, i >> 1 == 1 ? oh : 0u, i >> 1 == 2 ? oh : 0u, i >> 1 == 3 ? oh : 0u};
    cs = MFMA16(bx8(sel), bx8(wA), f32x4{});
  }
  BARRIER();
  const unsigned CS = L + 73728;
  if (lane < 32) ldsw<f32x4>(CS + wave * 512 + (lane & 15) * 32 + (lane >> 4) * 16, cs);
#pragma unroll
  for (int g = 0; g < 3; ++g) {
    const unsigned a = L + qp * 18432 + g * 1024 + vL;
    if (kq != 0) ldsw<f32x4>(a + (kq - 1) * 3072, f32x4{o0[4 * g], o0[4 * g + 1], o0[4 * g + 2], o0[4 * g + 3]});
    if (kq != 1) ldsw<f32x4>(a + 9216 + (kq ? kq - 1 : 0) * 3072, f32x4{o1[4 * g], o1[4 * g + 1], o1[4 * g + 2], o1[4 * g + 3]});
  }
  LBARRIER();
  if (kq >= 2) {
    if (kq == 2 && qp < 2 && b < 240) {
      const int c = qp * 64 + lane;
      float s = 0.f;
#pragma unroll
      for (int w = 0; w < 16; ++w) s += ldsr<float>(CS + w * 512 + c * 4);
      const int col = cb * 128 + c, ct = col / 96;
      __hip_atomic_store((float*)(ws + WS_CS + ct * 8192 + (rg * 192 + col - ct * 96 + mat * 96) * 4), s, __ATOMIC_RELAXED, __HIP_MEMORY_SCOPE_AGENT);
    }
    return;
  }
  f32x4 o[3];
#pragma unroll
  for (int g = 0; g < 3; ++g) {
    const unsigned a = L + qp * 18432 + kq * 9216 + g * 1024 + vL;
#pragma unroll
    for (int e = 0; e < 4; ++e) o[g][e] = kq ? o1[4 * g + e] : o0[4 * g + e];
    o[g] += ldsr<f32x4>(a) + (ldsr<f32x4>(a + 3072) + ldsr<f32x4>(a + 6144));
  }
  float l = __shfl(o[2][0], r + 32);
  bool ok = l <= 0x1p100f && l >= 0x1p-100f;
#pragma unroll
  for (int i = 0; i < 12; ++i) ok = ok && (__builtin_fabsf(o[i >> 2][i & 3]) <= 0x1p126f);
  if (__builtin_amdgcn_ballot_w64(!ok) != 0ull) {
    const char* kv = rec + (size_t)(32 + head) * REC_HEAD;
    const bf16x8 ql = *(const bf16x8*)(qrec + kq * REC + hh * 512 + r * 16);
    const u32x2 t = *(const u32x2*)(qrec + kq * REC + 1024 + r * 8);
    const bf16x8 qh = bx8(hh ? u32x4{} : u32x4{t.x, t.y, 0u, 0u});
    float m = -__builtin_inff();
    f32x16 oo = {};
    for (int t = 0; t < 64; ++t) {
      const char* q = kv + t * REC;
      const u32x2 kh = *(const u32x2*)(q + aKh);
      f32x16 s = {};
      s = MFMA32(*(const bf16x8*)(q + aK), ql, s);
      s = MFMA32(bx8(u32x4{kh.x, kh.y, 0u, 0u}), qh, s);
      float mn = fmaxf(m, max16(s));
      float c = __builtin_amdgcn_exp2f(m - mn);
      m = mn;
#pragma unroll
      for (int i = 0; i < 16; ++i) { s[i] -= mn; oo[i] *= c; }
      unsigned pp[8];
#pragma unroll
      for (int g = 0; g < 4; ++g) exp4(s, g, pp + 2 * g);
      u32x4 a0 = {vc, vc, vc, vc}, a1 = a0;
      if (r < 20) { a0 = *(const u32x4*)(q + aV); a1 = *(const u32x4*)(q + aV + REC_VS); }
      oo = MFMA32(bx8(a0), px8(pp), oo);
      oo = MFMA32(bx8(a1), px8(pp + 4), oo);
    }
#pragma unroll
    for (int i = 0; i < 12; ++i) o[i >> 2][i & 3] = oo[i];
    l = __shfl(oo[8], r + 32);
  }
  const float inv = 1.f / l;
  const int row = qb * 256 + (qp * 2 + kq) * 32 + r;
  const i32x4 so = mk_srd(ws + WS_O, S_ * D_ * 2);
#pragma unroll
  for (int g = 0; g < 3; ++g) {
    if (g < 2 || hh == 0) {
      const int col = head * 20 + 8 * g + 4 * hh;
      u32x2 v = {pk2(o[g][0] * inv, o[g][1] * inv), pk2(o[g][2] * inv, o[g][3] * inv)};
      st8(v, so, (col >> 3) * 32768 + row * 16 + (col & 7) * 2, 0);
    }
  }
  TS(1, 3);
}

__global__ __launch_bounds__(1024, 4) void k2_attn(char* __restrict__ ws, const u16* __restrict__ wo, const u16* __restrict__ wg,
                                                   const u16* __restrict__ wu, const u16* __restrict__ wd) {
  __shared__ __attribute__((aligned(1024))) char smem[REC_HEAD];
  const unsigned L = (unsigned)(uintptr_t)(LDS3 char*)smem;
  const int tid = threadIdx.x, lane = tid & 63, wave = __builtin_amdgcn_readfirstlane(tid >> 6);
  const int b = blockIdx.x, xcd = b & 7, bi = b >> 3;
  const int head = xcd * 4 + (bi & 3), qb = bi >> 2;
  const int qp = wave & 3, kq = wave >> 2;
  const int r = lane & 31, hh = lane >> 5;
  const char* rec = ws + WS_REC;
  const i32x4 skv = mk_srd(rec + (size_t)(32 + head) * REC_HEAD - L, REC_HEAD + L);
  const unsigned vL = lane * 16, pc = L + kq * 40960 + qp * 1024;
  TS(1, 0);
  u32x4 ql0, ql1, qh0, qh1;
  {
    const i32x4 sq = mk_srd(rec, 32 * REC_HEAD);
    asm volatile("v_mov_b64 v[92:93], 0\n\tv_mov_b64 v[94:95], 0\n\tv_mov_b64 v[100:101], 0\n\tv_mov_b64 v[102:103], 0\n\t"
                 "buffer_load_dwordx4 v[88:91], %4, %6, %7 offen\n\tbuffer_load_dwordx4 v[96:99], %4, %6, %7 offen offset:2560\n\t"
                 "s_mov_b32 exec_hi, 0\n\t"
                 "buffer_load_dwordx2 v[92:93], %5, %6, %7 offen offset:1024\n\tbuffer_load_dwordx2 v[100:101], %5, %6, %7 offen offset:3584\n\t"
                 "s_mov_b32 exec_hi, -1"
                 : "=&{v[88:91]}"(ql0), "=&{v[92:95]}"(qh0), "=&{v[96:99]}"(ql1), "=&{v[100:103]}"(qh1)
                 : "v"(hh * 512 + r * 16), "v"(r * 8), "s"(sq), "s"((head * 64 + qb * 8 + qp * 2) * REC));
  }
  dma16(pc, vL, skv, pc);
  BARRIER();
  dma16(pc + 4096, vL, skv, pc + 4096);
  dma16(pc + 8192, vL, skv, pc + 8192);
  const int mat = b >= 120, rg = (b - mat * 120) / 12, cb = b - mat * 120 - rg * 12, gw = b * 16 + wave;
  unsigned srcA, dstA, srcB, dstB;
  {
    const int pr = wave * 4 + (lane >> 4), cc = cb * 16 + (lane & 15), ct = (cc * 171) >> 11, c12 = cc - ct * 12;
    srcA = (rg * 64 + pr) * 3072 + cc * 16;
    dstA = (ct * 10 + rg) * 24576 + mat * 12288 + pr * 192 + (c12 ^ (mat * 2)) * 16;
    if (b >= 240) srcA = dstA = 0x80000000u;
    const int id = (gw < 800 ? gw : gw - 800) * 64 + lane, R = id / 80, c80 = id - R * 80, c8 = (c80 * 205) >> 11, k = R & 127;
    srcB = id * 16;
    dstB = (gw < 800 ? PK_O + c8 * (5 * 20480) : PK_D + c8 * (12 * 20480)) + (R >> 7) * 20480 + (k ^ ((k >> 1) & 4)) * 160 + (c80 - c8 * 10) * 16;
    if (gw >= 2720) srcB = dstB = 0x80000000u;
  }
  const i32x4 sa = mk_srd(mat ? wu : wg, D_ * F_ * 2), sb = mk_srd(gw < 800 ? wo : wd, gw < 800 ? D_ * D_ * 2 : D_ * F_ * 2);
  const i32x4 sp = mk_srd(ws + WS_PK, PK_SIZE);
  unsigned aK = L + kq * 40960 + hh * 512 + r * 16, aKh = L + kq * 40960 + 1024 + r * 8, aV = L + kq * 40960 + REC_V + hh * REC_VH + r * 16;
  const unsigned vc = r == 20 ? 0x3f803f80u : 0u;
  u32x4 v0 = {vc, vc, vc, vc}, v1 = v0;
  asm volatile("s_waitcnt vmcnt(2)\n\ts_barrier"
               : "+{v[88:91]}"(ql0), "+{v[92:95]}"(qh0), "+{v[96:99]}"(ql1), "+{v[100:103]}"(qh1), "+{v116}"(srcA), "+{v118}"(dstA),
                 "+{v117}"(srcB), "+{v119}"(dstB), "+{v[80:83]}"(v0), "+{v[84:87]}"(v1), "+{v104}"(aK), "+{v105}"(aKh), "+{v106}"(aV)
               :: "memory");
  TS(1, 1);
  f32x16 o0, o1;
  u32x4 wA;
  unsigned pcs = pc + 8192;
  const int wi = bi * 16 + wave;
  const unsigned pf = wi < 240 ? (xcd & 1) * (8 * 10 * 24576) + wi * 8192 : PK_O + (wi < 340 ? wi - 240 : 0) * 8192;
  asm volatile(K2_ASM
               : "=&{v[0:15]}"(o0), "=&{v[16:31]}"(o1), "+{v[80:83]}"(v0), "+{v[84:87]}"(v1), "=&{v[108:111]}"(wA),
                 "+{v[92:95]}"(qh0), "+{v[100:103]}"(qh1), [pc] "+s"(pcs), "+{v116}"(srcA), "+{v117}"(srcB)
               : "{v[88:91]}"(ql0), "{v[96:99]}"(ql1), "{v104}"(aK), "{v105}"(aKh), "{v106}"(aV), "{v107}"(vL),
                 "{v118}"(dstA), "{v119}"(dstB), [pf] "s"(pf),
                 [mv] "s"(0x000fffff000fffffull), [sa] "s"(sa), [sb] "s"(sb), [sp] "s"(sp), [skv] "s"(skv), [ko] "s"(kq * 40960)
               : "memory", "scc", "v32", "v33", "v34", "v35", "v36", "v37", "v38", "v39", "v40", "v41", "v42", "v43", "v44", "v45", "v46", "v47",
                 "v48", "v49", "v50", "v51", "v52", "v53", "v54", "v55", "v56", "v57", "v58", "v59", "v60", "v61", "v62", "v63",
                 "v64", "v65", "v66", "v67", "v68", "v69", "v70", "v71", "v72", "v73", "v74", "v75", "v76", "v77", "v78", "v79",
                 "v112", "v113", "v114", "v115");
  TS(1, 2);
  k2_tail(ws, L, o0, o1, wA, wave, head, qb, mat, rg, cb);
}

struct Frag3 { bf16x8 a[2]; s16x4 b[5][2]; };
constexpr int K3_STAGE = 16384 + 20480;

template <int KD, bool FIRST>
__device__ __forceinline__ void proj_body(const char* __restrict__ A, const char* __restrict__ W, const char* __restrict__ R,
                                          char* __restrict__ C, char* __restrict__ stats, unsigned L) {
  constexpr int NS = KD / 128;
  const int tid = threadIdx.x, lane = tid & 63, wave = __builtin_amdgcn_readfirstlane(tid >> 6);
  const int b = blockIdx.x, xcd = b & 7, bi = b >> 3;
  const int rt = xcd * 4 + (bi & 3), ct = bi >> 2;
  const int m0 = rt * 64, n0 = ct * 80;
  const unsigned RS = L + 4 * K3_STAGE;
  TS(FIRST ? 2 : 4, 0);
  if (wave >= 4) {
    const int pw = wave - 4;
    const i32x4 sa = mk_srd(A, S_ * KD * 2), sw = mk_srd(W, KD * D_ * 2), sr = mk_srd(R, S_ * D_ * 2);
    const unsigned vL = lane * 16;
    auto issue = [&](int s) {
#pragma unroll
      for (int i = 0; i < 5; ++i) {
        if (i < 4) dma16(L + (s & 3) * K3_STAGE + (pw * 4 + i) * 1024, vL, sa, (s * 16 + pw * 4 + i) * 32768 + m0 * 16);
        dma16(L + (s & 3) * K3_STAGE + 16384 + (pw * 5 + i) * 1024, vL, sw, (ct * NS + s) * 20480 + (pw * 5 + i) * 1024);
      }
    };
    issue(0);
    BARRIER();
    issue(1);
    WAIT_V(9); BARRIER();
    issue(2);
#pragma unroll
    for (int i = 0; i < 3; ++i) {
      int p = pw * 3 + i; p = p > 9 ? 9 : p;
      if (FIRST) { int idx = p * 64 + lane, row = idx / 10, c = idx - row * 10; dma16(RS + p * 1024, row * 1280 + c * 16, sr, m0 * 1280 + n0 * 2); }
      else dma16(RS + p * 1024, vL, sr, (ct * 10 + p) * 32768 + m0 * 16);
    }
    issue(3);
    for (int s = 0; s < NS - 1; ++s) {
      if (s == 0) WAIT_V(21); else if (s < NS - 3) WAIT_V(18); else if (s == NS - 3) WAIT_V(9); else WAIT_V(0);
      BARRIER();
      if (s < NS - 4) issue(s + 4);
    }
    return;
  }
  const int wm = wave & 1, wk = wave >> 1, g = lane >> 4;
  const unsigned aA = L + (wk * 8 + g) * 1024 + (wm * 32 + (lane & 15)) * 16;
  unsigned aB[2];
#pragma unroll
  for (int t = 0; t < 2; ++t) aB[t] = L + 16384 + (wk * 64 + 8 * g + ((4 * t) ^ (4 * (g & 1))) + ((lane >> 2) & 3)) * 160 + (lane & 3) * 8;
  auto rd = [&](Frag3& f, unsigned sl, int ks, int t) {
    if (t == 0) f.a[0] = ldsr<bf16x8>(aA + sl + ks * 4096);
    else if (t == 3) f.a[1] = ldsr<bf16x8>(aA + sl + ks * 4096 + 256);
    else { const int u = t < 3 ? t - 1 : t - 2; f.b[u >> 1][u & 1] = ldstr(aB[u & 1] + sl + ks * 5120 + (u >> 1) * 32); }
  };
  f32x4 acc[2][5] = {};
  auto mm = [&](const Frag3& f, int t) { acc[t & 1][t >> 1] = MFMA16(cat8(f.b[t >> 1][0], f.b[t >> 1][1]), f.a[t & 1], acc[t & 1][t >> 1]); };
  auto body = [&](const Frag3& f, Frag3& n, unsigned sl, int ks) {
#pragma unroll
    for (int t = 1; t < 10; ++t) {
      SB();
      if (t <= 3) { rd(n, sl, ks, 2 * t - 2); rd(n, sl, ks, 2 * t - 1); } else rd(n, sl, ks, t + 2);
      SB(); mm(f, t);
    }
    SB();
  };
  Frag3 f0, f1;
  BARRIER(); BARRIER();
  TS(FIRST ? 2 : 4, 1);
#pragma unroll
  for (int t = 0; t < 12; ++t) rd(f0, 0, 0, t);
  SB();
  for (int s = 0; s < NS; ++s) {
    const unsigned sl = (s & 3) * K3_STAGE, nsl = ((s + 1) & 3) * K3_STAGE;
    mm(f0, 0); body(f0, f1, sl, 1);
    mm(f1, 0); SB();
    if (s < NS - 1) LBARRIER();
    body(f1, f0, nsl, 0);
  }
  TS(FIRST ? 2 : 4, 2);
  const unsigned X = L + ((NS + 1) & 3) * K3_STAGE;
#pragma unroll
  for (int j = 0; j < 5; ++j) ldsw<f32x4>(X + (wave * 5 + j) * 1024 + lane * 16, wk ? acc[0][j] : acc[1][j]);
  LBARRIER();
  const int lr = wm * 32 + wk * 16 + (lane & 15);
  const i32x4 rc = mk_srd(C, S_ * D_ * 2);
  const unsigned vo = FIRST ? (g >> 1) * 32768 + lr * 16 + (g & 1) * 8 : lr * 1280 + g * 8;
  f32x2 sum = {}, sq = {};
#pragma unroll
  for (int j = 0; j < 5; ++j) {
    f32x4 v = (wk ? acc[1][j] : acc[0][j]) + ldsr<f32x4>(X + ((wave ^ 2) * 5 + j) * 1024 + lane * 16);
    u32x2 rr = FIRST ? ldsr<u32x2>(RS + lr * 160 + j * 32 + g * 8) : ldsr<u32x2>(RS + (2 * j + (g >> 1)) * 1024 + lr * 16 + (g & 1) * 8);
    u32x2 o = {pk2(v[0] + bflo(rr.x), v[1] + bfhi(rr.x)), pk2(v[2] + bflo(rr.y), v[3] + bfhi(rr.y))};
    if (FIRST) {
      st8(o, rc, vo, (ct * 10 + 2 * j) * 32768 + m0 * 16);
      f32x2 h0 = {bflo(o.x), bfhi(o.x)}, h1 = {bflo(o.y), bfhi(o.y)};
      sum += h0 + h1;
      sq = fma2(h0, h0, fma2(h1, h1, sq));
    } else {
      st8(o, rc, vo, m0 * 1280 + (n0 + 16 * j) * 2);
    }
  }
  if (FIRST) {
    float s1 = sum.x + sum.y, s2 = sq.x + sq.y;
    s1 += __shfl_xor(s1, 16); s2 += __shfl_xor(s2, 16);
    s1 += __shfl_xor(s1, 32); s2 += __shfl_xor(s2, 32);
    if (lane < 16) st8(u32x2{__float_as_uint(s1), __float_as_uint(s2)}, mk_srd(stats, S_ * 64), lr * 64, m0 * 64 + ct * 8);
  }
  TS(FIRST ? 2 : 4, 3);
}

__global__ __launch_bounds__(512, 2) void k3_oproj(const u16* __restrict__ x, char* __restrict__ ws) {
  __shared__ __attribute__((aligned(1024))) char smem[4 * K3_STAGE + 10240];
  proj_body<D_, true>(ws + WS_O, ws + WS_PK + PK_O, (const char*)x, ws + WS_H1, ws + WS_ST, (unsigned)(uintptr_t)(LDS3 char*)smem);
}
__global__ __launch_bounds__(512, 2) void k5_down(char* __restrict__ ws, u16* __restrict__ out) {
  __shared__ __attribute__((aligned(1024))) char smem[4 * K3_STAGE + 10240];
  proj_body<F_, false>(ws + WS_HH, ws + WS_PK + PK_D, ws + WS_H1, (char*)out, nullptr, (unsigned)(uintptr_t)(LDS3 char*)smem);
}

struct Frag4 { bf16x8 a[2]; s16x4 b[3][2]; };
constexpr int K4_STAGE = 16384 + 24576;

__global__ __launch_bounds__(512, 2) void k4_gateup(char* __restrict__ ws) {
  __shared__ __attribute__((aligned(1024))) char smem[4 * K4_STAGE];
  const unsigned L = (unsigned)(uintptr_t)(LDS3 char*)smem;
  const int tid = threadIdx.x, lane = tid & 63, wave = __builtin_amdgcn_readfirstlane(tid >> 6);
  const int b = blockIdx.x, xcd = b & 7, bi = b >> 3;
  const int rt = (xcd >> 1) * 4 + (bi & 3), ct = (xcd & 1) * 8 + (bi >> 2);
  const int m0 = rt * 128;
  const unsigned T = L + 2 * K4_STAGE;
  TS(3, 0);
  if (wave >= 4) {
    const int pw = wave - 4;
    const i32x4 sa = mk_srd(ws + WS_H1, S_ * D_ * 2), sw = mk_srd(ws + WS_PK + PK_G, PK_O);
    const i32x4 sst = mk_srd(ws + WS_ST, S_ * 64 + 16 * 8192);
    const i32x4 sd = mk_srd(ws + WS_PK + PK_D, PK_SIZE - PK_D);
    const unsigned vL = lane * 16;
    auto issue = [&](int s) {
      const unsigned sl = L + (s & 3) * K4_STAGE;
#pragma unroll
      for (int i = 0; i < 6; ++i) {
        const int p = pw * 4 + i, q = pw * 6 + i;
        if (i < 4) K4A(sl + p * 1024, vL, sa, (s * 8 + (p >> 1)) * 32768 + (m0 + (p & 1) * 64) * 16);
        K4B(sl + 16384 + q * 1024, vL, sw, (ct * 10 + s) * 24576 + q * 1024);
      }
    };
    issue(0);
    BARRIER();
    issue(1);
    WAIT_V(10); BARRIER();
    issue(2); issue(3);
#pragma unroll
    for (int s = 0; s < 9; ++s) {
      if (s == 0) WAIT_V(20); else if (s == 1) WAIT_V(21); else if (s == 2) WAIT_V(22); else if (s < 7) WAIT_V(23);
      else if (s == 7) WAIT_V(16); else WAIT_V(0);
      BARRIER();
      if (s < 6) {
        issue(s + 4);
        const int n = s * 4 + pw;
        unsigned t;
        asm volatile("buffer_load_dword %0, %1, %2, %3 offen" : "=v"(t) : "v"(lane * (PFD * 2)), "s"(sd), "s"((bi * 3 + (n >> 3)) * 20480 + (n & 7) * (PFD * 128)));
      }
      if (s == 6) {
        dma16(T + pw * 2048, vL, sst, m0 * 64 + pw * 2048);
        dma16(T + pw * 2048 + 1024, vL, sst, m0 * 64 + pw * 2048 + 1024);
        dma16(T + 8192 + pw * 2048, vL, sst, S_ * 64 + ct * 8192 + pw * 2048);
        dma16(T + 8192 + pw * 2048 + 1024, vL, sst, S_ * 64 + ct * 8192 + pw * 2048 + 1024);
      }
    }
    const int pt = tid - 256;
    if (pt < 128) {
      f32x4 t0 = ldsr<f32x4>(T + pt * 64), t1 = ldsr<f32x4>(T + pt * 64 + 16), t2 = ldsr<f32x4>(T + pt * 64 + 32), t3 = ldsr<f32x4>(T + pt * 64 + 48);
      f32x4 t = (t0 + t1) + (t2 + t3);
      float mean = (t[0] + t[2]) * (1.f / D_);
      float var = fmaxf((t[1] + t[3]) * (1.f / D_) - mean * mean, 0.f);
      ldsw<f32x2>(T + 16384 + pt * 8, f32x2{__builtin_amdgcn_rsqf(var + LN_EPS), -mean});
    } else if (pt < 128 + 96) {
      const unsigned a = T + 8192 + (pt - 128) * 8;
      f32x2 c = {};
#pragma unroll
      for (int k = 0; k < 10; ++k) c += ldsr<f32x2>(a + k * 768);
      ldsw<f32x2>(T + 17408 + (pt - 128) * 8, c);
    }
    LBARRIER();
    return;
  }
  const int wm = wave >> 1, wn = wave & 1, r = lane & 31, hh = lane >> 5;
  const unsigned aA = L + hh * 2048 + (wm * 64 + r) * 16;
  unsigned aB[3];
  {
    const int q = (lane >> 2) & 3, p = lane & 3, up = (lane >> 4) & 1;
#pragma unroll
    for (int j = 0; j < 3; ++j) aB[j] = L + 16384 + up * 12288 + (8 * hh + q) * 192 + (((wn * 3 + j) ^ up) * 32) + p * 8;
  }
  auto rd = [&](Frag4& f, unsigned sl, int ks, int t) {
    if (t == 0) { f.a[0] = ldsr<bf16x8>(aA + sl + ks * 4096); f.b[0][0] = ldstr(aB[0] + sl + ks * 3072); }
    if (t == 1) { f.b[0][1] = ldstr(aB[0] + sl + ks * 3072 + 768); f.a[1] = ldsr<bf16x8>(aA + sl + ks * 4096 + 512); }
    if (t >= 2) { f.b[t - 1][0] = ldstr(aB[t - 1] + sl + ks * 3072); f.b[t - 1][1] = ldstr(aB[t - 1] + sl + ks * 3072 + 768); }
  };
  f32x16 acc[2][3] = {};
  auto mm = [&](const Frag4& f, int t) { acc[t & 1][t >> 1] = MFMA32(cat8(f.b[t >> 1][0], f.b[t >> 1][1]), f.a[t & 1], acc[t & 1][t >> 1]); };
  auto body = [&](const Frag4& f, Frag4& n, unsigned sl, int ks) {
#pragma unroll
    for (int t = 1; t < 6; ++t) {
      SB();
      if (t <= 4) rd(n, sl, ks, t - 1);
      SB(); mm(f, t);
    }
    SB();
  };
  Frag4 f0, f1;
  BARRIER(); BARRIER();
  TS(3, 1);
#pragma unroll
  for (int t = 0; t < 4; ++t) rd(f0, 0, 0, t);
  SB();
  for (int s = 0; s < 10; ++s) {
    const unsigned sl = (s & 3) * K4_STAGE, nsl = ((s + 1) & 3) * K4_STAGE;
    mm(f0, 0); body(f0, f1, sl, 1);
    mm(f1, 0); body(f1, f0, sl, 2);
    mm(f0, 0); body(f0, f1, sl, 3);
    mm(f1, 0); SB();
    if (s < 9) LBARRIER();
    body(f1, f0, nsl, 0);
  }
  BARRIER();
  TS(3, 2);
  const float k1 = -2.f * 0.7978845608028654f * 1.4426950408889634f, k2 = k1 * 0.044715f;
  const i32x4 ro = mk_srd(ws + WS_HH, S_ * F_ * 2);
  const unsigned vo = hh * 32768 + r * 16;
#pragma unroll
  for (int j = 0; j < 3; ++j) {
    const int fc = (wn * 3 + j) * 16 + 4 * hh;
    f32x4 cg[2], cu[2];
#pragma unroll
    for (int g = 0; g < 2; ++g) { cg[g] = ldsr<f32x4>(T + 17408 + (fc + 8 * g) * 4); cu[g] = ldsr<f32x4>(T + 17408 + 384 + (fc + 8 * g) * 4); }
#pragma unroll
    for (int i = 0; i < 2; ++i) {
      const f32x2 ab = ldsr<f32x2>(T + 16384 + (wm * 64 + i * 32 + r) * 8);
      const f32x2 a = {ab.x, ab.x}, nm = {ab.y, ab.y};
      unsigned pk[4];
#pragma unroll
      for (int e = 0; e < 4; ++e) {
        const f32x2 gt = fma2(nm, sub2(cg[e >> 1], (e & 1) * 2), sub2(acc[i][j], 2 * e)) * a;
        const f32x2 up = fma2(nm, sub2(cu[e >> 1], (e & 1) * 2), sub2(acc[i][j], 8 + 2 * e)) * a;
        const f32x2 z = gt * fma2(gt * gt, f32x2{k2, k2}, f32x2{k1, k1});
        const f32x2 dn = f32x2{__builtin_amdgcn_exp2f(z.x), __builtin_amdgcn_exp2f(z.y)} + f32x2{1.f, 1.f};
        const f32x2 sg2 = {__builtin_amdgcn_rcpf(dn.x), __builtin_amdgcn_rcpf(dn.y)};
        pk[e] = pk2(gt * up * sg2);
      }
      auto s0 = __builtin_amdgcn_permlane32_swap(pk[0], pk[2], false, false);
      auto s1 = __builtin_amdgcn_permlane32_swap(pk[1], pk[3], false, false);
      st16(u32x4{s0[0], s1[0], s0[1], s1[1]}, ro, vo, (ct * 12 + (wn * 3 + j) * 2) * 32768 + (m0 + wm * 64 + i * 32) * 16);
    }
  }
  TS(3, 3);
}

extern "C" __global__ void k_end() {}

extern "C" void kernel_launch(void* const* d_in, const int* in_sizes, int n_in, void* d_out, int out_size, void* d_ws,
                              size_t ws_size, hipStream_t stream) {
  const u16* x = (const u16*)d_in[0];
  const u16* wqkv = (const u16*)d_in[1];
  const u16* wo = (const u16*)d_in[2];
  const u16* wg = (const u16*)d_in[4];
  const u16* wu = (const u16*)d_in[5];
  const u16* wd = (const u16*)d_in[6];
  char* ws = (char*)d_ws;
  k1_qkv<<<256, 512, 0, stream>>>(x, wqkv, ws);
  k2_attn<<<256, 1024, 0, stream>>>(ws, wo, wg, wu, wd);
  k3_oproj<<<256, 512, 0, stream>>>(x, ws);
  k4_gateup<<<256, 512, 0, stream>>>(ws);
  k5_down<<<256, 512, 0, stream>>>(ws, (u16*)d_out);
}
```

```cpp
#include <hip/hip_runtime.h>
#include <stdint.h>

typedef unsigned short u16;
typedef __bf16 bf16x8 __attribute__((ext_vector_type(8)));
typedef __bf16 bf16x2 __attribute__((ext_vector_type(2)));
typedef short s16x4 __attribute__((ext_vector_type(4)));
typedef short s16x8 __attribute__((ext_vector_type(8)));
typedef float f32x2 __attribute__((ext_vector_type(2)));
typedef float f32x4 __attribute__((ext_vector_type(4)));
typedef float f32x16 __attribute__((ext_vector_type(16)));
typedef int i32x4 __attribute__((ext_vector_type(4)));
typedef unsigned u32x2 __attribute__((ext_vector_type(2)));
typedef unsigned u32x4 __attribute__((ext_vector_type(4)));
#define LDS3 __attribute__((address_space(3)))

constexpr int S_ = 2048, D_ = 640, F_ = 1536;
constexpr int REC = 2560, REC_V = 1280, REC_VS = 640, REC_VH = 320;
constexpr int REC_HEAD = 64 * REC;
constexpr size_t WS_REC = 0;
constexpr size_t WS_O = WS_REC + (size_t)64 * REC_HEAD;
constexpr size_t WS_H1 = WS_O + (size_t)S_ * D_ * 2;
constexpr size_t WS_HH = WS_H1 + (size_t)S_ * D_ * 2;
constexpr size_t WS_ST = WS_HH + (size_t)S_ * F_ * 2;
constexpr size_t WS_CS = WS_ST + (size_t)S_ * 64;
constexpr size_t WS_PK = WS_CS + 16 * 8192;
constexpr int PK_G = 0, PK_O = 16 * 10 * 24576, PK_D = PK_O + 8 * 5 * 20480, PK_SIZE = PK_D + 8 * 12 * 20480;
constexpr float LN_EPS = 1e-5f;

#define TS(k, i)
#define STPOL " sc1"
#define SB() __builtin_amdgcn_sched_barrier(0)
#define WAIT_V(n) asm volatile("s_waitcnt vmcnt(%0)" ::"n"(n) : "memory")
#define WAIT_VL(n) asm volatile("s_waitcnt vmcnt(%0) lgkmcnt(0)" ::"n"(n) : "memory")
#define BARRIER() asm volatile("s_barrier" ::: "memory")
#define LBARRIER() asm volatile("s_waitcnt lgkmcnt(0)\n\ts_barrier" ::: "memory")

__device__ __forceinline__ i32x4 mk_srd(const void* p, unsigned bytes) {
  unsigned long long a = (unsigned long long)p;
  i32x4 r;
  r.x = (int)(unsigned)a; r.y = (int)((unsigned)(a >> 32) & 0xffffu); r.z = (int)bytes; r.w = 0x00020000;
  return r;
}
__device__ __forceinline__ void st16(u32x4 v, i32x4 srd, unsigned voff, unsigned soff) {
  asm volatile("buffer_store_dwordx4 %0, %1, %2, %3 offen" STPOL "\n\ts_nop 1" ::"v"(v), "v"(voff), "s"(srd), "s"(soff));
}
__device__ __forceinline__ void st8(u32x2 v, i32x4 srd, unsigned voff, unsigned soff) {
  asm volatile("buffer_store_dwordx2 %0, %1, %2, %3 offen" STPOL ::"v"(v), "v"(voff), "s"(srd), "s"(soff));
}
__device__ __forceinline__ void dma16(unsigned lds, unsigned voff, i32x4 srd, unsigned soff) {
  asm volatile("s_mov_b32 m0, %0\n\ts_nop 0\n\tbuffer_load_dwordx4 %1, %2, %3 offen lds"
               ::"s"(lds), "v"(voff), "s"(srd), "s"(soff) : "memory");
}
__device__ __forceinline__ void dma16nt(unsigned lds, unsigned voff, i32x4 srd, unsigned soff) {
  asm volatile("s_mov_b32 m0, %0\n\ts_nop 0\n\tbuffer_load_dwordx4 %1, %2, %3 offen nt lds"
               ::"s"(lds), "v"(voff), "s"(srd), "s"(soff) : "memory");
}
#define PFD 12
#define K4A dma16
#define K4B dma16
__device__ __forceinline__ void dma16_masked(unsigned lds, unsigned voff, i32x4 srd, unsigned soff, unsigned long long mask) {
  asm volatile("s_mov_b32 m0, %0\n\ts_mov_b64 exec, %4\n\tbuffer_load_dwordx4 %1, %2, %3 offen lds\n\ts_mov_b64 exec, -1"
               ::"s"(lds), "v"(voff), "s"(srd), "s"(soff), "s"(mask) : "memory");
}
template <typename T> __device__ __forceinline__ T ldsr(unsigned a) { return *(LDS3 const T*)(uintptr_t)a; }
template <typename T> __device__ __forceinline__ void ldsw(unsigned a, T v) { *(LDS3 T*)(uintptr_t)a = v; }
__device__ __forceinline__ s16x4 ldstr(unsigned a) { return __builtin_amdgcn_ds_read_tr16_b64_v4i16((LDS3 s16x4*)(uintptr_t)a); }
__device__ __forceinline__ bf16x8 cat8(s16x4 lo, s16x4 hi) {
  s16x8 r = __builtin_shufflevector(lo, hi, 0, 1, 2, 3, 4, 5, 6, 7);
  return __builtin_bit_cast(bf16x8, r);
}
__device__ __forceinline__ unsigned pk2(f32x2 v) { return __builtin_bit_cast(unsigned, __builtin_convertvector(v, bf16x2)); }
__device__ __forceinline__ unsigned pk2(float a, float b) { return pk2(f32x2{a, b}); }
__device__ __forceinline__ float bflo(unsigned v) { return __uint_as_float(v << 16); }
__device__ __forceinline__ float bfhi(unsigned v) { return __uint_as_float(v & 0xffff0000u); }
__device__ __forceinline__ s16x4 lo4(bf16x8 v) { s16x8 t = __builtin_bit_cast(s16x8, v); return __builtin_shufflevector(t, t, 0, 1, 2, 3); }
__device__ __forceinline__ s16x4 hi4(bf16x8 v) { s16x8 t = __builtin_bit_cast(s16x8, v); return __builtin_shufflevector(t, t, 4, 5, 6, 7); }
__device__ __forceinline__ f32x2 sub2(const f32x16& v, int i) { return f32x2{v[i], v[i + 1]}; }
__device__ __forceinline__ f32x2 sub2(const f32x4& v, int i) { return f32x2{v[i], v[i + 1]}; }
__device__ __forceinline__ f32x2 fma2(f32x2 a, f32x2 b, f32x2 c) { return __builtin_elementwise_fma(a, b, c); }
#define MFMA32(a, b, c) __builtin_amdgcn_mfma_f32_32x32x16_bf16(a, b, c, 0, 0, 0)
#define MFMA16(a, b, c) __builtin_amdgcn_mfma_f32_16x16x32_bf16(a, b, c, 0, 0, 0)
#define MFMA32K8(a, b, c) __builtin_amdgcn_mfma_f32_32x32x8bf16_1k(a, b, c, 0, 0, 0)
#define MFMA4(a, b, c) __builtin_amdgcn_mfma_f32_4x4x4bf16_1k(a, b, c, 0, 0, 0)

struct Frag1 { bf16x8 a[2]; s16x4 b[2][2]; };

template <bool SWAP>
__device__ __forceinline__ void k1_consumer(char* __restrict__ rec, unsigned L, int rt, int ct, int wave) {
  const int tid = threadIdx.x - 256, lane = tid & 63;
  const int wm = wave >> 1, wn = wave & 1, r = lane & 31, hh = lane >> 5;
  unsigned aA[2][4], aB[2];
#pragma unroll
  for (int i = 0; i < 2; ++i) {
    int row = wm * 64 + ((i ^ wn) * 32) + r;
#pragma unroll
    for (int ks = 0; ks < 4; ++ks) aA[i][ks] = L + row * 128 + (((2 * ks + hh) ^ ((row >> 1) & 7)) << 4);
    int q = (lane >> 2) & 3, p = lane & 3, ch = (lane >> 4) & 1;
    int c = wn * 8 + ((i ^ wm) * 4) + 2 * ch + (p >> 1);
    aB[i] = L + 16384 + (8 * hh + q) * 256 + ((c ^ (q << 2)) << 4) + 8 * (p & 1);
  }
  auto rd = [&](Frag1& f, unsigned sl, int ks, int t) {
    if (t == 0) { f.a[0] = ldsr<bf16x8>(aA[0][ks] + sl); f.b[0][0] = ldstr(aB[0] + sl + ks * 4096); }
    if (t == 1) { f.b[0][1] = ldstr(aB[0] + sl + ks * 4096 + 1024); f.b[1][0] = ldstr(aB[1] + sl + ks * 4096); }
    if (t == 2) { f.b[1][1] = ldstr(aB[1] + sl + ks * 4096 + 1024); f.a[1] = ldsr<bf16x8>(aA[1][ks] + sl); }
  };
  f32x16 acc[2][2] = {};
  f32x4 sq0 = {}, sq1 = {}, cs0 = {}, cs1 = {};
  const s16x4 ones = {0x3f80, 0x3f80, 0x3f80, 0x3f80};
  auto mm = [&](const Frag1& f, int t) {
    const int i = t >> 1, j = t & 1;
    const bf16x8 b = cat8(f.b[j][0], f.b[j][1]);
    acc[i][j] = SWAP ? MFMA32(b, f.a[i], acc[i][j]) : MFMA32(f.a[i], b, acc[i][j]);
    if (t == 3) {
      sq0 = MFMA4(lo4(f.a[0]), lo4(f.a[0]), sq0); cs0 = MFMA4(ones, f.b[0][0], cs0);
      sq1 = MFMA4(hi4(f.a[0]), hi4(f.a[0]), sq1); cs1 = MFMA4(ones, f.b[0][1], cs1);
    }
  };
  auto kstep = [&](const Frag1& f, Frag1& n, unsigned sl, int ks) {
    mm(f, 0); SB(); rd(n, sl, ks, 0); SB(); mm(f, 1); SB(); rd(n, sl, ks, 1); SB(); mm(f, 2); SB(); rd(n, sl, ks, 2); SB(); mm(f, 3); SB();
  };
  Frag1 f0, f1;
  BARRIER(); BARRIER();
  TS(0, 1);
  rd(f0, 0, 0, 0); rd(f0, 0, 0, 1); rd(f0, 0, 0, 2);
  SB();
  for (int s = 0; s < 10; ++s) {
    const unsigned sl = (s & 3) * 32768, nsl = ((s + 1) & 3) * 32768;
    kstep(f0, f1, sl, 1);
    kstep(f1, f0, sl, 2);
    kstep(f0, f1, sl, 3);
    mm(f1, 0); SB();
    if (s < 9) LBARRIER();
    SB(); rd(f0, nsl, 0, 0); SB(); mm(f1, 1); SB(); rd(f0, nsl, 0, 1); SB(); mm(f1, 2); SB(); rd(f0, nsl, 0, 2); SB(); mm(f1, 3); SB();
  }
  TS(0, 2);
  const unsigned T = L + 131072;
  {
    f32x4 sq = sq0 + sq1;
    int e = lane & 3;
    float sv = e == 0 ? sq[0] : e == 1 ? sq[1] : e == 2 ? sq[2] : sq[3];
    ldsw<float>(T + hh * 512 + (wm * 64 + wn * 32 + r) * 4, sv);
    ldsw<float>(T + 1024 + hh * 512 + (wn * 64 + wm * 32 + r) * 4, cs0[0] + cs1[0]);
    if (wn == 1) {
#pragma unroll
      for (int i = 0; i < 2; ++i) {
        const int rb = wm * 64 + (i ^ 1) * 32;
        if (SWAP) {
          float v = wm ? acc[i][0][12] : acc[i][1][12];
          if (hh == 0) ldsw<float>(T + 2048 + (rb + r) * 4, v);
        } else if (r == 24) {
#pragma unroll
          for (int g = 0; g < 4; ++g) {
            f32x4 v;
#pragma unroll
            for (int e2 = 0; e2 < 4; ++e2) v[e2] = wm ? acc[i][0][4 * g + e2] : acc[i][1][4 * g + e2];
            ldsw<f32x4>(T + 2048 + (rb + 8 * g + 4 * hh) * 4, v);
          }
        }
      }
    }
  }
  LBARRIER();
  if (tid < 128) {
    float sum = ldsr<float>(T + 2048 + tid * 4);
    float sq = ldsr<float>(T + tid * 4) + ldsr<float>(T + 512 + tid * 4);
    float mean = sum * (1.f / D_);
    float var = fmaxf(sq * (1.f / D_) - mean * mean, 0.f);
    float rs = __builtin_amdgcn_rsqf(var + LN_EPS);
    if (SWAP) ldsw<f32x2>(T + 2560 + tid * 8, f32x2{rs, -mean});
    else { ldsw<float>(T + 2560 + tid * 4, rs); ldsw<float>(T + 3072 + tid * 4, -mean); }
  } else {
    int c = tid - 128;
    ldsw<float>(T + 3584 + c * 4, ldsr<float>(T + 1024 + c * 4) + ldsr<float>(T + 1536 + c * 4));
  }
  LBARRIER();
  const i32x4 ro = mk_srd(rec, 64 * REC_HEAD);
  const int tile0 = rt * 4 + wm * 2;
  if (SWAP) {
    const float qs = 0.22360679774997896f * 1.4426950408889634f;
    f32x2 ab[2];
#pragma unroll
    for (int i = 0; i < 2; ++i) ab[i] = ldsr<f32x2>(T + 2560 + (wm * 64 + (i ^ wn) * 32 + r) * 8);
    const unsigned vlo = (hh ^ wn) * REC + r * 16;
    const unsigned vhi = hh * REC_HEAD + 1024 + r * 8;
    const unsigned X = L + wave * 8192;
#pragma unroll
    for (int j = 0; j < 2; ++j) {
#pragma unroll
      for (int g = 0; g < 4; ++g) {
        const int P = wn * 8 + (j ^ wm) * 4 + g;
        if (P < 15) {
          const int hp = (P * 13) >> 6, t = P - hp * 5, hA = ct * 6 + 2 * hp, od = (t + 1) >> 2;
          const f32x4 cs = ldsr<f32x4>(T + 3584 + (P * 8 + 4 * hh) * 4);
          const float sc = hA < 32 ? qs : 1.f;
          u32x2 v[2];
#pragma unroll
          for (int i = 0; i < 2; ++i) {
            const f32x2 a = {ab[i].x * sc, ab[i].x * sc}, nm = {ab[i].y, ab[i].y};
            v[i].x = pk2(fma2(nm, sub2(cs, 0), sub2(acc[i][j], 4 * g)) * a);
            v[i].y = pk2(fma2(nm, sub2(cs, 2), sub2(acc[i][j], 4 * g + 2)) * a);
          }
          if (hA >= 64) {
#pragma unroll
            for (int i = 0; i < 2; ++i) ldsw<u32x2>(X + ((i ^ wn) * 32 + r) * 104 + (t * 8 + 4 * hh) * 2, v[i]);
          } else if (t == 2) {
#pragma unroll
            for (int i = 0; i < 2; ++i) st8(v[i], ro, vhi, hA * REC_HEAD + (tile0 + (i ^ wn)) * REC);
          } else {
            auto s0 = __builtin_amdgcn_permlane32_swap(v[0].x, v[1].x, false, false);
            auto s1 = __builtin_amdgcn_permlane32_swap(v[0].y, v[1].y, false, false);
            st16(u32x4{s0[0], s1[0], s0[1], s1[1]}, ro, vlo, (hA + od) * REC_HEAD + tile0 * REC + (t - 3 * od) * 512);
          }
        }
      }
    }
    if (ct == 10 && wn == 1) {
      const int g4 = lane >> 4, c16 = lane & 15;
      const unsigned ra = X + (16 * (g4 >> 1) + 4 * (g4 & 1) + ((lane >> 2) & 3)) * 104 + (lane & 3) * 8;
#pragma unroll
      for (int mb = 0; mb < 2; ++mb) {
#pragma unroll
        for (int c = 0; c < 3; ++c) {
          s16x4 lo = ldstr(ra + mb * 32 * 104 + c * 32), hi = ldstr(ra + mb * 32 * 104 + 8 * 104 + c * 32);
          const int col = c * 16 + c16, hd = col >= 20, d = col - 20 * hd;
          const unsigned vo = hd * REC_HEAD + (g4 >> 1) * REC_VS + (g4 & 1) * REC_VH + d * 16;
          const unsigned so = 32 * REC_HEAD + (tile0 + mb) * REC + REC_V;
          if (col < 40) {
            st16(__builtin_bit_cast(u32x4, cat8(lo, hi)), ro, vo, so);
          }
        }
      }
    }
  } else {
#pragma unroll
    for (int i = 0; i < 2; ++i) {
      const int rb = wm * 64 + (i ^ wn) * 32;
      f32x4 av[4], bv[4];
#pragma unroll
      for (int g = 0; g < 4; ++g) {
        av[g] = ldsr<f32x4>(T + 2560 + (rb + 8 * g + 4 * hh) * 4);
        bv[g] = ldsr<f32x4>(T + 3072 + (rb + 8 * g + 4 * hh) * 4);
      }
#pragma unroll
      for (int j = 0; j < 2; ++j) {
        const int lc = wn * 64 + (j ^ wm) * 32 + r;
        if (lc < 120) {
          const float c1 = ldsr<float>(T + 3584 + lc * 4);
          const f32x2 cs = {c1, c1};
          const int gc = ct * 120 + lc - 1280;
          const int hd = (gc * 205) >> 12, d = gc - hd * 20;
          u32x4 v[2];
#pragma unroll
          for (int g = 0; g < 4; ++g) {
            v[g >> 1][(g & 1) * 2] = pk2(fma2(sub2(bv[g], 0), cs, sub2(acc[i][j], 4 * g)) * sub2(av[g], 0));
            v[g >> 1][(g & 1) * 2 + 1] = pk2(fma2(sub2(bv[g], 2), cs, sub2(acc[i][j], 4 * g + 2)) * sub2(av[g], 2));
          }
          const unsigned vo = hd * REC_HEAD + hh * REC_VH + d * 16, so = 32 * REC_HEAD + (tile0 + (i ^ wn)) * REC + REC_V;
          st16(v[0], ro, vo, so); st16(v[1], ro, vo + REC_VS, so);
        }
      }
    }
  }
  TS(0, 3);
}
constexpr bool chk_div() {
  for (int i = 0; i < 640; ++i) if (((i * 205) >> 12) != i / 20) return false;
  return true;
}
static_assert(chk_div(), "magic division");
constexpr bool chk_div2() {
  for (int i = 0; i < 192; ++i) if (((i * 171) >> 11) != i / 12) return false;
  for (int i = 0; i < 80; ++i) if (((i * 205) >> 11) != i / 10) return false;
  return true;
}
static_assert(chk_div2(), "magic division");

__global__ __launch_bounds__(512, 2) void k1_qkv(const u16* __restrict__ x, const u16* __restrict__ w, char* __restrict__ ws) {
  __shared__ __attribute__((aligned(1024))) char smem[131072 + 4096];
  const unsigned L = (unsigned)(uintptr_t)(LDS3 char*)smem;
  const int b = blockIdx.x, xcd = b & 7, bi = b >> 3;
  const int rt = (xcd >> 1) * 4 + (bi & 3), ct = (xcd & 1) * 8 + (bi >> 2);
  const int lane = threadIdx.x & 63, wraw = __builtin_amdgcn_readfirstlane(threadIdx.x >> 6), wave = wraw - 4;
  TS(0, 0);
  auto producer = [&]() __attribute__((always_inline)) {
    const int pw = wraw;
    const i32x4 sx = mk_srd(x, S_ * D_ * 2), sw = mk_srd(w, D_ * 1920 * 2);
    unsigned vA[4], vB[4];
#pragma unroll
    for (int i = 0; i < 4; ++i) {
      int row = (pw * 4 + i) * 8 + (lane >> 3);
      vA[i] = row * 1280 + (((lane & 7) ^ ((row >> 1) & 7)) << 4);
      int k = (pw * 4 + i) * 4 + (lane >> 4);
      vB[i] = k * 3840 + (((lane & 15) ^ ((k & 3) << 2)) << 4);
    }
    const unsigned long long bmask = ~((1ull << 15) | (1ull << 27) | (1ull << 39) | (1ull << 51));
    const unsigned oA = rt * 128 * 1280, oB = ct * 240;
    auto issue = [&](int s) {
#pragma unroll
      for (int i = 0; i < 4; ++i) {
        dma16(L + (s & 3) * 32768 + (pw * 4 + i) * 1024, vA[i], sx, oA + s * 128);
        dma16_masked(L + (s & 3) * 32768 + 16384 + (pw * 4 + i) * 1024, vB[i], sw, oB + s * (64 * 3840), bmask);
      }
    };
    issue(0);
    ldsw<u32x4>(L + pw * 32768 + 16384 + lane * 256 + ((15 ^ ((lane & 3) << 2)) << 4), u32x4{0x3f80u, 0u, 0u, 0u});
    BARRIER();
    issue(1);
    WAIT_VL(8); BARRIER();
    issue(2); issue(3);
    for (int s = 0; s < 9; ++s) {
      if (s < 7) WAIT_V(16); else if (s == 7) WAIT_V(8); else WAIT_V(0);
      BARRIER();
      if (s < 6) issue(s + 4);
    }
  };
  if (wraw >= 4) {
  if (wave == 0) {
    unsigned long long pc; unsigned len, t;
    asm volatile("s_getpc_b64 %0\n.Lpf_here:\n\ts_mov_b32 %1, k_end-_Z6k1_qkvPKtS0_Pc\n\ts_mov_b32 %2, .Lpf_here-_Z6k1_qkvPKtS0_Pc" : "=s"(pc), "=s"(len), "=s"(t));
    i32x4 sc = mk_srd((const void*)(pc - t), len);
    asm volatile("buffer_load_dword %0, %1, %2, 0 offen\n\ts_waitcnt vmcnt(0)" : "=v"(t) : "v"((bi * 16 + (lane & 15)) * 128), "s"(sc));
  }
  if (ct <= 10) k1_consumer<true>(ws + WS_REC, L, rt, ct, wave);
  else k1_consumer<false>(ws + WS_REC, L, rt, ct, wave);
  return;
  }
  producer();
}

__device__ __forceinline__ void exp4(const f32x16& s, int g, unsigned* p) {
  float e0 = __builtin_amdgcn_exp2f(s[4 * g + 0]), e1 = __builtin_amdgcn_exp2f(s[4 * g + 1]);
  float e2 = __builtin_amdgcn_exp2f(s[4 * g + 2]), e3 = __builtin_amdgcn_exp2f(s[4 * g + 3]);
  p[0] = pk2(e0, e1); p[1] = pk2(e2, e3);
}
__device__ __forceinline__ bf16x8 px8(const unsigned* p) { u32x4 v = {p[0], p[1], p[2], p[3]}; return __builtin_bit_cast(bf16x8, v); }
__device__ __forceinline__ bf16x8 bx8(u32x4 v) { return __builtin_bit_cast(bf16x8, v); }
__device__ __forceinline__ float max16(const f32x16& s) {
  float m = fmaxf(fmaxf(fmaxf(s[0], s[1]), fmaxf(s[2], s[3])), fmaxf(fmaxf(s[4], s[5]), fmaxf(s[6], s[7])));
  float n = fmaxf(fmaxf(fmaxf(s[8], s[9]), fmaxf(s[10], s[11])), fmaxf(fmaxf(s[12], s[13]), fmaxf(s[14], s[15])));
  m = fmaxf(m, n);
  return fmaxf(m, __shfl_xor(m, 32));
}

#define K2_ASM \
  ".macro XE r\n v_exp_f32 v[\\r], v[\\r]\n.endm\n" \
  ".macro XC p, r\n v_cvt_pk_bf16_f32 v[\\p], v[\\r], v[\\r+1]\n.endm\n" \
  ".macro XM d, a, b, c\n v_mfma_f32_32x32x16_bf16 v[\\d:\\d+15], v[\\a:\\a+3], v[\\b:\\b+3], \\c\n.endm\n" \
  ".macro XPH s, n, q, op, oc, t, b\n" \
  " XE \\s\n XE \\s+1\n" \
  " .if \\t + \\b > 0\n XM \\op, 84, 68, v[\\op:\\op+15]\n .endif\n" \
  " .if \\b == 0\n" \
  "  .if \\t == 1\n s_waitcnt vmcnt(9)\n s_barrier\n .endif\n" \
  "  .if \\t == 4\n s_waitcnt vmcnt(0)\n s_barrier\n" \
  "   buffer_store_dwordx4 v[108:111], v118, %[sp], 0 offen sc1\n buffer_store_dwordx4 v[112:115], v119, %[sp], 0 offen sc1\n .endif\n" \
  "  .if \\t == 12\n v_lshlrev_b32 v116, 3, v107\n s_nop 0\n buffer_load_dword v117, v116, %[sp], %[pf] offen\n .endif\n" \
  "  s_mov_b64 exec, %[mv]\n ds_read_b128 v[80:83], v106 offset:\\t*2560\n ds_read_b128 v[84:87], v106 offset:\\t*2560+640\n s_mov_b64 exec, -1\n" \
  " .endif\n" \
  " XE \\s+2\n XE \\s+3\n XC 64, \\s\n XC 65, \\s+2\n XE \\s+4\n XE \\s+5\n" \
  " .if \\b == 1\n s_waitcnt lgkmcnt(0)\n .endif\n" \
  " .if \\t + \\b < 16\n XM \\n, 72, \\q, 0\n .endif\n" \
  " XE \\s+6\n XE \\s+7\n XC 66, \\s+4\n XC 67, \\s+6\n XE \\s+8\n" \
  " .if \\t + \\b < 16\n XM \\n, 76, \\q+4, v[\\n:\\n+15]\n .endif\n" \
  " .if (\\b == 0) && (\\t < 15)\n ds_read_b128 v[72:75], v104 offset:(\\t+1)*2560\n ds_read_b64 v[76:77], v105 offset:(\\t+1)*2560\n .endif\n" \
  " XE \\s+9\n XE \\s+10\n XE \\s+11\n XC 68, \\s+8\n XC 69, \\s+10\n XE \\s+12\n XE \\s+13\n" \
  " .if \\b == 0\n .if \\t < 15\n s_waitcnt lgkmcnt(2)\n .else\n s_waitcnt lgkmcnt(0)\n .endif\n .endif\n" \
  " .if \\t == 0\n XM \\oc, 80, 64, 0\n .else\n XM \\oc, 80, 64, v[\\oc:\\oc+15]\n .endif\n" \
  " XE \\s+14\n XE \\s+15\n XC 70, \\s+12\n XC 71, \\s+14\n" \
  ".endm\n" \
  ".macro XD\n s_add_u32 %[pc], %[pc], 4096\n s_mov_b32 m0, %[pc]\n s_nop 0\n buffer_load_dwordx4 v107, %[skv], %[pc] offen lds\n.endm\n" \
  ".macro XMAX d, s\n v_max3_f32 v[\\d], v[\\s], v[\\s+1], v[\\s+2]\n v_max3_f32 v[\\d+1], v[\\s+3], v[\\s+4], v[\\s+5]\n" \
  " v_max3_f32 v[\\d], v[\\d], v[\\s+6], v[\\s+7]\n v_max3_f32 v[\\d+1], v[\\d+1], v[\\s+8], v[\\s+9]\n" \
  " v_max3_f32 v[\\d], v[\\d], v[\\s+10], v[\\s+11]\n v_max3_f32 v[\\d+1], v[\\d+1], v[\\s+12], v[\\s+13]\n" \
  " v_max3_f32 v[\\d], v[\\d], v[\\s+14], v[\\s+15]\n v_max_f32 v[\\d], v[\\d], v[\\d+1]\n.endm\n" \
  "v_subrev_u32 v64, %[ko], v104\n v_subrev_u32 v65, %[ko], v105\n" \
  "ds_read_b128 v[72:75], v64\n ds_read_b64 v[76:77], v65\n" \
  "v_mov_b32 v78, 0x3f803f80\n v_mov_b32 v79, 0\n" \
  "XD\n XD\n s_waitcnt lgkmcnt(0)\n" \
  "XM 32, 72, 88, 0\n XM 48, 72, 96, 0\n XM 32, 76, 92, v[32:47]\n XM 48, 76, 100, v[48:63]\n" \
  "ds_read_b128 v[72:75], v104\n ds_read_b64 v[76:77], v105\n" \
  "XD\n XD\n XD\n XD\n XD\n" \
  "buffer_load_dwordx4 v[108:111], v116, %[sa], 0 offen\n" \
  "buffer_load_dwordx4 v[112:115], v117, %[sb], 0 offen\n" \
  "s_mov_b32 exec_hi, 0\n XMAX 64, 32\n XMAX 66, 48\n" \
  "v_cvt_pk_bf16_f32 v65, -v64, 0\n v_cvt_pk_bf16_f32 v67, -v66, 0\n v_lshlrev_b32 v65, 16, v65\n v_lshlrev_b32 v67, 16, v67\n" \
  "v_sub_f32 v65, -v64, v65\n v_sub_f32 v67, -v66, v67\n v_cvt_pk_bf16_f32 v94, -v64, v65\n v_cvt_pk_bf16_f32 v102, -v66, v67\n" \
  "s_mov_b32 exec_hi, -1\n s_waitcnt lgkmcnt(0)\n s_nop 0\n" \
  "XM 32, 72, 88, 0\n XM 32, 76, 92, v[32:47]\n s_nop 7\n s_nop 3\n" \
  ".irp t,0,1,2,3,4,5,6,7,8,9,10,11,12,13,14,15\n XPH 32, 48, 96, 16, 0, \\t, 0\n XPH 48, 32, 88, 0, 16, \\t, 1\n.endr\n" \
  "s_nop 1\n XM 16, 84, 68, v[16:31]\n s_waitcnt vmcnt(0)\n s_nop 7\n s_nop 3\n" \
  ".purgem XE\n.purgem XC\n.purgem XM\n.purgem XPH\n.purgem XD\n.purgem XMAX\n"

__device__ __forceinline__ void k2_tail(char* __restrict__ ws, unsigned L, const f32x16& o0, const f32x16& o1, u32x4 wA,
                                        int wave, int head, int qb, int mat, int rg, int cb) {
  int lane;
  asm volatile("v_mbcnt_lo_u32_b32 %0, -1, 0\n\tv_mbcnt_hi_u32_b32 %0, -1, %0" : "=v"(lane));
  const int b = blockIdx.x, qp = wave & 3, kq = wave >> 2, r = lane & 31, hh = lane >> 5;
  const char* rec = ws + WS_REC;
  const char* qrec = rec + (size_t)(head * 64 + qb * 8 + qp * 2) * REC;
  const unsigned vL = lane * 16, aK = hh * 512 + r * 16, aKh = 1024 + r * 8, aV = REC_V + hh * REC_VH + r * 16;
  const unsigned vc = r == 20 ? 0x3f803f80u : 0u;
  f32x4 cs;
  {
    const int i = lane & 15;
    const unsigned oh = (i & 1) ? 0x3f800000u : 0x3f80u;
    const u32x4 sel = {i >> 1 == 0 ? oh : 0u, i >> 1 == 1 ? oh : 0u, i >> 1 == 2 ? oh : 0u, i >> 1 == 3 ? oh : 0u};
    cs = MFMA16(bx8(sel), bx8(wA), f32x4{});
  }
  BARRIER();
  const unsigned CS = L + 73728;
  if (lane < 32) ldsw<f32x4>(CS + wave * 512 + (lane & 15) * 32 + (lane >> 4) * 16, cs);
#pragma unroll
  for (int g = 0; g < 3; ++g) {
    const unsigned a = L + qp * 18432 + g * 1024 + vL;
    if (kq != 0) ldsw<f32x4>(a + (kq - 1) * 3072, f32x4{o0[4 * g], o0[4 * g + 1], o0[4 * g + 2], o0[4 * g + 3]});
    if (kq != 1) ldsw<f32x4>(a + 9216 + (kq ? kq - 1 : 0) * 3072, f32x4{o1[4 * g], o1[4 * g + 1], o1[4 * g + 2], o1[4 * g + 3]});
  }
  LBARRIER();
  if (kq >= 2) {
    if (kq == 2 && qp < 2 && b < 240) {
      const int c = qp * 64 + lane;
      float s = 0.f;
#pragma unroll
      for (int w = 0; w < 16; ++w) s += ldsr<float>(CS + w * 512 + c * 4);
      const int col = cb * 128 + c, ct = col / 96;
      __hip_atomic_store((float*)(ws + WS_CS + ct * 8192 + (rg * 192 + col - ct * 96 + mat * 96) * 4), s, __ATOMIC_RELAXED, __HIP_MEMORY_SCOPE_AGENT);
    }
    return;
  }
  f32x4 o[3];
#pragma unroll
  for (int g = 0; g < 3; ++g) {
    const unsigned a = L + qp * 18432 + kq * 9216 + g * 1024 + vL;
#pragma unroll
    for (int e = 0; e < 4; ++e) o[g][e] = kq ? o1[4 * g + e] : o0[4 * g + e];
    o[g] += ldsr<f32x4>(a) + (ldsr<f32x4>(a + 3072) + ldsr<f32x4>(a + 6144));
  }
  float l = __shfl(o[2][0], r + 32);
  bool ok = l <= 0x1p100f && l >= 0x1p-100f;
#pragma unroll
  for (int i = 0; i < 12; ++i) ok = ok && (__builtin_fabsf(o[i >> 2][i & 3]) <= 0x1p126f);
  if (__builtin_amdgcn_ballot_w64(!ok) != 0ull) {
    const char* kv = rec + (size_t)(32 + head) * REC_HEAD;
    const bf16x8 ql = *(const bf16x8*)(qrec + kq * REC + hh * 512 + r * 16);
    const u32x2 t = *(const u32x2*)(qrec + kq * REC + 1024 + r * 8);
    const bf16x8 qh = bx8(hh ? u32x4{} : u32x4{t.x, t.y, 0u, 0u});
    float m = -__builtin_inff();
    f32x16 oo = {};
    for (int t = 0; t < 64; ++t) {
      const char* q = kv + t * REC;
      const u32x2 kh = *(const u32x2*)(q + aKh);
      f32x16 s = {};
      s = MFMA32(*(const bf16x8*)(q + aK), ql, s);
      s = MFMA32(bx8(u32x4{kh.x, kh.y, 0u, 0u}), qh, s);
      float mn = fmaxf(m, max16(s));
      float c = __builtin_amdgcn_exp2f(m - mn);
      m = mn;
#pragma unroll
      for (int i = 0; i < 16; ++i) { s[i] -= mn; oo[i] *= c; }
      unsigned pp[8];
#pragma unroll
      for (int g = 0; g < 4; ++g) exp4(s, g, pp + 2 * g);
      u32x4 a0 = {vc, vc, vc, vc}, a1 = a0;
      if (r < 20) { a0 = *(const u32x4*)(q + aV); a1 = *(const u32x4*)(q + aV + REC_VS); }
      oo = MFMA32(bx8(a0), px8(pp), oo);
      oo = MFMA32(bx8(a1), px8(pp + 4), oo);
    }
#pragma unroll
    for (int i = 0; i < 12; ++i) o[i >> 2][i & 3] = oo[i];
    l = __shfl(oo[8], r + 32);
  }
  const float inv = 1.f / l;
  const int row = qb * 256 + (qp * 2 + kq) * 32 + r;
  const i32x4 so = mk_srd(ws + WS_O, S_ * D_ * 2);
#pragma unroll
  for (int g = 0; g < 3; ++g) {
    if (g < 2 || hh == 0) {
      const int col = head * 20 + 8 * g + 4 * hh;
      u32x2 v = {pk2(o[g][0] * inv, o[g][1] * inv), pk2(o[g][2] * inv, o[g][3] * inv)};
      st8(v, so, (col >> 3) * 32768 + row * 16 + (col & 7) * 2, 0);
    }
  }
  TS(1, 3);
}

__global__ __launch_bounds__(1024, 4) void k2_attn(char* __restrict__ ws, const u16* __restrict__ wo, const u16* __restrict__ wg,
                                                   const u16* __restrict__ wu, const u16* __restrict__ wd) {
  __shared__ __attribute__((aligned(1024))) char smem[REC_HEAD];
  const unsigned L = (unsigned)(uintptr_t)(LDS3 char*)smem;
  const int tid = threadIdx.x, lane = tid & 63, wave = __builtin_amdgcn_readfirstlane(tid >> 6);
  const int b = blockIdx.x, xcd = b & 7, bi = b >> 3;
  const int head = xcd * 4 + (bi & 3), qb = bi >> 2;
  const int qp = wave & 3, kq = wave >> 2;
  const int r = lane & 31, hh = lane >> 5;
  const char* rec = ws + WS_REC;
  const i32x4 skv = mk_srd(rec + (size_t)(32 + head) * REC_HEAD - L, REC_HEAD + L);
  const unsigned vL = lane * 16, pc = L + kq * 40960 + qp * 1024;
  TS(1, 0);
  u32x4 ql0, ql1, qh0, qh1;
  {
    const i32x4 sq = mk_srd(rec, 32 * REC_HEAD);
    asm volatile("v_mov_b64 v[92:93], 0\n\tv_mov_b64 v[94:95], 0\n\tv_mov_b64 v[100:101], 0\n\tv_mov_b64 v[102:103], 0\n\t"
                 "buffer_load_dwordx4 v[88:91], %4, %6, %7 offen\n\tbuffer_load_dwordx4 v[96:99], %4, %6, %7 offen offset:2560\n\t"
                 "s_mov_b32 exec_hi, 0\n\t"
                 "buffer_load_dwordx2 v[92:93], %5, %6, %7 offen offset:1024\n\tbuffer_load_dwordx2 v[100:101], %5, %6, %7 offen offset:3584\n\t"
                 "s_mov_b32 exec_hi, -1"
                 : "=&{v[88:91]}"(ql0), "=&{v[92:95]}"(qh0), "=&{v[96:99]}"(ql1), "=&{v[100:103]}"(qh1)
                 : "v"(hh * 512 + r * 16), "v"(r * 8), "s"(sq), "s"((head * 64 + qb * 8 + qp * 2) * REC));
  }
  dma16(pc, vL, skv, pc);
  BARRIER();
  dma16(pc + 4096, vL, skv, pc + 4096);
  dma16(pc + 8192, vL, skv, pc + 8192);
  const int mat = b >= 120, rg = (b - mat * 120) / 12, cb = b - mat * 120 - rg * 12, gw = b * 16 + wave;
  unsigned srcA, dstA, srcB, dstB;
  {
    const int pr = wave * 4 + (lane >> 4), cc = cb * 16 + (lane & 15), ct = (cc * 171) >> 11, c12 = cc - ct * 12;
    srcA = (rg * 64 + pr) * 3072 + cc * 16;
    dstA = (ct * 10 + rg) * 24576 + mat * 12288 + pr * 192 + (c12 ^ (mat * 2)) * 16;
    if (b >= 240) srcA = dstA = 0x80000000u;
    const int id = (gw < 800 ? gw : gw - 800) * 64 + lane, R = id / 80, c80 = id - R * 80, c8 = (c80 * 205) >> 11, k = R & 127;
    srcB = id * 16;
    dstB = (gw < 800 ? PK_O + c8 * (5 * 20480) : PK_D + c8 * (12 * 20480)) + (R >> 7) * 20480 + (k ^ ((k >> 1) & 4)) * 160 + (c80 - c8 * 10) * 16;
    if (gw >= 2720) srcB = dstB = 0x80000000u;
  }
  const i32x4 sa = mk_srd(mat ? wu : wg, D_ * F_ * 2), sb = mk_srd(gw < 800 ? wo : wd, gw < 800 ? D_ * D_ * 2 : D_ * F_ * 2);
  const i32x4 sp = mk_srd(ws + WS_PK, PK_SIZE);
  unsigned aK = L + kq * 40960 + hh * 512 + r * 16, aKh = L + kq * 40960 + 1024 + r * 8, aV = L + kq * 40960 + REC_V + hh * REC_VH + r * 16;
  const unsigned vc = r == 20 ? 0x3f803f80u : 0u;
  u32x4 v0 = {vc, vc, vc, vc}, v1 = v0;
  asm volatile("s_waitcnt vmcnt(2)\n\ts_barrier"
               : "+{v[88:91]}"(ql0), "+{v[92:95]}"(qh0), "+{v[96:99]}"(ql1), "+{v[100:103]}"(qh1), "+{v116}"(srcA), "+{v118}"(dstA),
                 "+{v117}"(srcB), "+{v119}"(dstB), "+{v[80:83]}"(v0), "+{v[84:87]}"(v1), "+{v104}"(aK), "+{v105}"(aKh), "+{v106}"(aV)
               :: "memory");
  TS(1, 1);
  f32x16 o0, o1;
  u32x4 wA;
  unsigned pcs = pc + 8192;
  const int wi = bi * 16 + wave;
  const unsigned pf = wi < 240 ? (xcd & 1) * (8 * 10 * 24576) + wi * 8192 : PK_O + (wi < 340 ? wi - 240 : 0) * 8192;
  asm volatile(K2_ASM
               : "=&{v[0:15]}"(o0), "=&{v[16:31]}"(o1), "+{v[80:83]}"(v0), "+{v[84:87]}"(v1), "=&{v[108:111]}"(wA),
                 "+{v[92:95]}"(qh0), "+{v[100:103]}"(qh1), [pc] "+s"(pcs), "+{v116}"(srcA), "+{v117}"(srcB)
               : "{v[88:91]}"(ql0), "{v[96:99]}"(ql1), "{v104}"(aK), "{v105}"(aKh), "{v106}"(aV), "{v107}"(vL),
                 "{v118}"(dstA), "{v119}"(dstB), [pf] "s"(pf),
                 [mv] "s"(0x000fffff000fffffull), [sa] "s"(sa), [sb] "s"(sb), [sp] "s"(sp), [skv] "s"(skv), [ko] "s"(kq * 40960)
               : "memory", "scc", "v32", "v33", "v34", "v35", "v36", "v37", "v38", "v39", "v40", "v41", "v42", "v43", "v44", "v45", "v46", "v47",
                 "v48", "v49", "v50", "v51", "v52", "v53", "v54", "v55", "v56", "v57", "v58", "v59", "v60", "v61", "v62", "v63",
                 "v64", "v65", "v66", "v67", "v68", "v69", "v70", "v71", "v72", "v73", "v74", "v75", "v76", "v77", "v78", "v79",
                 "v112", "v113", "v114", "v115");
  TS(1, 2);
  k2_tail(ws, L, o0, o1, wA, wave, head, qb, mat, rg, cb);
}

struct Frag3 { bf16x8 a[2]; s16x4 b[5][2]; };
constexpr int K3_STAGE = 16384 + 20480;

template <int KD, bool FIRST>
__device__ __forceinline__ void proj_body(const char* __restrict__ A, const char* __restrict__ W, const char* __restrict__ R,
                                          char* __restrict__ C, char* __restrict__ stats, unsigned L) {
  constexpr int NS = KD / 128;
  const int tid = threadIdx.x, lane = tid & 63, wraw = __builtin_amdgcn_readfirstlane(tid >> 6), wave = wraw - 4;
  const int b = blockIdx.x, xcd = b & 7, bi = b >> 3;
  const int rt = xcd * 4 + (bi & 3), ct = bi >> 2;
  const int m0 = rt * 64, n0 = ct * 80;
  const unsigned RS = L + 4 * K3_STAGE;
  TS(FIRST ? 2 : 4, 0);
  auto producer = [&]() __attribute__((always_inline)) {
    const int pw = wraw;
    const i32x4 sa = mk_srd(A, S_ * KD * 2), sw = mk_srd(W, KD * D_ * 2), sr = mk_srd(R, S_ * D_ * 2);
    const unsigned vL = lane * 16;
    auto issue = [&](int s) {
#pragma unroll
      for (int i = 0; i < 5; ++i) {
        if (i < 4) dma16(L + (s & 3) * K3_STAGE + (pw * 4 + i) * 1024, vL, sa, (s * 16 + pw * 4 + i) * 32768 + m0 * 16);
        dma16(L + (s & 3) * K3_STAGE + 16384 + (pw * 5 + i) * 1024, vL, sw, (ct * NS + s) * 20480 + (pw * 5 + i) * 1024);
      }
    };
    issue(0);
    BARRIER();
    issue(1);
    WAIT_V(9); BARRIER();
    issue(2);
#pragma unroll
    for (int i = 0; i < 3; ++i) {
      int p = pw * 3 + i; p = p > 9 ? 9 : p;
      if (FIRST) { int idx = p * 64 + lane, row = idx / 10, c = idx - row * 10; dma16(RS + p * 1024, row * 1280 + c * 16, sr, m0 * 1280 + n0 * 2); }
      else dma16(RS + p * 1024, vL, sr, (ct * 10 + p) * 32768 + m0 * 16);
    }
    issue(3);
    for (int s = 0; s < NS - 1; ++s) {
      if (s == 0) WAIT_V(21); else if (s < NS - 3) WAIT_V(18); else if (s == NS - 3) WAIT_V(9); else WAIT_V(0);
      BARRIER();
      if (s < NS - 4) issue(s + 4);
    }
  };
  if (wraw >= 4) {
  const int wm = wave & 1, wk = wave >> 1, g = lane >> 4;
  const unsigned aA = L + (wk * 8 + g) * 1024 + (wm * 32 + (lane & 15)) * 16;
  unsigned aB[2];
#pragma unroll
  for (int t = 0; t < 2; ++t) aB[t] = L + 16384 + (wk * 64 + 8 * g + ((4 * t) ^ (4 * (g & 1))) + ((lane >> 2) & 3)) * 160 + (lane & 3) * 8;
  auto rd = [&](Frag3& f, unsigned sl, int ks, int t) {
    if (t == 0) f.a[0] = ldsr<bf16x8>(aA + sl + ks * 4096);
    else if (t == 3) f.a[1] = ldsr<bf16x8>(aA + sl + ks * 4096 + 256);
    else { const int u = t < 3 ? t - 1 : t - 2; f.b[u >> 1][u & 1] = ldstr(aB[u & 1] + sl + ks * 5120 + (u >> 1) * 32); }
  };
  f32x4 acc[2][5] = {};
  auto mm = [&](const Frag3& f, int t) { acc[t & 1][t >> 1] = MFMA16(cat8(f.b[t >> 1][0], f.b[t >> 1][1]), f.a[t & 1], acc[t & 1][t >> 1]); };
  auto body = [&](const Frag3& f, Frag3& n, unsigned sl, int ks) {
#pragma unroll
    for (int t = 1; t < 10; ++t) {
      SB();
      if (t <= 3) { rd(n, sl, ks, 2 * t - 2); rd(n, sl, ks, 2 * t - 1); } else rd(n, sl, ks, t + 2);
      SB(); mm(f, t);
    }
    SB();
  };
  Frag3 f0, f1;
  BARRIER(); BARRIER();
  TS(FIRST ? 2 : 4, 1);
#pragma unroll
  for (int t = 0; t < 12; ++t) rd(f0, 0, 0, t);
  SB();
  for (int s = 0; s < NS; ++s) {
    const unsigned sl = (s & 3) * K3_STAGE, nsl = ((s + 1) & 3) * K3_STAGE;
    mm(f0, 0); body(f0, f1, sl, 1);
    mm(f1, 0); SB();
    if (s < NS - 1) LBARRIER();
    body(f1, f0, nsl, 0);
  }
  TS(FIRST ? 2 : 4, 2);
  const unsigned X = L + ((NS + 1) & 3) * K3_STAGE;
#pragma unroll
  for (int j = 0; j < 5; ++j) ldsw<f32x4>(X + (wave * 5 + j) * 1024 + lane * 16, wk ? acc[0][j] : acc[1][j]);
  LBARRIER();
  const int lr = wm * 32 + wk * 16 + (lane & 15);
  const i32x4 rc = mk_srd(C, S_ * D_ * 2);
  const unsigned vo = FIRST ? (g >> 1) * 32768 + lr * 16 + (g & 1) * 8 : lr * 1280 + g * 8;
  f32x2 sum = {}, sq = {};
#pragma unroll
  for (int j = 0; j < 5; ++j) {
    f32x4 v = (wk ? acc[1][j] : acc[0][j]) + ldsr<f32x4>(X + ((wave ^ 2) * 5 + j) * 1024 + lane * 16);
    u32x2 rr = FIRST ? ldsr<u32x2>(RS + lr * 160 + j * 32 + g * 8) : ldsr<u32x2>(RS + (2 * j + (g >> 1)) * 1024 + lr * 16 + (g & 1) * 8);
    u32x2 o = {pk2(v[0] + bflo(rr.x), v[1] + bfhi(rr.x)), pk2(v[2] + bflo(rr.y), v[3] + bfhi(rr.y))};
    if (FIRST) {
      st8(o, rc, vo, (ct * 10 + 2 * j) * 32768 + m0 * 16);
      f32x2 h0 = {bflo(o.x), bfhi(o.x)}, h1 = {bflo(o.y), bfhi(o.y)};
      sum += h0 + h1;
      sq = fma2(h0, h0, fma2(h1, h1, sq));
    } else {
      st8(o, rc, vo, m0 * 1280 + (n0 + 16 * j) * 2);
    }
  }
  if (FIRST) {
    float s1 = sum.x + sum.y, s2 = sq.x + sq.y;
    s1 += __shfl_xor(s1, 16); s2 += __shfl_xor(s2, 16);
    s1 += __shfl_xor(s1, 32); s2 += __shfl_xor(s2, 32);
    if (lane < 16) st8(u32x2{__float_as_uint(s1), __float_as_uint(s2)}, mk_srd(stats, S_ * 64), lr * 64, m0 * 64 + ct * 8);
  }
  TS(FIRST ? 2 : 4, 3);
  return;
  }
  producer();
}

__global__ __launch_bounds__(512, 2) void k3_oproj(const u16* __restrict__ x, char* __restrict__ ws) {
  __shared__ __attribute__((aligned(1024))) char smem[4 * K3_STAGE + 10240];
  proj_body<D_, true>(ws + WS_O, ws + WS_PK + PK_O, (const char*)x, ws + WS_H1, ws + WS_ST, (unsigned)(uintptr_t)(LDS3 char*)smem);
}
__global__ __launch_bounds__(512, 2) void k5_down(char* __restrict__ ws, u16* __restrict__ out) {
  __shared__ __attribute__((aligned(1024))) char smem[4 * K3_STAGE + 10240];
  proj_body<F_, false>(ws + WS_HH, ws + WS_PK + PK_D, ws + WS_H1, (char*)out, nullptr, (unsigned)(uintptr_t)(LDS3 char*)smem);
}

struct Frag4 { bf16x8 a[2]; s16x4 b[3][2]; };
constexpr int K4_STAGE = 16384 + 24576;

__global__ __launch_bounds__(512, 2) void k4_gateup(char* __restrict__ ws) {
  __shared__ __attribute__((aligned(1024))) char smem[4 * K4_STAGE];
  const unsigned L = (unsigned)(uintptr_t)(LDS3 char*)smem;
  const int tid = threadIdx.x, lane = tid & 63, wraw = __builtin_amdgcn_readfirstlane(tid >> 6), wave = wraw - 4;
  const int b = blockIdx.x, xcd = b & 7, bi = b >> 3;
  const int rt = (xcd >> 1) * 4 + (bi & 3), ct = (xcd & 1) * 8 + (bi >> 2);
  const int m0 = rt * 128;
  const unsigned T = L + 2 * K4_STAGE;
  TS(3, 0);
  auto producer = [&]() __attribute__((always_inline)) {
    const int pw = wraw;
    const i32x4 sa = mk_srd(ws + WS_H1, S_ * D_ * 2), sw = mk_srd(ws + WS_PK + PK_G, PK_O);
    const i32x4 sst = mk_srd(ws + WS_ST, S_ * 64 + 16 * 8192);
    const i32x4 sd = mk_srd(ws + WS_PK + PK_D, PK_SIZE - PK_D);
    const unsigned vL = lane * 16;
    auto issue = [&](int s) {
      const unsigned sl = L + (s & 3) * K4_STAGE;
#pragma unroll
      for (int i = 0; i < 6; ++i) {
        const int p = pw * 4 + i, q = pw * 6 + i;
        if (i < 4) K4A(sl + p * 1024, vL, sa, (s * 8 + (p >> 1)) * 32768 + (m0 + (p & 1) * 64) * 16);
        K4B(sl + 16384 + q * 1024, vL, sw, (ct * 10 + s) * 24576 + q * 1024);
      }
    };
    issue(0);
    BARRIER();
    issue(1);
    WAIT_V(10); BARRIER();
    issue(2); issue(3);
#pragma unroll
    for (int s = 0; s < 9; ++s) {
      if (s == 0) WAIT_V(20); else if (s == 1) WAIT_V(21); else if (s == 2) WAIT_V(22); else if (s < 7) WAIT_V(23);
      else if (s == 7) WAIT_V(16); else WAIT_V(0);
      BARRIER();
      if (s < 6) {
        issue(s + 4);
        const int n = s * 4 + pw;
        unsigned t;
        asm volatile("buffer_load_dword %0, %1, %2, %3 offen" : "=v"(t) : "v"(lane * (PFD * 2)), "s"(sd), "s"((bi * 3 + (n >> 3)) * 20480 + (n & 7) * (PFD * 128)));
      }
      if (s == 6) {
        dma16(T + pw * 2048, vL, sst, m0 * 64 + pw * 2048);
        dma16(T + pw * 2048 + 1024, vL, sst, m0 * 64 + pw * 2048 + 1024);
        dma16(T + 8192 + pw * 2048, vL, sst, S_ * 64 + ct * 8192 + pw * 2048);
        dma16(T + 8192 + pw * 2048 + 1024, vL, sst, S_ * 64 + ct * 8192 + pw * 2048 + 1024);
      }
    }
    const int pt = tid;
    if (pt < 128) {
      f32x4 t0 = ldsr<f32x4>(T + pt * 64), t1 = ldsr<f32x4>(T + pt * 64 + 16), t2 = ldsr<f32x4>(T + pt * 64 + 32), t3 = ldsr<f32x4>(T + pt * 64 + 48);
      f32x4 t = (t0 + t1) + (t2 + t3);
      float mean = (t[0] + t[2]) * (1.f / D_);
      float var = fmaxf((t[1] + t[3]) * (1.f / D_) - mean * mean, 0.f);
      ldsw<f32x2>(T + 16384 + pt * 8, f32x2{__builtin_amdgcn_rsqf(var + LN_EPS), -mean});
    } else if (pt < 128 + 96) {
      const unsigned a = T + 8192 + (pt - 128) * 8;
      f32x2 c = {};
#pragma unroll
      for (int k = 0; k < 10; ++k) c += ldsr<f32x2>(a + k * 768);
      ldsw<f32x2>(T + 17408 + (pt - 128) * 8, c);
    }
    LBARRIER();
  };
  if (wraw >= 4) {
  const int wm = wave >> 1, wn = wave & 1, r = lane & 31, hh = lane >> 5;
  const unsigned aA = L + hh * 2048 + (wm * 64 + r) * 16;
  unsigned aB[3];
  {
    const int q = (lane >> 2) & 3, p = lane & 3, up = (lane >> 4) & 1;
#pragma unroll
    for (int j = 0; j < 3; ++j) aB[j] = L + 16384 + up * 12288 + (8 * hh + q) * 192 + (((wn * 3 + j) ^ up) * 32) + p * 8;
  }
  auto rd = [&](Frag4& f, unsigned sl, int ks, int t) {
    if (t == 0) { f.a[0] = ldsr<bf16x8>(aA + sl + ks * 4096); f.b[0][0] = ldstr(aB[0] + sl + ks * 3072); }
    if (t == 1) { f.b[0][1] = ldstr(aB[0] + sl + ks * 3072 + 768); f.a[1] = ldsr<bf16x8>(aA + sl + ks * 4096 + 512); }
    if (t >= 2) { f.b[t - 1][0] = ldstr(aB[t - 1] + sl + ks * 3072); f.b[t - 1][1] = ldstr(aB[t - 1] + sl + ks * 3072 + 768); }
  };
  f32x16 acc[2][3] = {};
  auto mm = [&](const Frag4& f, int t) { acc[t & 1][t >> 1] = MFMA32(cat8(f.b[t >> 1][0], f.b[t >> 1][1]), f.a[t & 1], acc[t & 1][t >> 1]); };
  auto body = [&](const Frag4& f, Frag4& n, unsigned sl, int ks) {
#pragma unroll
    for (int t = 1; t < 6; ++t) {
      SB();
      if (t <= 4) rd(n, sl, ks, t - 1);
      SB(); mm(f, t);
    }
    SB();
  };
  Frag4 f0, f1;
  BARRIER(); BARRIER();
  TS(3, 1);
#pragma unroll
  for (int t = 0; t < 4; ++t) rd(f0, 0, 0, t);
  SB();
  for (int s = 0; s < 10; ++s) {
    const unsigned sl = (s & 3) * K4_STAGE, nsl = ((s + 1) & 3) * K4_STAGE;
    mm(f0, 0); body(f0, f1, sl, 1);
    mm(f1, 0); body(f1, f0, sl, 2);
    mm(f0, 0); body(f0, f1, sl, 3);
    mm(f1, 0); SB();
    if (s < 9) LBARRIER();
    body(f1, f0, nsl, 0);
  }
  BARRIER();
  TS(3, 2);
  const float k1 = -2.f * 0.7978845608028654f * 1.4426950408889634f, k2 = k1 * 0.044715f;
  const i32x4 ro = mk_srd(ws + WS_HH, S_ * F_ * 2);
  const unsigned vo = hh * 32768 + r * 16;
#pragma unroll
  for (int j = 0; j < 3; ++j) {
    const int fc = (wn * 3 + j) * 16 + 4 * hh;
    f32x4 cg[2], cu[2];
#pragma unroll
    for (int g = 0; g < 2; ++g) { cg[g] = ldsr<f32x4>(T + 17408 + (fc + 8 * g) * 4); cu[g] = ldsr<f32x4>(T + 17408 + 384 + (fc + 8 * g) * 4); }
#pragma unroll
    for (int i = 0; i < 2; ++i) {
      const f32x2 ab = ldsr<f32x2>(T + 16384 + (wm * 64 + i * 32 + r) * 8);
      const f32x2 a = {ab.x, ab.x}, nm = {ab.y, ab.y};
      unsigned pk[4];
#pragma unroll
      for (int e = 0; e < 4; ++e) {
        const f32x2 gt = fma2(nm, sub2(cg[e >> 1], (e & 1) * 2), sub2(acc[i][j], 2 * e)) * a;
        const f32x2 up = fma2(nm, sub2(cu[e >> 1], (e & 1) * 2), sub2(acc[i][j], 8 + 2 * e)) * a;
        const f32x2 z = gt * fma2(gt * gt, f32x2{k2, k2}, f32x2{k1, k1});
        const f32x2 dn = f32x2{__builtin_amdgcn_exp2f(z.x), __builtin_amdgcn_exp2f(z.y)} + f32x2{1.f, 1.f};
        const f32x2 sg2 = {__builtin_amdgcn_rcpf(dn.x), __builtin_amdgcn_rcpf(dn.y)};
        pk[e] = pk2(gt * up * sg2);
      }
      auto s0 = __builtin_amdgcn_permlane32_swap(pk[0], pk[2], false, false);
      auto s1 = __builtin_amdgcn_permlane32_swap(pk[1], pk[3], false, false);
      st16(u32x4{s0[0], s1[0], s0[1], s1[1]}, ro, vo, (ct * 12 + (wn * 3 + j) * 2) * 32768 + (m0 + wm * 64 + i * 32) * 16);
    }
  }
  TS(3, 3);
  return;
  }
  producer();
}

extern "C" __global__ void k_end() {}

extern "C" void kernel_launch(void* const* d_in, const int* in_sizes, int n_in, void* d_out, int out_size, void* d_ws,
                              size_t ws_size, hipStream_t stream) {
  const u16* x = (const u16*)d_in[0];
  const u16* wqkv = (const u16*)d_in[1];
  const u16* wo = (const u16*)d_in[2];
  const u16* wg = (const u16*)d_in[4];
  const u16* wu = (const u16*)d_in[5];
  const u16* wd = (const u16*)d_in[6];
  char* ws = (char*)d_ws;
  k1_qkv<<<256, 512, 0, stream>>>(x, wqkv, ws);
  k2_attn<<<256, 1024, 0, stream>>>(ws, wo, wg, wu, wd);
  k3_oproj<<<256, 512, 0, stream>>>(x, ws);
  k4_gateup<<<256, 512, 0, stream>>>(ws);
  k5_down<<<256, 512, 0, stream>>>(ws, (u16*)d_out);
}
```

```cpp
#include <hip/hip_runtime.h>
#include <stdint.h>

typedef unsigned short u16;
typedef __bf16 bf16x8 __attribute__((ext_vector_type(8)));
typedef __bf16 bf16x2 __attribute__((ext_vector_type(2)));
typedef short s16x4 __attribute__((ext_vector_type(4)));
typedef short s16x8 __attribute__((ext_vector_type(8)));
typedef float f32x2 __attribute__((ext_vector_type(2)));
typedef float f32x4 __attribute__((ext_vector_type(4)));
typedef float f32x16 __attribute__((ext_vector_type(16)));
typedef int i32x4 __attribute__((ext_vector_type(4)));
typedef unsigned u32x2 __attribute__((ext_vector_type(2)));
typedef unsigned u32x4 __attribute__((ext_vector_type(4)));
#define LDS3 __attribute__((address_space(3)))

constexpr int S_ = 2048, D_ = 640, F_ = 1536;
constexpr int REC = 2560, REC_V = 1280, REC_VS = 640, REC_VH = 320;
constexpr int REC_HEAD = 64 * REC;
constexpr size_t WS_REC = 0;
constexpr size_t WS_O = WS_REC + (size_t)64 * REC_HEAD;
constexpr size_t WS_H1 = WS_O + (size_t)S_ * D_ * 2;
constexpr size_t WS_HH = WS_H1 + (size_t)S_ * D_ * 2;
constexpr size_t WS_ST = WS_HH + (size_t)S_ * F_ * 2;
constexpr size_t WS_CS = WS_ST + (size_t)S_ * 64;
constexpr size_t WS_PK = WS_CS + 16 * 8192;
constexpr int PK_G = 0, PK_O = 16 * 10 * 24576, PK_D = PK_O + 8 * 5 * 20480, PK_SIZE = PK_D + 8 * 12 * 20480;
constexpr float LN_EPS = 1e-5f;

#define TS(k, i)
#define STPOL " sc1"
#define SB() __builtin_amdgcn_sched_barrier(0)
#define WAIT_V(n) asm volatile("s_waitcnt vmcnt(%0)" ::"n"(n) : "memory")
#define WAIT_VL(n) asm volatile("s_waitcnt vmcnt(%0) lgkmcnt(0)" ::"n"(n) : "memory")
#define BARRIER() asm volatile("s_barrier" ::: "memory")
#define LBARRIER() asm volatile("s_waitcnt lgkmcnt(0)\n\ts_barrier" ::: "memory")

__device__ __forceinline__ i32x4 mk_srd(const void* p, unsigned bytes) {
  unsigned long long a = (unsigned long long)p;
  i32x4 r;
  r.x = (int)(unsigned)a; r.y = (int)((unsigned)(a >> 32) & 0xffffu); r.z = (int)bytes; r.w = 0x00020000;
  return r;
}
__device__ __forceinline__ void st16(u32x4 v, i32x4 srd, unsigned voff, unsigned soff) {
  asm volatile("buffer_store_dwordx4 %0, %1, %2, %3 offen" STPOL "\n\ts_nop 1" ::"v"(v), "v"(voff), "s"(srd), "s"(soff));
}
__device__ __forceinline__ void st8(u32x2 v, i32x4 srd, unsigned voff, unsigned soff) {
  asm volatile("buffer_store_dwordx2 %0, %1, %2, %3 offen" STPOL ::"v"(v), "v"(voff), "s"(srd), "s"(soff));
}
__device__ __forceinline__ void dma16(unsigned lds, unsigned voff, i32x4 srd, unsigned soff) {
  asm volatile("s_mov_b32 m0, %0\n\ts_nop 0\n\tbuffer_load_dwordx4 %1, %2, %3 offen lds"
               ::"s"(lds), "v"(voff), "s"(srd), "s"(soff) : "memory");
}
__device__ __forceinline__ void dma16nt(unsigned lds, unsigned voff, i32x4 srd, unsigned soff) {
  asm volatile("s_mov_b32 m0, %0\n\ts_nop 0\n\tbuffer_load_dwordx4 %1, %2, %3 offen nt lds"
               ::"s"(lds), "v"(voff), "s"(srd), "s"(soff) : "memory");
}
#define PFD 12
#define K4A dma16
#define K4B dma16
__device__ __forceinline__ void dma16_masked(unsigned lds, unsigned voff, i32x4 srd, unsigned soff, unsigned long long mask) {
  asm volatile("s_mov_b32 m0, %0\n\ts_mov_b64 exec, %4\n\tbuffer_load_dwordx4 %1, %2, %3 offen lds\n\ts_mov_b64 exec, -1"
               ::"s"(lds), "v"(voff), "s"(srd), "s"(soff), "s"(mask) : "memory");
}
template <typename T> __device__ __forceinline__ T ldsr(unsigned a) { return *(LDS3 const T*)(uintptr_t)a; }
template <typename T> __device__ __forceinline__ void ldsw(unsigned a, T v) { *(LDS3 T*)(uintptr_t)a = v; }
__device__ __forceinline__ s16x4 ldstr(unsigned a) { return __builtin_amdgcn_ds_read_tr16_b64_v4i16((LDS3 s16x4*)(uintptr_t)a); }
__device__ __forceinline__ bf16x8 cat8(s16x4 lo, s16x4 hi) {
  s16x8 r = __builtin_shufflevector(lo, hi, 0, 1, 2, 3, 4, 5, 6, 7);
  return __builtin_bit_cast(bf16x8, r);
}
__device__ __forceinline__ unsigned pk2(f32x2 v) { return __builtin_bit_cast(unsigned, __builtin_convertvector(v, bf16x2)); }
__device__ __forceinline__ unsigned pk2(float a, float b) { return pk2(f32x2{a, b}); }
__device__ __forceinline__ float bflo(unsigned v) { return __uint_as_float(v << 16); }
__device__ __forceinline__ float bfhi(unsigned v) { return __uint_as_float(v & 0xffff0000u); }
__device__ __forceinline__ s16x4 lo4(bf16x8 v) { s16x8 t = __builtin_bit_cast(s16x8, v); return __builtin_shufflevector(t, t, 0, 1, 2, 3); }
__device__ __forceinline__ s16x4 hi4(bf16x8 v) { s16x8 t = __builtin_bit_cast(s16x8, v); return __builtin_shufflevector(t, t, 4, 5, 6, 7); }
__device__ __forceinline__ f32x2 sub2(const f32x16& v, int i) { return f32x2{v[i], v[i + 1]}; }
__device__ __forceinline__ f32x2 sub2(const f32x4& v, int i) { return f32x2{v[i], v[i + 1]}; }
__device__ __forceinline__ f32x2 fma2(f32x2 a, f32x2 b, f32x2 c) { return __builtin_elementwise_fma(a, b, c); }
#define MFMA32(a, b, c) __builtin_amdgcn_mfma_f32_32x32x16_bf16(a, b, c, 0, 0, 0)
#define MFMA16(a, b, c) __builtin_amdgcn_mfma_f32_16x16x32_bf16(a, b, c, 0, 0, 0)
#define MFMA32K8(a, b, c) __builtin_amdgcn_mfma_f32_32x32x8bf16_1k(a, b, c, 0, 0, 0)
#define MFMA4(a, b, c) __builtin_amdgcn_mfma_f32_4x4x4bf16_1k(a, b, c, 0, 0, 0)

__device__ unsigned long long ptab[8 * 16];
typedef __attribute__((address_space(4))) const unsigned long long* cptr;
#define PT(i, arg) (((cptr)(uintptr_t)ptab)[(blockIdx.x & 7) * 16 + (i)])
#define CODE_PF(sym, off) do { unsigned long long pc_; int d_; unsigned t_; \
  asm volatile("s_getpc_b64 %0\n.Lpf%=:\n\ts_mov_b32 %1, " sym "-.Lpf%=" : "=s"(pc_), "=s"(d_)); \
  unsigned u_; \
  asm volatile("buffer_load_dword %0, %2, %3, 0 offen\n\tglobal_load_dword %1, %4, %5\n\ts_waitcnt vmcnt(0)" : "=&v"(t_), "=&v"(u_) \
               : "v"((threadIdx.x & 63) * 128), "s"(mk_srd((const void*)(pc_ + d_ + (off)), 8192)), "v"(0), "s"(ptab + (blockIdx.x & 7) * 16)); } while (0)

struct Frag1 { bf16x8 a[2]; s16x4 b[2][2]; };

template <bool SWAP>
__device__ __forceinline__ void k1_consumer(char* __restrict__ rec, unsigned L, int rt, int ct, int wave) {
  const int tid = threadIdx.x - 256, lane = tid & 63;
  const int wm = wave >> 1, wn = wave & 1, r = lane & 31, hh = lane >> 5;
  unsigned aA[2][4], aB[2];
#pragma unroll
  for (int i = 0; i < 2; ++i) {
    int row = wm * 64 + ((i ^ wn) * 32) + r;
#pragma unroll
    for (int ks = 0; ks < 4; ++ks) aA[i][ks] = L + row * 128 + (((2 * ks + hh) ^ ((row >> 1) & 7)) << 4);
    int q = (lane >> 2) & 3, p = lane & 3, ch = (lane >> 4) & 1;
    int c = wn * 8 + ((i ^ wm) * 4) + 2 * ch + (p >> 1);
    aB[i] = L + 16384 + (8 * hh + q) * 256 + ((c ^ (q << 2)) << 4) + 8 * (p & 1);
  }
  auto rd = [&](Frag1& f, unsigned sl, int ks, int t) {
    if (t == 0) { f.a[0] = ldsr<bf16x8>(aA[0][ks] + sl); f.b[0][0] = ldstr(aB[0] + sl + ks * 4096); }
    if (t == 1) { f.b[0][1] = ldstr(aB[0] + sl + ks * 4096 + 1024); f.b[1][0] = ldstr(aB[1] + sl + ks * 4096); }
    if (t == 2) { f.b[1][1] = ldstr(aB[1] + sl + ks * 4096 + 1024); f.a[1] = ldsr<bf16x8>(aA[1][ks] + sl); }
  };
  f32x16 acc[2][2] = {};
  f32x4 sq0 = {}, sq1 = {}, cs0 = {}, cs1 = {};
  const s16x4 ones = {0x3f80, 0x3f80, 0x3f80, 0x3f80};
  auto mm = [&](const Frag1& f, int t) {
    const int i = t >> 1, j = t & 1;
    const bf16x8 b = cat8(f.b[j][0], f.b[j][1]);
    acc[i][j] = SWAP ? MFMA32(b, f.a[i], acc[i][j]) : MFMA32(f.a[i], b, acc[i][j]);
    if (t == 3) {
      sq0 = MFMA4(lo4(f.a[0]), lo4(f.a[0]), sq0); cs0 = MFMA4(ones, f.b[0][0], cs0);
      sq1 = MFMA4(hi4(f.a[0]), hi4(f.a[0]), sq1); cs1 = MFMA4(ones, f.b[0][1], cs1);
    }
  };
  auto kstep = [&](const Frag1& f, Frag1& n, unsigned sl, int ks) {
    mm(f, 0); SB(); rd(n, sl, ks, 0); SB(); mm(f, 1); SB(); rd(n, sl, ks, 1); SB(); mm(f, 2); SB(); rd(n, sl, ks, 2); SB(); mm(f, 3); SB();
  };
  Frag1 f0, f1;
  BARRIER(); BARRIER();
  TS(0, 1);
  rd(f0, 0, 0, 0); rd(f0, 0, 0, 1); rd(f0, 0, 0, 2);
  SB();
  for (int s = 0; s < 10; ++s) {
    const unsigned sl = (s & 3) * 32768, nsl = ((s + 1) & 3) * 32768;
    kstep(f0, f1, sl, 1);
    kstep(f1, f0, sl, 2);
    kstep(f0, f1, sl, 3);
    mm(f1, 0); SB();
    if (s < 9) LBARRIER();
    SB(); rd(f0, nsl, 0, 0); SB(); mm(f1, 1); SB(); rd(f0, nsl, 0, 1); SB(); mm(f1, 2); SB(); rd(f0, nsl, 0, 2); SB(); mm(f1, 3); SB();
  }
  TS(0, 2);
  const unsigned T = L + 131072;
  {
    f32x4 sq = sq0 + sq1;
    int e = lane & 3;
    float sv = e == 0 ? sq[0] : e == 1 ? sq[1] : e == 2 ? sq[2] : sq[3];
    ldsw<float>(T + hh * 512 + (wm * 64 + wn * 32 + r) * 4, sv);
    ldsw<float>(T + 1024 + hh * 512 + (wn * 64 + wm * 32 + r) * 4, cs0[0] + cs1[0]);
    if (wn == 1) {
#pragma unroll
      for (int i = 0; i < 2; ++i) {
        const int rb = wm * 64 + (i ^ 1) * 32;
        if (SWAP) {
          float v = wm ? acc[i][0][12] : acc[i][1][12];
          if (hh == 0) ldsw<float>(T + 2048 + (rb + r) * 4, v);
        } else if (r == 24) {
#pragma unroll
          for (int g = 0; g < 4; ++g) {
            f32x4 v;
#pragma unroll
            for (int e2 = 0; e2 < 4; ++e2) v[e2] = wm ? acc[i][0][4 * g + e2] : acc[i][1][4 * g + e2];
            ldsw<f32x4>(T + 2048 + (rb + 8 * g + 4 * hh) * 4, v);
          }
        }
      }
    }
  }
  LBARRIER();
  if (tid < 128) {
    float sum = ldsr<float>(T + 2048 + tid * 4);
    float sq = ldsr<float>(T + tid * 4) + ldsr<float>(T + 512 + tid * 4);
    float mean = sum * (1.f / D_);
    float var = fmaxf(sq * (1.f / D_) - mean * mean, 0.f);
    float rs = __builtin_amdgcn_rsqf(var + LN_EPS);
    if (SWAP) ldsw<f32x2>(T + 2560 + tid * 8, f32x2{rs, -mean});
    else { ldsw<float>(T + 2560 + tid * 4, rs); ldsw<float>(T + 3072 + tid * 4, -mean); }
  } else {
    int c = tid - 128;
    ldsw<float>(T + 3584 + c * 4, ldsr<float>(T + 1024 + c * 4) + ldsr<float>(T + 1536 + c * 4));
  }
  LBARRIER();
  const i32x4 ro = mk_srd(rec, 64 * REC_HEAD);
  const int tile0 = rt * 4 + wm * 2;
  if (SWAP) {
    const float qs = 0.22360679774997896f * 1.4426950408889634f;
    f32x2 ab[2];
#pragma unroll
    for (int i = 0; i < 2; ++i) ab[i] = ldsr<f32x2>(T + 2560 + (wm * 64 + (i ^ wn) * 32 + r) * 8);
    const unsigned vlo = (hh ^ wn) * REC + r * 16;
    const unsigned vhi = hh * REC_HEAD + 1024 + r * 8;
    const unsigned X = L + wave * 8192;
#pragma unroll
    for (int j = 0; j < 2; ++j) {
#pragma unroll
      for (int g = 0; g < 4; ++g) {
        const int P = wn * 8 + (j ^ wm) * 4 + g;
        if (P < 15) {
          const int hp = (P * 13) >> 6, t = P - hp * 5, hA = ct * 6 + 2 * hp, od = (t + 1) >> 2;
          const f32x4 cs = ldsr<f32x4>(T + 3584 + (P * 8 + 4 * hh) * 4);
          const float sc = hA < 32 ? qs : 1.f;
          u32x2 v[2];
#pragma unroll
          for (int i = 0; i < 2; ++i) {
            const f32x2 a = {ab[i].x * sc, ab[i].x * sc}, nm = {ab[i].y, ab[i].y};
            v[i].x = pk2(fma2(nm, sub2(cs, 0), sub2(acc[i][j], 4 * g)) * a);
            v[i].y = pk2(fma2(nm, sub2(cs, 2), sub2(acc[i][j], 4 * g + 2)) * a);
          }
          if (hA >= 64) {
#pragma unroll
            for (int i = 0; i < 2; ++i) ldsw<u32x2>(X + ((i ^ wn) * 32 + r) * 104 + (t * 8 + 4 * hh) * 2, v[i]);
          } else if (t == 2) {
#pragma unroll
            for (int i = 0; i < 2; ++i) st8(v[i], ro, vhi, hA * REC_HEAD + (tile0 + (i ^ wn)) * REC);
          } else {
            auto s0 = __builtin_amdgcn_permlane32_swap(v[0].x, v[1].x, false, false);
            auto s1 = __builtin_amdgcn_permlane32_swap(v[0].y, v[1].y, false, false);
            st16(u32x4{s0[0], s1[0], s0[1], s1[1]}, ro, vlo, (hA + od) * REC_HEAD + tile0 * REC + (t - 3 * od) * 512);
          }
        }
      }
    }
    if (ct == 10 && wn == 1) {
      const int g4 = lane >> 4, c16 = lane & 15;
      const unsigned ra = X + (16 * (g4 >> 1) + 4 * (g4 & 1) + ((lane >> 2) & 3)) * 104 + (lane & 3) * 8;
#pragma unroll
      for (int mb = 0; mb < 2; ++mb) {
#pragma unroll
        for (int c = 0; c < 3; ++c) {
          s16x4 lo = ldstr(ra + mb * 32 * 104 + c * 32), hi = ldstr(ra + mb * 32 * 104 + 8 * 104 + c * 32);
          const int col = c * 16 + c16, hd = col >= 20, d = col - 20 * hd;
          const unsigned vo = hd * REC_HEAD + (g4 >> 1) * REC_VS + (g4 & 1) * REC_VH + d * 16;
          const unsigned so = 32 * REC_HEAD + (tile0 + mb) * REC + REC_V;
          if (col < 40) {
            st16(__builtin_bit_cast(u32x4, cat8(lo, hi)), ro, vo, so);
          }
        }
      }
    }
  } else {
#pragma unroll
    for (int i = 0; i < 2; ++i) {
      const int rb = wm * 64 + (i ^ wn) * 32;
      f32x4 av[4], bv[4];
#pragma unroll
      for (int g = 0; g < 4; ++g) {
        av[g] = ldsr<f32x4>(T + 2560 + (rb + 8 * g + 4 * hh) * 4);
        bv[g] = ldsr<f32x4>(T + 3072 + (rb + 8 * g + 4 * hh) * 4);
      }
#pragma unroll
      for (int j = 0; j < 2; ++j) {
        const int lc = wn * 64 + (j ^ wm) * 32 + r;
        if (lc < 120) {
          const float c1 = ldsr<float>(T + 3584 + lc * 4);
          const f32x2 cs = {c1, c1};
          const int gc = ct * 120 + lc - 1280;
          const int hd = (gc * 205) >> 12, d = gc - hd * 20;
          u32x4 v[2];
#pragma unroll
          for (int g = 0; g < 4; ++g) {
            v[g >> 1][(g & 1) * 2] = pk2(fma2(sub2(bv[g], 0), cs, sub2(acc[i][j], 4 * g)) * sub2(av[g], 0));
            v[g >> 1][(g & 1) * 2 + 1] = pk2(fma2(sub2(bv[g], 2), cs, sub2(acc[i][j], 4 * g + 2)) * sub2(av[g], 2));
          }
          const unsigned vo = hd * REC_HEAD + hh * REC_VH + d * 16, so = 32 * REC_HEAD + (tile0 + (i ^ wn)) * REC + REC_V;
          st16(v[0], ro, vo, so); st16(v[1], ro, vo + REC_VS, so);
        }
      }
    }
  }
  TS(0, 3);
}
constexpr bool chk_div() {
  for (int i = 0; i < 640; ++i) if (((i * 205) >> 12) != i / 20) return false;
  return true;
}
static_assert(chk_div(), "magic division");
constexpr bool chk_div2() {
  for (int i = 0; i < 192; ++i) if (((i * 171) >> 11) != i / 12) return false;
  for (int i = 0; i < 80; ++i) if (((i * 205) >> 11) != i / 10) return false;
  return true;
}
static_assert(chk_div2(), "magic division");

extern "C" __global__ __launch_bounds__(512, 2) void k1_qkv(const u16* __restrict__ x, const u16* __restrict__ w, char* __restrict__ ws, u16* out,
                                                            const u16* wo, const u16* wg, const u16* wu, const u16* wd) {
  __shared__ __attribute__((aligned(1024))) char smem[131072 + 4096];
  const unsigned L = (unsigned)(uintptr_t)(LDS3 char*)smem;
  const int b = blockIdx.x, xcd = b & 7, bi = b >> 3;
  const int rt = (xcd >> 1) * 4 + (bi & 3), ct = (xcd & 1) * 8 + (bi >> 2);
  const int lane = threadIdx.x & 63, wraw = __builtin_amdgcn_readfirstlane(threadIdx.x >> 6), wave = wraw - 4;
  TS(0, 0);
  auto producer = [&]() __attribute__((always_inline)) {
    const int pw = wraw;
    const i32x4 sx = mk_srd(x, S_ * D_ * 2), sw = mk_srd(w, D_ * 1920 * 2);
    unsigned vA[4], vB[4];
#pragma unroll
    for (int i = 0; i < 4; ++i) {
      int row = (pw * 4 + i) * 8 + (lane >> 3);
      vA[i] = row * 1280 + (((lane & 7) ^ ((row >> 1) & 7)) << 4);
      int k = (pw * 4 + i) * 4 + (lane >> 4);
      vB[i] = k * 3840 + (((lane & 15) ^ ((k & 3) << 2)) << 4);
    }
    const unsigned long long bmask = ~((1ull << 15) | (1ull << 27) | (1ull << 39) | (1ull << 51));
    const unsigned oA = rt * 128 * 1280, oB = ct * 240;
    auto issue = [&](int s) {
#pragma unroll
      for (int i = 0; i < 4; ++i) {
        dma16(L + (s & 3) * 32768 + (pw * 4 + i) * 1024, vA[i], sx, oA + s * 128);
        dma16_masked(L + (s & 3) * 32768 + 16384 + (pw * 4 + i) * 1024, vB[i], sw, oB + s * (64 * 3840), bmask);
      }
    };
    issue(0);
    ldsw<u32x4>(L + pw * 32768 + 16384 + lane * 256 + ((15 ^ ((lane & 3) << 2)) << 4), u32x4{0x3f80u, 0u, 0u, 0u});
    BARRIER();
    issue(1);
    WAIT_VL(8); BARRIER();
    issue(2); issue(3);
    for (int s = 0; s < 9; ++s) {
      if (s < 7) WAIT_V(16); else if (s == 7) WAIT_V(8); else WAIT_V(0);
      BARRIER();
      if (s < 6) issue(s + 4);
    }
  };
  if (wraw >= 4) {
  if (wave == 0) {
    unsigned long long pc; unsigned len, t;
    asm volatile("s_getpc_b64 %0\n.Lpf_here:\n\ts_mov_b32 %1, k_end-k1_qkv\n\ts_mov_b32 %2, .Lpf_here-k1_qkv" : "=s"(pc), "=s"(len), "=s"(t));
    i32x4 sc = mk_srd((const void*)(pc - t), len);
    asm volatile("buffer_load_dword %0, %1, %2, 0 offen\n\ts_waitcnt vmcnt(0)" : "=v"(t) : "v"((bi * 16 + (lane & 15)) * 128), "s"(sc));
  }
  if (wave == 1 && lane == 0) {
    unsigned long long* t = ptab + xcd * 16;
    t[0] = (unsigned long long)ws; t[1] = (unsigned long long)x; t[2] = (unsigned long long)out; t[3] = (unsigned long long)wo;
    t[4] = (unsigned long long)wg; t[5] = (unsigned long long)wu; t[6] = (unsigned long long)wd;
  }
  if (ct <= 10) k1_consumer<true>(ws + WS_REC, L, rt, ct, wave);
  else k1_consumer<false>(ws + WS_REC, L, rt, ct, wave);
  return;
  }
  producer();
}

__device__ __forceinline__ void exp4(const f32x16& s, int g, unsigned* p) {
  float e0 = __builtin_amdgcn_exp2f(s[4 * g + 0]), e1 = __builtin_amdgcn_exp2f(s[4 * g + 1]);
  float e2 = __builtin_amdgcn_exp2f(s[4 * g + 2]), e3 = __builtin_amdgcn_exp2f(s[4 * g + 3]);
  p[0] = pk2(e0, e1); p[1] = pk2(e2, e3);
}
__device__ __forceinline__ bf16x8 px8(const unsigned* p) { u32x4 v = {p[0], p[1], p[2], p[3]}; return __builtin_bit_cast(bf16x8, v); }
__device__ __forceinline__ bf16x8 bx8(u32x4 v) { return __builtin_bit_cast(bf16x8, v); }
__device__ __forceinline__ float max16(const f32x16& s) {
  float m = fmaxf(fmaxf(fmaxf(s[0], s[1]), fmaxf(s[2], s[3])), fmaxf(fmaxf(s[4], s[5]), fmaxf(s[6], s[7])));
  float n = fmaxf(fmaxf(fmaxf(s[8], s[9]), fmaxf(s[10], s[11])), fmaxf(fmaxf(s[12], s[13]), fmaxf(s[14], s[15])));
  m = fmaxf(m, n);
  return fmaxf(m, __shfl_xor(m, 32));
}

#define K2_ASM \
  ".macro XE r\n v_exp_f32 v[\\r], v[\\r]\n.endm\n" \
  ".macro XC p, r\n v_cvt_pk_bf16_f32 v[\\p], v[\\r], v[\\r+1]\n.endm\n" \
  ".macro XM d, a, b, c\n v_mfma_f32_32x32x16_bf16 v[\\d:\\d+15], v[\\a:\\a+3], v[\\b:\\b+3], \\c\n.endm\n" \
  ".macro XPH s, n, q, op, oc, t, b\n" \
  " XE \\s\n XE \\s+1\n" \
  " .if \\t + \\b > 0\n XM \\op, 84, 68, v[\\op:\\op+15]\n .endif\n" \
  " .if \\b == 0\n" \
  "  .if \\t == 1\n s_waitcnt vmcnt(9)\n s_barrier\n .endif\n" \
  "  .if \\t == 4\n s_waitcnt vmcnt(0)\n s_barrier\n" \
  "   buffer_store_dwordx4 v[108:111], v118, %[sp], 0 offen sc1\n buffer_store_dwordx4 v[112:115], v119, %[sp], 0 offen sc1\n .endif\n" \
  "  .if \\t == 12\n v_lshlrev_b32 v116, 3, v107\n s_nop 0\n buffer_load_dword v117, v116, %[sp], %[pf] offen\n .endif\n" \
  "  s_mov_b64 exec, %[mv]\n ds_read_b128 v[80:83], v106 offset:\\t*2560\n ds_read_b128 v[84:87], v106 offset:\\t*2560+640\n s_mov_b64 exec, -1\n" \
  " .endif\n" \
  " XE \\s+2\n XE \\s+3\n XC 64, \\s\n XC 65, \\s+2\n XE \\s+4\n XE \\s+5\n" \
  " .if \\b == 1\n s_waitcnt lgkmcnt(0)\n .endif\n" \
  " .if \\t + \\b < 16\n XM \\n, 72, \\q, 0\n .endif\n" \
  " XE \\s+6\n XE \\s+7\n XC 66, \\s+4\n XC 67, \\s+6\n XE \\s+8\n" \
  " .if \\t + \\b < 16\n XM \\n, 76, \\q+4, v[\\n:\\n+15]\n .endif\n" \
  " .if (\\b == 0) && (\\t < 15)\n ds_read_b128 v[72:75], v104 offset:(\\t+1)*2560\n ds_read_b64 v[76:77], v105 offset:(\\t+1)*2560\n .endif\n" \
  " XE \\s+9\n XE \\s+10\n XE \\s+11\n XC 68, \\s+8\n XC 69, \\s+10\n XE \\s+12\n XE \\s+13\n" \
  " .if \\b == 0\n .if \\t < 15\n s_waitcnt lgkmcnt(2)\n .else\n s_waitcnt lgkmcnt(0)\n .endif\n .endif\n" \
  " .if \\t == 0\n XM \\oc, 80, 64, 0\n .else\n XM \\oc, 80, 64, v[\\oc:\\oc+15]\n .endif\n" \
  " XE \\s+14\n XE \\s+15\n XC 70, \\s+12\n XC 71, \\s+14\n" \
  ".endm\n" \
  ".macro XD\n s_add_u32 %[pc], %[pc], 4096\n s_mov_b32 m0, %[pc]\n s_nop 0\n buffer_load_dwordx4 v107, %[skv], %[pc] offen lds\n.endm\n" \
  ".macro XMAX d, s\n v_max3_f32 v[\\d], v[\\s], v[\\s+1], v[\\s+2]\n v_max3_f32 v[\\d+1], v[\\s+3], v[\\s+4], v[\\s+5]\n" \
  " v_max3_f32 v[\\d], v[\\d], v[\\s+6], v[\\s+7]\n v_max3_f32 v[\\d+1], v[\\d+1], v[\\s+8], v[\\s+9]\n" \
  " v_max3_f32 v[\\d], v[\\d], v[\\s+10], v[\\s+11]\n v_max3_f32 v[\\d+1], v[\\d+1], v[\\s+12], v[\\s+13]\n" \
  " v_max3_f32 v[\\d], v[\\d], v[\\s+14], v[\\s+15]\n v_max_f32 v[\\d], v[\\d], v[\\d+1]\n.endm\n" \
  "v_subrev_u32 v64, %[ko], v104\n v_subrev_u32 v65, %[ko], v105\n" \
  "ds_read_b128 v[72:75], v64\n ds_read_b64 v[76:77], v65\n" \
  "v_mov_b32 v78, 0x3f803f80\n v_mov_b32 v79, 0\n" \
  "XD\n XD\n s_waitcnt lgkmcnt(0)\n" \
  "XM 32, 72, 88, 0\n XM 48, 72, 96, 0\n XM 32, 76, 92, v[32:47]\n XM 48, 76, 100, v[48:63]\n" \
  "ds_read_b128 v[72:75], v104\n ds_read_b64 v[76:77], v105\n" \
  "XD\n XD\n XD\n XD\n XD\n" \
  "buffer_load_dwordx4 v[108:111], v116, %[sa], 0 offen\n" \
  "buffer_load_dwordx4 v[112:115], v117, %[sb], 0 offen\n" \
  "s_mov_b32 exec_hi, 0\n XMAX 64, 32\n XMAX 66, 48\n" \
  "v_cvt_pk_bf16_f32 v65, -v64, 0\n v_cvt_pk_bf16_f32 v67, -v66, 0\n v_lshlrev_b32 v65, 16, v65\n v_lshlrev_b32 v67, 16, v67\n" \
  "v_sub_f32 v65, -v64, v65\n v_sub_f32 v67, -v66, v67\n v_cvt_pk_bf16_f32 v94, -v64, v65\n v_cvt_pk_bf16_f32 v102, -v66, v67\n" \
  "s_mov_b32 exec_hi, -1\n s_waitcnt lgkmcnt(0)\n s_nop 0\n" \
  "XM 32, 72, 88, 0\n XM 32, 76, 92, v[32:47]\n s_nop 7\n s_nop 3\n" \
  ".irp t,0,1,2,3,4,5,6,7,8,9,10,11,12,13,14,15\n XPH 32, 48, 96, 16, 0, \\t, 0\n XPH 48, 32, 88, 0, 16, \\t, 1\n.endr\n" \
  "s_nop 1\n XM 16, 84, 68, v[16:31]\n s_waitcnt vmcnt(0)\n s_nop 7\n s_nop 3\n" \
  ".purgem XE\n.purgem XC\n.purgem XM\n.purgem XPH\n.purgem XD\n.purgem XMAX\n"

__device__ __forceinline__ void k2_tail(char* __restrict__ ws, unsigned L, const f32x16& o0, const f32x16& o1, u32x4 wA,
                                        int wave, int head, int qb, int mat, int rg, int cb) {
  int lane;
  asm volatile("v_mbcnt_lo_u32_b32 %0, -1, 0\n\tv_mbcnt_hi_u32_b32 %0, -1, %0" : "=v"(lane));
  const int b = blockIdx.x, qp = wave & 3, kq = wave >> 2, r = lane & 31, hh = lane >> 5;
  const char* rec = ws + WS_REC;
  const char* qrec = rec + (size_t)(head * 64 + qb * 8 + qp * 2) * REC;
  const unsigned vL = lane * 16, aK = hh * 512 + r * 16, aKh = 1024 + r * 8, aV = REC_V + hh * REC_VH + r * 16;
  const unsigned vc = r == 20 ? 0x3f803f80u : 0u;
  f32x4 cs;
  {
    const int i = lane & 15;
    const unsigned oh = (i & 1) ? 0x3f800000u : 0x3f80u;
    const u32x4 sel = {i >> 1 == 0 ? oh : 0u, i >> 1 == 1 ? oh : 0u, i >> 1 == 2 ? oh : 0u, i >> 1 == 3 ? oh : 0u};
    cs = MFMA16(bx8(sel), bx8(wA), f32x4{});
  }
  BARRIER();
  const unsigned CS = L + 73728;
  if (lane < 32) ldsw<f32x4>(CS + wave * 512 + (lane & 15) * 32 + (lane >> 4) * 16, cs);
#pragma unroll
  for (int g = 0; g < 3; ++g) {
    const unsigned a = L + qp * 18432 + g * 1024 + vL;
    if (kq != 0) ldsw<f32x4>(a + (kq - 1) * 3072, f32x4{o0[4 * g], o0[4 * g + 1], o0[4 * g + 2], o0[4 * g + 3]});
    if (kq != 1) ldsw<f32x4>(a + 9216 + (kq ? kq - 1 : 0) * 3072, f32x4{o1[4 * g], o1[4 * g + 1], o1[4 * g + 2], o1[4 * g + 3]});
  }
  LBARRIER();
  if (kq >= 2) {
    if (wave == 15) CODE_PF("k3_oproj", 0);
    if (kq == 2 && qp < 2 && b < 240) {
      const int c = qp * 64 + lane;
      float s = 0.f;
#pragma unroll
      for (int w = 0; w < 16; ++w) s += ldsr<float>(CS + w * 512 + c * 4);
      const int col = cb * 128 + c, ct = col / 96;
      __hip_atomic_store((float*)(ws + WS_CS + ct * 8192 + (rg * 192 + col - ct * 96 + mat * 96) * 4), s, __ATOMIC_RELAXED, __HIP_MEMORY_SCOPE_AGENT);
    }
    return;
  }
  f32x4 o[3];
#pragma unroll
  for (int g = 0; g < 3; ++g) {
    const unsigned a = L + qp * 18432 + kq * 9216 + g * 1024 + vL;
#pragma unroll
    for (int e = 0; e < 4; ++e) o[g][e] = kq ? o1[4 * g + e] : o0[4 * g + e];
    o[g] += ldsr<f32x4>(a) + (ldsr<f32x4>(a + 3072) + ldsr<f32x4>(a + 6144));
  }
  float l = __shfl(o[2][0], r + 32);
  bool ok = l <= 0x1p100f && l >= 0x1p-100f;
#pragma unroll
  for (int i = 0; i < 12; ++i) ok = ok && (__builtin_fabsf(o[i >> 2][i & 3]) <= 0x1p126f);
  if (__builtin_amdgcn_ballot_w64(!ok) != 0ull) {
    const char* kv = rec + (size_t)(32 + head) * REC_HEAD;
    const bf16x8 ql = *(const bf16x8*)(qrec + kq * REC + hh * 512 + r * 16);
    const u32x2 t = *(const u32x2*)(qrec + kq * REC + 1024 + r * 8);
    const bf16x8 qh = bx8(hh ? u32x4{} : u32x4{t.x, t.y, 0u, 0u});
    float m = -__builtin_inff();
    f32x16 oo = {};
    for (int t = 0; t < 64; ++t) {
      const char* q = kv + t * REC;
      const u32x2 kh = *(const u32x2*)(q + aKh);
      f32x16 s = {};
      s = MFMA32(*(const bf16x8*)(q + aK), ql, s);
      s = MFMA32(bx8(u32x4{kh.x, kh.y, 0u, 0u}), qh, s);
      float mn = fmaxf(m, max16(s));
      float c = __builtin_amdgcn_exp2f(m - mn);
      m = mn;
#pragma unroll
      for (int i = 0; i < 16; ++i) { s[i] -= mn; oo[i] *= c; }
      unsigned pp[8];
#pragma unroll
      for (int g = 0; g < 4; ++g) exp4(s, g, pp + 2 * g);
      u32x4 a0 = {vc, vc, vc, vc}, a1 = a0;
      if (r < 20) { a0 = *(const u32x4*)(q + aV); a1 = *(const u32x4*)(q + aV + REC_VS); }
      oo = MFMA32(bx8(a0), px8(pp), oo);
      oo = MFMA32(bx8(a1), px8(pp + 4), oo);
    }
#pragma unroll
    for (int i = 0; i < 12; ++i) o[i >> 2][i & 3] = oo[i];
    l = __shfl(oo[8], r + 32);
  }
  const float inv = 1.f / l;
  const int row = qb * 256 + (qp * 2 + kq) * 32 + r;
  const i32x4 so = mk_srd(ws + WS_O, S_ * D_ * 2);
#pragma unroll
  for (int g = 0; g < 3; ++g) {
    if (g < 2 || hh == 0) {
      const int col = head * 20 + 8 * g + 4 * hh;
      u32x2 v = {pk2(o[g][0] * inv, o[g][1] * inv), pk2(o[g][2] * inv, o[g][3] * inv)};
      st8(v, so, (col >> 3) * 32768 + row * 16 + (col & 7) * 2, 0);
    }
  }
  TS(1, 3);
}

extern "C" __global__ __launch_bounds__(1024, 4) void k2_attn(char* ws_, const u16* wo_, const u16* wg_, const u16* wu_, const u16* wd_) {
  char* __restrict__ ws = (char*)PT(0, ws_);
  const u16 *wo = (const u16*)PT(3, wo_), *wg = (const u16*)PT(4, wg_), *wu = (const u16*)PT(5, wu_), *wd = (const u16*)PT(6, wd_);
  __shared__ __attribute__((aligned(1024))) char smem[REC_HEAD];
  const unsigned L = (unsigned)(uintptr_t)(LDS3 char*)smem;
  const int tid = threadIdx.x, lane = tid & 63, wave = __builtin_amdgcn_readfirstlane(tid >> 6);
  const int b = blockIdx.x, xcd = b & 7, bi = b >> 3;
  const int head = xcd * 4 + (bi & 3), qb = bi >> 2;
  const int qp = wave & 3, kq = wave >> 2;
  const int r = lane & 31, hh = lane >> 5;
  const char* rec = ws + WS_REC;
  const i32x4 skv = mk_srd(rec + (size_t)(32 + head) * REC_HEAD - L, REC_HEAD + L);
  const unsigned vL = lane * 16, pc = L + kq * 40960 + qp * 1024;
  TS(1, 0);
  u32x4 ql0, ql1, qh0, qh1;
  {
    const i32x4 sq = mk_srd(rec, 32 * REC_HEAD);
    asm volatile("v_mov_b64 v[92:93], 0\n\tv_mov_b64 v[94:95], 0\n\tv_mov_b64 v[100:101], 0\n\tv_mov_b64 v[102:103], 0\n\t"
                 "buffer_load_dwordx4 v[88:91], %4, %6, %7 offen\n\tbuffer_load_dwordx4 v[96:99], %4, %6, %7 offen offset:2560\n\t"
                 "s_mov_b32 exec_hi, 0\n\t"
                 "buffer_load_dwordx2 v[92:93], %5, %6, %7 offen offset:1024\n\tbuffer_load_dwordx2 v[100:101], %5, %6, %7 offen offset:3584\n\t"
                 "s_mov_b32 exec_hi, -1"
                 : "=&{v[88:91]}"(ql0), "=&{v[92:95]}"(qh0), "=&{v[96:99]}"(ql1), "=&{v[100:103]}"(qh1)
                 : "v"(hh * 512 + r * 16), "v"(r * 8), "s"(sq), "s"((head * 64 + qb * 8 + qp * 2) * REC));
  }
  dma16(pc, vL, skv, pc);
  BARRIER();
  dma16(pc + 4096, vL, skv, pc + 4096);
  dma16(pc + 8192, vL, skv, pc + 8192);
  const int mat = b >= 120, rg = (b - mat * 120) / 12, cb = b - mat * 120 - rg * 12, gw = b * 16 + wave;
  unsigned srcA, dstA, srcB, dstB;
  {
    const int pr = wave * 4 + (lane >> 4), cc = cb * 16 + (lane & 15), ct = (cc * 171) >> 11, c12 = cc - ct * 12;
    srcA = (rg * 64 + pr) * 3072 + cc * 16;
    dstA = (ct * 10 + rg) * 24576 + mat * 12288 + pr * 192 + (c12 ^ (mat * 2)) * 16;
    if (b >= 240) srcA = dstA = 0x80000000u;
    const int id = (gw < 800 ? gw : gw - 800) * 64 + lane, R = id / 80, c80 = id - R * 80, c8 = (c80 * 205) >> 11, k = R & 127;
    srcB = id * 16;
    dstB = (gw < 800 ? PK_O + c8 * (5 * 20480) : PK_D + c8 * (12 * 20480)) + (R >> 7) * 20480 + (k ^ ((k >> 1) & 4)) * 160 + (c80 - c8 * 10) * 16;
    if (gw >= 2720) srcB = dstB = 0x80000000u;
  }
  const i32x4 sa = mk_srd(mat ? wu : wg, D_ * F_ * 2), sb = mk_srd(gw < 800 ? wo : wd, gw < 800 ? D_ * D_ * 2 : D_ * F_ * 2);
  const i32x4 sp = mk_srd(ws + WS_PK, PK_SIZE);
  unsigned aK = L + kq * 40960 + hh * 512 + r * 16, aKh = L + kq * 40960 + 1024 + r * 8, aV = L + kq * 40960 + REC_V + hh * REC_VH + r * 16;
  const unsigned vc = r == 20 ? 0x3f803f80u : 0u;
  u32x4 v0 = {vc, vc, vc, vc}, v1 = v0;
  asm volatile("s_waitcnt vmcnt(2)\n\ts_barrier"
               : "+{v[88:91]}"(ql0), "+{v[92:95]}"(qh0), "+{v[96:99]}"(ql1), "+{v[100:103]}"(qh1), "+{v116}"(srcA), "+{v118}"(dstA),
                 "+{v117}"(srcB), "+{v119}"(dstB), "+{v[80:83]}"(v0), "+{v[84:87]}"(v1), "+{v104}"(aK), "+{v105}"(aKh), "+{v106}"(aV)
               :: "memory");
  TS(1, 1);
  f32x16 o0, o1;
  u32x4 wA;
  unsigned pcs = pc + 8192;
  const int wi = bi * 16 + wave;
  const unsigned pf = wi < 240 ? (xcd & 1) * (8 * 10 * 24576) + wi * 8192 : PK_O + (wi < 340 ? wi - 240 : 0) * 8192;
  asm volatile(K2_ASM
               : "=&{v[0:15]}"(o0), "=&{v[16:31]}"(o1), "+{v[80:83]}"(v0), "+{v[84:87]}"(v1), "=&{v[108:111]}"(wA),
                 "+{v[92:95]}"(qh0), "+{v[100:103]}"(qh1), [pc] "+s"(pcs), "+{v116}"(srcA), "+{v117}"(srcB)
               : "{v[88:91]}"(ql0), "{v[96:99]}"(ql1), "{v104}"(aK), "{v105}"(aKh), "{v106}"(aV), "{v107}"(vL),
                 "{v118}"(dstA), "{v119}"(dstB), [pf] "s"(pf),
                 [mv] "s"(0x000fffff000fffffull), [sa] "s"(sa), [sb] "s"(sb), [sp] "s"(sp), [skv] "s"(skv), [ko] "s"(kq * 40960)
               : "memory", "scc", "v32", "v33", "v34", "v35", "v36", "v37", "v38", "v39", "v40", "v41", "v42", "v43", "v44", "v45", "v46", "v47",
                 "v48", "v49", "v50", "v51", "v52", "v53", "v54", "v55", "v56", "v57", "v58", "v59", "v60", "v61", "v62", "v63",
                 "v64", "v65", "v66", "v67", "v68", "v69", "v70", "v71", "v72", "v73", "v74", "v75", "v76", "v77", "v78", "v79",
                 "v112", "v113", "v114", "v115");
  TS(1, 2);
  k2_tail(ws, L, o0, o1, wA, wave, head, qb, mat, rg, cb);
}

struct Frag3 { bf16x8 a[2]; s16x4 b[5][2]; };
constexpr int K3_STAGE = 16384 + 20480;

template <int KD, bool FIRST>
__device__ __forceinline__ void proj_body(const char* __restrict__ A, const char* __restrict__ W, const char* __restrict__ R,
                                          char* __restrict__ C, char* __restrict__ stats, unsigned L) {
  constexpr int NS = KD / 128;
  const int tid = threadIdx.x, lane = tid & 63, wraw = __builtin_amdgcn_readfirstlane(tid >> 6), wave = wraw - 4;
  const int b = blockIdx.x, xcd = b & 7, bi = b >> 3;
  const int rt = xcd * 4 + (bi & 3), ct = bi >> 2;
  const int m0 = rt * 64, n0 = ct * 80;
  const unsigned RS = L + 4 * K3_STAGE;
  TS(FIRST ? 2 : 4, 0);
  auto producer = [&]() __attribute__((always_inline)) {
    const int pw = wraw;
    const i32x4 sa = mk_srd(A, S_ * KD * 2), sw = mk_srd(W, KD * D_ * 2), sr = mk_srd(R, S_ * D_ * 2);
    const unsigned vL = lane * 16;
    auto issue = [&](int s) {
#pragma unroll
      for (int i = 0; i < 5; ++i) {
        if (i < 4) dma16(L + (s & 3) * K3_STAGE + (pw * 4 + i) * 1024, vL, sa, (s * 16 + pw * 4 + i) * 32768 + m0 * 16);
        dma16(L + (s & 3) * K3_STAGE + 16384 + (pw * 5 + i) * 1024, vL, sw, (ct * NS + s) * 20480 + (pw * 5 + i) * 1024);
      }
    };
    issue(0);
    BARRIER();
    issue(1);
    WAIT_V(9); BARRIER();
    issue(2);
#pragma unroll
    for (int i = 0; i < 3; ++i) {
      int p = pw * 3 + i; p = p > 9 ? 9 : p;
      if (FIRST) { int idx = p * 64 + lane, row = idx / 10, c = idx - row * 10; dma16(RS + p * 1024, row * 1280 + c * 16, sr, m0 * 1280 + n0 * 2); }
      else dma16(RS + p * 1024, vL, sr, (ct * 10 + p) * 32768 + m0 * 16);
    }
    issue(3);
    for (int s = 0; s < NS - 1; ++s) {
      if (s == 0) WAIT_V(21); else if (s < NS - 3) WAIT_V(18); else if (s == NS - 3) WAIT_V(9); else WAIT_V(0);
      BARRIER();
      if (s < NS - 4) issue(s + 4);
    }
    if (FIRST && pw < 2) CODE_PF("k4_gateup", pw * 4096);
  };
  if (wraw >= 4) {
  const int wm = wave & 1, wk = wave >> 1, g = lane >> 4;
  const unsigned aA = L + (wk * 8 + g) * 1024 + (wm * 32 + (lane & 15)) * 16;
  unsigned aB[2];
#pragma unroll
  for (int t = 0; t < 2; ++t) aB[t] = L + 16384 + (wk * 64 + 8 * g + ((4 * t) ^ (4 * (g & 1))) + ((lane >> 2) & 3)) * 160 + (lane & 3) * 8;
  auto rd = [&](Frag3& f, unsigned sl, int ks, int t) {
    if (t == 0) f.a[0] = ldsr<bf16x8>(aA + sl + ks * 4096);
    else if (t == 3) f.a[1] = ldsr<bf16x8>(aA + sl + ks * 4096 + 256);
    else { const int u = t < 3 ? t - 1 : t - 2; f.b[u >> 1][u & 1] = ldstr(aB[u & 1] + sl + ks * 5120 + (u >> 1) * 32); }
  };
  f32x4 acc[2][5] = {};
  auto mm = [&](const Frag3& f, int t) { acc[t & 1][t >> 1] = MFMA16(cat8(f.b[t >> 1][0], f.b[t >> 1][1]), f.a[t & 1], acc[t & 1][t >> 1]); };
  auto body = [&](const Frag3& f, Frag3& n, unsigned sl, int ks) {
#pragma unroll
    for (int t = 1; t < 10; ++t) {
      SB();
      if (t <= 3) { rd(n, sl, ks, 2 * t - 2); rd(n, sl, ks, 2 * t - 1); } else rd(n, sl, ks, t + 2);
      SB(); mm(f, t);
    }
    SB();
  };
  Frag3 f0, f1;
  BARRIER(); BARRIER();
  TS(FIRST ? 2 : 4, 1);
#pragma unroll
  for (int t = 0; t < 12; ++t) rd(f0, 0, 0, t);
  SB();
  for (int s = 0; s < NS; ++s) {
    const unsigned sl = (s & 3) * K3_STAGE, nsl = ((s + 1) & 3) * K3_STAGE;
    mm(f0, 0); body(f0, f1, sl, 1);
    mm(f1, 0); SB();
    if (s < NS - 1) LBARRIER();
    body(f1, f0, nsl, 0);
  }
  TS(FIRST ? 2 : 4, 2);
  const unsigned X = L + ((NS + 1) & 3) * K3_STAGE;
#pragma unroll
  for (int j = 0; j < 5; ++j) ldsw<f32x4>(X + (wave * 5 + j) * 1024 + lane * 16, wk ? acc[0][j] : acc[1][j]);
  LBARRIER();
  const int lr = wm * 32 + wk * 16 + (lane & 15);
  const i32x4 rc = mk_srd(C, S_ * D_ * 2);
  const unsigned vo = FIRST ? (g >> 1) * 32768 + lr * 16 + (g & 1) * 8 : lr * 1280 + g * 8;
  f32x2 sum = {}, sq = {};
#pragma unroll
  for (int j = 0; j < 5; ++j) {
    f32x4 v = (wk ? acc[1][j] : acc[0][j]) + ldsr<f32x4>(X + ((wave ^ 2) * 5 + j) * 1024 + lane * 16);
    u32x2 rr = FIRST ? ldsr<u32x2>(RS + lr * 160 + j * 32 + g * 8) : ldsr<u32x2>(RS + (2 * j + (g >> 1)) * 1024 + lr * 16 + (g & 1) * 8);
    u32x2 o = {pk2(v[0] + bflo(rr.x), v[1] + bfhi(rr.x)), pk2(v[2] + bflo(rr.y), v[3] + bfhi(rr.y))};
    if (FIRST) {
      st8(o, rc, vo, (ct * 10 + 2 * j) * 32768 + m0 * 16);
      f32x2 h0 = {bflo(o.x), bfhi(o.x)}, h1 = {bflo(o.y), bfhi(o.y)};
      sum += h0 + h1;
      sq = fma2(h0, h0, fma2(h1, h1, sq));
    } else {
      st8(o, rc, vo, m0 * 1280 + (n0 + 16 * j) * 2);
    }
  }
  if (FIRST) {
    float s1 = sum.x + sum.y, s2 = sq.x + sq.y;
    s1 += __shfl_xor(s1, 16); s2 += __shfl_xor(s2, 16);
    s1 += __shfl_xor(s1, 32); s2 += __shfl_xor(s2, 32);
    if (lane < 16) st8(u32x2{__float_as_uint(s1), __float_as_uint(s2)}, mk_srd(stats, S_ * 64), lr * 64, m0 * 64 + ct * 8);
  }
  TS(FIRST ? 2 : 4, 3);
  return;
  }
  producer();
}

extern "C" __global__ __launch_bounds__(512, 2) void k3_oproj(const u16* x_, char* ws_) {
  char* ws = (char*)PT(0, ws_); const u16* x = (const u16*)PT(1, x_);
  __shared__ __attribute__((aligned(1024))) char smem[4 * K3_STAGE + 10240];
  proj_body<D_, true>(ws + WS_O, ws + WS_PK + PK_O, (const char*)x, ws + WS_H1, ws + WS_ST, (unsigned)(uintptr_t)(LDS3 char*)smem);
}
extern "C" __global__ __launch_bounds__(512, 2) void k5_down(char* ws_, u16* out_) {
  char* ws = (char*)PT(0, ws_); u16* out = (u16*)PT(2, out_);
  __shared__ __attribute__((aligned(1024))) char smem[4 * K3_STAGE + 10240];
  proj_body<F_, false>(ws + WS_HH, ws + WS_PK + PK_D, ws + WS_H1, (char*)out, nullptr, (unsigned)(uintptr_t)(LDS3 char*)smem);
}

struct Frag4 { bf16x8 a[2]; s16x4 b[3][2]; };
constexpr int K4_STAGE = 16384 + 24576;

extern "C" __global__ __launch_bounds__(512, 2) void k4_gateup(char* ws_) {
  char* __restrict__ ws = (char*)PT(0, ws_);
  __shared__ __attribute__((aligned(1024))) char smem[4 * K4_STAGE];
  const unsigned L = (unsigned)(uintptr_t)(LDS3 char*)smem;
  const int tid = threadIdx.x, lane = tid & 63, wraw = __builtin_amdgcn_readfirstlane(tid >> 6), wave = wraw - 4;
  const int b = blockIdx.x, xcd = b & 7, bi = b >> 3;
  const int rt = (xcd >> 1) * 4 + (bi & 3), ct = (xcd & 1) * 8 + (bi >> 2);
  const int m0 = rt * 128;
  const unsigned T = L + 2 * K4_STAGE;
  TS(3, 0);
  auto producer = [&]() __attribute__((always_inline)) {
    const int pw = wraw;
    const i32x4 sa = mk_srd(ws + WS_H1, S_ * D_ * 2), sw = mk_srd(ws + WS_PK + PK_G, PK_O);
    const i32x4 sst = mk_srd(ws + WS_ST, S_ * 64 + 16 * 8192);
    const i32x4 sd = mk_srd(ws + WS_PK + PK_D, PK_SIZE - PK_D);
    const unsigned vL = lane * 16;
    auto issue = [&](int s) {
      const unsigned sl = L + (s & 3) * K4_STAGE;
#pragma unroll
      for (int i = 0; i < 6; ++i) {
        const int p = pw * 4 + i, q = pw * 6 + i;
        if (i < 4) K4A(sl + p * 1024, vL, sa, (s * 8 + (p >> 1)) * 32768 + (m0 + (p & 1) * 64) * 16);
        K4B(sl + 16384 + q * 1024, vL, sw, (ct * 10 + s) * 24576 + q * 1024);
      }
    };
    issue(0);
    BARRIER();
    issue(1);
    WAIT_V(10); BARRIER();
    issue(2); issue(3);
#pragma unroll
    for (int s = 0; s < 9; ++s) {
      if (s == 0) WAIT_V(20); else if (s == 1) WAIT_V(21); else if (s == 2) WAIT_V(22); else if (s < 7) WAIT_V(23);
      else if (s == 7) WAIT_V(16); else WAIT_V(0);
      BARRIER();
      if (s < 6) {
        issue(s + 4);
        const int n = s * 4 + pw;
        unsigned t;
        asm volatile("buffer_load_dword %0, %1, %2, %3 offen" : "=v"(t) : "v"(lane * (PFD * 2)), "s"(sd), "s"((bi * 3 + (n >> 3)) * 20480 + (n & 7) * (PFD * 128)));
      }
      if (s == 6) {
        dma16(T + pw * 2048, vL, sst, m0 * 64 + pw * 2048);
        dma16(T + pw * 2048 + 1024, vL, sst, m0 * 64 + pw * 2048 + 1024);
        dma16(T + 8192 + pw * 2048, vL, sst, S_ * 64 + ct * 8192 + pw * 2048);
        dma16(T + 8192 + pw * 2048 + 1024, vL, sst, S_ * 64 + ct * 8192 + pw * 2048 + 1024);
      }
    }
    const int pt = tid;
    if (pt < 128) {
      f32x4 t0 = ldsr<f32x4>(T + pt * 64), t1 = ldsr<f32x4>(T + pt * 64 + 16), t2 = ldsr<f32x4>(T + pt * 64 + 32), t3 = ldsr<f32x4>(T + pt * 64 + 48);
      f32x4 t = (t0 + t1) + (t2 + t3);
      float mean = (t[0] + t[2]) * (1.f / D_);
      float var = fmaxf((t[1] + t[3]) * (1.f / D_) - mean * mean, 0.f);
      ldsw<f32x2>(T + 16384 + pt * 8, f32x2{__builtin_amdgcn_rsqf(var + LN_EPS), -mean});
    } else if (pt < 128 + 96) {
      const unsigned a = T + 8192 + (pt - 128) * 8;
      f32x2 c = {};
#pragma unroll
      for (int k = 0; k < 10; ++k) c += ldsr<f32x2>(a + k * 768);
      ldsw<f32x2>(T + 17408 + (pt - 128) * 8, c);
    }
    LBARRIER();
    if (pw == 0) CODE_PF("k5_down", 0);
  };
  if (wraw >= 4) {
  const int wm = wave >> 1, wn = wave & 1, r = lane & 31, hh = lane >> 5;
  const unsigned aA = L + hh * 2048 + (wm * 64 + r) * 16;
  unsigned aB[3];
  {
    const int q = (lane >> 2) & 3, p = lane & 3, up = (lane >> 4) & 1;
#pragma unroll
    for (int j = 0; j < 3; ++j) aB[j] = L + 16384 + up * 12288 + (8 * hh + q) * 192 + (((wn * 3 + j) ^ up) * 32) + p * 8;
  }
  auto rd = [&](Frag4& f, unsigned sl, int ks, int t) {
    if (t == 0) { f.a[0] = ldsr<bf16x8>(aA + sl + ks * 4096); f.b[0][0] = ldstr(aB[0] + sl + ks * 3072); }
    if (t == 1) { f.b[0][1] = ldstr(aB[0] + sl + ks * 3072 + 768); f.a[1] = ldsr<bf16x8>(aA + sl + ks * 4096 + 512); }
    if (t >= 2) { f.b[t - 1][0] = ldstr(aB[t - 1] + sl + ks * 3072); f.b[t - 1][1] = ldstr(aB[t - 1] + sl + ks * 3072 + 768); }
  };
  f32x16 acc[2][3] = {};
  auto mm = [&](const Frag4& f, int t) { acc[t & 1][t >> 1] = MFMA32(cat8(f.b[t >> 1][0], f.b[t >> 1][1]), f.a[t & 1], acc[t & 1][t >> 1]); };
  auto body = [&](const Frag4& f, Frag4& n, unsigned sl, int ks) {
#pragma unroll
    for (int t = 1; t < 6; ++t) {
      SB();
      if (t <= 4) rd(n, sl, ks, t - 1);
      SB(); mm(f, t);
    }
    SB();
  };
  Frag4 f0, f1;
  BARRIER(); BARRIER();
  TS(3, 1);
#pragma unroll
  for (int t = 0; t < 4; ++t) rd(f0, 0, 0, t);
  SB();
  for (int s = 0; s < 10; ++s) {
    const unsigned sl = (s & 3) * K4_STAGE, nsl = ((s + 1) & 3) * K4_STAGE;
    mm(f0, 0); body(f0, f1, sl, 1);
    mm(f1, 0); body(f1, f0, sl, 2);
    mm(f0, 0); body(f0, f1, sl, 3);
    mm(f1, 0); SB();
    if (s < 9) LBARRIER();
    body(f1, f0, nsl, 0);
  }
  BARRIER();
  TS(3, 2);
  const float k1 = -2.f * 0.7978845608028654f * 1.4426950408889634f, k2 = k1 * 0.044715f;
  const i32x4 ro = mk_srd(ws + WS_HH, S_ * F_ * 2);
  const unsigned vo = hh * 32768 + r * 16;
#pragma unroll
  for (int j = 0; j < 3; ++j) {
    const int fc = (wn * 3 + j) * 16 + 4 * hh;
    f32x4 cg[2], cu[2];
#pragma unroll
    for (int g = 0; g < 2; ++g) { cg[g] = ldsr<f32x4>(T + 17408 + (fc + 8 * g) * 4); cu[g] = ldsr<f32x4>(T + 17408 + 384 + (fc + 8 * g) * 4); }
#pragma unroll
    for (int i = 0; i < 2; ++i) {
      const f32x2 ab = ldsr<f32x2>(T + 16384 + (wm * 64 + i * 32 + r) * 8);
      const f32x2 a = {ab.x, ab.x}, nm = {ab.y, ab.y};
      unsigned pk[4];
#pragma unroll
      for (int e = 0; e < 4; ++e) {
        const f32x2 gt = fma2(nm, sub2(cg[e >> 1], (e & 1) * 2), sub2(acc[i][j], 2 * e)) * a;
        const f32x2 up = fma2(nm, sub2(cu[e >> 1], (e & 1) * 2), sub2(acc[i][j], 8 + 2 * e)) * a;
        const f32x2 z = gt * fma2(gt * gt, f32x2{k2, k2}, f32x2{k1, k1});
        const f32x2 dn = f32x2{__builtin_amdgcn_exp2f(z.x), __builtin_amdgcn_exp2f(z.y)} + f32x2{1.f, 1.f};
        const f32x2 sg2 = {__builtin_amdgcn_rcpf(dn.x), __builtin_amdgcn_rcpf(dn.y)};
        pk[e] = pk2(gt * up * sg2);
      }
      auto s0 = __builtin_amdgcn_permlane32_swap(pk[0], pk[2], false, false);
      auto s1 = __builtin_amdgcn_permlane32_swap(pk[1], pk[3], false, false);
      st16(u32x4{s0[0], s1[0], s0[1], s1[1]}, ro, vo, (ct * 12 + (wn * 3 + j) * 2) * 32768 + (m0 + wm * 64 + i * 32) * 16);
    }
  }
  TS(3, 3);
  return;
  }
  producer();
}

extern "C" __global__ void k_end() {}

extern "C" void kernel_launch(void* const* d_in, const int* in_sizes, int n_in, void* d_out, int out_size, void* d_ws,
                              size_t ws_size, hipStream_t stream) {
  const u16* x = (const u16*)d_in[0];
  const u16* wqkv = (const u16*)d_in[1];
  const u16* wo = (const u16*)d_in[2];
  const u16* wg = (const u16*)d_in[4];
  const u16* wu = (const u16*)d_in[5];
  const u16* wd = (const u16*)d_in[6];
  char* ws = (char*)d_ws;
  k1_qkv<<<256, 512, 0, stream>>>(x, wqkv, ws, (u16*)d_out, wo, wg, wu, wd);
  k2_attn<<<256, 1024, 0, stream>>>(ws, wo, wg, wu, wd);
  k3_oproj<<<256, 512, 0, stream>>>(x, ws);
  k4_gateup<<<256, 512, 0, stream>>>(ws);
  k5_down<<<256, 512, 0, stream>>>(ws, (u16*)d_out);
}
```

```cpp
#include <hip/hip_runtime.h>
#include <stdint.h>

typedef unsigned short u16;
typedef __bf16 bf16x8 __attribute__((ext_vector_type(8)));
typedef __bf16 bf16x2 __attribute__((ext_vector_type(2)));
typedef float f32x2 __attribute__((ext_vector_type(2)));
typedef float f32x4 __attribute__((ext_vector_type(4)));
typedef float f32x16 __attribute__((ext_vector_type(16)));
typedef int i32x4 __attribute__((ext_vector_type(4)));
typedef unsigned u32x2 __attribute__((ext_vector_type(2)));
typedef unsigned u32x4 __attribute__((ext_vector_type(4)));
#define LDS3 __attribute__((address_space(3)))

constexpr int S_ = 2048, D_ = 640, F_ = 1536;
constexpr int REC = 2560, REC_V = 1280, REC_VS = 640, REC_VH = 320;
constexpr int REC_HEAD = 64 * REC;
constexpr size_t WS_REC = 0;
constexpr size_t WS_O = WS_REC + (size_t)64 * REC_HEAD;
constexpr size_t WS_H1 = WS_O + (size_t)S_ * D_ * 2;
constexpr size_t WS_HH = WS_H1 + (size_t)S_ * D_ * 2;
constexpr size_t WS_ST = WS_HH + (size_t)S_ * F_ * 2;
constexpr size_t WS_CS = WS_ST + (size_t)S_ * 64;
constexpr size_t WS_PK = WS_CS + 16 * 8192 + 16384;
constexpr int PK_G = 0, PK_O = 16 * 10 * 24576, PK_D = PK_O + 8 * 5 * 20480, PK_SIZE = PK_D + 8 * 12 * 20480;
__device__ __attribute__((aligned(4096))) char wsg[WS_PK + PK_SIZE];

__device__ __forceinline__ unsigned lane_id() {
  unsigned l;
  asm volatile("v_mbcnt_lo_u32_b32 %0, -1, 0\n\tv_mbcnt_hi_u32_b32 %0, -1, %0" : "=v"(l));
  return l;
}
#define WAIT_V(n) asm volatile("s_waitcnt vmcnt(%0)" ::"i"(n) : "memory")
#define WAIT_VL(n) asm volatile("s_waitcnt vmcnt(%0) lgkmcnt(0)" ::"i"(n) : "memory")
#define BARRIER() asm volatile("s_barrier" ::: "memory")

__device__ __forceinline__ i32x4 mk_srd(const void* p, unsigned bytes) {
  unsigned long long a = (unsigned long long)p;
  i32x4 r;
  r.x = (int)(unsigned)a; r.y = (int)((unsigned)(a >> 32) & 0xffffu); r.z = (int)bytes; r.w = 0x00020000;
  return r;
}
__device__ __forceinline__ void st8(u32x2 v, i32x4 srd, unsigned voff, unsigned soff) {
  asm volatile("buffer_store_dwordx2 %0, %1, %2, %3 offen sc1" ::"v"(v), "v"(voff), "s"(srd), "s"(soff));
}
__device__ __forceinline__ void dma16(unsigned lds, unsigned voff, i32x4 srd, unsigned soff) {
  asm volatile("s_mov_b32 m0, %0\n\ts_nop 0\n\tbuffer_load_dwordx4 %1, %2, %3 offen lds"
               ::"s"(lds), "v"(voff), "s"(srd), "s"(soff) : "memory");
}
__device__ __forceinline__ void dma16s(unsigned lds, unsigned voff, i32x4 srd, unsigned soff) {
  asm volatile("s_mov_b32 m0, %0\n\ts_nop 0\n\tbuffer_load_dwordx4 %1, %2, %3 offen sc1 lds"
               ::"s"(lds), "v"(voff), "s"(srd), "s"(soff) : "memory");
}
template <typename T> __device__ __forceinline__ T ldsr(unsigned a) { return *(LDS3 const T*)(uintptr_t)a; }
template <typename T> __device__ __forceinline__ void ldsw(unsigned a, T v) { *(LDS3 T*)(uintptr_t)a = v; }
__device__ __forceinline__ unsigned pk2(f32x2 v) { return __builtin_bit_cast(unsigned, __builtin_convertvector(v, bf16x2)); }
__device__ __forceinline__ unsigned pk2(float a, float b) { return pk2(f32x2{a, b}); }
#define MFMA32(a, b, c) __builtin_amdgcn_mfma_f32_32x32x16_bf16(a, b, c, 0, 0, 0)

#define FS 16
constexpr int F_K1 = 6144 * FS, F_EP = 7936 * FS;

#define K1_ASM_QK \
  "v_mov_b64 v[0:1], 0\nv_mov_b64 v[2:3], 0\nv_mov_b64 v[4:5], 0\nv_mov_b64 v[6:7], 0\nv_mov_b64 v[8:9], 0\nv_mov_b64 v[10:11], 0\n" \
  "v_mov_b64 v[12:13], 0\nv_mov_b64 v[14:15], 0\nv_mov_b64 v[16:17], 0\nv_mov_b64 v[18:19], 0\nv_mov_b64 v[20:21], 0\nv_mov_b64 v[22:23], 0\n" \
  "v_mov_b64 v[24:25], 0\nv_mov_b64 v[26:27], 0\nv_mov_b64 v[28:29], 0\nv_mov_b64 v[30:31], 0\nv_mov_b64 v[32:33], 0\nv_mov_b64 v[34:35], 0\n" \
  "v_mov_b64 v[36:37], 0\nv_mov_b64 v[38:39], 0\nv_mov_b64 v[40:41], 0\nv_mov_b64 v[42:43], 0\nv_mov_b64 v[44:45], 0\nv_mov_b64 v[46:47], 0\n" \
  "v_mov_b64 v[48:49], 0\nv_mov_b64 v[50:51], 0\nv_mov_b64 v[52:53], 0\nv_mov_b64 v[54:55], 0\nv_mov_b64 v[56:57], 0\nv_mov_b64 v[58:59], 0\n" \
  "v_mov_b64 v[60:61], 0\nv_mov_b64 v[62:63], 0\nv_mov_b64 v[64:65], 0\nv_mov_b64 v[66:67], 0\nv_mov_b64 v[68:69], 0\nv_mov_b64 v[70:71], 0\n" \
  "v_mov_b64 v[72:73], 0\nv_mov_b64 v[74:75], 0\nv_mov_b64 v[76:77], 0\nv_mov_b64 v[78:79], 0\nv_mov_b64 v[80:81], 0\nv_mov_b64 v[82:83], 0\n" \
  "v_mov_b64 v[84:85], 0\nv_mov_b64 v[86:87], 0\nv_mov_b32 v88, 0x3f803f80\nv_mov_b32 v89, 0x3f803f80\nv_mov_b32 v90, 0x3f803f80\n" \
  "v_mov_b32 v91, 0x3f803f80\ns_barrier\ns_barrier\nds_read_b128 v[92:95], v156 offset:0\nds_read_b64_tr_b16 v[108:109], v160 offset:0\n" \
  "ds_read_b64_tr_b16 v[110:111], v160 offset:1024\nds_read_b64_tr_b16 v[112:113], v161 offset:0\nds_read_b64_tr_b16 v[114:115], v161 offset:1024\n" \
  "ds_read_b64_tr_b16 v[116:117], v162 offset:0\nds_read_b64_tr_b16 v[118:119], v162 offset:1024\nds_read_b64_tr_b16 v[120:121], v163 offset:0\n" \
  "ds_read_b64_tr_b16 v[122:123], v163 offset:1024\nds_read_b128 v[96:99], v156 offset:2048\nds_read_b128 v[100:103], v157 offset:0\n" \
  "ds_read_b128 v[104:107], v157 offset:2048\n.Lk1s_%=:\ns_waitcnt lgkmcnt(9)\nv_mfma_f32_16x16x32_bf16 v[0:3], v[108:111], v[92:95], v[0:3]\n" \
  "s_waitcnt lgkmcnt(7)\nv_mfma_f32_16x16x32_bf16 v[4:7], v[112:115], v[92:95], v[4:7]\ns_waitcnt lgkmcnt(5)\n" \
  "v_mfma_f32_16x16x32_bf16 v[8:11], v[116:119], v[92:95], v[8:11]\ns_waitcnt lgkmcnt(3)\n" \
  "v_mfma_f32_16x16x32_bf16 v[12:15], v[120:123], v[92:95], v[12:15]\ns_waitcnt lgkmcnt(0)\nds_read_b128 v[124:127], v158 offset:0\n" \
  "v_mfma_f32_16x16x32_bf16 v[16:19], v[108:111], v[96:99], v[16:19]\nds_read_b64_tr_b16 v[140:141], v160 offset:8192\n" \
  "v_mfma_f32_16x16x32_bf16 v[20:23], v[112:115], v[96:99], v[20:23]\nds_read_b64_tr_b16 v[142:143], v160 offset:9216\n" \
  "v_mfma_f32_16x16x32_bf16 v[24:27], v[116:119], v[96:99], v[24:27]\nds_read_b64_tr_b16 v[144:145], v161 offset:8192\n" \
  "v_mfma_f32_16x16x32_bf16 v[28:31], v[120:123], v[96:99], v[28:31]\nds_read_b64_tr_b16 v[146:147], v161 offset:9216\n" \
  "v_mfma_f32_16x16x32_bf16 v[32:35], v[108:111], v[100:103], v[32:35]\nds_read_b64_tr_b16 v[148:149], v162 offset:8192\n" \
  "v_mfma_f32_16x16x32_bf16 v[36:39], v[112:115], v[100:103], v[36:39]\nds_read_b64_tr_b16 v[150:151], v162 offset:9216\n" \
  "v_mfma_f32_16x16x32_bf16 v[40:43], v[116:119], v[100:103], v[40:43]\nds_read_b64_tr_b16 v[152:153], v163 offset:8192\n" \
  "v_mfma_f32_16x16x32_bf16 v[44:47], v[120:123], v[100:103], v[44:47]\nds_read_b64_tr_b16 v[154:155], v163 offset:9216\n" \
  "v_mfma_f32_16x16x32_bf16 v[48:51], v[108:111], v[104:107], v[48:51]\nds_read_b128 v[128:131], v158 offset:2048\n" \
  "v_mfma_f32_16x16x32_bf16 v[52:55], v[112:115], v[104:107], v[52:55]\nds_read_b128 v[132:135], v159 offset:0\n" \
  "v_mfma_f32_16x16x32_bf16 v[56:59], v[116:119], v[104:107], v[56:59]\nds_read_b128 v[136:139], v159 offset:2048\n" \
  "v_mfma_f32_16x16x32_bf16 v[60:63], v[120:123], v[104:107], v[60:63]\nv_mfma_f32_16x16x32_bf16 v[64:67], v[92:95], v[92:95], v[64:67]\n" \
  "v_mfma_f32_16x16x32_bf16 v[68:71], v[96:99], v[96:99], v[68:71]\nv_mfma_f32_16x16x32_bf16 v[72:75], v[88:91], v[92:95], v[72:75]\n" \
  "v_mfma_f32_16x16x32_bf16 v[76:79], v[88:91], v[96:99], v[76:79]\nv_mfma_f32_16x16x32_bf16 v[80:83], v[88:91], v[108:111], v[80:83]\n" \
  "v_mfma_f32_16x16x32_bf16 v[84:87], v[88:91], v[112:115], v[84:87]\ns_waitcnt lgkmcnt(9)\n" \
  "v_mfma_f32_16x16x32_bf16 v[0:3], v[140:143], v[124:127], v[0:3]\ns_waitcnt lgkmcnt(7)\n" \
  "v_mfma_f32_16x16x32_bf16 v[4:7], v[144:147], v[124:127], v[4:7]\ns_waitcnt lgkmcnt(5)\n" \
  "v_mfma_f32_16x16x32_bf16 v[8:11], v[148:151], v[124:127], v[8:11]\ns_waitcnt lgkmcnt(3)\n" \
  "v_mfma_f32_16x16x32_bf16 v[12:15], v[152:155], v[124:127], v[12:15]\ns_waitcnt lgkmcnt(0)\ns_barrier\nds_read_b128 v[92:95], v156 offset:32768\n" \
  "v_mfma_f32_16x16x32_bf16 v[16:19], v[140:143], v[128:131], v[16:19]\nds_read_b64_tr_b16 v[108:109], v160 offset:32768\n" \
  "v_mfma_f32_16x16x32_bf16 v[20:23], v[144:147], v[128:131], v[20:23]\nds_read_b64_tr_b16 v[110:111], v160 offset:33792\n" \
  "v_mfma_f32_16x16x32_bf16 v[24:27], v[148:151], v[128:131], v[24:27]\nds_read_b64_tr_b16 v[112:113], v161 offset:32768\n" \
  "v_mfma_f32_16x16x32_bf16 v[28:31], v[152:155], v[128:131], v[28:31]\nds_read_b64_tr_b16 v[114:115], v161 offset:33792\n" \
  "v_mfma_f32_16x16x32_bf16 v[32:35], v[140:143], v[132:135], v[32:35]\nds_read_b64_tr_b16 v[116:117], v162 offset:32768\n" \
  "v_mfma_f32_16x16x32_bf16 v[36:39], v[144:147], v[132:135], v[36:39]\nds_read_b64_tr_b16 v[118:119], v162 offset:33792\n" \
  "v_mfma_f32_16x16x32_bf16 v[40:43], v[148:151], v[132:135], v[40:43]\nds_read_b64_tr_b16 v[120:121], v163 offset:32768\n" \
  "v_mfma_f32_16x16x32_bf16 v[44:47], v[152:155], v[132:135], v[44:47]\nds_read_b64_tr_b16 v[122:123], v163 offset:33792\n" \
  "v_mfma_f32_16x16x32_bf16 v[48:51], v[140:143], v[136:139], v[48:51]\nds_read_b128 v[96:99], v156 offset:34816\n" \
  "v_mfma_f32_16x16x32_bf16 v[52:55], v[144:147], v[136:139], v[52:55]\nds_read_b128 v[100:103], v157 offset:32768\n" \
  "v_mfma_f32_16x16x32_bf16 v[56:59], v[148:151], v[136:139], v[56:59]\nds_read_b128 v[104:107], v157 offset:34816\n" \
  "v_mfma_f32_16x16x32_bf16 v[60:63], v[152:155], v[136:139], v[60:63]\nv_mfma_f32_16x16x32_bf16 v[64:67], v[124:127], v[124:127], v[64:67]\n" \
  "v_mfma_f32_16x16x32_bf16 v[68:71], v[128:131], v[128:131], v[68:71]\nv_mfma_f32_16x16x32_bf16 v[72:75], v[88:91], v[124:127], v[72:75]\n" \
  "v_mfma_f32_16x16x32_bf16 v[76:79], v[88:91], v[128:131], v[76:79]\nv_mfma_f32_16x16x32_bf16 v[80:83], v[88:91], v[140:143], v[80:83]\n" \
  "v_mfma_f32_16x16x32_bf16 v[84:87], v[88:91], v[144:147], v[84:87]\ns_waitcnt lgkmcnt(9)\n" \
  "v_mfma_f32_16x16x32_bf16 v[0:3], v[108:111], v[92:95], v[0:3]\ns_waitcnt lgkmcnt(7)\n" \
  "v_mfma_f32_16x16x32_bf16 v[4:7], v[112:115], v[92:95], v[4:7]\ns_waitcnt lgkmcnt(5)\n" \
  "v_mfma_f32_16x16x32_bf16 v[8:11], v[116:119], v[92:95], v[8:11]\ns_waitcnt lgkmcnt(3)\n" \
  "v_mfma_f32_16x16x32_bf16 v[12:15], v[120:123], v[92:95], v[12:15]\ns_waitcnt lgkmcnt(0)\nds_read_b128 v[124:127], v158 offset:32768\n" \
  "v_mfma_f32_16x16x32_bf16 v[16:19], v[108:111], v[96:99], v[16:19]\nds_read_b64_tr_b16 v[140:141], v160 offset:40960\n" \
  "v_mfma_f32_16x16x32_bf16 v[20:23], v[112:115], v[96:99], v[20:23]\nds_read_b64_tr_b16 v[142:143], v160 offset:41984\n" \
  "v_mfma_f32_16x16x32_bf16 v[24:27], v[116:119], v[96:99], v[24:27]\nds_read_b64_tr_b16 v[144:145], v161 offset:40960\n" \
  "v_mfma_f32_16x16x32_bf16 v[28:31], v[120:123], v[96:99], v[28:31]\nds_read_b64_tr_b16 v[146:147], v161 offset:41984\n" \
  "v_mfma_f32_16x16x32_bf16 v[32:35], v[108:111], v[100:103], v[32:35]\nds_read_b64_tr_b16 v[148:149], v162 offset:40960\n" \
  "v_mfma_f32_16x16x32_bf16 v[36:39], v[112:115], v[100:103], v[36:39]\nds_read_b64_tr_b16 v[150:151], v162 offset:41984\n" \
  "v_mfma_f32_16x16x32_bf16 v[40:43], v[116:119], v[100:103], v[40:43]\nds_read_b64_tr_b16 v[152:153], v163 offset:40960\n" \
  "v_mfma_f32_16x16x32_bf16 v[44:47], v[120:123], v[100:103], v[44:47]\nds_read_b64_tr_b16 v[154:155], v163 offset:41984\n" \
  "v_mfma_f32_16x16x32_bf16 v[48:51], v[108:111], v[104:107], v[48:51]\nds_read_b128 v[128:131], v158 offset:34816\n" \
  "v_mfma_f32_16x16x32_bf16 v[52:55], v[112:115], v[104:107], v[52:55]\nds_read_b128 v[132:135], v159 offset:32768\n" \
  "v_mfma_f32_16x16x32_bf16 v[56:59], v[116:119], v[104:107], v[56:59]\nds_read_b128 v[136:139], v159 offset:34816\n" \
  "v_mfma_f32_16x16x32_bf16 v[60:63], v[120:123], v[104:107], v[60:63]\nv_xor_b32 v156, 0x10000, v156\nv_xor_b32 v157, 0x10000, v157\n" \
  "v_mfma_f32_16x16x32_bf16 v[64:67], v[92:95], v[92:95], v[64:67]\nv_xor_b32 v158, 0x10000, v158\nv_xor_b32 v159, 0x10000, v159\n" \
  "v_mfma_f32_16x16x32_bf16 v[68:71], v[96:99], v[96:99], v[68:71]\nv_xor_b32 v160, 0x10000, v160\nv_xor_b32 v161, 0x10000, v161\n" \
  "v_mfma_f32_16x16x32_bf16 v[72:75], v[88:91], v[92:95], v[72:75]\nv_xor_b32 v162, 0x10000, v162\nv_xor_b32 v163, 0x10000, v163\n" \
  "v_mfma_f32_16x16x32_bf16 v[76:79], v[88:91], v[96:99], v[76:79]\nv_mfma_f32_16x16x32_bf16 v[80:83], v[88:91], v[108:111], v[80:83]\n" \
  "v_mfma_f32_16x16x32_bf16 v[84:87], v[88:91], v[112:115], v[84:87]\ns_waitcnt lgkmcnt(9)\n" \
  "v_mfma_f32_16x16x32_bf16 v[0:3], v[140:143], v[124:127], v[0:3]\ns_waitcnt lgkmcnt(7)\n" \
  "v_mfma_f32_16x16x32_bf16 v[4:7], v[144:147], v[124:127], v[4:7]\ns_waitcnt lgkmcnt(5)\n" \
  "v_mfma_f32_16x16x32_bf16 v[8:11], v[148:151], v[124:127], v[8:11]\ns_waitcnt lgkmcnt(3)\n" \
  "v_mfma_f32_16x16x32_bf16 v[12:15], v[152:155], v[124:127], v[12:15]\ns_waitcnt lgkmcnt(0)\ns_barrier\nds_read_b128 v[92:95], v156 offset:0\n" \
  "v_mfma_f32_16x16x32_bf16 v[16:19], v[140:143], v[128:131], v[16:19]\nds_read_b64_tr_b16 v[108:109], v160 offset:0\n" \
  "v_mfma_f32_16x16x32_bf16 v[20:23], v[144:147], v[128:131], v[20:23]\nds_read_b64_tr_b16 v[110:111], v160 offset:1024\n" \
  "v_mfma_f32_16x16x32_bf16 v[24:27], v[148:151], v[128:131], v[24:27]\nds_read_b64_tr_b16 v[112:113], v161 offset:0\n" \
  "v_mfma_f32_16x16x32_bf16 v[28:31], v[152:155], v[128:131], v[28:31]\nds_read_b64_tr_b16 v[114:115], v161 offset:1024\n" \
  "v_mfma_f32_16x16x32_bf16 v[32:35], v[140:143], v[132:135], v[32:35]\nds_read_b64_tr_b16 v[116:117], v162 offset:0\n" \
  "v_mfma_f32_16x16x32_bf16 v[36:39], v[144:147], v[132:135], v[36:39]\nds_read_b64_tr_b16 v[118:119], v162 offset:1024\n" \
  "v_mfma_f32_16x16x32_bf16 v[40:43], v[148:151], v[132:135], v[40:43]\nds_read_b64_tr_b16 v[120:121], v163 offset:0\n" \
  "v_mfma_f32_16x16x32_bf16 v[44:47], v[152:155], v[132:135], v[44:47]\nds_read_b64_tr_b16 v[122:123], v163 offset:1024\n" \
  "v_mfma_f32_16x16x32_bf16 v[48:51], v[140:143], v[136:139], v[48:51]\nds_read_b128 v[96:99], v156 offset:2048\n" \
  "v_mfma_f32_16x16x32_bf16 v[52:55], v[144:147], v[136:139], v[52:55]\nds_read_b128 v[100:103], v157 offset:0\n" \
  "v_mfma_f32_16x16x32_bf16 v[56:59], v[148:151], v[136:139], v[56:59]\nds_read_b128 v[104:107], v157 offset:2048\n" \
  "v_mfma_f32_16x16x32_bf16 v[60:63], v[152:155], v[136:139], v[60:63]\nv_mfma_f32_16x16x32_bf16 v[64:67], v[124:127], v[124:127], v[64:67]\n" \
  "v_mfma_f32_16x16x32_bf16 v[68:71], v[128:131], v[128:131], v[68:71]\nv_mfma_f32_16x16x32_bf16 v[72:75], v[88:91], v[124:127], v[72:75]\n" \
  "v_mfma_f32_16x16x32_bf16 v[76:79], v[88:91], v[128:131], v[76:79]\nv_mfma_f32_16x16x32_bf16 v[80:83], v[88:91], v[140:143], v[80:83]\n" \
  "v_mfma_f32_16x16x32_bf16 v[84:87], v[88:91], v[144:147], v[84:87]\ns_add_u32 %[cnt], %[cnt], -1\ns_cbranch_scc1 .Lk1s_%=\ns_waitcnt lgkmcnt(9)\n" \
  "v_mfma_f32_16x16x32_bf16 v[0:3], v[108:111], v[92:95], v[0:3]\ns_waitcnt lgkmcnt(7)\n" \
  "v_mfma_f32_16x16x32_bf16 v[4:7], v[112:115], v[92:95], v[4:7]\ns_waitcnt lgkmcnt(5)\n" \
  "v_mfma_f32_16x16x32_bf16 v[8:11], v[116:119], v[92:95], v[8:11]\ns_waitcnt lgkmcnt(3)\n" \
  "v_mfma_f32_16x16x32_bf16 v[12:15], v[120:123], v[92:95], v[12:15]\ns_waitcnt lgkmcnt(0)\nds_read_b128 v[124:127], v158 offset:0\n" \
  "v_mfma_f32_16x16x32_bf16 v[16:19], v[108:111], v[96:99], v[16:19]\nds_read_b64_tr_b16 v[140:141], v160 offset:8192\n" \
  "v_mfma_f32_16x16x32_bf16 v[20:23], v[112:115], v[96:99], v[20:23]\nds_read_b64_tr_b16 v[142:143], v160 offset:9216\n" \
  "v_mfma_f32_16x16x32_bf16 v[24:27], v[116:119], v[96:99], v[24:27]\nds_read_b64_tr_b16 v[144:145], v161 offset:8192\n" \
  "v_mfma_f32_16x16x32_bf16 v[28:31], v[120:123], v[96:99], v[28:31]\nds_read_b64_tr_b16 v[146:147], v161 offset:9216\n" \
  "v_mfma_f32_16x16x32_bf16 v[32:35], v[108:111], v[100:103], v[32:35]\nds_read_b64_tr_b16 v[148:149], v162 offset:8192\n" \
  "v_mfma_f32_16x16x32_bf16 v[36:39], v[112:115], v[100:103], v[36:39]\nds_read_b64_tr_b16 v[150:151], v162 offset:9216\n" \
  "v_mfma_f32_16x16x32_bf16 v[40:43], v[116:119], v[100:103], v[40:43]\nds_read_b64_tr_b16 v[152:153], v163 offset:8192\n" \
  "v_mfma_f32_16x16x32_bf16 v[44:47], v[120:123], v[100:103], v[44:47]\nds_read_b64_tr_b16 v[154:155], v163 offset:9216\n" \
  "v_mfma_f32_16x16x32_bf16 v[48:51], v[108:111], v[104:107], v[48:51]\nds_read_b128 v[128:131], v158 offset:2048\n" \
  "v_mfma_f32_16x16x32_bf16 v[52:55], v[112:115], v[104:107], v[52:55]\nds_read_b128 v[132:135], v159 offset:0\n" \
  "v_mfma_f32_16x16x32_bf16 v[56:59], v[116:119], v[104:107], v[56:59]\nds_read_b128 v[136:139], v159 offset:2048\n" \
  "v_mfma_f32_16x16x32_bf16 v[60:63], v[120:123], v[104:107], v[60:63]\nv_mfma_f32_16x16x32_bf16 v[64:67], v[92:95], v[92:95], v[64:67]\n" \
  "v_mfma_f32_16x16x32_bf16 v[68:71], v[96:99], v[96:99], v[68:71]\nv_mfma_f32_16x16x32_bf16 v[72:75], v[88:91], v[92:95], v[72:75]\n" \
  "v_mfma_f32_16x16x32_bf16 v[76:79], v[88:91], v[96:99], v[76:79]\nv_mfma_f32_16x16x32_bf16 v[80:83], v[88:91], v[108:111], v[80:83]\n" \
  "v_mfma_f32_16x16x32_bf16 v[84:87], v[88:91], v[112:115], v[84:87]\ns_waitcnt lgkmcnt(9)\n" \
  "v_mfma_f32_16x16x32_bf16 v[0:3], v[140:143], v[124:127], v[0:3]\ns_waitcnt lgkmcnt(7)\n" \
  "v_mfma_f32_16x16x32_bf16 v[4:7], v[144:147], v[124:127], v[4:7]\ns_waitcnt lgkmcnt(5)\n" \
  "v_mfma_f32_16x16x32_bf16 v[8:11], v[148:151], v[124:127], v[8:11]\ns_waitcnt lgkmcnt(3)\n" \
  "v_mfma_f32_16x16x32_bf16 v[12:15], v[152:155], v[124:127], v[12:15]\ns_waitcnt lgkmcnt(0)\ns_barrier\nds_read_b128 v[92:95], v156 offset:32768\n" \
  "v_mfma_f32_16x16x32_bf16 v[16:19], v[140:143], v[128:131], v[16:19]\nds_read_b64_tr_b16 v[108:109], v160 offset:32768\n" \
  "v_mfma_f32_16x16x32_bf16 v[20:23], v[144:147], v[128:131], v[20:23]\nds_read_b64_tr_b16 v[110:111], v160 offset:33792\n" \
  "v_mfma_f32_16x16x32_bf16 v[24:27], v[148:151], v[128:131], v[24:27]\nds_read_b64_tr_b16 v[112:113], v161 offset:32768\n" \
  "v_mfma_f32_16x16x32_bf16 v[28:31], v[152:155], v[128:131], v[28:31]\nds_read_b64_tr_b16 v[114:115], v161 offset:33792\n" \
  "v_mfma_f32_16x16x32_bf16 v[32:35], v[140:143], v[132:135], v[32:35]\nds_read_b64_tr_b16 v[116:117], v162 offset:32768\n" \
  "v_mfma_f32_16x16x32_bf16 v[36:39], v[144:147], v[132:135], v[36:39]\nds_read_b64_tr_b16 v[118:119], v162 offset:33792\n" \
  "v_mfma_f32_16x16x32_bf16 v[40:43], v[148:151], v[132:135], v[40:43]\nds_read_b64_tr_b16 v[120:121], v163 offset:32768\n" \
  "v_mfma_f32_16x16x32_bf16 v[44:47], v[152:155], v[132:135], v[44:47]\nds_read_b64_tr_b16 v[122:123], v163 offset:33792\n" \
  "v_mfma_f32_16x16x32_bf16 v[48:51], v[140:143], v[136:139], v[48:51]\nds_read_b128 v[96:99], v156 offset:34816\n" \
  "v_mfma_f32_16x16x32_bf16 v[52:55], v[144:147], v[136:139], v[52:55]\nds_read_b128 v[100:103], v157 offset:32768\n" \
  "v_mfma_f32_16x16x32_bf16 v[56:59], v[148:151], v[136:139], v[56:59]\nds_read_b128 v[104:107], v157 offset:34816\n" \
  "v_mfma_f32_16x16x32_bf16 v[60:63], v[152:155], v[136:139], v[60:63]\nv_mfma_f32_16x16x32_bf16 v[64:67], v[124:127], v[124:127], v[64:67]\n" \
  "v_mfma_f32_16x16x32_bf16 v[68:71], v[128:131], v[128:131], v[68:71]\nv_mfma_f32_16x16x32_bf16 v[72:75], v[88:91], v[124:127], v[72:75]\n" \
  "v_mfma_f32_16x16x32_bf16 v[76:79], v[88:91], v[128:131], v[76:79]\nv_mfma_f32_16x16x32_bf16 v[80:83], v[88:91], v[140:143], v[80:83]\n" \
  "v_mfma_f32_16x16x32_bf16 v[84:87], v[88:91], v[144:147], v[84:87]\ns_waitcnt lgkmcnt(9)\n" \
  "v_mfma_f32_16x16x32_bf16 v[0:3], v[108:111], v[92:95], v[0:3]\ns_waitcnt lgkmcnt(7)\n" \
  "v_mfma_f32_16x16x32_bf16 v[4:7], v[112:115], v[92:95], v[4:7]\ns_waitcnt lgkmcnt(5)\n" \
  "v_mfma_f32_16x16x32_bf16 v[8:11], v[116:119], v[92:95], v[8:11]\ns_waitcnt lgkmcnt(3)\n" \
  "v_mfma_f32_16x16x32_bf16 v[12:15], v[120:123], v[92:95], v[12:15]\ns_waitcnt lgkmcnt(0)\nds_read_b128 v[124:127], v158 offset:32768\n" \
  "v_mfma_f32_16x16x32_bf16 v[16:19], v[108:111], v[96:99], v[16:19]\nds_read_b64_tr_b16 v[140:141], v160 offset:40960\n" \
  "v_mfma_f32_16x16x32_bf16 v[20:23], v[112:115], v[96:99], v[20:23]\nds_read_b64_tr_b16 v[142:143], v160 offset:41984\n" \
  "v_mfma_f32_16x16x32_bf16 v[24:27], v[116:119], v[96:99], v[24:27]\nds_read_b64_tr_b16 v[144:145], v161 offset:40960\n" \
  "v_mfma_f32_16x16x32_bf16 v[28:31], v[120:123], v[96:99], v[28:31]\nds_read_b64_tr_b16 v[146:147], v161 offset:41984\n" \
  "v_mfma_f32_16x16x32_bf16 v[32:35], v[108:111], v[100:103], v[32:35]\nds_read_b64_tr_b16 v[148:149], v162 offset:40960\n" \
  "v_mfma_f32_16x16x32_bf16 v[36:39], v[112:115], v[100:103], v[36:39]\nds_read_b64_tr_b16 v[150:151], v162 offset:41984\n" \
  "v_mfma_f32_16x16x32_bf16 v[40:43], v[116:119], v[100:103], v[40:43]\nds_read_b64_tr_b16 v[152:153], v163 offset:40960\n" \
  "v_mfma_f32_16x16x32_bf16 v[44:47], v[120:123], v[100:103], v[44:47]\nds_read_b64_tr_b16 v[154:155], v163 offset:41984\n" \
  "v_mfma_f32_16x16x32_bf16 v[48:51], v[108:111], v[104:107], v[48:51]\nds_read_b128 v[128:131], v158 offset:34816\n" \
  "v_mfma_f32_16x16x32_bf16 v[52:55], v[112:115], v[104:107], v[52:55]\nds_read_b128 v[132:135], v159 offset:32768\n" \
  "v_mfma_f32_16x16x32_bf16 v[56:59], v[116:119], v[104:107], v[56:59]\nds_read_b128 v[136:139], v159 offset:34816\n" \
  "v_mfma_f32_16x16x32_bf16 v[60:63], v[120:123], v[104:107], v[60:63]\nv_mfma_f32_16x16x32_bf16 v[64:67], v[92:95], v[92:95], v[64:67]\n" \
  "v_mfma_f32_16x16x32_bf16 v[68:71], v[96:99], v[96:99], v[68:71]\nv_mfma_f32_16x16x32_bf16 v[72:75], v[88:91], v[92:95], v[72:75]\n" \
  "v_mfma_f32_16x16x32_bf16 v[76:79], v[88:91], v[96:99], v[76:79]\nv_mfma_f32_16x16x32_bf16 v[80:83], v[88:91], v[108:111], v[80:83]\n" \
  "v_mfma_f32_16x16x32_bf16 v[84:87], v[88:91], v[112:115], v[84:87]\ns_waitcnt lgkmcnt(0)\n" \
  "v_mfma_f32_16x16x32_bf16 v[64:67], v[124:127], v[124:127], v[64:67]\nv_mfma_f32_16x16x32_bf16 v[68:71], v[128:131], v[128:131], v[68:71]\n" \
  "v_mfma_f32_16x16x32_bf16 v[72:75], v[88:91], v[124:127], v[72:75]\nv_mfma_f32_16x16x32_bf16 v[76:79], v[88:91], v[128:131], v[76:79]\n" \
  "v_mfma_f32_16x16x32_bf16 v[80:83], v[88:91], v[140:143], v[80:83]\nv_mfma_f32_16x16x32_bf16 v[84:87], v[88:91], v[144:147], v[84:87]\n" \
  "v_mfma_f32_16x16x32_bf16 v[0:3], v[140:143], v[124:127], v[0:3]\nv_mfma_f32_16x16x32_bf16 v[4:7], v[144:147], v[124:127], v[4:7]\n" \
  "v_mfma_f32_16x16x32_bf16 v[8:11], v[148:151], v[124:127], v[8:11]\nv_cndmask_b32_e64 v178, v64, v65, %[m1]\n" \
  "v_cndmask_b32_e64 v179, v68, v69, %[m1]\nv_cndmask_b32_e64 v180, v66, v67, %[m1]\nv_cndmask_b32_e64 v181, v70, v71, %[m1]\n" \
  "v_mfma_f32_16x16x32_bf16 v[12:15], v[152:155], v[124:127], v[12:15]\nv_mul_f32 v183, 0xbacccccd, v72\nv_mul_f32 v185, 0xbacccccd, v76\n" \
  "v_cndmask_b32_e64 v178, v178, v180, %[m2]\nv_cndmask_b32_e64 v179, v179, v181, %[m2]\n" \
  "v_mfma_f32_16x16x32_bf16 v[16:19], v[140:143], v[128:131], v[16:19]\nv_mul_f32_e64 v180, -v183, v183\nv_mul_f32_e64 v181, -v185, v185\n" \
  "v_fmamk_f32 v178, v178, 0x3acccccd, v180\nv_fmamk_f32 v179, v179, 0x3acccccd, v181\n" \
  "v_mfma_f32_16x16x32_bf16 v[20:23], v[144:147], v[128:131], v[20:23]\nv_max_f32 v178, 0, v178\nv_max_f32 v179, 0, v179\n" \
  "v_add_f32 v178, 0x3727c5ac, v178\nv_add_f32 v179, 0x3727c5ac, v179\nv_mfma_f32_16x16x32_bf16 v[24:27], v[148:151], v[128:131], v[24:27]\n" \
  "v_rsq_f32 v178, v178\nv_rsq_f32 v179, v179\nv_mul_f32 v182, %[sc], v178\nv_mul_f32 v184, %[sc], v179\n" \
  "v_mfma_f32_16x16x32_bf16 v[28:31], v[152:155], v[128:131], v[28:31]\ns_mov_b64 exec, %[md]\nds_write_b32 v169, v80 offset:0\n" \
  "ds_write_b32 v169, v84 offset:64\nds_write_b64 v168, v[182:183] offset:0\nds_write_b64 v168, v[184:185] offset:128\ns_mov_b64 exec, -1\n" \
  "v_mfma_f32_16x16x32_bf16 v[32:35], v[140:143], v[132:135], v[32:35]\nv_mfma_f32_16x16x32_bf16 v[36:39], v[144:147], v[132:135], v[36:39]\n" \
  "s_waitcnt vmcnt(0) lgkmcnt(0)\ns_barrier\nv_readfirstlane_b32 %[e], v187\nds_read_b64 v[92:93], v170 offset:0\n" \
  "ds_read_b64 v[94:95], v170 offset:128\nds_read_b128 v[100:103], v172 offset:0\nds_read_b128 v[104:107], v172 offset:64\n" \
  "ds_read_b128 v[108:111], v173 offset:0\nds_read_b128 v[112:115], v173 offset:64\nds_read_b64 v[96:97], v171 offset:0\n" \
  "ds_read_b64 v[98:99], v171 offset:128\nv_mfma_f32_16x16x32_bf16 v[40:43], v[148:151], v[132:135], v[40:43]\n" \
  "v_mfma_f32_16x16x32_bf16 v[44:47], v[152:155], v[132:135], v[44:47]\nv_mfma_f32_16x16x32_bf16 v[48:51], v[140:143], v[136:139], v[48:51]\n" \
  "v_mfma_f32_16x16x32_bf16 v[52:55], v[144:147], v[136:139], v[52:55]\nv_mfma_f32_16x16x32_bf16 v[56:59], v[148:151], v[136:139], v[56:59]\n" \
  "v_mfma_f32_16x16x32_bf16 v[60:63], v[152:155], v[136:139], v[60:63]\ns_waitcnt lgkmcnt(5)\n" \
  "v_pk_fma_f32 v[0:1], v[92:93], v[100:101], v[0:1] op_sel:[1,0,0]\nv_pk_fma_f32 v[2:3], v[92:93], v[102:103], v[2:3] op_sel:[1,0,0]\n" \
  "v_pk_fma_f32 v[16:17], v[94:95], v[100:101], v[16:17] op_sel:[1,0,0]\nv_pk_fma_f32 v[18:19], v[94:95], v[102:103], v[18:19] op_sel:[1,0,0]\n" \
  "v_pk_mul_f32 v[0:1], v[92:93], v[0:1] op_sel_hi:[0,1]\nv_pk_mul_f32 v[2:3], v[92:93], v[2:3] op_sel_hi:[0,1]\n" \
  "v_pk_mul_f32 v[16:17], v[94:95], v[16:17] op_sel_hi:[0,1]\nv_pk_mul_f32 v[18:19], v[94:95], v[18:19] op_sel_hi:[0,1]\n" \
  "v_cvt_pk_bf16_f32 v124, v0, v1\nv_cvt_pk_bf16_f32 v125, v2, v3\nv_cvt_pk_bf16_f32 v126, v16, v17\nv_cvt_pk_bf16_f32 v127, v18, v19\n" \
  "s_waitcnt lgkmcnt(4)\nv_pk_fma_f32 v[4:5], v[92:93], v[104:105], v[4:5] op_sel:[1,0,0]\n" \
  "v_pk_fma_f32 v[6:7], v[92:93], v[106:107], v[6:7] op_sel:[1,0,0]\nv_permlane16_swap_b32 v124, v126\nv_permlane16_swap_b32 v125, v127\n" \
  "v_pk_fma_f32 v[20:21], v[94:95], v[104:105], v[20:21] op_sel:[1,0,0]\nv_pk_fma_f32 v[22:23], v[94:95], v[106:107], v[22:23] op_sel:[1,0,0]\n" \
  "v_pk_mul_f32 v[4:5], v[92:93], v[4:5] op_sel_hi:[0,1]\nv_pk_mul_f32 v[6:7], v[92:93], v[6:7] op_sel_hi:[0,1]\ns_mov_b64 vcc, %[no0]\n" \
  "v_cndmask_b32_dpp v0, v126, v124, vcc quad_perm:[1,0,3,2] row_mask:0xf bank_mask:0xf\n" \
  "v_cndmask_b32_dpp v1, v127, v125, vcc quad_perm:[1,0,3,2] row_mask:0xf bank_mask:0xf\ns_mov_b64 vcc, %[ne0]\n" \
  "v_cndmask_b32_dpp v2, v124, v126, vcc quad_perm:[1,0,3,2] row_mask:0xf bank_mask:0xf\n" \
  "v_cndmask_b32_dpp v3, v125, v127, vcc quad_perm:[1,0,3,2] row_mask:0xf bank_mask:0xf\nbuffer_store_dwordx4 v[0:3], v164, %[ro], %[so0] offen sc1\n" \
  "v_pk_mul_f32 v[20:21], v[94:95], v[20:21] op_sel_hi:[0,1]\nv_pk_mul_f32 v[22:23], v[94:95], v[22:23] op_sel_hi:[0,1]\n" \
  "v_cvt_pk_bf16_f32 v128, v4, v5\nv_cvt_pk_bf16_f32 v129, v6, v7\nv_cvt_pk_bf16_f32 v130, v20, v21\nv_cvt_pk_bf16_f32 v131, v22, v23\n" \
  "s_waitcnt lgkmcnt(3)\nv_pk_fma_f32 v[8:9], v[92:93], v[108:109], v[8:9] op_sel:[1,0,0]\n" \
  "v_pk_fma_f32 v[10:11], v[92:93], v[110:111], v[10:11] op_sel:[1,0,0]\nv_permlane16_swap_b32 v128, v130\nv_permlane16_swap_b32 v129, v131\n" \
  "v_pk_fma_f32 v[24:25], v[94:95], v[108:109], v[24:25] op_sel:[1,0,0]\nv_pk_fma_f32 v[26:27], v[94:95], v[110:111], v[26:27] op_sel:[1,0,0]\n" \
  "v_pk_mul_f32 v[8:9], v[92:93], v[8:9] op_sel_hi:[0,1]\nv_pk_mul_f32 v[10:11], v[92:93], v[10:11] op_sel_hi:[0,1]\ns_mov_b64 vcc, %[no1]\n" \
  "v_cndmask_b32_dpp v4, v130, v128, vcc quad_perm:[1,0,3,2] row_mask:0xf bank_mask:0xf\n" \
  "v_cndmask_b32_dpp v5, v131, v129, vcc quad_perm:[1,0,3,2] row_mask:0xf bank_mask:0xf\ns_mov_b64 vcc, %[ne1]\n" \
  "v_cndmask_b32_dpp v6, v128, v130, vcc quad_perm:[1,0,3,2] row_mask:0xf bank_mask:0xf\n" \
  "v_cndmask_b32_dpp v7, v129, v131, vcc quad_perm:[1,0,3,2] row_mask:0xf bank_mask:0xf\nbuffer_store_dwordx4 v[4:7], v165, %[ro], %[so0] offen sc1\n" \
  "v_pk_mul_f32 v[24:25], v[94:95], v[24:25] op_sel_hi:[0,1]\nv_pk_mul_f32 v[26:27], v[94:95], v[26:27] op_sel_hi:[0,1]\n" \
  "v_cvt_pk_bf16_f32 v132, v8, v9\nv_cvt_pk_bf16_f32 v133, v10, v11\nv_cvt_pk_bf16_f32 v134, v24, v25\nv_cvt_pk_bf16_f32 v135, v26, v27\n" \
  "s_waitcnt lgkmcnt(2)\nv_pk_fma_f32 v[12:13], v[92:93], v[112:113], v[12:13] op_sel:[1,0,0]\n" \
  "v_pk_fma_f32 v[14:15], v[92:93], v[114:115], v[14:15] op_sel:[1,0,0]\nv_permlane16_swap_b32 v132, v134\nv_permlane16_swap_b32 v133, v135\n" \
  "v_pk_fma_f32 v[28:29], v[94:95], v[112:113], v[28:29] op_sel:[1,0,0]\nv_pk_fma_f32 v[30:31], v[94:95], v[114:115], v[30:31] op_sel:[1,0,0]\n" \
  "v_pk_mul_f32 v[12:13], v[92:93], v[12:13] op_sel_hi:[0,1]\nv_pk_mul_f32 v[14:15], v[92:93], v[14:15] op_sel_hi:[0,1]\ns_mov_b64 vcc, %[no2]\n" \
  "v_cndmask_b32_dpp v8, v134, v132, vcc quad_perm:[1,0,3,2] row_mask:0xf bank_mask:0xf\n" \
  "v_cndmask_b32_dpp v9, v135, v133, vcc quad_perm:[1,0,3,2] row_mask:0xf bank_mask:0xf\ns_mov_b64 vcc, %[ne2]\n" \
  "v_cndmask_b32_dpp v10, v132, v134, vcc quad_perm:[1,0,3,2] row_mask:0xf bank_mask:0xf\n" \
  "v_cndmask_b32_dpp v11, v133, v135, vcc quad_perm:[1,0,3,2] row_mask:0xf bank_mask:0xf\n" \
  "buffer_store_dwordx4 v[8:11], v166, %[ro], %[so0] offen sc1\nv_pk_mul_f32 v[28:29], v[94:95], v[28:29] op_sel_hi:[0,1]\n" \
  "v_pk_mul_f32 v[30:31], v[94:95], v[30:31] op_sel_hi:[0,1]\nv_cvt_pk_bf16_f32 v136, v12, v13\nv_cvt_pk_bf16_f32 v137, v14, v15\n" \
  "v_cvt_pk_bf16_f32 v138, v28, v29\nv_cvt_pk_bf16_f32 v139, v30, v31\ns_waitcnt lgkmcnt(0)\n" \
  "v_pk_fma_f32 v[32:33], v[96:97], v[100:101], v[32:33] op_sel:[1,0,0]\nv_pk_fma_f32 v[34:35], v[96:97], v[102:103], v[34:35] op_sel:[1,0,0]\n" \
  "v_permlane16_swap_b32 v136, v138\nv_permlane16_swap_b32 v137, v139\nv_pk_fma_f32 v[48:49], v[98:99], v[100:101], v[48:49] op_sel:[1,0,0]\n" \
  "v_pk_fma_f32 v[50:51], v[98:99], v[102:103], v[50:51] op_sel:[1,0,0]\nv_pk_mul_f32 v[32:33], v[96:97], v[32:33] op_sel_hi:[0,1]\n" \
  "v_pk_mul_f32 v[34:35], v[96:97], v[34:35] op_sel_hi:[0,1]\ns_mov_b64 vcc, %[no3]\n" \
  "v_cndmask_b32_dpp v12, v138, v136, vcc quad_perm:[1,0,3,2] row_mask:0xf bank_mask:0xf\n" \
  "v_cndmask_b32_dpp v13, v139, v137, vcc quad_perm:[1,0,3,2] row_mask:0xf bank_mask:0xf\ns_mov_b64 vcc, %[ne3]\n" \
  "v_cndmask_b32_dpp v14, v136, v138, vcc quad_perm:[1,0,3,2] row_mask:0xf bank_mask:0xf\n" \
  "v_cndmask_b32_dpp v15, v137, v139, vcc quad_perm:[1,0,3,2] row_mask:0xf bank_mask:0xf\n" \
  "buffer_store_dwordx4 v[12:15], v167, %[ro], %[so0] offen sc1\nv_pk_mul_f32 v[48:49], v[98:99], v[48:49] op_sel_hi:[0,1]\n" \
  "v_pk_mul_f32 v[50:51], v[98:99], v[50:51] op_sel_hi:[0,1]\nv_cvt_pk_bf16_f32 v140, v32, v33\nv_cvt_pk_bf16_f32 v141, v34, v35\n" \
  "v_cvt_pk_bf16_f32 v142, v48, v49\nv_cvt_pk_bf16_f32 v143, v50, v51\nv_pk_fma_f32 v[36:37], v[96:97], v[104:105], v[36:37] op_sel:[1,0,0]\n" \
  "v_pk_fma_f32 v[38:39], v[96:97], v[106:107], v[38:39] op_sel:[1,0,0]\nv_permlane16_swap_b32 v140, v142\nv_permlane16_swap_b32 v141, v143\n" \
  "v_pk_fma_f32 v[52:53], v[98:99], v[104:105], v[52:53] op_sel:[1,0,0]\nv_pk_fma_f32 v[54:55], v[98:99], v[106:107], v[54:55] op_sel:[1,0,0]\n" \
  "v_pk_mul_f32 v[36:37], v[96:97], v[36:37] op_sel_hi:[0,1]\nv_pk_mul_f32 v[38:39], v[96:97], v[38:39] op_sel_hi:[0,1]\ns_mov_b64 vcc, %[no0]\n" \
  "v_cndmask_b32_dpp v32, v142, v140, vcc quad_perm:[1,0,3,2] row_mask:0xf bank_mask:0xf\n" \
  "v_cndmask_b32_dpp v33, v143, v141, vcc quad_perm:[1,0,3,2] row_mask:0xf bank_mask:0xf\ns_mov_b64 vcc, %[ne0]\n" \
  "v_cndmask_b32_dpp v34, v140, v142, vcc quad_perm:[1,0,3,2] row_mask:0xf bank_mask:0xf\n" \
  "v_cndmask_b32_dpp v35, v141, v143, vcc quad_perm:[1,0,3,2] row_mask:0xf bank_mask:0xf\n" \
  "buffer_store_dwordx4 v[32:35], v164, %[ro], %[so1] offen sc1\nv_pk_mul_f32 v[52:53], v[98:99], v[52:53] op_sel_hi:[0,1]\n" \
  "v_pk_mul_f32 v[54:55], v[98:99], v[54:55] op_sel_hi:[0,1]\nv_cvt_pk_bf16_f32 v144, v36, v37\nv_cvt_pk_bf16_f32 v145, v38, v39\n" \
  "v_cvt_pk_bf16_f32 v146, v52, v53\nv_cvt_pk_bf16_f32 v147, v54, v55\nv_pk_fma_f32 v[40:41], v[96:97], v[108:109], v[40:41] op_sel:[1,0,0]\n" \
  "v_pk_fma_f32 v[42:43], v[96:97], v[110:111], v[42:43] op_sel:[1,0,0]\nv_permlane16_swap_b32 v144, v146\nv_permlane16_swap_b32 v145, v147\n" \
  "v_pk_fma_f32 v[56:57], v[98:99], v[108:109], v[56:57] op_sel:[1,0,0]\nv_pk_fma_f32 v[58:59], v[98:99], v[110:111], v[58:59] op_sel:[1,0,0]\n" \
  "v_pk_mul_f32 v[40:41], v[96:97], v[40:41] op_sel_hi:[0,1]\nv_pk_mul_f32 v[42:43], v[96:97], v[42:43] op_sel_hi:[0,1]\ns_mov_b64 vcc, %[no1]\n" \
  "v_cndmask_b32_dpp v36, v146, v144, vcc quad_perm:[1,0,3,2] row_mask:0xf bank_mask:0xf\n" \
  "v_cndmask_b32_dpp v37, v147, v145, vcc quad_perm:[1,0,3,2] row_mask:0xf bank_mask:0xf\ns_mov_b64 vcc, %[ne1]\n" \
  "v_cndmask_b32_dpp v38, v144, v146, vcc quad_perm:[1,0,3,2] row_mask:0xf bank_mask:0xf\n" \
  "v_cndmask_b32_dpp v39, v145, v147, vcc quad_perm:[1,0,3,2] row_mask:0xf bank_mask:0xf\n" \
  "buffer_store_dwordx4 v[36:39], v165, %[ro], %[so1] offen sc1\nv_pk_mul_f32 v[56:57], v[98:99], v[56:57] op_sel_hi:[0,1]\n" \
  "v_pk_mul_f32 v[58:59], v[98:99], v[58:59] op_sel_hi:[0,1]\nv_cvt_pk_bf16_f32 v148, v40, v41\nv_cvt_pk_bf16_f32 v149, v42, v43\n" \
  "v_cvt_pk_bf16_f32 v150, v56, v57\nv_cvt_pk_bf16_f32 v151, v58, v59\nv_pk_fma_f32 v[44:45], v[96:97], v[112:113], v[44:45] op_sel:[1,0,0]\n" \
  "v_pk_fma_f32 v[46:47], v[96:97], v[114:115], v[46:47] op_sel:[1,0,0]\nv_permlane16_swap_b32 v148, v150\nv_permlane16_swap_b32 v149, v151\n" \
  "v_pk_fma_f32 v[60:61], v[98:99], v[112:113], v[60:61] op_sel:[1,0,0]\nv_pk_fma_f32 v[62:63], v[98:99], v[114:115], v[62:63] op_sel:[1,0,0]\n" \
  "v_pk_mul_f32 v[44:45], v[96:97], v[44:45] op_sel_hi:[0,1]\nv_pk_mul_f32 v[46:47], v[96:97], v[46:47] op_sel_hi:[0,1]\ns_mov_b64 vcc, %[no2]\n" \
  "v_cndmask_b32_dpp v40, v150, v148, vcc quad_perm:[1,0,3,2] row_mask:0xf bank_mask:0xf\n" \
  "v_cndmask_b32_dpp v41, v151, v149, vcc quad_perm:[1,0,3,2] row_mask:0xf bank_mask:0xf\ns_mov_b64 vcc, %[ne2]\n" \
  "v_cndmask_b32_dpp v42, v148, v150, vcc quad_perm:[1,0,3,2] row_mask:0xf bank_mask:0xf\n" \
  "v_cndmask_b32_dpp v43, v149, v151, vcc quad_perm:[1,0,3,2] row_mask:0xf bank_mask:0xf\n" \
  "buffer_store_dwordx4 v[40:43], v166, %[ro], %[so1] offen sc1\nv_pk_mul_f32 v[60:61], v[98:99], v[60:61] op_sel_hi:[0,1]\n" \
  "v_pk_mul_f32 v[62:63], v[98:99], v[62:63] op_sel_hi:[0,1]\nv_cvt_pk_bf16_f32 v152, v44, v45\nv_cvt_pk_bf16_f32 v153, v46, v47\n" \
  "v_cvt_pk_bf16_f32 v154, v60, v61\nv_cvt_pk_bf16_f32 v155, v62, v63\ns_nop 1\nv_permlane16_swap_b32 v152, v154\nv_permlane16_swap_b32 v153, v155\n" \
  "s_nop 1\ns_mov_b64 vcc, %[no3]\nv_cndmask_b32_dpp v44, v154, v152, vcc quad_perm:[1,0,3,2] row_mask:0xf bank_mask:0xf\n" \
  "v_cndmask_b32_dpp v45, v155, v153, vcc quad_perm:[1,0,3,2] row_mask:0xf bank_mask:0xf\ns_mov_b64 vcc, %[ne3]\n" \
  "v_cndmask_b32_dpp v46, v152, v154, vcc quad_perm:[1,0,3,2] row_mask:0xf bank_mask:0xf\n" \
  "v_cndmask_b32_dpp v47, v153, v155, vcc quad_perm:[1,0,3,2] row_mask:0xf bank_mask:0xf\n" \
  "buffer_store_dwordx4 v[44:47], v167, %[ro], %[so1] offen sc1\n"
#define K1_ASM_V \
  "v_mov_b64 v[0:1], 0\nv_mov_b64 v[2:3], 0\nv_mov_b64 v[4:5], 0\nv_mov_b64 v[6:7], 0\nv_mov_b64 v[8:9], 0\nv_mov_b64 v[10:11], 0\n" \
  "v_mov_b64 v[12:13], 0\nv_mov_b64 v[14:15], 0\nv_mov_b64 v[16:17], 0\nv_mov_b64 v[18:19], 0\nv_mov_b64 v[20:21], 0\nv_mov_b64 v[22:23], 0\n" \
  "v_mov_b64 v[24:25], 0\nv_mov_b64 v[26:27], 0\nv_mov_b64 v[28:29], 0\nv_mov_b64 v[30:31], 0\nv_mov_b64 v[32:33], 0\nv_mov_b64 v[34:35], 0\n" \
  "v_mov_b64 v[36:37], 0\nv_mov_b64 v[38:39], 0\nv_mov_b64 v[40:41], 0\nv_mov_b64 v[42:43], 0\nv_mov_b64 v[44:45], 0\nv_mov_b64 v[46:47], 0\n" \
  "v_mov_b64 v[48:49], 0\nv_mov_b64 v[50:51], 0\nv_mov_b64 v[52:53], 0\nv_mov_b64 v[54:55], 0\nv_mov_b64 v[56:57], 0\nv_mov_b64 v[58:59], 0\n" \
  "v_mov_b64 v[60:61], 0\nv_mov_b64 v[62:63], 0\nv_mov_b64 v[64:65], 0\nv_mov_b64 v[66:67], 0\nv_mov_b64 v[68:69], 0\nv_mov_b64 v[70:71], 0\n" \
  "v_mov_b64 v[72:73], 0\nv_mov_b64 v[74:75], 0\nv_mov_b64 v[76:77], 0\nv_mov_b64 v[78:79], 0\nv_mov_b64 v[80:81], 0\nv_mov_b64 v[82:83], 0\n" \
  "v_mov_b64 v[84:85], 0\nv_mov_b64 v[86:87], 0\nv_mov_b32 v88, 0x3f803f80\nv_mov_b32 v89, 0x3f803f80\nv_mov_b32 v90, 0x3f803f80\n" \
  "v_mov_b32 v91, 0x3f803f80\ns_barrier\ns_barrier\nds_read_b128 v[92:95], v156 offset:0\nds_read_b64_tr_b16 v[108:109], v160 offset:0\n" \
  "ds_read_b64_tr_b16 v[110:111], v160 offset:1024\nds_read_b64_tr_b16 v[112:113], v161 offset:0\nds_read_b64_tr_b16 v[114:115], v161 offset:1024\n" \
  "ds_read_b64_tr_b16 v[116:117], v162 offset:0\nds_read_b64_tr_b16 v[118:119], v162 offset:1024\nds_read_b64_tr_b16 v[120:121], v163 offset:0\n" \
  "ds_read_b64_tr_b16 v[122:123], v163 offset:1024\nds_read_b128 v[96:99], v156 offset:2048\nds_read_b128 v[100:103], v157 offset:0\n" \
  "ds_read_b128 v[104:107], v157 offset:2048\n.Lk1v_%=:\ns_waitcnt lgkmcnt(9)\nv_mfma_f32_16x16x32_bf16 v[0:3], v[92:95], v[108:111], v[0:3]\n" \
  "s_waitcnt lgkmcnt(7)\nv_mfma_f32_16x16x32_bf16 v[4:7], v[92:95], v[112:115], v[4:7]\ns_waitcnt lgkmcnt(5)\n" \
  "v_mfma_f32_16x16x32_bf16 v[8:11], v[92:95], v[116:119], v[8:11]\ns_waitcnt lgkmcnt(3)\n" \
  "v_mfma_f32_16x16x32_bf16 v[12:15], v[92:95], v[120:123], v[12:15]\ns_waitcnt lgkmcnt(0)\nds_read_b128 v[124:127], v158 offset:0\n" \
  "v_mfma_f32_16x16x32_bf16 v[16:19], v[96:99], v[108:111], v[16:19]\nds_read_b64_tr_b16 v[140:141], v160 offset:8192\n" \
  "v_mfma_f32_16x16x32_bf16 v[20:23], v[96:99], v[112:115], v[20:23]\nds_read_b64_tr_b16 v[142:143], v160 offset:9216\n" \
  "v_mfma_f32_16x16x32_bf16 v[24:27], v[96:99], v[116:119], v[24:27]\nds_read_b64_tr_b16 v[144:145], v161 offset:8192\n" \
  "v_mfma_f32_16x16x32_bf16 v[28:31], v[96:99], v[120:123], v[28:31]\nds_read_b64_tr_b16 v[146:147], v161 offset:9216\n" \
  "v_mfma_f32_16x16x32_bf16 v[32:35], v[100:103], v[108:111], v[32:35]\nds_read_b64_tr_b16 v[148:149], v162 offset:8192\n" \
  "v_mfma_f32_16x16x32_bf16 v[36:39], v[100:103], v[112:115], v[36:39]\nds_read_b64_tr_b16 v[150:151], v162 offset:9216\n" \
  "v_mfma_f32_16x16x32_bf16 v[40:43], v[100:103], v[116:119], v[40:43]\nds_read_b64_tr_b16 v[152:153], v163 offset:8192\n" \
  "v_mfma_f32_16x16x32_bf16 v[44:47], v[100:103], v[120:123], v[44:47]\nds_read_b64_tr_b16 v[154:155], v163 offset:9216\n" \
  "v_mfma_f32_16x16x32_bf16 v[48:51], v[104:107], v[108:111], v[48:51]\nds_read_b128 v[128:131], v158 offset:2048\n" \
  "v_mfma_f32_16x16x32_bf16 v[52:55], v[104:107], v[112:115], v[52:55]\nds_read_b128 v[132:135], v159 offset:0\n" \
  "v_mfma_f32_16x16x32_bf16 v[56:59], v[104:107], v[116:119], v[56:59]\nds_read_b128 v[136:139], v159 offset:2048\n" \
  "v_mfma_f32_16x16x32_bf16 v[60:63], v[104:107], v[120:123], v[60:63]\nv_mfma_f32_16x16x32_bf16 v[64:67], v[92:95], v[92:95], v[64:67]\n" \
  "v_mfma_f32_16x16x32_bf16 v[68:71], v[96:99], v[96:99], v[68:71]\nv_mfma_f32_16x16x32_bf16 v[72:75], v[88:91], v[92:95], v[72:75]\n" \
  "v_mfma_f32_16x16x32_bf16 v[76:79], v[88:91], v[96:99], v[76:79]\nv_mfma_f32_16x16x32_bf16 v[80:83], v[88:91], v[108:111], v[80:83]\n" \
  "v_mfma_f32_16x16x32_bf16 v[84:87], v[88:91], v[112:115], v[84:87]\ns_waitcnt lgkmcnt(9)\n" \
  "v_mfma_f32_16x16x32_bf16 v[0:3], v[124:127], v[140:143], v[0:3]\ns_waitcnt lgkmcnt(7)\n" \
  "v_mfma_f32_16x16x32_bf16 v[4:7], v[124:127], v[144:147], v[4:7]\ns_waitcnt lgkmcnt(5)\n" \
  "v_mfma_f32_16x16x32_bf16 v[8:11], v[124:127], v[148:151], v[8:11]\ns_waitcnt lgkmcnt(3)\n" \
  "v_mfma_f32_16x16x32_bf16 v[12:15], v[124:127], v[152:155], v[12:15]\ns_waitcnt lgkmcnt(0)\ns_barrier\nds_read_b128 v[92:95], v156 offset:32768\n" \
  "v_mfma_f32_16x16x32_bf16 v[16:19], v[128:131], v[140:143], v[16:19]\nds_read_b64_tr_b16 v[108:109], v160 offset:32768\n" \
  "v_mfma_f32_16x16x32_bf16 v[20:23], v[128:131], v[144:147], v[20:23]\nds_read_b64_tr_b16 v[110:111], v160 offset:33792\n" \
  "v_mfma_f32_16x16x32_bf16 v[24:27], v[128:131], v[148:151], v[24:27]\nds_read_b64_tr_b16 v[112:113], v161 offset:32768\n" \
  "v_mfma_f32_16x16x32_bf16 v[28:31], v[128:131], v[152:155], v[28:31]\nds_read_b64_tr_b16 v[114:115], v161 offset:33792\n" \
  "v_mfma_f32_16x16x32_bf16 v[32:35], v[132:135], v[140:143], v[32:35]\nds_read_b64_tr_b16 v[116:117], v162 offset:32768\n" \
  "v_mfma_f32_16x16x32_bf16 v[36:39], v[132:135], v[144:147], v[36:39]\nds_read_b64_tr_b16 v[118:119], v162 offset:33792\n" \
  "v_mfma_f32_16x16x32_bf16 v[40:43], v[132:135], v[148:151], v[40:43]\nds_read_b64_tr_b16 v[120:121], v163 offset:32768\n" \
  "v_mfma_f32_16x16x32_bf16 v[44:47], v[132:135], v[152:155], v[44:47]\nds_read_b64_tr_b16 v[122:123], v163 offset:33792\n" \
  "v_mfma_f32_16x16x32_bf16 v[48:51], v[136:139], v[140:143], v[48:51]\nds_read_b128 v[96:99], v156 offset:34816\n" \
  "v_mfma_f32_16x16x32_bf16 v[52:55], v[136:139], v[144:147], v[52:55]\nds_read_b128 v[100:103], v157 offset:32768\n" \
  "v_mfma_f32_16x16x32_bf16 v[56:59], v[136:139], v[148:151], v[56:59]\nds_read_b128 v[104:107], v157 offset:34816\n" \
  "v_mfma_f32_16x16x32_bf16 v[60:63], v[136:139], v[152:155], v[60:63]\nv_mfma_f32_16x16x32_bf16 v[64:67], v[124:127], v[124:127], v[64:67]\n" \
  "v_mfma_f32_16x16x32_bf16 v[68:71], v[128:131], v[128:131], v[68:71]\nv_mfma_f32_16x16x32_bf16 v[72:75], v[88:91], v[124:127], v[72:75]\n" \
  "v_mfma_f32_16x16x32_bf16 v[76:79], v[88:91], v[128:131], v[76:79]\nv_mfma_f32_16x16x32_bf16 v[80:83], v[88:91], v[140:143], v[80:83]\n" \
  "v_mfma_f32_16x16x32_bf16 v[84:87], v[88:91], v[144:147], v[84:87]\ns_waitcnt lgkmcnt(9)\n" \
  "v_mfma_f32_16x16x32_bf16 v[0:3], v[92:95], v[108:111], v[0:3]\ns_waitcnt lgkmcnt(7)\n" \
  "v_mfma_f32_16x16x32_bf16 v[4:7], v[92:95], v[112:115], v[4:7]\ns_waitcnt lgkmcnt(5)\n" \
  "v_mfma_f32_16x16x32_bf16 v[8:11], v[92:95], v[116:119], v[8:11]\ns_waitcnt lgkmcnt(3)\n" \
  "v_mfma_f32_16x16x32_bf16 v[12:15], v[92:95], v[120:123], v[12:15]\ns_waitcnt lgkmcnt(0)\nds_read_b128 v[124:127], v158 offset:32768\n" \
  "v_mfma_f32_16x16x32_bf16 v[16:19], v[96:99], v[108:111], v[16:19]\nds_read_b64_tr_b16 v[140:141], v160 offset:40960\n" \
  "v_mfma_f32_16x16x32_bf16 v[20:23], v[96:99], v[112:115], v[20:23]\nds_read_b64_tr_b16 v[142:143], v160 offset:41984\n" \
  "v_mfma_f32_16x16x32_bf16 v[24:27], v[96:99], v[116:119], v[24:27]\nds_read_b64_tr_b16 v[144:145], v161 offset:40960\n" \
  "v_mfma_f32_16x16x32_bf16 v[28:31], v[96:99], v[120:123], v[28:31]\nds_read_b64_tr_b16 v[146:147], v161 offset:41984\n" \
  "v_mfma_f32_16x16x32_bf16 v[32:35], v[100:103], v[108:111], v[32:35]\nds_read_b64_tr_b16 v[148:149], v162 offset:40960\n" \
  "v_mfma_f32_16x16x32_bf16 v[36:39], v[100:103], v[112:115], v[36:39]\nds_read_b64_tr_b16 v[150:151], v162 offset:41984\n" \
  "v_mfma_f32_16x16x32_bf16 v[40:43], v[100:103], v[116:119], v[40:43]\nds_read_b64_tr_b16 v[152:153], v163 offset:40960\n" \
  "v_mfma_f32_16x16x32_bf16 v[44:47], v[100:103], v[120:123], v[44:47]\nds_read_b64_tr_b16 v[154:155], v163 offset:41984\n" \
  "v_mfma_f32_16x16x32_bf16 v[48:51], v[104:107], v[108:111], v[48:51]\nds_read_b128 v[128:131], v158 offset:34816\n" \
  "v_mfma_f32_16x16x32_bf16 v[52:55], v[104:107], v[112:115], v[52:55]\nds_read_b128 v[132:135], v159 offset:32768\n" \
  "v_mfma_f32_16x16x32_bf16 v[56:59], v[104:107], v[116:119], v[56:59]\nds_read_b128 v[136:139], v159 offset:34816\n" \
  "v_mfma_f32_16x16x32_bf16 v[60:63], v[104:107], v[120:123], v[60:63]\nv_xor_b32 v156, 0x10000, v156\nv_xor_b32 v157, 0x10000, v157\n" \
  "v_mfma_f32_16x16x32_bf16 v[64:67], v[92:95], v[92:95], v[64:67]\nv_xor_b32 v158, 0x10000, v158\nv_xor_b32 v159, 0x10000, v159\n" \
  "v_mfma_f32_16x16x32_bf16 v[68:71], v[96:99], v[96:99], v[68:71]\nv_xor_b32 v160, 0x10000, v160\nv_xor_b32 v161, 0x10000, v161\n" \
  "v_mfma_f32_16x16x32_bf16 v[72:75], v[88:91], v[92:95], v[72:75]\nv_xor_b32 v162, 0x10000, v162\nv_xor_b32 v163, 0x10000, v163\n" \
  "v_mfma_f32_16x16x32_bf16 v[76:79], v[88:91], v[96:99], v[76:79]\nv_mfma_f32_16x16x32_bf16 v[80:83], v[88:91], v[108:111], v[80:83]\n" \
  "v_mfma_f32_16x16x32_bf16 v[84:87], v[88:91], v[112:115], v[84:87]\ns_waitcnt lgkmcnt(9)\n" \
  "v_mfma_f32_16x16x32_bf16 v[0:3], v[124:127], v[140:143], v[0:3]\ns_waitcnt lgkmcnt(7)\n" \
  "v_mfma_f32_16x16x32_bf16 v[4:7], v[124:127], v[144:147], v[4:7]\ns_waitcnt lgkmcnt(5)\n" \
  "v_mfma_f32_16x16x32_bf16 v[8:11], v[124:127], v[148:151], v[8:11]\ns_waitcnt lgkmcnt(3)\n" \
  "v_mfma_f32_16x16x32_bf16 v[12:15], v[124:127], v[152:155], v[12:15]\ns_waitcnt lgkmcnt(0)\ns_barrier\nds_read_b128 v[92:95], v156 offset:0\n" \
  "v_mfma_f32_16x16x32_bf16 v[16:19], v[128:131], v[140:143], v[16:19]\nds_read_b64_tr_b16 v[108:109], v160 offset:0\n" \
  "v_mfma_f32_16x16x32_bf16 v[20:23], v[128:131], v[144:147], v[20:23]\nds_read_b64_tr_b16 v[110:111], v160 offset:1024\n" \
  "v_mfma_f32_16x16x32_bf16 v[24:27], v[128:131], v[148:151], v[24:27]\nds_read_b64_tr_b16 v[112:113], v161 offset:0\n" \
  "v_mfma_f32_16x16x32_bf16 v[28:31], v[128:131], v[152:155], v[28:31]\nds_read_b64_tr_b16 v[114:115], v161 offset:1024\n" \
  "v_mfma_f32_16x16x32_bf16 v[32:35], v[132:135], v[140:143], v[32:35]\nds_read_b64_tr_b16 v[116:117], v162 offset:0\n" \
  "v_mfma_f32_16x16x32_bf16 v[36:39], v[132:135], v[144:147], v[36:39]\nds_read_b64_tr_b16 v[118:119], v162 offset:1024\n" \
  "v_mfma_f32_16x16x32_bf16 v[40:43], v[132:135], v[148:151], v[40:43]\nds_read_b64_tr_b16 v[120:121], v163 offset:0\n" \
  "v_mfma_f32_16x16x32_bf16 v[44:47], v[132:135], v[152:155], v[44:47]\nds_read_b64_tr_b16 v[122:123], v163 offset:1024\n" \
  "v_mfma_f32_16x16x32_bf16 v[48:51], v[136:139], v[140:143], v[48:51]\nds_read_b128 v[96:99], v156 offset:2048\n" \
  "v_mfma_f32_16x16x32_bf16 v[52:55], v[136:139], v[144:147], v[52:55]\nds_read_b128 v[100:103], v157 offset:0\n" \
  "v_mfma_f32_16x16x32_bf16 v[56:59], v[136:139], v[148:151], v[56:59]\nds_read_b128 v[104:107], v157 offset:2048\n" \
  "v_mfma_f32_16x16x32_bf16 v[60:63], v[136:139], v[152:155], v[60:63]\nv_mfma_f32_16x16x32_bf16 v[64:67], v[124:127], v[124:127], v[64:67]\n" \
  "v_mfma_f32_16x16x32_bf16 v[68:71], v[128:131], v[128:131], v[68:71]\nv_mfma_f32_16x16x32_bf16 v[72:75], v[88:91], v[124:127], v[72:75]\n" \
  "v_mfma_f32_16x16x32_bf16 v[76:79], v[88:91], v[128:131], v[76:79]\nv_mfma_f32_16x16x32_bf16 v[80:83], v[88:91], v[140:143], v[80:83]\n" \
  "v_mfma_f32_16x16x32_bf16 v[84:87], v[88:91], v[144:147], v[84:87]\ns_add_u32 %[cnt], %[cnt], -1\ns_cbranch_scc1 .Lk1v_%=\ns_waitcnt lgkmcnt(9)\n" \
  "v_mfma_f32_16x16x32_bf16 v[0:3], v[92:95], v[108:111], v[0:3]\ns_waitcnt lgkmcnt(7)\n" \
  "v_mfma_f32_16x16x32_bf16 v[4:7], v[92:95], v[112:115], v[4:7]\ns_waitcnt lgkmcnt(5)\n" \
  "v_mfma_f32_16x16x32_bf16 v[8:11], v[92:95], v[116:119], v[8:11]\ns_waitcnt lgkmcnt(3)\n" \
  "v_mfma_f32_16x16x32_bf16 v[12:15], v[92:95], v[120:123], v[12:15]\ns_waitcnt lgkmcnt(0)\nds_read_b128 v[124:127], v158 offset:0\n" \
  "v_mfma_f32_16x16x32_bf16 v[16:19], v[96:99], v[108:111], v[16:19]\nds_read_b64_tr_b16 v[140:141], v160 offset:8192\n" \
  "v_mfma_f32_16x16x32_bf16 v[20:23], v[96:99], v[112:115], v[20:23]\nds_read_b64_tr_b16 v[142:143], v160 offset:9216\n" \
  "v_mfma_f32_16x16x32_bf16 v[24:27], v[96:99], v[116:119], v[24:27]\nds_read_b64_tr_b16 v[144:145], v161 offset:8192\n" \
  "v_mfma_f32_16x16x32_bf16 v[28:31], v[96:99], v[120:123], v[28:31]\nds_read_b64_tr_b16 v[146:147], v161 offset:9216\n" \
  "v_mfma_f32_16x16x32_bf16 v[32:35], v[100:103], v[108:111], v[32:35]\nds_read_b64_tr_b16 v[148:149], v162 offset:8192\n" \
  "v_mfma_f32_16x16x32_bf16 v[36:39], v[100:103], v[112:115], v[36:39]\nds_read_b64_tr_b16 v[150:151], v162 offset:9216\n" \
  "v_mfma_f32_16x16x32_bf16 v[40:43], v[100:103], v[116:119], v[40:43]\nds_read_b64_tr_b16 v[152:153], v163 offset:8192\n" \
  "v_mfma_f32_16x16x32_bf16 v[44:47], v[100:103], v[120:123], v[44:47]\nds_read_b64_tr_b16 v[154:155], v163 offset:9216\n" \
  "v_mfma_f32_16x16x32_bf16 v[48:51], v[104:107], v[108:111], v[48:51]\nds_read_b128 v[128:131], v158 offset:2048\n" \
  "v_mfma_f32_16x16x32_bf16 v[52:55], v[104:107], v[112:115], v[52:55]\nds_read_b128 v[132:135], v159 offset:0\n" \
  "v_mfma_f32_16x16x32_bf16 v[56:59], v[104:107], v[116:119], v[56:59]\nds_read_b128 v[136:139], v159 offset:2048\n" \
  "v_mfma_f32_16x16x32_bf16 v[60:63], v[104:107], v[120:123], v[60:63]\nv_mfma_f32_16x16x32_bf16 v[64:67], v[92:95], v[92:95], v[64:67]\n" \
  "v_mfma_f32_16x16x32_bf16 v[68:71], v[96:99], v[96:99], v[68:71]\nv_mfma_f32_16x16x32_bf16 v[72:75], v[88:91], v[92:95], v[72:75]\n" \
  "v_mfma_f32_16x16x32_bf16 v[76:79], v[88:91], v[96:99], v[76:79]\nv_mfma_f32_16x16x32_bf16 v[80:83], v[88:91], v[108:111], v[80:83]\n" \
  "v_mfma_f32_16x16x32_bf16 v[84:87], v[88:91], v[112:115], v[84:87]\ns_waitcnt lgkmcnt(9)\n" \
  "v_mfma_f32_16x16x32_bf16 v[0:3], v[124:127], v[140:143], v[0:3]\ns_waitcnt lgkmcnt(7)\n" \
  "v_mfma_f32_16x16x32_bf16 v[4:7], v[124:127], v[144:147], v[4:7]\ns_waitcnt lgkmcnt(5)\n" \
  "v_mfma_f32_16x16x32_bf16 v[8:11], v[124:127], v[148:151], v[8:11]\ns_waitcnt lgkmcnt(3)\n" \
  "v_mfma_f32_16x16x32_bf16 v[12:15], v[124:127], v[152:155], v[12:15]\ns_waitcnt lgkmcnt(0)\ns_barrier\nds_read_b128 v[92:95], v156 offset:32768\n" \
  "v_mfma_f32_16x16x32_bf16 v[16:19], v[128:131], v[140:143], v[16:19]\nds_read_b64_tr_b16 v[108:109], v160 offset:32768\n" \
  "v_mfma_f32_16x16x32_bf16 v[20:23], v[128:131], v[144:147], v[20:23]\nds_read_b64_tr_b16 v[110:111], v160 offset:33792\n" \
  "v_mfma_f32_16x16x32_bf16 v[24:27], v[128:131], v[148:151], v[24:27]\nds_read_b64_tr_b16 v[112:113], v161 offset:32768\n" \
  "v_mfma_f32_16x16x32_bf16 v[28:31], v[128:131], v[152:155], v[28:31]\nds_read_b64_tr_b16 v[114:115], v161 offset:33792\n" \
  "v_mfma_f32_16x16x32_bf16 v[32:35], v[132:135], v[140:143], v[32:35]\nds_read_b64_tr_b16 v[116:117], v162 offset:32768\n" \
  "v_mfma_f32_16x16x32_bf16 v[36:39], v[132:135], v[144:147], v[36:39]\nds_read_b64_tr_b16 v[118:119], v162 offset:33792\n" \
  "v_mfma_f32_16x16x32_bf16 v[40:43], v[132:135], v[148:151], v[40:43]\nds_read_b64_tr_b16 v[120:121], v163 offset:32768\n" \
  "v_mfma_f32_16x16x32_bf16 v[44:47], v[132:135], v[152:155], v[44:47]\nds_read_b64_tr_b16 v[122:123], v163 offset:33792\n" \
  "v_mfma_f32_16x16x32_bf16 v[48:51], v[136:139], v[140:143], v[48:51]\nds_read_b128 v[96:99], v156 offset:34816\n" \
  "v_mfma_f32_16x16x32_bf16 v[52:55], v[136:139], v[144:147], v[52:55]\nds_read_b128 v[100:103], v157 offset:32768\n" \
  "v_mfma_f32_16x16x32_bf16 v[56:59], v[136:139], v[148:151], v[56:59]\nds_read_b128 v[104:107], v157 offset:34816\n" \
  "v_mfma_f32_16x16x32_bf16 v[60:63], v[136:139], v[152:155], v[60:63]\nv_mfma_f32_16x16x32_bf16 v[64:67], v[124:127], v[124:127], v[64:67]\n" \
  "v_mfma_f32_16x16x32_bf16 v[68:71], v[128:131], v[128:131], v[68:71]\nv_mfma_f32_16x16x32_bf16 v[72:75], v[88:91], v[124:127], v[72:75]\n" \
  "v_mfma_f32_16x16x32_bf16 v[76:79], v[88:91], v[128:131], v[76:79]\nv_mfma_f32_16x16x32_bf16 v[80:83], v[88:91], v[140:143], v[80:83]\n" \
  "v_mfma_f32_16x16x32_bf16 v[84:87], v[88:91], v[144:147], v[84:87]\ns_waitcnt lgkmcnt(9)\n" \
  "v_mfma_f32_16x16x32_bf16 v[0:3], v[92:95], v[108:111], v[0:3]\ns_waitcnt lgkmcnt(7)\n" \
  "v_mfma_f32_16x16x32_bf16 v[4:7], v[92:95], v[112:115], v[4:7]\ns_waitcnt lgkmcnt(5)\n" \
  "v_mfma_f32_16x16x32_bf16 v[8:11], v[92:95], v[116:119], v[8:11]\ns_waitcnt lgkmcnt(3)\n" \
  "v_mfma_f32_16x16x32_bf16 v[12:15], v[92:95], v[120:123], v[12:15]\ns_waitcnt lgkmcnt(0)\nds_read_b128 v[124:127], v158 offset:32768\n" \
  "v_mfma_f32_16x16x32_bf16 v[16:19], v[96:99], v[108:111], v[16:19]\nds_read_b64_tr_b16 v[140:141], v160 offset:40960\n" \
  "v_mfma_f32_16x16x32_bf16 v[20:23], v[96:99], v[112:115], v[20:23]\nds_read_b64_tr_b16 v[142:143], v160 offset:41984\n" \
  "v_mfma_f32_16x16x32_bf16 v[24:27], v[96:99], v[116:119], v[24:27]\nds_read_b64_tr_b16 v[144:145], v161 offset:40960\n" \
  "v_mfma_f32_16x16x32_bf16 v[28:31], v[96:99], v[120:123], v[28:31]\nds_read_b64_tr_b16 v[146:147], v161 offset:41984\n" \
  "v_mfma_f32_16x16x32_bf16 v[32:35], v[100:103], v[108:111], v[32:35]\nds_read_b64_tr_b16 v[148:149], v162 offset:40960\n" \
  "v_mfma_f32_16x16x32_bf16 v[36:39], v[100:103], v[112:115], v[36:39]\nds_read_b64_tr_b16 v[150:151], v162 offset:41984\n" \
  "v_mfma_f32_16x16x32_bf16 v[40:43], v[100:103], v[116:119], v[40:43]\nds_read_b64_tr_b16 v[152:153], v163 offset:40960\n" \
  "v_mfma_f32_16x16x32_bf16 v[44:47], v[100:103], v[120:123], v[44:47]\nds_read_b64_tr_b16 v[154:155], v163 offset:41984\n" \
  "v_mfma_f32_16x16x32_bf16 v[48:51], v[104:107], v[108:111], v[48:51]\nds_read_b128 v[128:131], v158 offset:34816\n" \
  "v_mfma_f32_16x16x32_bf16 v[52:55], v[104:107], v[112:115], v[52:55]\nds_read_b128 v[132:135], v159 offset:32768\n" \
  "v_mfma_f32_16x16x32_bf16 v[56:59], v[104:107], v[116:119], v[56:59]\nds_read_b128 v[136:139], v159 offset:34816\n" \
  "v_mfma_f32_16x16x32_bf16 v[60:63], v[104:107], v[120:123], v[60:63]\nv_mfma_f32_16x16x32_bf16 v[64:67], v[92:95], v[92:95], v[64:67]\n" \
  "v_mfma_f32_16x16x32_bf16 v[68:71], v[96:99], v[96:99], v[68:71]\nv_mfma_f32_16x16x32_bf16 v[72:75], v[88:91], v[92:95], v[72:75]\n" \
  "v_mfma_f32_16x16x32_bf16 v[76:79], v[88:91], v[96:99], v[76:79]\nv_mfma_f32_16x16x32_bf16 v[80:83], v[88:91], v[108:111], v[80:83]\n" \
  "v_mfma_f32_16x16x32_bf16 v[84:87], v[88:91], v[112:115], v[84:87]\ns_waitcnt lgkmcnt(0)\n" \
  "v_mfma_f32_16x16x32_bf16 v[64:67], v[124:127], v[124:127], v[64:67]\nv_mfma_f32_16x16x32_bf16 v[68:71], v[128:131], v[128:131], v[68:71]\n" \
  "v_mfma_f32_16x16x32_bf16 v[72:75], v[88:91], v[124:127], v[72:75]\nv_mfma_f32_16x16x32_bf16 v[76:79], v[88:91], v[128:131], v[76:79]\n" \
  "v_mfma_f32_16x16x32_bf16 v[80:83], v[88:91], v[140:143], v[80:83]\nv_mfma_f32_16x16x32_bf16 v[84:87], v[88:91], v[144:147], v[84:87]\n" \
  "v_mfma_f32_16x16x32_bf16 v[0:3], v[124:127], v[140:143], v[0:3]\nv_mfma_f32_16x16x32_bf16 v[4:7], v[124:127], v[144:147], v[4:7]\n" \
  "v_mfma_f32_16x16x32_bf16 v[8:11], v[124:127], v[148:151], v[8:11]\nv_cndmask_b32_e64 v178, v64, v65, %[m1]\n" \
  "v_cndmask_b32_e64 v179, v68, v69, %[m1]\nv_cndmask_b32_e64 v180, v66, v67, %[m1]\nv_cndmask_b32_e64 v181, v70, v71, %[m1]\n" \
  "v_mfma_f32_16x16x32_bf16 v[12:15], v[124:127], v[152:155], v[12:15]\nv_mul_f32 v183, 0xbacccccd, v72\nv_mul_f32 v185, 0xbacccccd, v76\n" \
  "v_cndmask_b32_e64 v178, v178, v180, %[m2]\nv_cndmask_b32_e64 v179, v179, v181, %[m2]\n" \
  "v_mfma_f32_16x16x32_bf16 v[16:19], v[128:131], v[140:143], v[16:19]\nv_mul_f32_e64 v180, -v183, v183\nv_mul_f32_e64 v181, -v185, v185\n" \
  "v_fmamk_f32 v178, v178, 0x3acccccd, v180\nv_fmamk_f32 v179, v179, 0x3acccccd, v181\n" \
  "v_mfma_f32_16x16x32_bf16 v[20:23], v[128:131], v[144:147], v[20:23]\nv_max_f32 v178, 0, v178\nv_max_f32 v179, 0, v179\n" \
  "v_add_f32 v178, 0x3727c5ac, v178\nv_add_f32 v179, 0x3727c5ac, v179\nv_mfma_f32_16x16x32_bf16 v[24:27], v[128:131], v[148:151], v[24:27]\n" \
  "v_rsq_f32 v178, v178\nv_rsq_f32 v179, v179\nv_mul_f32 v182, %[sc], v178\nv_mul_f32 v184, %[sc], v179\n" \
  "v_mfma_f32_16x16x32_bf16 v[28:31], v[128:131], v[152:155], v[28:31]\ns_mov_b64 exec, %[md]\nds_write_b32 v169, v80 offset:0\n" \
  "ds_write_b32 v169, v84 offset:64\nds_write_b32 v168, v182 offset:0\nds_write_b32 v168, v183 offset:512\nds_write_b32 v168, v184 offset:64\n" \
  "ds_write_b32 v168, v185 offset:576\ns_mov_b64 exec, -1\nv_mfma_f32_16x16x32_bf16 v[32:35], v[132:135], v[140:143], v[32:35]\n" \
  "v_mfma_f32_16x16x32_bf16 v[36:39], v[132:135], v[144:147], v[36:39]\ns_waitcnt vmcnt(0) lgkmcnt(0)\ns_barrier\nv_readfirstlane_b32 %[e], v187\n" \
  "ds_read_b32 v174, v172 offset:0\nds_read_b32 v175, v172 offset:64\nds_read_b32 v176, v173 offset:0\nds_read_b32 v177, v173 offset:64\n" \
  "ds_read_b128 v[108:111], v170 offset:512\nds_read_b128 v[92:95], v170 offset:0\nds_read_b128 v[112:115], v170 offset:576\n" \
  "ds_read_b128 v[96:99], v170 offset:64\nds_read_b128 v[116:119], v171 offset:512\nds_read_b128 v[100:103], v171 offset:0\n" \
  "ds_read_b128 v[120:123], v171 offset:576\nds_read_b128 v[104:107], v171 offset:64\n" \
  "v_mfma_f32_16x16x32_bf16 v[40:43], v[132:135], v[148:151], v[40:43]\nv_mfma_f32_16x16x32_bf16 v[44:47], v[132:135], v[152:155], v[44:47]\n" \
  "v_mfma_f32_16x16x32_bf16 v[48:51], v[136:139], v[140:143], v[48:51]\nv_mfma_f32_16x16x32_bf16 v[52:55], v[136:139], v[144:147], v[52:55]\n" \
  "v_mfma_f32_16x16x32_bf16 v[56:59], v[136:139], v[148:151], v[56:59]\nv_mfma_f32_16x16x32_bf16 v[60:63], v[136:139], v[152:155], v[60:63]\n" \
  "s_waitcnt lgkmcnt(4)\nv_pk_fma_f32 v[0:1], v[174:175], v[108:109], v[0:1] op_sel_hi:[0,1,1]\n" \
  "v_pk_fma_f32 v[2:3], v[174:175], v[110:111], v[2:3] op_sel_hi:[0,1,1]\nv_pk_fma_f32 v[16:17], v[174:175], v[112:113], v[16:17] op_sel_hi:[0,1,1]\n" \
  "v_pk_fma_f32 v[18:19], v[174:175], v[114:115], v[18:19] op_sel_hi:[0,1,1]\nv_pk_mul_f32 v[0:1], v[92:93], v[0:1]\n" \
  "v_pk_mul_f32 v[2:3], v[94:95], v[2:3]\nv_pk_mul_f32 v[16:17], v[96:97], v[16:17]\nv_pk_mul_f32 v[18:19], v[98:99], v[18:19]\n" \
  "v_cvt_pk_bf16_f32 v124, v0, v1\nv_cvt_pk_bf16_f32 v125, v2, v3\nv_cvt_pk_bf16_f32 v126, v16, v17\nv_cvt_pk_bf16_f32 v127, v18, v19\n" \
  "v_pk_fma_f32 v[4:5], v[174:175], v[108:109], v[4:5] op_sel:[1,0,0]\nv_pk_fma_f32 v[6:7], v[174:175], v[110:111], v[6:7] op_sel:[1,0,0]\n" \
  "v_permlane32_swap_b32 v124, v126\nv_permlane32_swap_b32 v125, v127\nv_pk_fma_f32 v[20:21], v[174:175], v[112:113], v[20:21] op_sel:[1,0,0]\n" \
  "v_pk_fma_f32 v[22:23], v[174:175], v[114:115], v[22:23] op_sel:[1,0,0]\nv_pk_mul_f32 v[4:5], v[92:93], v[4:5]\n" \
  "v_pk_mul_f32 v[6:7], v[94:95], v[6:7]\nbuffer_store_dwordx4 v[124:127], v164, %[ro], %[so0] offen sc1\nv_pk_mul_f32 v[20:21], v[96:97], v[20:21]\n" \
  "v_pk_mul_f32 v[22:23], v[98:99], v[22:23]\nv_cvt_pk_bf16_f32 v128, v4, v5\nv_cvt_pk_bf16_f32 v129, v6, v7\nv_cvt_pk_bf16_f32 v130, v20, v21\n" \
  "v_cvt_pk_bf16_f32 v131, v22, v23\nv_pk_fma_f32 v[8:9], v[176:177], v[108:109], v[8:9] op_sel_hi:[0,1,1]\n" \
  "v_pk_fma_f32 v[10:11], v[176:177], v[110:111], v[10:11] op_sel_hi:[0,1,1]\nv_permlane32_swap_b32 v128, v130\nv_permlane32_swap_b32 v129, v131\n" \
  "v_pk_fma_f32 v[24:25], v[176:177], v[112:113], v[24:25] op_sel_hi:[0,1,1]\n" \
  "v_pk_fma_f32 v[26:27], v[176:177], v[114:115], v[26:27] op_sel_hi:[0,1,1]\nv_pk_mul_f32 v[8:9], v[92:93], v[8:9]\n" \
  "v_pk_mul_f32 v[10:11], v[94:95], v[10:11]\nbuffer_store_dwordx4 v[128:131], v165, %[ro], %[so0] offen sc1\n" \
  "v_pk_mul_f32 v[24:25], v[96:97], v[24:25]\nv_pk_mul_f32 v[26:27], v[98:99], v[26:27]\nv_cvt_pk_bf16_f32 v132, v8, v9\n" \
  "v_cvt_pk_bf16_f32 v133, v10, v11\nv_cvt_pk_bf16_f32 v134, v24, v25\nv_cvt_pk_bf16_f32 v135, v26, v27\n" \
  "v_pk_fma_f32 v[12:13], v[176:177], v[108:109], v[12:13] op_sel:[1,0,0]\nv_pk_fma_f32 v[14:15], v[176:177], v[110:111], v[14:15] op_sel:[1,0,0]\n" \
  "v_permlane32_swap_b32 v132, v134\nv_permlane32_swap_b32 v133, v135\nv_pk_fma_f32 v[28:29], v[176:177], v[112:113], v[28:29] op_sel:[1,0,0]\n" \
  "v_pk_fma_f32 v[30:31], v[176:177], v[114:115], v[30:31] op_sel:[1,0,0]\nv_pk_mul_f32 v[12:13], v[92:93], v[12:13]\n" \
  "v_pk_mul_f32 v[14:15], v[94:95], v[14:15]\nbuffer_store_dwordx4 v[132:135], v166, %[ro], %[so0] offen sc1\n" \
  "v_pk_mul_f32 v[28:29], v[96:97], v[28:29]\nv_pk_mul_f32 v[30:31], v[98:99], v[30:31]\nv_cvt_pk_bf16_f32 v136, v12, v13\n" \
  "v_cvt_pk_bf16_f32 v137, v14, v15\nv_cvt_pk_bf16_f32 v138, v28, v29\nv_cvt_pk_bf16_f32 v139, v30, v31\ns_waitcnt lgkmcnt(0)\n" \
  "v_pk_fma_f32 v[32:33], v[174:175], v[116:117], v[32:33] op_sel_hi:[0,1,1]\n" \
  "v_pk_fma_f32 v[34:35], v[174:175], v[118:119], v[34:35] op_sel_hi:[0,1,1]\nv_permlane32_swap_b32 v136, v138\nv_permlane32_swap_b32 v137, v139\n" \
  "v_pk_fma_f32 v[48:49], v[174:175], v[120:121], v[48:49] op_sel_hi:[0,1,1]\n" \
  "v_pk_fma_f32 v[50:51], v[174:175], v[122:123], v[50:51] op_sel_hi:[0,1,1]\nv_pk_mul_f32 v[32:33], v[100:101], v[32:33]\n" \
  "v_pk_mul_f32 v[34:35], v[102:103], v[34:35]\nbuffer_store_dwordx4 v[136:139], v167, %[ro], %[so0] offen sc1\n" \
  "v_pk_mul_f32 v[48:49], v[104:105], v[48:49]\nv_pk_mul_f32 v[50:51], v[106:107], v[50:51]\nv_cvt_pk_bf16_f32 v140, v32, v33\n" \
  "v_cvt_pk_bf16_f32 v141, v34, v35\nv_cvt_pk_bf16_f32 v142, v48, v49\nv_cvt_pk_bf16_f32 v143, v50, v51\n" \
  "v_pk_fma_f32 v[36:37], v[174:175], v[116:117], v[36:37] op_sel:[1,0,0]\nv_pk_fma_f32 v[38:39], v[174:175], v[118:119], v[38:39] op_sel:[1,0,0]\n" \
  "v_permlane32_swap_b32 v140, v142\nv_permlane32_swap_b32 v141, v143\nv_pk_fma_f32 v[52:53], v[174:175], v[120:121], v[52:53] op_sel:[1,0,0]\n" \
  "v_pk_fma_f32 v[54:55], v[174:175], v[122:123], v[54:55] op_sel:[1,0,0]\nv_pk_mul_f32 v[36:37], v[100:101], v[36:37]\n" \
  "v_pk_mul_f32 v[38:39], v[102:103], v[38:39]\nbuffer_store_dwordx4 v[140:143], v164, %[ro], %[so1] offen sc1\n" \
  "v_pk_mul_f32 v[52:53], v[104:105], v[52:53]\nv_pk_mul_f32 v[54:55], v[106:107], v[54:55]\nv_cvt_pk_bf16_f32 v144, v36, v37\n" \
  "v_cvt_pk_bf16_f32 v145, v38, v39\nv_cvt_pk_bf16_f32 v146, v52, v53\nv_cvt_pk_bf16_f32 v147, v54, v55\n" \
  "v_pk_fma_f32 v[40:41], v[176:177], v[116:117], v[40:41] op_sel_hi:[0,1,1]\n" \
  "v_pk_fma_f32 v[42:43], v[176:177], v[118:119], v[42:43] op_sel_hi:[0,1,1]\nv_permlane32_swap_b32 v144, v146\nv_permlane32_swap_b32 v145, v147\n" \
  "v_pk_fma_f32 v[56:57], v[176:177], v[120:121], v[56:57] op_sel_hi:[0,1,1]\n" \
  "v_pk_fma_f32 v[58:59], v[176:177], v[122:123], v[58:59] op_sel_hi:[0,1,1]\nv_pk_mul_f32 v[40:41], v[100:101], v[40:41]\n" \
  "v_pk_mul_f32 v[42:43], v[102:103], v[42:43]\nbuffer_store_dwordx4 v[144:147], v165, %[ro], %[so1] offen sc1\n" \
  "v_pk_mul_f32 v[56:57], v[104:105], v[56:57]\nv_pk_mul_f32 v[58:59], v[106:107], v[58:59]\nv_cvt_pk_bf16_f32 v148, v40, v41\n" \
  "v_cvt_pk_bf16_f32 v149, v42, v43\nv_cvt_pk_bf16_f32 v150, v56, v57\nv_cvt_pk_bf16_f32 v151, v58, v59\n" \
  "v_pk_fma_f32 v[44:45], v[176:177], v[116:117], v[44:45] op_sel:[1,0,0]\nv_pk_fma_f32 v[46:47], v[176:177], v[118:119], v[46:47] op_sel:[1,0,0]\n" \
  "v_permlane32_swap_b32 v148, v150\nv_permlane32_swap_b32 v149, v151\nv_pk_fma_f32 v[60:61], v[176:177], v[120:121], v[60:61] op_sel:[1,0,0]\n" \
  "v_pk_fma_f32 v[62:63], v[176:177], v[122:123], v[62:63] op_sel:[1,0,0]\nv_pk_mul_f32 v[44:45], v[100:101], v[44:45]\n" \
  "v_pk_mul_f32 v[46:47], v[102:103], v[46:47]\nbuffer_store_dwordx4 v[148:151], v166, %[ro], %[so1] offen sc1\n" \
  "v_pk_mul_f32 v[60:61], v[104:105], v[60:61]\nv_pk_mul_f32 v[62:63], v[106:107], v[62:63]\nv_cvt_pk_bf16_f32 v152, v44, v45\n" \
  "v_cvt_pk_bf16_f32 v153, v46, v47\nv_cvt_pk_bf16_f32 v154, v60, v61\nv_cvt_pk_bf16_f32 v155, v62, v63\ns_nop 1\nv_permlane32_swap_b32 v152, v154\n" \
  "v_permlane32_swap_b32 v153, v155\ns_nop 1\nbuffer_store_dwordx4 v[152:155], v167, %[ro], %[so1] offen sc1\n"
#define K1_CLOBBER \
  "memory", "scc", "vcc", "v0", "v1", "v2", "v3", "v4", "v5", "v6", "v7", "v8", "v9", "v10", "v11", "v12", "v13", "v14", "v15", "v16", "v17", "v18", "v19", \
  "v20", "v21", "v22", "v23", "v24", "v25", "v26", "v27", "v28", "v29", "v30", "v31", "v32", "v33", "v34", "v35", "v36", "v37", "v38", "v39", \
  "v40", "v41", "v42", "v43", "v44", "v45", "v46", "v47", "v48", "v49", "v50", "v51", "v52", "v53", "v54", "v55", "v56", "v57", "v58", "v59", \
  "v60", "v61", "v62", "v63", "v64", "v65", "v66", "v67", "v68", "v69", "v70", "v71", "v72", "v73", "v74", "v75", "v76", "v77", "v78", "v79", \
  "v80", "v81", "v82", "v83", "v84", "v85", "v86", "v87", "v88", "v89", "v90", "v91", "v92", "v93", "v94", "v95", "v96", "v97", "v98", "v99", \
  "v100", "v101", "v102", "v103", "v104", "v105", "v106", "v107", "v108", "v109", "v110", "v111", "v112", "v113", "v114", "v115", "v116", \
  "v117", "v118", "v119", "v120", "v121", "v122", "v123", "v124", "v125", "v126", "v127", "v128", "v129", "v130", "v131", "v132", "v133", \
  "v134", "v135", "v136", "v137", "v138", "v139", "v140", "v141", "v142", "v143", "v144", "v145", "v146", "v147", "v148", "v149", "v150", \
  "v151", "v152", "v153", "v154", "v155", "v174", "v175", "v176", "v177", "v178", "v179", "v180", "v181", "v182", "v183", "v184", "v185"

template <bool QK>
__device__ __forceinline__ unsigned k1_consumer(char* __restrict__ rec, int rt, int ct, int wave, int lane, unsigned p0, unsigned p1) {
  const int wm = wave >> 1, wn = wave & 1, c = lane & 15, g = lane >> 4;
  u32x4 aA, aB, vo, rd;
  u32x2 wr;
  unsigned long long ne[4], no[4];
  const unsigned T = 131072;
#pragma unroll
  for (int t = 0; t < 4; ++t) {
    aA[t] = (wm * 64 + ((t & 1) ^ wn) * 32 + c) * 128 + ((((t >> 1) * 4 + g) ^ (c >> 1)) << 4);
    const int j = wn * 4 + (t ^ (2 * wm)), q = (lane >> 2) & 3;
    aB[t] = 16384 + (8 * g + q) * 256 + ((j ^ (q | ((g & 1) << 2))) << 5) + (lane & 3) * 8;
    if (QK) {
      const int P = ct * 16 + 2 * j + (lane >> 5), hp = (P * 205) >> 10, u = P - 5 * hp, r = lane & 31, od = u == 2 ? lane & 1 : u > 2;
      vo[t] = (2 * hp + od) * REC_HEAD + (u == 2 ? 1024 + (r & 30) * 8 : (u - 3 * od) * 512 + r * 16);
      ne[t] = ~__builtin_amdgcn_ballot_w64(u == 2 && !(lane & 1));
      no[t] = ~__builtin_amdgcn_ballot_w64(u == 2 && (lane & 1));
    } else {
      const int gc = (ct - 10) * 128 + j * 16 + c, hd = (gc * 205) >> 12, d = gc - hd * 20 - (hd & 1) * 4;
      vo[t] = (32 + hd) * REC_HEAD + REC_V + (lane >> 5) * REC_VS + (g & 1) * REC_VH + (d < 0 ? d + 20 : d) * 16;
    }
  }
  wr[0] = T + (wm * 64 + wn * 32 + c) * (QK ? 8 : 4);
  wr[1] = T + 1024 + (wn * 64 + wm * 32 + c) * 4;
#pragma unroll
  for (int h = 0; h < 2; ++h) {
    rd[h] = QK ? T + (wm * 64 + (h ^ wn) * 32 + c) * 8 : T + (wm * 64 + (h ^ wn) * 32 + 4 * g) * 4;
    rd[2 + h] = T + 1024 + (wn * 64 + (h ^ wm) * 32 + (QK ? 4 * g : c)) * 4;
  }
  const i32x4 ro = mk_srd(rec, 64 * REC_HEAD);
  const int tile0 = rt * 4 + wm * 2;
  const unsigned so0 = (tile0 + wn) * REC, so1 = (tile0 + (wn ^ 1)) * REC;
  unsigned cnt = 3, e;
  if (QK) {
    const float sc = ct < 5 ? 0.22360679774997896f * 1.4426950408889634f : 1.f;
    asm volatile(K1_ASM_QK
                 : "+{v[156:159]}"(aA), "+{v[160:163]}"(aB), [cnt] "+s"(cnt), [e] "=&s"(e)
                 : "{v[164:167]}"(vo), "{v[168:169]}"(wr), "{v[170:173]}"(rd), [ro] "s"(ro), [so0] "s"(so0), [so1] "s"(so1), [sc] "s"(sc),
                   [m1] "s"(0xaaaaaaaaaaaaaaaaull), [m2] "s"(0xccccccccccccccccull), [md] "s"(0xf0000f0000f0000full),
                   [ne0] "s"(ne[0]), [ne1] "s"(ne[1]), [ne2] "s"(ne[2]), [ne3] "s"(ne[3]),
                   [no0] "s"(no[0]), [no1] "s"(no[1]), [no2] "s"(no[2]), [no3] "s"(no[3]), "{v186}"(p0), "{v187}"(p1)
                 : K1_CLOBBER);
  } else {
    asm volatile(K1_ASM_V
                 : "+{v[156:159]}"(aA), "+{v[160:163]}"(aB), [cnt] "+s"(cnt), [e] "=&s"(e)
                 : "{v[164:167]}"(vo), "{v[168:169]}"(wr), "{v[170:173]}"(rd), [ro] "s"(ro), [so0] "s"(so0), [so1] "s"(so1), [sc] "s"(1.f),
                   [m1] "s"(0xaaaaaaaaaaaaaaaaull), [m2] "s"(0xccccccccccccccccull), [md] "s"(0xf0000f0000f0000full), "{v186}"(p0), "{v187}"(p1)
                 : K1_CLOBBER);
  }


  return e;
}
constexpr bool chk_div() {
  for (int i = 0; i < 640; ++i) if (((i * 205) >> 12) != i / 20) return false;
  for (int i = 0; i < 160; ++i) if (((i * 205) >> 10) != i / 5) return false;
  for (int i = 0; i < 192; ++i) if (((i * 171) >> 11) != i / 12) return false;
  for (int i = 0; i < 80; ++i) if (((i * 205) >> 11) != i / 10) return false;
  return true;
}
static_assert(chk_div(), "magic division");

__device__ __forceinline__ i32x4 flag_srd(char* fl) {
  asm volatile("" : "+s"(fl));
  return mk_srd(fl, 8192 * FS);
}
#define EPOCH(e) asm volatile("buffer_load_dword v1, off, %1, %2 sc1\n\ts_waitcnt vmcnt(0)\n\tv_readfirstlane_b32 %0, v1" : "=s"(e) : "s"(flag_srd(fl)), "s"(F_EP) : "v1")
__device__ __forceinline__ unsigned k1_qkv(const u16* __restrict__ x, const u16* __restrict__ w, char* fl, int wraw) {
  const int b = blockIdx.x, xcd = b & 7, bi = b >> 3, id = (xcd & 1) * 30 + bi;
  const int rt = (xcd >> 1) * 4 + (id & 3), ct = id >> 2;
  const int lane = threadIdx.x & 63;
  unsigned e;

  if (bi >= 30) {
    unsigned long long pc; unsigned len, t;
    unsigned q0 = 0, q1 = 0;
    if (wraw >= 4) {
      const unsigned i = (wraw - 4) * 64 + lane;
      const i32x4 sq = bi & 1 ? mk_srd(x, S_ * D_ * 2) : mk_srd(w, D_ * 1920 * 2);
      const unsigned v = bi & 1 ? i * 1280 : (i >> 4) * (4 * 3840) + (i & 15) * 128, o = bi & 1 ? xcd * 256 * 1280 : (xcd >> 1) * 3840 + (xcd & 1) * 7 * 256, d = bi & 1 ? 128 : 64 * 3840;
      asm volatile("buffer_load_dword %0, %1, %2, %3 offen" : "=&v"(q0) : "v"(v), "s"(sq), "s"(o + d));
      asm volatile("buffer_load_dword %0, %1, %2, %3 offen" : "=&v"(q1) : "v"(v), "s"(sq), "s"(o + 2 * d));
    }
    asm volatile("s_getpc_b64 %0\n.Lpf_here:\n\ts_mov_b32 %1, k_end-k_all\n\ts_mov_b32 %2, .Lpf_here-k_all" : "=s"(pc), "=s"(len), "=s"(t));
    asm volatile("buffer_load_dword %0, %1, %2, 0 offen\n\ts_waitcnt vmcnt(0)"
                 : "=v"(t) : "v"((threadIdx.x * 2 + bi - 30) * 128), "s"(mk_srd((const void*)(pc - t), len)), "v"(q0), "v"(q1));
#define FW 1
    if (FW && wraw == 0) asm volatile("buffer_load_dword %0, %1, %2, 0 offen sc1" : "=v"(t) : "v"(((b - 240) * 64 + lane) * 128), "s"(flag_srd(fl)));
#define CW 1
    if (CW && wraw == 1) asm volatile("buffer_load_dword %0, %1, %2, 0 offen sc1" : "=v"(t) : "v"(((b - 240) * 64 + lane) * 128), "s"(mk_srd(wsg + WS_CS, 16 * 8192)));
    EPOCH(e);
    asm volatile("" ::"v"(t));

    return e;
  }
  if (wraw >= 4) {
    asm volatile("" : "+s"(x), "+s"(w));
    const i32x4 sx = mk_srd(x, S_ * D_ * 2), sw = mk_srd(w, D_ * 1920 * 2);
    const int wv = wraw - 4;
    const unsigned i = bi * 64 + lane + 128, wx = (xcd & 1) * 7 * 256;
    unsigned p0 = 0;
#define TOUCH1(v, srd, so) asm volatile("buffer_load_dword %0, %1, %2, %3 offen" : "={v186}"(p0) : "v"(v), "s"(srd), "s"(so))
    if (wv == 0) TOUCH1((i >> 4) * (4 * 3840) + (i & 15) * 128, sw, (128 + (xcd >> 1)) * 3840 + wx);
    else if (wv == 1) TOUCH1((i & 255) * 1280 + (i >> 8) * 128, sx, xcd * 256 * 1280 + 256);
    unsigned p1;
    asm volatile("buffer_load_dword %0, off, %1, %2 sc1" : "={v187}"(p1) : "s"(flag_srd(fl)), "s"(F_EP));
    return ct < 10 ? k1_consumer<true>(wsg + WS_REC, rt, ct, wraw - 4, lane, p0, p1) : k1_consumer<false>(wsg + WS_REC, rt, ct, wraw - 4, lane, p0, p1);
  }
  const int pw = wraw;
  unsigned vA[2], vB[2];
#pragma unroll
  for (int i = 0; i < 2; ++i) {
    vA[i] = (i * 8 + (lane >> 3)) * 1280 + (((lane & 7) ^ (4 * i + (lane >> 4))) << 4);
    vB[i] = (lane >> 4) * 3840 + (((lane & 15) ^ (((lane >> 4) | (i << 2)) << 1)) << 4);
  }
  const unsigned oA = (rt * 128 + pw * 32) * 1280, oB = ct * 256 + pw * 16 * 3840;
  asm volatile("" : "+s"(x), "+s"(w) : "v"(vA[0]), "v"(vA[1]), "v"(vB[0]), "v"(vB[1]), "s"(oA), "s"(oB), "s"(pw * 4096));
  const i32x4 sx = mk_srd(x, S_ * D_ * 2), sw = mk_srd(w, D_ * 1920 * 2);
  auto issue = [&](int s) {
#pragma unroll
    for (int i = 0; i < 4; ++i) {
      dma16((s & 3) * 32768 + (pw * 4 + i) * 1024, vA[i & 1], sx, oA + (i >> 1) * (16 * 1280) + s * 128);
      dma16((s & 3) * 32768 + 16384 + (pw * 4 + i) * 1024, vB[i >> 1], sw, oB + i * (4 * 3840) + s * (64 * 3840));
    }
  };

  issue(0);

  BARRIER();
  issue(1);

  WAIT_V(8); BARRIER();
  issue(2);
  WAIT_V(8); BARRIER();
  issue(3);
  WAIT_V(8); BARRIER();
  issue(4);
#pragma unroll
  for (int s = 2; s < 9; ++s) {

    if (s < 8) WAIT_V(8); else WAIT_V(0);

    BARRIER();

    if (s < 7) issue(s + 3);
  }

  BARRIER();
  EPOCH(e);
  return e;
}

__device__ __forceinline__ void exp4(const f32x16& s, int g, unsigned* p) {
  float e0 = __builtin_amdgcn_exp2f(s[4 * g + 0]), e1 = __builtin_amdgcn_exp2f(s[4 * g + 1]);
  float e2 = __builtin_amdgcn_exp2f(s[4 * g + 2]), e3 = __builtin_amdgcn_exp2f(s[4 * g + 3]);
  p[0] = pk2(e0, e1); p[1] = pk2(e2, e3);
}
__device__ __forceinline__ bf16x8 px8(const unsigned* p) { u32x4 v = {p[0], p[1], p[2], p[3]}; return __builtin_bit_cast(bf16x8, v); }
__device__ __forceinline__ bf16x8 bx8(u32x4 v) { return __builtin_bit_cast(bf16x8, v); }
__device__ __forceinline__ float max16(const f32x16& s) {
  float m = fmaxf(fmaxf(fmaxf(s[0], s[1]), fmaxf(s[2], s[3])), fmaxf(fmaxf(s[4], s[5]), fmaxf(s[6], s[7])));
  float n = fmaxf(fmaxf(fmaxf(s[8], s[9]), fmaxf(s[10], s[11])), fmaxf(fmaxf(s[12], s[13]), fmaxf(s[14], s[15])));
  m = fmaxf(m, n);
  return fmaxf(m, __shfl_xor(m, 32));
}

#define K2_ASM \
  "s_nop 1\ns_getpc_b64 s[40:41]\ns_add_u32 s40, s40, wsg@rel32@lo+4\ns_addc_u32 s41, s41, wsg@rel32@hi+12\nv_readfirstlane_b32 s76, %[tid]\n" \
  "s_lshl_b32 s59, %[b], 2\ns_and_b32 s59, s59, 28\ns_bfe_u32 s61, %[b], 0x20003\ns_or_b32 s59, s59, s61\nv_lshlrev_b32 v0, 4, %[tid]\n" \
  "s_mul_i32 s84, s59, 0x28000\ns_lshr_b32 s54, s76, 8\ns_bfe_u32 s60, s76, 0x20006\nv_and_b32 v0, 0x3f0, v0\ns_add_u32 s61, s84, 0x500000\n" \
  "s_xor_b32 s56, s54, 1\ns_mul_i32 s56, s56, 0x14000\ns_lshl_b32 s62, s60, 10\nv_add_u32 v107, s61, v0\ns_or_b32 s57, s56, s62\ns_brev_b32 s42, -2\n" \
  "s_mov_b32 s43, 0x20000\ns_mul_i32 s55, s60, 0xa000\ns_add_u32 s58, s55, 0x9400\ns_lshr_b32 s61, %[b], 5\ns_mul_i32 s61, s61, 0x5000\n" \
  "s_add_u32 s84, s84, s61\ns_lshr_b32 s61, s55, 3\ns_add_u32 s84, s84, s61\ns_mov_b32 s86, s58\nv_add_u32 v6, 0x500, v0\n" \
  "v_cmp_le_i32 vcc, 0x100, v0\ns_add_u32 s85, s84, 0xd00\nv_cndmask_b32 v6, v0, v6, vcc\nv_and_b32 v1, 0x1f0, v0\nv_lshrrev_b32 v65, 1, v1\n" \
  "v_or_b32 v65, 0x400, v65\ns_lshr_b32 s76, s76, 6\nv_mov_b32 v64, v0\ns_cmp_gt_u32 %[b], 119\nv_add_u32 v66, s58, v0\ns_cselect_b32 s69, 120, 0\n" \
  "v_add_u32 v67, s58, v65\ns_sub_u32 s65, %[b], s69\nv_add_u32 v104, s56, v0\ns_mul_i32 s64, s65, 171\nv_add_u32 v105, s56, v65\n" \
  "s_lshr_b32 s64, s64, 11\nv_lshrrev_b32 v2, 9, v0\ns_mul_i32 s61, s64, 12\nv_mov_b32 v78, 0x3f803f80\ns_sub_u32 s65, s65, s61\nv_mov_b32 v79, 0\n" \
  "s_mul_i32 s70, s65, 43776\nv_cmp_eq_u32 vcc, 0x140, v1\ns_lshl_b32 s68, %[b], 4\nv_mul_u32_u24 v5, 0x140, v2\ns_add_u32 s68, s68, s76\n" \
  "v_cndmask_b32 v80, 0, v78, vcc\ns_cmp_lt_u32 s68, 800\nv_add_u32 v5, 0x500, v5\ns_cselect_b32 s61, 0, 800\nv_mov_b32 v81, v80\n" \
  "s_mov_b32 s62, 102240\nv_add3_u32 v106, v5, v1, s56\ns_cselect_b32 s73, s62, 245600\nv_mov_b64 v[82:83], v[80:81]\ns_sub_u32 s67, s68, s61\n" \
  "v_mov_b64 v[84:85], v[80:81]\ns_mul_i32 s66, s67, 52429\nv_mov_b64 v[86:87], v[80:81]\ns_lshr_b32 s66, s66, 18\nv_mov_b64 v[92:93], 0\n" \
  "s_mul_i32 s61, s66, 5\nv_mov_b64 v[94:95], 0\ns_sub_u32 s67, s67, s61\nv_mov_b64 v[100:101], 0\ns_mul_i32 s71, s67, 52480\n" \
  "v_mov_b64 v[102:103], 0\ns_add_u32 s78, s67, 8\nv_add_u32 v128, 0xa000, v104\ns_mul_i32 s77, s78, 205\nv_add_u32 v129, 0xa000, v105\n" \
  "s_lshr_b32 s77, s77, 10\nv_add_u32 v130, 0xa000, v106\ns_mul_i32 s61, s77, 5\nv_mov_b32 v127, v0\ns_sub_u32 s78, s78, s61\n" \
  "v_lshl_add_u32 v124, v2, 15, v1\ns_add_u32 s77, s77, s66\nv_mov_b32 v125, v1\ns_mul_i32 s79, s78, 52480\nv_and_b32 v5, 16, v0\n" \
  "s_lshl_b32 s83, s60, 12\nv_lshrrev_b32 v2, 16, v78\ns_add_u32 s83, s83, s56\nv_lshlrev_b32 v5, v5, v2\ns_mul_i32 s63, s60, 0x1800\n" \
  "v_bfe_u32 v2, v0, 5, 3\ns_lshl_b32 s72, s60, 4\nv_cmp_eq_u32 vcc, 0, v2\ns_and_b32 s72, s72, 32\nv_cndmask_b32 v120, 0, v5, vcc\n" \
  "s_mov_b32 s52, 0xfffff\nv_cmp_eq_u32 vcc, 1, v2\ns_mov_b32 s53, 0xfffff\nv_cndmask_b32 v121, 0, v5, vcc\ns_movk_i32 s61, 0xc00\n" \
  "v_cmp_eq_u32 vcc, 2, v2\ns_movk_i32 s62, 0x500\nv_cndmask_b32 v122, 0, v5, vcc\ns_movk_i32 s74, 0xc0\nv_cmp_eq_u32 vcc, 3, v2\n" \
  "s_movk_i32 s75, 0xa0\nv_cndmask_b32 v123, 0, v5, vcc\ns_mov_b32 s44, 245568\nv_and_b32 v3, 0xf0, v0\nv_lshrrev_b32 v4, 8, v0\n" \
  "v_add_u32 v126, s63, v127\nv_mad_u32_u24 v116, v4, s61, v3\nv_mad_u32_u24 v117, v4, s62, v3\nv_mul_u32_u24 v5, 171, v3\nv_mul_u32_u24 v2, 205, v3\n" \
  "v_xor_b32 v118, s72, v3\nv_add_u32 v5, s70, v5\nv_add_u32 v2, s71, v2\nv_mad_u32_u24 v118, v4, s74, v118\nv_mad_u32_u24 v119, v4, s75, v3\n" \
  "v_lshrrev_b32 v5, 15, v5\nv_lshrrev_b32 v2, 15, v2\nv_mad_u32_u24 v118, v5, s44, v118\nv_mul_u32_u24 v5, 205, v3\nv_add_u32 v5, s79, v5\n" \
  "v_lshrrev_b32 v5, 15, v5\nv_mad_u32_u24 v131, v5, s73, v119\nv_mad_u32_u24 v119, v2, s73, v119\ns_mov_b32 s73, 26230784\ns_cmp_lt_u32 s68, 800\n" \
  "s_cselect_b32 s62, 24, 48\ns_cselect_b32 s73, s73, 27049984\ns_load_dwordx2 s[48:49], %[ka], s62\ns_cmp_eq_u32 s69, 0\ns_cselect_b32 s62, 32, 40\n" \
  "s_cselect_b32 s72, 0, 0x3000\ns_load_dwordx2 s[44:45], %[ka], s62\ns_cmp_lt_u32 s68, 2720\ns_cselect_b32 s50, s42, 0\ns_mov_b32 s51, s43\n" \
  "s_lshl_b32 s70, s64, 4\ns_add_u32 s70, s70, s76\ns_mul_i32 s70, s70, 0x3000\ns_lshl_b32 s61, s65, 8\ns_add_u32 s70, s70, s61\n" \
  "s_add_u32 s72, s72, s61\ns_mul_i32 s61, s64, 0x6000\ns_add_u32 s72, s72, s61\ns_mul_i32 s61, s76, 0x300\ns_add_u32 s72, s72, s61\n" \
  "s_add_u32 s72, s72, 22298624\ns_add_u32 s81, s70, 0x18000\ns_add_u32 s82, s72, 0x1800\ns_mov_b32 s80, s73\ns_mul_i32 s71, s66, 0x1400\n" \
  "s_lshl_b32 s61, s67, 8\ns_add_u32 s71, s71, s61\ns_add_u32 s73, s73, s61\ns_mul_i32 s61, s66, 171\ns_lshr_b32 s61, s61, 11\n" \
  "s_mul_i32 s62, s61, 12\ns_sub_u32 s62, s66, s62\ns_lshl_b32 s61, s61, 2\ns_and_b32 s63, s62, 3\ns_add_u32 s61, s61, s63\ns_lshr_b32 s62, s62, 2\n" \
  "s_lshl_b32 s62, s62, 7\ns_add_u32 s61, s61, s62\ns_cmp_lt_u32 s68, 800\ns_cselect_b32 s66, s66, s61\ns_lshr_b32 s61, s66, 5\n" \
  "s_mul_i32 s61, s61, 0x5000\ns_add_u32 s73, s73, s61\ns_and_b32 s61, s66, 31\ns_bfe_u32 s62, s66, 0x10001\ns_xor_b32 s61, s61, s62\n" \
  "s_mul_i32 s61, s61, 640\ns_add_u32 s73, s73, s61\ns_mul_i32 s79, s77, 0x1400\ns_lshl_b32 s61, s78, 8\ns_add_u32 s79, s79, s61\n" \
  "s_add_u32 s80, s80, s61\ns_mul_i32 s61, s77, 171\ns_lshr_b32 s61, s61, 11\ns_mul_i32 s62, s61, 12\ns_sub_u32 s62, s77, s62\n" \
  "s_lshl_b32 s61, s61, 2\ns_and_b32 s63, s62, 3\ns_add_u32 s61, s61, s63\ns_lshr_b32 s62, s62, 2\ns_lshl_b32 s62, s62, 7\ns_add_u32 s61, s61, s62\n" \
  "s_cmp_lt_u32 s68, 800\ns_cselect_b32 s77, s77, s61\ns_lshr_b32 s61, s77, 5\ns_mul_i32 s61, s61, 0x5000\ns_add_u32 s80, s80, s61\n" \
  "s_and_b32 s61, s77, 31\ns_bfe_u32 s62, s77, 0x10001\ns_xor_b32 s61, s61, s62\ns_mul_i32 s61, s61, 640\ns_add_u32 s80, s80, s61\n" \
  "s_lshl_b32 s58, s76, 9\ns_add_u32 s58, s58, 0x14000\ns_lshr_b32 s61, %[b], 5\ns_lshl_b32 s61, s61, 12\ns_lshl_b32 s62, s60, 10\n" \
  "s_add_u32 s61, s61, s62\ns_add_u32 s61, s61, 10485760\ns_mul_i32 s74, s59, 5\ns_add_u32 s74, s74, 1\ns_lshr_b32 s74, s74, 1\n" \
  "s_lshl_b32 s74, s74, 15\ns_add_u32 s74, s74, s61\ns_lshr_b32 s75, s59, 1\ns_mul_i32 s75, s75, 0x28000\ns_add_u32 s75, s75, s61\n" \
  "s_and_b32 s62, s59, 1\ns_lshl_b32 s62, s62, 3\ns_add_u32 s75, s75, s62\ns_add_u32 s75, s75, 0x10000\nv_mov_b64 v[8:9], %[ep]\n" \
  "s_waitcnt vmcnt(0) lgkmcnt(0)\ns_barrier\ns_cmp_eq_u32 s76, 1\ns_cbranch_scc0 .Lk2f_%=\nbuffer_store_dwordx2 v[8:9], %[pa], %[rf], 0 offen sc1\n" \
  ".Lk2f_%=:\ns_cmp_eq_u32 s76, 0\ns_cbranch_scc0 .Lk2g_%=\ns_sleep 8\ns_mov_b32 s61, 0x40000\n.Lk2h_%=:\n" \
  "buffer_load_dwordx4 v[8:11], %[pa], %[rf], 0 offen sc1\ns_waitcnt vmcnt(0)\nv_cmp_ne_u64 vcc, %[ep], v[8:9]\n" \
  "v_cmp_ne_u64 s[88:89], %[ep], v[10:11]\ns_nop 3\ns_or_b64 vcc, vcc, s[88:89]\ns_cbranch_vccz .Lk2i_%=\ns_sleep 1\ns_sub_u32 s61, s61, 1\n" \
  "s_cbranch_scc0 .Lk2h_%=\n.Lk2i_%=:\ns_mov_b64 exec, -1\n.Lk2g_%=:\ns_mov_b32 m0, s57\ns_barrier\n" \
  "buffer_load_dwordx4 v107, s[40:43], m0 offen sc1 lds\ns_cmp_eq_u32 s54, 0\ns_cbranch_scc0 .Lk2p_%=\ns_mov_b32 m0, s86\ns_nop 0\n" \
  "buffer_load_dwordx4 v0, s[40:43], s84 offen sc1 lds\ns_add_u32 m0, s86, 0x800\ns_nop 0\nbuffer_load_dwordx4 v0, s[40:43], s85 offen sc1 lds\n" \
  "s_branch .Lk2q_%=\n.Lk2p_%=:\ns_add_u32 m0, s86, 0x400\ns_add_u32 s84, s84, 0x400\nbuffer_load_dwordx4 v6, s[40:43], s84 offen sc1 lds\n.Lk2q_%=:\n" \
  "s_barrier\ns_add_u32 m0, s57, 0x1000\ns_nop 0\nbuffer_load_dwordx4 v107, s[40:43], m0 offen sc1 lds\ns_add_u32 m0, s57, 0x2000\ns_nop 0\n" \
  "buffer_load_dwordx4 v107, s[40:43], m0 offen sc1 lds\ns_waitcnt vmcnt(2)\ns_barrier\nds_read_b128 v[72:75], v64\nds_read_b128 v[88:91], v66\n" \
  "ds_read_b128 v[96:99], v66 offset:1280\nds_read_b64 v[76:77], v65\ns_mov_b32 exec_hi, 0\nds_read_b64 v[92:93], v67\n" \
  "ds_read_b64 v[100:101], v67 offset:1280\ns_mov_b32 exec_hi, -1\ns_waitcnt lgkmcnt(4)\nv_mfma_f32_32x32x16_bf16 v[32:47], v[72:75], v[88:91], 0\n" \
  "s_waitcnt lgkmcnt(3)\nv_mfma_f32_32x32x16_bf16 v[48:63], v[72:75], v[96:99], 0\ns_waitcnt lgkmcnt(1)\n" \
  "v_mfma_f32_32x32x16_bf16 v[32:47], v[76:79], v[92:95], v[32:47]\ns_waitcnt lgkmcnt(0)\n" \
  "v_mfma_f32_32x32x16_bf16 v[48:63], v[76:79], v[100:103], v[48:63]\nds_read_b128 v[72:75], v104\nds_read_b64 v[76:77], v105\n" \
  "s_add_u32 m0, s83, 12288\ns_nop 0\nbuffer_load_dwordx4 v107, s[40:43], m0 offen offset:0 sc1 lds\n" \
  "buffer_load_dwordx4 v107, s[40:43], m0 offen offset:1024 sc1 lds\nbuffer_load_dwordx4 v107, s[40:43], m0 offen offset:2048 sc1 lds\n" \
  "buffer_load_dwordx4 v107, s[40:43], m0 offen offset:3072 sc1 lds\ns_mov_b32 exec_hi, 0\ns_nop 1\nv_max3_f32 v64, v32, v33, v34\n" \
  "v_cvt_pk_bf16_f32 v94, -v64, 0\ns_mov_b32 exec_hi, -1\ns_waitcnt lgkmcnt(0)\ns_nop 0\nv_mfma_f32_32x32x16_bf16 v[32:47], v[72:75], v[88:91], 0\n" \
  "v_mfma_f32_32x32x16_bf16 v[32:47], v[76:79], v[92:95], v[32:47]\ns_mov_b32 exec_hi, 0\nv_max3_f32 v64, v48, v49, v50\n" \
  "v_cvt_pk_bf16_f32 v102, -v64, 0\ns_mov_b32 exec_hi, -1\ns_nop 7\nv_exp_f32 v32, v32\nv_exp_f32 v33, v33\ns_mov_b64 exec, s[52:53]\n" \
  "ds_read_b128 v[80:83], v106 offset:0\nds_read_b128 v[84:87], v106 offset:640\ns_mov_b64 exec, -1\nv_exp_f32 v34, v34\nv_exp_f32 v35, v35\n" \
  "v_cvt_pk_bf16_f32 v64, v32, v33\nv_cvt_pk_bf16_f32 v65, v34, v35\nv_exp_f32 v36, v36\nv_exp_f32 v37, v37\n" \
  "v_mfma_f32_32x32x16_bf16 v[48:63], v[72:75], v[96:99], 0\nv_exp_f32 v38, v38\nv_exp_f32 v39, v39\nv_cvt_pk_bf16_f32 v66, v36, v37\n" \
  "v_cvt_pk_bf16_f32 v67, v38, v39\nv_exp_f32 v40, v40\nv_mfma_f32_32x32x16_bf16 v[48:63], v[76:79], v[100:103], v[48:63]\n" \
  "ds_read_b128 v[72:75], v104 offset:2560\nds_read_b64 v[76:77], v105 offset:2560\nv_exp_f32 v41, v41\nv_exp_f32 v42, v42\nv_exp_f32 v43, v43\n" \
  "v_cvt_pk_bf16_f32 v68, v40, v41\nv_cvt_pk_bf16_f32 v69, v42, v43\nv_exp_f32 v44, v44\nv_exp_f32 v45, v45\ns_waitcnt lgkmcnt(2)\n" \
  "v_mfma_f32_32x32x16_bf16 v[0:15], v[80:83], v[64:67], 0\nv_exp_f32 v46, v46\nv_exp_f32 v47, v47\nv_cvt_pk_bf16_f32 v70, v44, v45\n" \
  "v_cvt_pk_bf16_f32 v71, v46, v47\nv_exp_f32 v48, v48\nv_exp_f32 v49, v49\nv_mfma_f32_32x32x16_bf16 v[0:15], v[84:87], v[68:71], v[0:15]\n" \
  "v_exp_f32 v50, v50\nv_exp_f32 v51, v51\nv_cvt_pk_bf16_f32 v64, v48, v49\nv_cvt_pk_bf16_f32 v65, v50, v51\nv_exp_f32 v52, v52\nv_exp_f32 v53, v53\n" \
  "s_waitcnt lgkmcnt(0)\nv_mfma_f32_32x32x16_bf16 v[32:47], v[72:75], v[88:91], 0\nv_exp_f32 v54, v54\nv_exp_f32 v55, v55\n" \
  "v_cvt_pk_bf16_f32 v66, v52, v53\nv_cvt_pk_bf16_f32 v67, v54, v55\nv_exp_f32 v56, v56\n" \
  "v_mfma_f32_32x32x16_bf16 v[32:47], v[76:79], v[92:95], v[32:47]\nv_exp_f32 v57, v57\nv_exp_f32 v58, v58\nv_exp_f32 v59, v59\n" \
  "v_cvt_pk_bf16_f32 v68, v56, v57\nv_cvt_pk_bf16_f32 v69, v58, v59\nv_exp_f32 v60, v60\nv_exp_f32 v61, v61\n" \
  "v_mfma_f32_32x32x16_bf16 v[16:31], v[80:83], v[64:67], 0\nv_exp_f32 v62, v62\nv_exp_f32 v63, v63\nv_cvt_pk_bf16_f32 v70, v60, v61\n" \
  "v_cvt_pk_bf16_f32 v71, v62, v63\nv_exp_f32 v32, v32\nv_exp_f32 v33, v33\nv_mfma_f32_32x32x16_bf16 v[16:31], v[84:87], v[68:71], v[16:31]\n" \
  "s_waitcnt vmcnt(4)\ns_barrier\ns_add_u32 m0, s83, 28672\ns_nop 0\nbuffer_load_dwordx4 v107, s[40:43], m0 offen offset:0 sc1 lds\n" \
  "buffer_load_dwordx4 v107, s[40:43], m0 offen offset:1024 sc1 lds\nbuffer_load_dwordx4 v107, s[40:43], m0 offen offset:2048 sc1 lds\n" \
  "buffer_load_dwordx4 v107, s[40:43], m0 offen offset:3072 sc1 lds\ns_mov_b64 exec, s[52:53]\nds_read_b128 v[80:83], v106 offset:2560\n" \
  "ds_read_b128 v[84:87], v106 offset:3200\ns_mov_b64 exec, -1\nv_exp_f32 v34, v34\nv_exp_f32 v35, v35\nv_cvt_pk_bf16_f32 v64, v32, v33\n" \
  "v_cvt_pk_bf16_f32 v65, v34, v35\nv_exp_f32 v36, v36\nv_exp_f32 v37, v37\nv_mfma_f32_32x32x16_bf16 v[48:63], v[72:75], v[96:99], 0\n" \
  "v_exp_f32 v38, v38\nv_exp_f32 v39, v39\nv_cvt_pk_bf16_f32 v66, v36, v37\nv_cvt_pk_bf16_f32 v67, v38, v39\nv_exp_f32 v40, v40\n" \
  "v_mfma_f32_32x32x16_bf16 v[48:63], v[76:79], v[100:103], v[48:63]\nds_read_b128 v[72:75], v104 offset:5120\n" \
  "ds_read_b64 v[76:77], v105 offset:5120\nv_exp_f32 v41, v41\nv_exp_f32 v42, v42\nv_exp_f32 v43, v43\nv_cvt_pk_bf16_f32 v68, v40, v41\n" \
  "v_cvt_pk_bf16_f32 v69, v42, v43\nv_exp_f32 v44, v44\nv_exp_f32 v45, v45\ns_waitcnt lgkmcnt(2)\n" \
  "v_mfma_f32_32x32x16_bf16 v[0:15], v[80:83], v[64:67], v[0:15]\nv_exp_f32 v46, v46\nv_exp_f32 v47, v47\nv_cvt_pk_bf16_f32 v70, v44, v45\n" \
  "v_cvt_pk_bf16_f32 v71, v46, v47\nv_exp_f32 v48, v48\nv_exp_f32 v49, v49\nv_mfma_f32_32x32x16_bf16 v[0:15], v[84:87], v[68:71], v[0:15]\n" \
  "s_cmp_lt_u32 %[b], 240\ns_cselect_b32 s46, s42, 0\ns_mov_b32 s47, s43\nbuffer_load_dwordx4 v[108:111], v116, s[44:47], s70 offen\n" \
  "buffer_load_dwordx4 v[112:115], v117, s[48:51], s71 offen\nbuffer_load_dwordx4 v[132:135], v116, s[44:47], s81 offen\n" \
  "buffer_load_dwordx4 v[136:139], v117, s[48:51], s79 offen\nv_exp_f32 v50, v50\nv_exp_f32 v51, v51\nv_cvt_pk_bf16_f32 v64, v48, v49\n" \
  "v_cvt_pk_bf16_f32 v65, v50, v51\nv_exp_f32 v52, v52\nv_exp_f32 v53, v53\ns_waitcnt lgkmcnt(0)\n" \
  "v_mfma_f32_32x32x16_bf16 v[32:47], v[72:75], v[88:91], 0\nv_exp_f32 v54, v54\nv_exp_f32 v55, v55\nv_cvt_pk_bf16_f32 v66, v52, v53\n" \
  "v_cvt_pk_bf16_f32 v67, v54, v55\nv_exp_f32 v56, v56\nv_mfma_f32_32x32x16_bf16 v[32:47], v[76:79], v[92:95], v[32:47]\nv_exp_f32 v57, v57\n" \
  "v_exp_f32 v58, v58\nv_exp_f32 v59, v59\nv_cvt_pk_bf16_f32 v68, v56, v57\nv_cvt_pk_bf16_f32 v69, v58, v59\nv_exp_f32 v60, v60\nv_exp_f32 v61, v61\n" \
  "v_mfma_f32_32x32x16_bf16 v[16:31], v[80:83], v[64:67], v[16:31]\nv_exp_f32 v62, v62\nv_exp_f32 v63, v63\nv_cvt_pk_bf16_f32 v70, v60, v61\n" \
  "v_cvt_pk_bf16_f32 v71, v62, v63\nv_exp_f32 v32, v32\nv_exp_f32 v33, v33\nv_mfma_f32_32x32x16_bf16 v[16:31], v[84:87], v[68:71], v[16:31]\n" \
  "s_mov_b64 exec, s[52:53]\nds_read_b128 v[80:83], v106 offset:5120\nds_read_b128 v[84:87], v106 offset:5760\ns_mov_b64 exec, -1\n" \
  "v_add_u32 v116, s58, v127\nv_exp_f32 v34, v34\nv_exp_f32 v35, v35\nv_cvt_pk_bf16_f32 v64, v32, v33\nv_cvt_pk_bf16_f32 v65, v34, v35\n" \
  "v_exp_f32 v36, v36\nv_exp_f32 v37, v37\nv_mfma_f32_32x32x16_bf16 v[48:63], v[72:75], v[96:99], 0\nv_exp_f32 v38, v38\nv_exp_f32 v39, v39\n" \
  "v_cvt_pk_bf16_f32 v66, v36, v37\nv_cvt_pk_bf16_f32 v67, v38, v39\nv_exp_f32 v40, v40\n" \
  "v_mfma_f32_32x32x16_bf16 v[48:63], v[76:79], v[100:103], v[48:63]\nds_read_b128 v[72:75], v104 offset:7680\n" \
  "ds_read_b64 v[76:77], v105 offset:7680\nv_exp_f32 v41, v41\nv_exp_f32 v42, v42\nv_exp_f32 v43, v43\nv_cvt_pk_bf16_f32 v68, v40, v41\n" \
  "v_cvt_pk_bf16_f32 v69, v42, v43\nv_exp_f32 v44, v44\nv_exp_f32 v45, v45\ns_waitcnt lgkmcnt(2)\n" \
  "v_mfma_f32_32x32x16_bf16 v[0:15], v[80:83], v[64:67], v[0:15]\nv_exp_f32 v46, v46\nv_exp_f32 v47, v47\nv_cvt_pk_bf16_f32 v70, v44, v45\n" \
  "v_cvt_pk_bf16_f32 v71, v46, v47\nv_exp_f32 v48, v48\nv_exp_f32 v49, v49\nv_mfma_f32_32x32x16_bf16 v[0:15], v[84:87], v[68:71], v[0:15]\n" \
  "v_exp_f32 v50, v50\nv_exp_f32 v51, v51\nv_cvt_pk_bf16_f32 v64, v48, v49\nv_cvt_pk_bf16_f32 v65, v50, v51\nv_exp_f32 v52, v52\nv_exp_f32 v53, v53\n" \
  "s_waitcnt lgkmcnt(0)\nv_mfma_f32_32x32x16_bf16 v[32:47], v[72:75], v[88:91], 0\nv_exp_f32 v54, v54\nv_exp_f32 v55, v55\n" \
  "v_cvt_pk_bf16_f32 v66, v52, v53\nv_cvt_pk_bf16_f32 v67, v54, v55\nv_exp_f32 v56, v56\n" \
  "v_mfma_f32_32x32x16_bf16 v[32:47], v[76:79], v[92:95], v[32:47]\nv_exp_f32 v57, v57\nv_exp_f32 v58, v58\nv_exp_f32 v59, v59\n" \
  "v_cvt_pk_bf16_f32 v68, v56, v57\nv_cvt_pk_bf16_f32 v69, v58, v59\nv_exp_f32 v60, v60\nv_exp_f32 v61, v61\n" \
  "v_mfma_f32_32x32x16_bf16 v[16:31], v[80:83], v[64:67], v[16:31]\nv_exp_f32 v62, v62\nv_exp_f32 v63, v63\nv_cvt_pk_bf16_f32 v70, v60, v61\n" \
  "v_cvt_pk_bf16_f32 v71, v62, v63\nv_exp_f32 v32, v32\nv_exp_f32 v33, v33\nv_mfma_f32_32x32x16_bf16 v[16:31], v[84:87], v[68:71], v[16:31]\n" \
  "s_add_u32 m0, s83, 45056\ns_nop 0\nbuffer_load_dwordx4 v107, s[40:43], m0 offen offset:0 sc1 lds\n" \
  "buffer_load_dwordx4 v107, s[40:43], m0 offen offset:1024 sc1 lds\nbuffer_load_dwordx4 v107, s[40:43], m0 offen offset:2048 sc1 lds\n" \
  "buffer_load_dwordx4 v107, s[40:43], m0 offen offset:3072 sc1 lds\ns_mov_b64 exec, s[52:53]\nds_read_b128 v[80:83], v106 offset:7680\n" \
  "ds_read_b128 v[84:87], v106 offset:8320\ns_mov_b64 exec, -1\nv_exp_f32 v34, v34\nv_exp_f32 v35, v35\nv_cvt_pk_bf16_f32 v64, v32, v33\n" \
  "v_cvt_pk_bf16_f32 v65, v34, v35\nv_exp_f32 v36, v36\nv_exp_f32 v37, v37\nv_mfma_f32_32x32x16_bf16 v[48:63], v[72:75], v[96:99], 0\n" \
  "v_exp_f32 v38, v38\nv_exp_f32 v39, v39\nv_cvt_pk_bf16_f32 v66, v36, v37\nv_cvt_pk_bf16_f32 v67, v38, v39\nv_exp_f32 v40, v40\n" \
  "v_mfma_f32_32x32x16_bf16 v[48:63], v[76:79], v[100:103], v[48:63]\nds_read_b128 v[72:75], v104 offset:10240\n" \
  "ds_read_b64 v[76:77], v105 offset:10240\nv_exp_f32 v41, v41\nv_exp_f32 v42, v42\nv_exp_f32 v43, v43\nv_cvt_pk_bf16_f32 v68, v40, v41\n" \
  "v_cvt_pk_bf16_f32 v69, v42, v43\nv_exp_f32 v44, v44\nv_exp_f32 v45, v45\ns_waitcnt lgkmcnt(2)\n" \
  "v_mfma_f32_32x32x16_bf16 v[0:15], v[80:83], v[64:67], v[0:15]\nv_exp_f32 v46, v46\nv_exp_f32 v47, v47\nv_cvt_pk_bf16_f32 v70, v44, v45\n" \
  "v_cvt_pk_bf16_f32 v71, v46, v47\nv_exp_f32 v48, v48\nv_exp_f32 v49, v49\nv_mfma_f32_32x32x16_bf16 v[0:15], v[84:87], v[68:71], v[0:15]\n" \
  "v_exp_f32 v50, v50\nv_exp_f32 v51, v51\nv_cvt_pk_bf16_f32 v64, v48, v49\nv_cvt_pk_bf16_f32 v65, v50, v51\nv_exp_f32 v52, v52\nv_exp_f32 v53, v53\n" \
  "s_waitcnt lgkmcnt(0)\nv_mfma_f32_32x32x16_bf16 v[32:47], v[72:75], v[88:91], 0\nv_exp_f32 v54, v54\nv_exp_f32 v55, v55\n" \
  "v_cvt_pk_bf16_f32 v66, v52, v53\nv_cvt_pk_bf16_f32 v67, v54, v55\nv_exp_f32 v56, v56\n" \
  "v_mfma_f32_32x32x16_bf16 v[32:47], v[76:79], v[92:95], v[32:47]\nv_exp_f32 v57, v57\nv_exp_f32 v58, v58\nv_exp_f32 v59, v59\n" \
  "v_cvt_pk_bf16_f32 v68, v56, v57\nv_cvt_pk_bf16_f32 v69, v58, v59\nv_exp_f32 v60, v60\nv_exp_f32 v61, v61\n" \
  "v_mfma_f32_32x32x16_bf16 v[16:31], v[80:83], v[64:67], v[16:31]\nv_exp_f32 v62, v62\nv_exp_f32 v63, v63\nv_cvt_pk_bf16_f32 v70, v60, v61\n" \
  "v_cvt_pk_bf16_f32 v71, v62, v63\nv_exp_f32 v32, v32\nv_exp_f32 v33, v33\nv_mfma_f32_32x32x16_bf16 v[16:31], v[84:87], v[68:71], v[16:31]\n" \
  "s_waitcnt vmcnt(12)\ns_barrier\ns_mov_b64 exec, s[52:53]\nds_read_b128 v[80:83], v106 offset:10240\nds_read_b128 v[84:87], v106 offset:10880\n" \
  "s_mov_b64 exec, -1\nv_exp_f32 v34, v34\nv_exp_f32 v35, v35\nv_cvt_pk_bf16_f32 v64, v32, v33\nv_cvt_pk_bf16_f32 v65, v34, v35\nv_exp_f32 v36, v36\n" \
  "v_exp_f32 v37, v37\nv_mfma_f32_32x32x16_bf16 v[48:63], v[72:75], v[96:99], 0\nv_exp_f32 v38, v38\nv_exp_f32 v39, v39\n" \
  "v_cvt_pk_bf16_f32 v66, v36, v37\nv_cvt_pk_bf16_f32 v67, v38, v39\nv_exp_f32 v40, v40\n" \
  "v_mfma_f32_32x32x16_bf16 v[48:63], v[76:79], v[100:103], v[48:63]\nds_read_b128 v[72:75], v104 offset:12800\n" \
  "ds_read_b64 v[76:77], v105 offset:12800\nv_exp_f32 v41, v41\nv_exp_f32 v42, v42\nv_exp_f32 v43, v43\nv_cvt_pk_bf16_f32 v68, v40, v41\n" \
  "v_cvt_pk_bf16_f32 v69, v42, v43\nv_exp_f32 v44, v44\nv_exp_f32 v45, v45\ns_waitcnt lgkmcnt(2)\n" \
  "v_mfma_f32_32x32x16_bf16 v[0:15], v[80:83], v[64:67], v[0:15]\nv_exp_f32 v46, v46\nv_exp_f32 v47, v47\nv_cvt_pk_bf16_f32 v70, v44, v45\n" \
  "v_cvt_pk_bf16_f32 v71, v46, v47\nv_exp_f32 v48, v48\nv_exp_f32 v49, v49\nv_mfma_f32_32x32x16_bf16 v[0:15], v[84:87], v[68:71], v[0:15]\n" \
  "v_exp_f32 v50, v50\nv_exp_f32 v51, v51\nv_cvt_pk_bf16_f32 v64, v48, v49\nv_cvt_pk_bf16_f32 v65, v50, v51\nv_exp_f32 v52, v52\nv_exp_f32 v53, v53\n" \
  "s_waitcnt lgkmcnt(0)\nv_mfma_f32_32x32x16_bf16 v[32:47], v[72:75], v[88:91], 0\nv_exp_f32 v54, v54\nv_exp_f32 v55, v55\n" \
  "v_cvt_pk_bf16_f32 v66, v52, v53\nv_cvt_pk_bf16_f32 v67, v54, v55\nv_exp_f32 v56, v56\n" \
  "v_mfma_f32_32x32x16_bf16 v[32:47], v[76:79], v[92:95], v[32:47]\nv_exp_f32 v57, v57\nv_exp_f32 v58, v58\nv_exp_f32 v59, v59\n" \
  "v_cvt_pk_bf16_f32 v68, v56, v57\nv_cvt_pk_bf16_f32 v69, v58, v59\nv_exp_f32 v60, v60\nv_exp_f32 v61, v61\n" \
  "v_mfma_f32_32x32x16_bf16 v[16:31], v[80:83], v[64:67], v[16:31]\nv_exp_f32 v62, v62\nv_exp_f32 v63, v63\nv_cvt_pk_bf16_f32 v70, v60, v61\n" \
  "v_cvt_pk_bf16_f32 v71, v62, v63\nv_exp_f32 v32, v32\nv_exp_f32 v33, v33\nv_mfma_f32_32x32x16_bf16 v[16:31], v[84:87], v[68:71], v[16:31]\n" \
  "s_mov_b64 exec, s[52:53]\nds_read_b128 v[80:83], v106 offset:12800\nds_read_b128 v[84:87], v106 offset:13440\ns_mov_b64 exec, -1\n" \
  "v_exp_f32 v34, v34\nv_exp_f32 v35, v35\nv_cvt_pk_bf16_f32 v64, v32, v33\nv_cvt_pk_bf16_f32 v65, v34, v35\nv_exp_f32 v36, v36\nv_exp_f32 v37, v37\n" \
  "v_mfma_f32_32x32x16_bf16 v[48:63], v[72:75], v[96:99], 0\nv_exp_f32 v38, v38\nv_exp_f32 v39, v39\nv_cvt_pk_bf16_f32 v66, v36, v37\n" \
  "v_cvt_pk_bf16_f32 v67, v38, v39\nv_exp_f32 v40, v40\nv_mfma_f32_32x32x16_bf16 v[48:63], v[76:79], v[100:103], v[48:63]\n" \
  "ds_read_b128 v[72:75], v104 offset:15360\nds_read_b64 v[76:77], v105 offset:15360\nv_exp_f32 v41, v41\nv_exp_f32 v42, v42\nv_exp_f32 v43, v43\n" \
  "v_cvt_pk_bf16_f32 v68, v40, v41\nv_cvt_pk_bf16_f32 v69, v42, v43\nv_exp_f32 v44, v44\nv_exp_f32 v45, v45\ns_waitcnt lgkmcnt(2)\n" \
  "v_mfma_f32_32x32x16_bf16 v[0:15], v[80:83], v[64:67], v[0:15]\nv_exp_f32 v46, v46\nv_exp_f32 v47, v47\nv_cvt_pk_bf16_f32 v70, v44, v45\n" \
  "v_cvt_pk_bf16_f32 v71, v46, v47\nv_exp_f32 v48, v48\nv_exp_f32 v49, v49\nv_mfma_f32_32x32x16_bf16 v[0:15], v[84:87], v[68:71], v[0:15]\n" \
  "v_exp_f32 v50, v50\nv_exp_f32 v51, v51\nv_cvt_pk_bf16_f32 v64, v48, v49\nv_cvt_pk_bf16_f32 v65, v50, v51\nv_exp_f32 v52, v52\nv_exp_f32 v53, v53\n" \
  "s_waitcnt lgkmcnt(0)\nv_mfma_f32_32x32x16_bf16 v[32:47], v[72:75], v[88:91], 0\nv_exp_f32 v54, v54\nv_exp_f32 v55, v55\n" \
  "v_cvt_pk_bf16_f32 v66, v52, v53\nv_cvt_pk_bf16_f32 v67, v54, v55\nv_exp_f32 v56, v56\n" \
  "v_mfma_f32_32x32x16_bf16 v[32:47], v[76:79], v[92:95], v[32:47]\nv_exp_f32 v57, v57\nv_exp_f32 v58, v58\nv_exp_f32 v59, v59\n" \
  "v_cvt_pk_bf16_f32 v68, v56, v57\nv_cvt_pk_bf16_f32 v69, v58, v59\nv_exp_f32 v60, v60\nv_exp_f32 v61, v61\n" \
  "v_mfma_f32_32x32x16_bf16 v[16:31], v[80:83], v[64:67], v[16:31]\nv_exp_f32 v62, v62\nv_exp_f32 v63, v63\nv_cvt_pk_bf16_f32 v70, v60, v61\n" \
  "v_cvt_pk_bf16_f32 v71, v62, v63\nv_exp_f32 v32, v32\nv_exp_f32 v33, v33\nv_mfma_f32_32x32x16_bf16 v[16:31], v[84:87], v[68:71], v[16:31]\n" \
  "s_add_u32 m0, s83, 61440\ns_nop 0\nbuffer_load_dwordx4 v107, s[40:43], m0 offen offset:0 sc1 lds\n" \
  "buffer_load_dwordx4 v107, s[40:43], m0 offen offset:1024 sc1 lds\nbuffer_load_dwordx4 v107, s[40:43], m0 offen offset:2048 sc1 lds\n" \
  "buffer_load_dwordx4 v107, s[40:43], m0 offen offset:3072 sc1 lds\ns_mov_b64 exec, s[52:53]\nds_read_b128 v[80:83], v106 offset:15360\n" \
  "ds_read_b128 v[84:87], v106 offset:16000\ns_mov_b64 exec, -1\nv_exp_f32 v34, v34\nv_exp_f32 v35, v35\nv_cvt_pk_bf16_f32 v64, v32, v33\n" \
  "v_cvt_pk_bf16_f32 v65, v34, v35\nv_exp_f32 v36, v36\nv_exp_f32 v37, v37\nv_mfma_f32_32x32x16_bf16 v[48:63], v[72:75], v[96:99], 0\n" \
  "v_exp_f32 v38, v38\nv_exp_f32 v39, v39\nv_cvt_pk_bf16_f32 v66, v36, v37\nv_cvt_pk_bf16_f32 v67, v38, v39\nv_exp_f32 v40, v40\n" \
  "v_mfma_f32_32x32x16_bf16 v[48:63], v[76:79], v[100:103], v[48:63]\nds_read_b128 v[72:75], v104 offset:17920\n" \
  "ds_read_b64 v[76:77], v105 offset:17920\nv_exp_f32 v41, v41\nv_exp_f32 v42, v42\nv_exp_f32 v43, v43\nv_cvt_pk_bf16_f32 v68, v40, v41\n" \
  "v_cvt_pk_bf16_f32 v69, v42, v43\nv_exp_f32 v44, v44\nv_exp_f32 v45, v45\ns_waitcnt lgkmcnt(2)\n" \
  "v_mfma_f32_32x32x16_bf16 v[0:15], v[80:83], v[64:67], v[0:15]\nv_exp_f32 v46, v46\nv_exp_f32 v47, v47\nv_cvt_pk_bf16_f32 v70, v44, v45\n" \
  "v_cvt_pk_bf16_f32 v71, v46, v47\nv_exp_f32 v48, v48\nv_exp_f32 v49, v49\nv_mfma_f32_32x32x16_bf16 v[0:15], v[84:87], v[68:71], v[0:15]\n" \
  "v_exp_f32 v50, v50\nv_exp_f32 v51, v51\nv_cvt_pk_bf16_f32 v64, v48, v49\nv_cvt_pk_bf16_f32 v65, v50, v51\nv_exp_f32 v52, v52\nv_exp_f32 v53, v53\n" \
  "s_waitcnt lgkmcnt(0)\nv_mfma_f32_32x32x16_bf16 v[32:47], v[72:75], v[88:91], 0\nv_exp_f32 v54, v54\nv_exp_f32 v55, v55\n" \
  "v_cvt_pk_bf16_f32 v66, v52, v53\nv_cvt_pk_bf16_f32 v67, v54, v55\nv_exp_f32 v56, v56\n" \
  "v_mfma_f32_32x32x16_bf16 v[32:47], v[76:79], v[92:95], v[32:47]\nv_exp_f32 v57, v57\nv_exp_f32 v58, v58\nv_exp_f32 v59, v59\n" \
  "v_cvt_pk_bf16_f32 v68, v56, v57\nv_cvt_pk_bf16_f32 v69, v58, v59\nv_exp_f32 v60, v60\nv_exp_f32 v61, v61\n" \
  "v_mfma_f32_32x32x16_bf16 v[16:31], v[80:83], v[64:67], v[16:31]\nv_exp_f32 v62, v62\nv_exp_f32 v63, v63\nv_cvt_pk_bf16_f32 v70, v60, v61\n" \
  "v_cvt_pk_bf16_f32 v71, v62, v63\nv_exp_f32 v32, v32\nv_exp_f32 v33, v33\nv_mfma_f32_32x32x16_bf16 v[16:31], v[84:87], v[68:71], v[16:31]\n" \
  "s_add_u32 m0, s57, 77824\ns_nop 0\nbuffer_load_dwordx4 v107, s[40:43], m0 offen sc1 lds\ns_waitcnt vmcnt(9)\ns_cmp_lt_u32 %[b], 240\n" \
  "s_cselect_b64 exec, -1, 0\nbuffer_store_dwordx4 v[108:111], v118, s[40:43], s72 offen sc1\n" \
  "buffer_store_dwordx4 v[132:135], v118, s[40:43], s82 offen sc1\ns_cmp_lt_u32 s68, 2720\ns_cselect_b64 exec, -1, 0\n" \
  "buffer_store_dwordx4 v[112:115], v119, s[40:43], s73 offen sc1\nbuffer_store_dwordx4 v[136:139], v131, s[40:43], s80 offen sc1\n" \
  "s_mov_b64 exec, -1\ns_mov_b64 exec, s[52:53]\nds_read_b128 v[80:83], v106 offset:17920\nds_read_b128 v[84:87], v106 offset:18560\n" \
  "s_mov_b64 exec, -1\nv_exp_f32 v34, v34\nv_exp_f32 v35, v35\nv_cvt_pk_bf16_f32 v64, v32, v33\nv_cvt_pk_bf16_f32 v65, v34, v35\nv_exp_f32 v36, v36\n" \
  "v_exp_f32 v37, v37\nv_mfma_f32_32x32x16_bf16 v[48:63], v[72:75], v[96:99], 0\nv_exp_f32 v38, v38\nv_exp_f32 v39, v39\n" \
  "v_cvt_pk_bf16_f32 v66, v36, v37\nv_cvt_pk_bf16_f32 v67, v38, v39\nv_exp_f32 v40, v40\n" \
  "v_mfma_f32_32x32x16_bf16 v[48:63], v[76:79], v[100:103], v[48:63]\nds_read_b128 v[72:75], v104 offset:20480\n" \
  "ds_read_b64 v[76:77], v105 offset:20480\nv_exp_f32 v41, v41\nv_exp_f32 v42, v42\nv_exp_f32 v43, v43\nv_cvt_pk_bf16_f32 v68, v40, v41\n" \
  "v_cvt_pk_bf16_f32 v69, v42, v43\nv_exp_f32 v44, v44\nv_exp_f32 v45, v45\ns_waitcnt lgkmcnt(2)\n" \
  "v_mfma_f32_32x32x16_bf16 v[0:15], v[80:83], v[64:67], v[0:15]\nv_exp_f32 v46, v46\nv_exp_f32 v47, v47\nv_cvt_pk_bf16_f32 v70, v44, v45\n" \
  "v_cvt_pk_bf16_f32 v71, v46, v47\nv_exp_f32 v48, v48\nv_exp_f32 v49, v49\nv_mfma_f32_32x32x16_bf16 v[0:15], v[84:87], v[68:71], v[0:15]\n" \
  "v_exp_f32 v50, v50\nv_exp_f32 v51, v51\nv_cvt_pk_bf16_f32 v64, v48, v49\nv_cvt_pk_bf16_f32 v65, v50, v51\nv_exp_f32 v52, v52\nv_exp_f32 v53, v53\n" \
  "s_waitcnt lgkmcnt(0)\nv_mfma_f32_32x32x16_bf16 v[32:47], v[72:75], v[88:91], 0\nv_exp_f32 v54, v54\nv_exp_f32 v55, v55\n" \
  "v_cvt_pk_bf16_f32 v66, v52, v53\nv_cvt_pk_bf16_f32 v67, v54, v55\nv_exp_f32 v56, v56\n" \
  "v_mfma_f32_32x32x16_bf16 v[32:47], v[76:79], v[92:95], v[32:47]\nv_exp_f32 v57, v57\nv_exp_f32 v58, v58\nv_exp_f32 v59, v59\n" \
  "v_cvt_pk_bf16_f32 v68, v56, v57\nv_cvt_pk_bf16_f32 v69, v58, v59\nv_exp_f32 v60, v60\nv_exp_f32 v61, v61\n" \
  "v_mfma_f32_32x32x16_bf16 v[16:31], v[80:83], v[64:67], v[16:31]\nv_exp_f32 v62, v62\nv_exp_f32 v63, v63\nv_cvt_pk_bf16_f32 v70, v60, v61\n" \
  "v_cvt_pk_bf16_f32 v71, v62, v63\nv_exp_f32 v32, v32\nv_exp_f32 v33, v33\nv_mfma_f32_32x32x16_bf16 v[16:31], v[84:87], v[68:71], v[16:31]\n" \
  "s_barrier\nv_mfma_f32_16x16x32_bf16 v[140:143], v[120:123], v[108:111], 0\n" \
  "v_mfma_f32_16x16x32_bf16 v[140:143], v[120:123], v[132:135], v[140:143]\ns_mov_b64 exec, s[52:53]\nds_read_b128 v[80:83], v106 offset:20480\n" \
  "ds_read_b128 v[84:87], v106 offset:21120\ns_mov_b64 exec, -1\nv_exp_f32 v34, v34\nv_exp_f32 v35, v35\nv_cvt_pk_bf16_f32 v64, v32, v33\n" \
  "v_cvt_pk_bf16_f32 v65, v34, v35\nv_exp_f32 v36, v36\nv_exp_f32 v37, v37\nv_mfma_f32_32x32x16_bf16 v[48:63], v[72:75], v[96:99], 0\n" \
  "v_exp_f32 v38, v38\nv_exp_f32 v39, v39\nv_cvt_pk_bf16_f32 v66, v36, v37\nv_cvt_pk_bf16_f32 v67, v38, v39\nv_exp_f32 v40, v40\n" \
  "v_mfma_f32_32x32x16_bf16 v[48:63], v[76:79], v[100:103], v[48:63]\nds_read_b128 v[72:75], v104 offset:23040\n" \
  "ds_read_b64 v[76:77], v105 offset:23040\nv_exp_f32 v41, v41\nv_exp_f32 v42, v42\nv_exp_f32 v43, v43\nv_cvt_pk_bf16_f32 v68, v40, v41\n" \
  "v_cvt_pk_bf16_f32 v69, v42, v43\nv_exp_f32 v44, v44\nv_exp_f32 v45, v45\ns_waitcnt lgkmcnt(2)\n" \
  "v_mfma_f32_32x32x16_bf16 v[0:15], v[80:83], v[64:67], v[0:15]\nv_exp_f32 v46, v46\nv_exp_f32 v47, v47\nv_cvt_pk_bf16_f32 v70, v44, v45\n" \
  "v_cvt_pk_bf16_f32 v71, v46, v47\nv_exp_f32 v48, v48\nv_exp_f32 v49, v49\nv_mfma_f32_32x32x16_bf16 v[0:15], v[84:87], v[68:71], v[0:15]\n" \
  "v_exp_f32 v50, v50\nv_exp_f32 v51, v51\nv_cvt_pk_bf16_f32 v64, v48, v49\nv_cvt_pk_bf16_f32 v65, v50, v51\nv_exp_f32 v52, v52\nv_exp_f32 v53, v53\n" \
  "s_waitcnt lgkmcnt(0)\nv_mfma_f32_32x32x16_bf16 v[32:47], v[72:75], v[88:91], 0\nv_exp_f32 v54, v54\nv_exp_f32 v55, v55\n" \
  "v_cvt_pk_bf16_f32 v66, v52, v53\nv_cvt_pk_bf16_f32 v67, v54, v55\nv_exp_f32 v56, v56\n" \
  "v_mfma_f32_32x32x16_bf16 v[32:47], v[76:79], v[92:95], v[32:47]\nv_exp_f32 v57, v57\nv_exp_f32 v58, v58\nv_exp_f32 v59, v59\n" \
  "v_cvt_pk_bf16_f32 v68, v56, v57\nv_cvt_pk_bf16_f32 v69, v58, v59\nv_exp_f32 v60, v60\nv_exp_f32 v61, v61\n" \
  "v_mfma_f32_32x32x16_bf16 v[16:31], v[80:83], v[64:67], v[16:31]\nv_exp_f32 v62, v62\nv_exp_f32 v63, v63\nv_cvt_pk_bf16_f32 v70, v60, v61\n" \
  "v_cvt_pk_bf16_f32 v71, v62, v63\nv_exp_f32 v32, v32\nv_exp_f32 v33, v33\nv_mfma_f32_32x32x16_bf16 v[16:31], v[84:87], v[68:71], v[16:31]\n" \
  "s_mov_b32 exec_hi, 0\nds_write_b128 v116, v[140:143]\ns_mov_b32 exec_hi, -1\ns_mov_b64 exec, s[52:53]\nds_read_b128 v[80:83], v106 offset:23040\n" \
  "ds_read_b128 v[84:87], v106 offset:23680\ns_mov_b64 exec, -1\nv_exp_f32 v34, v34\nv_exp_f32 v35, v35\nv_cvt_pk_bf16_f32 v64, v32, v33\n" \
  "v_cvt_pk_bf16_f32 v65, v34, v35\nv_exp_f32 v36, v36\nv_exp_f32 v37, v37\nv_mfma_f32_32x32x16_bf16 v[48:63], v[72:75], v[96:99], 0\n" \
  "v_exp_f32 v38, v38\nv_exp_f32 v39, v39\nv_cvt_pk_bf16_f32 v66, v36, v37\nv_cvt_pk_bf16_f32 v67, v38, v39\nv_exp_f32 v40, v40\n" \
  "v_mfma_f32_32x32x16_bf16 v[48:63], v[76:79], v[100:103], v[48:63]\nds_read_b128 v[72:75], v104 offset:25600\n" \
  "ds_read_b64 v[76:77], v105 offset:25600\nv_exp_f32 v41, v41\nv_exp_f32 v42, v42\nv_exp_f32 v43, v43\nv_cvt_pk_bf16_f32 v68, v40, v41\n" \
  "v_cvt_pk_bf16_f32 v69, v42, v43\nv_exp_f32 v44, v44\nv_exp_f32 v45, v45\ns_waitcnt lgkmcnt(2)\n" \
  "v_mfma_f32_32x32x16_bf16 v[0:15], v[80:83], v[64:67], v[0:15]\nv_exp_f32 v46, v46\nv_exp_f32 v47, v47\nv_cvt_pk_bf16_f32 v70, v44, v45\n" \
  "v_cvt_pk_bf16_f32 v71, v46, v47\nv_exp_f32 v48, v48\nv_exp_f32 v49, v49\nv_mfma_f32_32x32x16_bf16 v[0:15], v[84:87], v[68:71], v[0:15]\n" \
  "v_exp_f32 v50, v50\nv_exp_f32 v51, v51\nv_cvt_pk_bf16_f32 v64, v48, v49\nv_cvt_pk_bf16_f32 v65, v50, v51\nv_exp_f32 v52, v52\nv_exp_f32 v53, v53\n" \
  "s_waitcnt lgkmcnt(0)\nv_mfma_f32_32x32x16_bf16 v[32:47], v[72:75], v[88:91], 0\nv_exp_f32 v54, v54\nv_exp_f32 v55, v55\n" \
  "v_cvt_pk_bf16_f32 v66, v52, v53\nv_cvt_pk_bf16_f32 v67, v54, v55\nv_exp_f32 v56, v56\n" \
  "v_mfma_f32_32x32x16_bf16 v[32:47], v[76:79], v[92:95], v[32:47]\nv_exp_f32 v57, v57\nv_exp_f32 v58, v58\nv_exp_f32 v59, v59\n" \
  "v_cvt_pk_bf16_f32 v68, v56, v57\nv_cvt_pk_bf16_f32 v69, v58, v59\nv_exp_f32 v60, v60\nv_exp_f32 v61, v61\n" \
  "v_mfma_f32_32x32x16_bf16 v[16:31], v[80:83], v[64:67], v[16:31]\nv_exp_f32 v62, v62\nv_exp_f32 v63, v63\nv_cvt_pk_bf16_f32 v70, v60, v61\n" \
  "v_cvt_pk_bf16_f32 v71, v62, v63\nv_exp_f32 v32, v32\nv_exp_f32 v33, v33\nv_mfma_f32_32x32x16_bf16 v[16:31], v[84:87], v[68:71], v[16:31]\n" \
  "s_mov_b64 exec, s[52:53]\nds_read_b128 v[80:83], v106 offset:25600\nds_read_b128 v[84:87], v106 offset:26240\ns_mov_b64 exec, -1\n" \
  "v_exp_f32 v34, v34\nv_exp_f32 v35, v35\nv_cvt_pk_bf16_f32 v64, v32, v33\nv_cvt_pk_bf16_f32 v65, v34, v35\nv_exp_f32 v36, v36\nv_exp_f32 v37, v37\n" \
  "v_mfma_f32_32x32x16_bf16 v[48:63], v[72:75], v[96:99], 0\nv_exp_f32 v38, v38\nv_exp_f32 v39, v39\nv_cvt_pk_bf16_f32 v66, v36, v37\n" \
  "v_cvt_pk_bf16_f32 v67, v38, v39\nv_exp_f32 v40, v40\nv_mfma_f32_32x32x16_bf16 v[48:63], v[76:79], v[100:103], v[48:63]\n" \
  "ds_read_b128 v[72:75], v104 offset:28160\nds_read_b64 v[76:77], v105 offset:28160\nv_exp_f32 v41, v41\nv_exp_f32 v42, v42\nv_exp_f32 v43, v43\n" \
  "v_cvt_pk_bf16_f32 v68, v40, v41\nv_cvt_pk_bf16_f32 v69, v42, v43\nv_exp_f32 v44, v44\nv_exp_f32 v45, v45\ns_waitcnt lgkmcnt(2)\n" \
  "v_mfma_f32_32x32x16_bf16 v[0:15], v[80:83], v[64:67], v[0:15]\nv_exp_f32 v46, v46\nv_exp_f32 v47, v47\nv_cvt_pk_bf16_f32 v70, v44, v45\n" \
  "v_cvt_pk_bf16_f32 v71, v46, v47\nv_exp_f32 v48, v48\nv_exp_f32 v49, v49\nv_mfma_f32_32x32x16_bf16 v[0:15], v[84:87], v[68:71], v[0:15]\n" \
  "v_exp_f32 v50, v50\nv_exp_f32 v51, v51\nv_cvt_pk_bf16_f32 v64, v48, v49\nv_cvt_pk_bf16_f32 v65, v50, v51\nv_exp_f32 v52, v52\nv_exp_f32 v53, v53\n" \
  "s_waitcnt lgkmcnt(0)\nv_mfma_f32_32x32x16_bf16 v[32:47], v[72:75], v[88:91], 0\nv_exp_f32 v54, v54\nv_exp_f32 v55, v55\n" \
  "v_cvt_pk_bf16_f32 v66, v52, v53\nv_cvt_pk_bf16_f32 v67, v54, v55\nv_exp_f32 v56, v56\n" \
  "v_mfma_f32_32x32x16_bf16 v[32:47], v[76:79], v[92:95], v[32:47]\nv_exp_f32 v57, v57\nv_exp_f32 v58, v58\nv_exp_f32 v59, v59\n" \
  "v_cvt_pk_bf16_f32 v68, v56, v57\nv_cvt_pk_bf16_f32 v69, v58, v59\nv_exp_f32 v60, v60\nv_exp_f32 v61, v61\n" \
  "v_mfma_f32_32x32x16_bf16 v[16:31], v[80:83], v[64:67], v[16:31]\nv_exp_f32 v62, v62\nv_exp_f32 v63, v63\nv_cvt_pk_bf16_f32 v70, v60, v61\n" \
  "v_cvt_pk_bf16_f32 v71, v62, v63\nv_exp_f32 v32, v32\nv_exp_f32 v33, v33\nv_mfma_f32_32x32x16_bf16 v[16:31], v[84:87], v[68:71], v[16:31]\n" \
  "s_mov_b64 exec, s[52:53]\nds_read_b128 v[80:83], v106 offset:28160\nds_read_b128 v[84:87], v106 offset:28800\ns_mov_b64 exec, -1\n" \
  "v_exp_f32 v34, v34\nv_exp_f32 v35, v35\nv_cvt_pk_bf16_f32 v64, v32, v33\nv_cvt_pk_bf16_f32 v65, v34, v35\nv_exp_f32 v36, v36\nv_exp_f32 v37, v37\n" \
  "v_mfma_f32_32x32x16_bf16 v[48:63], v[72:75], v[96:99], 0\nv_exp_f32 v38, v38\nv_exp_f32 v39, v39\nv_cvt_pk_bf16_f32 v66, v36, v37\n" \
  "v_cvt_pk_bf16_f32 v67, v38, v39\nv_exp_f32 v40, v40\nv_mfma_f32_32x32x16_bf16 v[48:63], v[76:79], v[100:103], v[48:63]\n" \
  "ds_read_b128 v[72:75], v104 offset:30720\nds_read_b64 v[76:77], v105 offset:30720\nv_exp_f32 v41, v41\nv_exp_f32 v42, v42\nv_exp_f32 v43, v43\n" \
  "v_cvt_pk_bf16_f32 v68, v40, v41\nv_cvt_pk_bf16_f32 v69, v42, v43\nv_exp_f32 v44, v44\nv_exp_f32 v45, v45\ns_waitcnt lgkmcnt(2)\n" \
  "v_mfma_f32_32x32x16_bf16 v[0:15], v[80:83], v[64:67], v[0:15]\nv_exp_f32 v46, v46\nv_exp_f32 v47, v47\nv_cvt_pk_bf16_f32 v70, v44, v45\n" \
  "v_cvt_pk_bf16_f32 v71, v46, v47\nv_exp_f32 v48, v48\nv_exp_f32 v49, v49\nv_mfma_f32_32x32x16_bf16 v[0:15], v[84:87], v[68:71], v[0:15]\n" \
  "v_exp_f32 v50, v50\nv_exp_f32 v51, v51\nv_cvt_pk_bf16_f32 v64, v48, v49\nv_cvt_pk_bf16_f32 v65, v50, v51\nv_exp_f32 v52, v52\nv_exp_f32 v53, v53\n" \
  "s_waitcnt lgkmcnt(0)\nv_mfma_f32_32x32x16_bf16 v[32:47], v[72:75], v[88:91], 0\nv_exp_f32 v54, v54\nv_exp_f32 v55, v55\n" \
  "v_cvt_pk_bf16_f32 v66, v52, v53\nv_cvt_pk_bf16_f32 v67, v54, v55\nv_exp_f32 v56, v56\n" \
  "v_mfma_f32_32x32x16_bf16 v[32:47], v[76:79], v[92:95], v[32:47]\nv_exp_f32 v57, v57\nv_exp_f32 v58, v58\nv_exp_f32 v59, v59\n" \
  "v_cvt_pk_bf16_f32 v68, v56, v57\nv_cvt_pk_bf16_f32 v69, v58, v59\nv_exp_f32 v60, v60\nv_exp_f32 v61, v61\n" \
  "v_mfma_f32_32x32x16_bf16 v[16:31], v[80:83], v[64:67], v[16:31]\nv_exp_f32 v62, v62\nv_exp_f32 v63, v63\nv_cvt_pk_bf16_f32 v70, v60, v61\n" \
  "v_cvt_pk_bf16_f32 v71, v62, v63\nv_exp_f32 v32, v32\nv_exp_f32 v33, v33\nv_mfma_f32_32x32x16_bf16 v[16:31], v[84:87], v[68:71], v[16:31]\n" \
  "s_barrier\ns_mov_b64 exec, s[52:53]\nds_read_b128 v[80:83], v106 offset:30720\nds_read_b128 v[84:87], v106 offset:31360\ns_mov_b64 exec, -1\n" \
  "v_exp_f32 v34, v34\nv_exp_f32 v35, v35\nv_cvt_pk_bf16_f32 v64, v32, v33\nv_cvt_pk_bf16_f32 v65, v34, v35\nv_exp_f32 v36, v36\nv_exp_f32 v37, v37\n" \
  "v_mfma_f32_32x32x16_bf16 v[48:63], v[72:75], v[96:99], 0\nv_exp_f32 v38, v38\nv_exp_f32 v39, v39\nv_cvt_pk_bf16_f32 v66, v36, v37\n" \
  "v_cvt_pk_bf16_f32 v67, v38, v39\nv_exp_f32 v40, v40\nv_mfma_f32_32x32x16_bf16 v[48:63], v[76:79], v[100:103], v[48:63]\n" \
  "ds_read_b128 v[72:75], v104 offset:33280\nds_read_b64 v[76:77], v105 offset:33280\nv_exp_f32 v41, v41\nv_exp_f32 v42, v42\nv_exp_f32 v43, v43\n" \
  "v_cvt_pk_bf16_f32 v68, v40, v41\nv_cvt_pk_bf16_f32 v69, v42, v43\nv_exp_f32 v44, v44\nv_exp_f32 v45, v45\ns_waitcnt lgkmcnt(2)\n" \
  "v_mfma_f32_32x32x16_bf16 v[0:15], v[80:83], v[64:67], v[0:15]\nv_exp_f32 v46, v46\nv_exp_f32 v47, v47\nv_cvt_pk_bf16_f32 v70, v44, v45\n" \
  "v_cvt_pk_bf16_f32 v71, v46, v47\nv_exp_f32 v48, v48\nv_exp_f32 v49, v49\nv_mfma_f32_32x32x16_bf16 v[0:15], v[84:87], v[68:71], v[0:15]\n" \
  "v_exp_f32 v50, v50\nv_exp_f32 v51, v51\nv_cvt_pk_bf16_f32 v64, v48, v49\nv_cvt_pk_bf16_f32 v65, v50, v51\nv_exp_f32 v52, v52\nv_exp_f32 v53, v53\n" \
  "s_waitcnt lgkmcnt(0)\nv_mfma_f32_32x32x16_bf16 v[32:47], v[72:75], v[88:91], 0\nv_exp_f32 v54, v54\nv_exp_f32 v55, v55\n" \
  "v_cvt_pk_bf16_f32 v66, v52, v53\nv_cvt_pk_bf16_f32 v67, v54, v55\nv_exp_f32 v56, v56\n" \
  "v_mfma_f32_32x32x16_bf16 v[32:47], v[76:79], v[92:95], v[32:47]\nv_exp_f32 v57, v57\nv_exp_f32 v58, v58\nv_exp_f32 v59, v59\n" \
  "v_cvt_pk_bf16_f32 v68, v56, v57\nv_cvt_pk_bf16_f32 v69, v58, v59\nv_exp_f32 v60, v60\nv_exp_f32 v61, v61\n" \
  "v_mfma_f32_32x32x16_bf16 v[16:31], v[80:83], v[64:67], v[16:31]\nv_exp_f32 v62, v62\nv_exp_f32 v63, v63\nv_cvt_pk_bf16_f32 v70, v60, v61\n" \
  "v_cvt_pk_bf16_f32 v71, v62, v63\nv_exp_f32 v32, v32\nv_exp_f32 v33, v33\nv_mfma_f32_32x32x16_bf16 v[16:31], v[84:87], v[68:71], v[16:31]\n" \
  "s_cmp_lt_u32 s76, 2\ns_cbranch_scc0 .Lk2s_%=\ns_cmp_lt_u32 %[b], 240\ns_cbranch_scc0 .Lk2s_%=\ns_lshl_b32 s61, s76, 6\n" \
  "v_lshrrev_b32 v108, 4, v127\nv_add_u32 v108, s61, v108\nv_and_b32 v109, 4, v108\nv_lshrrev_b32 v110, 3, v108\nv_lshlrev_b32 v109, 6, v109\n" \
  "v_and_b32 v111, 3, v108\nv_lshl_add_u32 v109, v110, 4, v109\nv_lshl_add_u32 v109, v111, 2, v109\nv_add_u32 v109, 0x14000, v109\n" \
  "ds_read_b32 v132, v109 offset:0\nds_read_b32 v133, v109 offset:512\nds_read_b32 v134, v109 offset:1024\nds_read_b32 v135, v109 offset:1536\n" \
  "ds_read_b32 v136, v109 offset:2048\nds_read_b32 v137, v109 offset:2560\nds_read_b32 v138, v109 offset:3072\nds_read_b32 v139, v109 offset:3584\n" \
  "s_lshl_b32 s61, s65, 7\nv_add_u32 v108, s61, v108\nv_mul_u32_u24 v110, 683, v108\nv_lshlrev_b32 v108, 2, v108\nv_lshrrev_b32 v110, 16, v110\n" \
  "s_movk_i32 s61, 0x1e80\nv_mad_u32_u24 v108, v110, s61, v108\ns_mul_i32 s61, s64, 768\ns_cmp_eq_u32 s69, 0\ns_cselect_b32 s62, 0, 384\n" \
  "s_add_u32 s61, s61, s62\ns_add_u32 s61, s61, 22151168\ns_waitcnt lgkmcnt(0)\nv_add_f32 v132, v132, v136\nv_add_f32 v133, v133, v137\n" \
  "v_add_f32 v134, v134, v138\nv_add_f32 v135, v135, v139\nv_add_f32 v132, v132, v134\nv_add_f32 v133, v133, v135\nv_add_f32 v132, v132, v133\n" \
  "buffer_store_dword v132, v108, s[40:43], s61 offen sc1\n.Lk2s_%=:\ns_mov_b64 exec, s[52:53]\nds_read_b128 v[80:83], v106 offset:33280\n" \
  "ds_read_b128 v[84:87], v106 offset:33920\ns_mov_b64 exec, -1\nv_exp_f32 v34, v34\nv_exp_f32 v35, v35\nv_cvt_pk_bf16_f32 v64, v32, v33\n" \
  "v_cvt_pk_bf16_f32 v65, v34, v35\nv_exp_f32 v36, v36\nv_exp_f32 v37, v37\nv_mfma_f32_32x32x16_bf16 v[48:63], v[72:75], v[96:99], 0\n" \
  "v_exp_f32 v38, v38\nv_exp_f32 v39, v39\nv_cvt_pk_bf16_f32 v66, v36, v37\nv_cvt_pk_bf16_f32 v67, v38, v39\nv_exp_f32 v40, v40\n" \
  "v_mfma_f32_32x32x16_bf16 v[48:63], v[76:79], v[100:103], v[48:63]\nds_read_b128 v[72:75], v104 offset:35840\n" \
  "ds_read_b64 v[76:77], v105 offset:35840\nv_exp_f32 v41, v41\nv_exp_f32 v42, v42\nv_exp_f32 v43, v43\nv_cvt_pk_bf16_f32 v68, v40, v41\n" \
  "v_cvt_pk_bf16_f32 v69, v42, v43\nv_exp_f32 v44, v44\nv_exp_f32 v45, v45\ns_waitcnt lgkmcnt(2)\n" \
  "v_mfma_f32_32x32x16_bf16 v[0:15], v[80:83], v[64:67], v[0:15]\nv_exp_f32 v46, v46\nv_exp_f32 v47, v47\nv_cvt_pk_bf16_f32 v70, v44, v45\n" \
  "v_cvt_pk_bf16_f32 v71, v46, v47\nv_exp_f32 v48, v48\nv_exp_f32 v49, v49\nv_mfma_f32_32x32x16_bf16 v[0:15], v[84:87], v[68:71], v[0:15]\n" \
  "v_exp_f32 v50, v50\nv_exp_f32 v51, v51\nv_cvt_pk_bf16_f32 v64, v48, v49\nv_cvt_pk_bf16_f32 v65, v50, v51\nv_exp_f32 v52, v52\nv_exp_f32 v53, v53\n" \
  "s_waitcnt lgkmcnt(0)\nv_mfma_f32_32x32x16_bf16 v[32:47], v[72:75], v[88:91], 0\nv_exp_f32 v54, v54\nv_exp_f32 v55, v55\n" \
  "v_cvt_pk_bf16_f32 v66, v52, v53\nv_cvt_pk_bf16_f32 v67, v54, v55\nv_exp_f32 v56, v56\n" \
  "v_mfma_f32_32x32x16_bf16 v[32:47], v[76:79], v[92:95], v[32:47]\nv_exp_f32 v57, v57\nv_exp_f32 v58, v58\nv_exp_f32 v59, v59\n" \
  "v_cvt_pk_bf16_f32 v68, v56, v57\nv_cvt_pk_bf16_f32 v69, v58, v59\nv_exp_f32 v60, v60\nv_exp_f32 v61, v61\n" \
  "v_mfma_f32_32x32x16_bf16 v[16:31], v[80:83], v[64:67], v[16:31]\nv_exp_f32 v62, v62\nv_exp_f32 v63, v63\nv_cvt_pk_bf16_f32 v70, v60, v61\n" \
  "v_cvt_pk_bf16_f32 v71, v62, v63\nv_exp_f32 v32, v32\nv_exp_f32 v33, v33\nv_mfma_f32_32x32x16_bf16 v[16:31], v[84:87], v[68:71], v[16:31]\n" \
  "s_mov_b64 exec, s[52:53]\nds_read_b128 v[80:83], v106 offset:35840\nds_read_b128 v[84:87], v106 offset:36480\ns_mov_b64 exec, -1\n" \
  "v_exp_f32 v34, v34\nv_exp_f32 v35, v35\nv_cvt_pk_bf16_f32 v64, v32, v33\nv_cvt_pk_bf16_f32 v65, v34, v35\nv_exp_f32 v36, v36\nv_exp_f32 v37, v37\n" \
  "v_mfma_f32_32x32x16_bf16 v[48:63], v[72:75], v[96:99], 0\nv_exp_f32 v38, v38\nv_exp_f32 v39, v39\nv_cvt_pk_bf16_f32 v66, v36, v37\n" \
  "v_cvt_pk_bf16_f32 v67, v38, v39\nv_exp_f32 v40, v40\nv_mfma_f32_32x32x16_bf16 v[48:63], v[76:79], v[100:103], v[48:63]\n" \
  "ds_read_b128 v[72:75], v104 offset:38400\nds_read_b64 v[76:77], v105 offset:38400\nv_exp_f32 v41, v41\nv_exp_f32 v42, v42\nv_exp_f32 v43, v43\n" \
  "v_cvt_pk_bf16_f32 v68, v40, v41\nv_cvt_pk_bf16_f32 v69, v42, v43\nv_exp_f32 v44, v44\nv_exp_f32 v45, v45\ns_waitcnt lgkmcnt(2)\n" \
  "v_mfma_f32_32x32x16_bf16 v[0:15], v[80:83], v[64:67], v[0:15]\nv_exp_f32 v46, v46\nv_exp_f32 v47, v47\nv_cvt_pk_bf16_f32 v70, v44, v45\n" \
  "v_cvt_pk_bf16_f32 v71, v46, v47\nv_exp_f32 v48, v48\nv_exp_f32 v49, v49\nv_mfma_f32_32x32x16_bf16 v[0:15], v[84:87], v[68:71], v[0:15]\n" \
  "v_exp_f32 v50, v50\nv_exp_f32 v51, v51\nv_cvt_pk_bf16_f32 v64, v48, v49\nv_cvt_pk_bf16_f32 v65, v50, v51\nv_exp_f32 v52, v52\nv_exp_f32 v53, v53\n" \
  "s_waitcnt lgkmcnt(0)\nv_mfma_f32_32x32x16_bf16 v[32:47], v[72:75], v[88:91], 0\nv_exp_f32 v54, v54\nv_exp_f32 v55, v55\n" \
  "v_cvt_pk_bf16_f32 v66, v52, v53\nv_cvt_pk_bf16_f32 v67, v54, v55\nv_exp_f32 v56, v56\n" \
  "v_mfma_f32_32x32x16_bf16 v[32:47], v[76:79], v[92:95], v[32:47]\nv_exp_f32 v57, v57\nv_exp_f32 v58, v58\nv_exp_f32 v59, v59\n" \
  "v_cvt_pk_bf16_f32 v68, v56, v57\nv_cvt_pk_bf16_f32 v69, v58, v59\nv_exp_f32 v60, v60\nv_exp_f32 v61, v61\n" \
  "v_mfma_f32_32x32x16_bf16 v[16:31], v[80:83], v[64:67], v[16:31]\nv_exp_f32 v62, v62\nv_exp_f32 v63, v63\nv_cvt_pk_bf16_f32 v70, v60, v61\n" \
  "v_cvt_pk_bf16_f32 v71, v62, v63\nv_exp_f32 v32, v32\nv_exp_f32 v33, v33\nv_mfma_f32_32x32x16_bf16 v[16:31], v[84:87], v[68:71], v[16:31]\n" \
  "s_mov_b64 exec, s[52:53]\nds_read_b128 v[80:83], v106 offset:38400\nds_read_b128 v[84:87], v106 offset:39040\ns_mov_b64 exec, -1\n" \
  "v_exp_f32 v34, v34\nv_exp_f32 v35, v35\nv_cvt_pk_bf16_f32 v64, v32, v33\nv_cvt_pk_bf16_f32 v65, v34, v35\nv_exp_f32 v36, v36\nv_exp_f32 v37, v37\n" \
  "v_mfma_f32_32x32x16_bf16 v[48:63], v[72:75], v[96:99], 0\nv_exp_f32 v38, v38\nv_exp_f32 v39, v39\nv_cvt_pk_bf16_f32 v66, v36, v37\n" \
  "v_cvt_pk_bf16_f32 v67, v38, v39\nv_exp_f32 v40, v40\nv_mfma_f32_32x32x16_bf16 v[48:63], v[76:79], v[100:103], v[48:63]\n" \
  "ds_read_b128 v[72:75], v128 offset:0\nds_read_b64 v[76:77], v129 offset:0\nv_exp_f32 v41, v41\nv_exp_f32 v42, v42\nv_exp_f32 v43, v43\n" \
  "v_cvt_pk_bf16_f32 v68, v40, v41\nv_cvt_pk_bf16_f32 v69, v42, v43\nv_exp_f32 v44, v44\nv_exp_f32 v45, v45\ns_waitcnt lgkmcnt(2)\n" \
  "v_mfma_f32_32x32x16_bf16 v[0:15], v[80:83], v[64:67], v[0:15]\nv_exp_f32 v46, v46\nv_exp_f32 v47, v47\nv_cvt_pk_bf16_f32 v70, v44, v45\n" \
  "v_cvt_pk_bf16_f32 v71, v46, v47\nv_exp_f32 v48, v48\nv_exp_f32 v49, v49\nv_mfma_f32_32x32x16_bf16 v[0:15], v[84:87], v[68:71], v[0:15]\n" \
  "v_exp_f32 v50, v50\nv_exp_f32 v51, v51\nv_cvt_pk_bf16_f32 v64, v48, v49\nv_cvt_pk_bf16_f32 v65, v50, v51\nv_exp_f32 v52, v52\nv_exp_f32 v53, v53\n" \
  "s_waitcnt lgkmcnt(0)\nv_mfma_f32_32x32x16_bf16 v[32:47], v[72:75], v[88:91], 0\nv_exp_f32 v54, v54\nv_exp_f32 v55, v55\n" \
  "v_cvt_pk_bf16_f32 v66, v52, v53\nv_cvt_pk_bf16_f32 v67, v54, v55\nv_exp_f32 v56, v56\n" \
  "v_mfma_f32_32x32x16_bf16 v[32:47], v[76:79], v[92:95], v[32:47]\nv_exp_f32 v57, v57\nv_exp_f32 v58, v58\nv_exp_f32 v59, v59\n" \
  "v_cvt_pk_bf16_f32 v68, v56, v57\nv_cvt_pk_bf16_f32 v69, v58, v59\nv_exp_f32 v60, v60\nv_exp_f32 v61, v61\n" \
  "v_mfma_f32_32x32x16_bf16 v[16:31], v[80:83], v[64:67], v[16:31]\nv_exp_f32 v62, v62\nv_exp_f32 v63, v63\nv_cvt_pk_bf16_f32 v70, v60, v61\n" \
  "v_cvt_pk_bf16_f32 v71, v62, v63\nv_exp_f32 v32, v32\nv_exp_f32 v33, v33\nv_mfma_f32_32x32x16_bf16 v[16:31], v[84:87], v[68:71], v[16:31]\n" \
  "s_waitcnt vmcnt(0)\ns_barrier\ns_mov_b64 exec, s[52:53]\nds_read_b128 v[80:83], v130 offset:0\nds_read_b128 v[84:87], v130 offset:640\n" \
  "s_mov_b64 exec, -1\nv_exp_f32 v34, v34\nv_exp_f32 v35, v35\nv_cvt_pk_bf16_f32 v64, v32, v33\nv_cvt_pk_bf16_f32 v65, v34, v35\nv_exp_f32 v36, v36\n" \
  "v_exp_f32 v37, v37\nv_mfma_f32_32x32x16_bf16 v[48:63], v[72:75], v[96:99], 0\nv_exp_f32 v38, v38\nv_exp_f32 v39, v39\n" \
  "v_cvt_pk_bf16_f32 v66, v36, v37\nv_cvt_pk_bf16_f32 v67, v38, v39\nv_exp_f32 v40, v40\n" \
  "v_mfma_f32_32x32x16_bf16 v[48:63], v[76:79], v[100:103], v[48:63]\nds_read_b128 v[72:75], v128 offset:2560\n" \
  "ds_read_b64 v[76:77], v129 offset:2560\nv_exp_f32 v41, v41\nv_exp_f32 v42, v42\nv_exp_f32 v43, v43\nv_cvt_pk_bf16_f32 v68, v40, v41\n" \
  "v_cvt_pk_bf16_f32 v69, v42, v43\nv_exp_f32 v44, v44\nv_exp_f32 v45, v45\ns_waitcnt lgkmcnt(2)\n" \
  "v_mfma_f32_32x32x16_bf16 v[0:15], v[80:83], v[64:67], v[0:15]\nv_exp_f32 v46, v46\nv_exp_f32 v47, v47\nv_cvt_pk_bf16_f32 v70, v44, v45\n" \
  "v_cvt_pk_bf16_f32 v71, v46, v47\nv_exp_f32 v48, v48\nv_exp_f32 v49, v49\nv_mfma_f32_32x32x16_bf16 v[0:15], v[84:87], v[68:71], v[0:15]\n" \
  "v_exp_f32 v50, v50\nv_exp_f32 v51, v51\nv_cvt_pk_bf16_f32 v64, v48, v49\nv_cvt_pk_bf16_f32 v65, v50, v51\nv_exp_f32 v52, v52\nv_exp_f32 v53, v53\n" \
  "s_waitcnt lgkmcnt(0)\nv_mfma_f32_32x32x16_bf16 v[32:47], v[72:75], v[88:91], 0\nv_exp_f32 v54, v54\nv_exp_f32 v55, v55\n" \
  "v_cvt_pk_bf16_f32 v66, v52, v53\nv_cvt_pk_bf16_f32 v67, v54, v55\nv_exp_f32 v56, v56\n" \
  "v_mfma_f32_32x32x16_bf16 v[32:47], v[76:79], v[92:95], v[32:47]\nv_exp_f32 v57, v57\nv_exp_f32 v58, v58\nv_exp_f32 v59, v59\n" \
  "v_cvt_pk_bf16_f32 v68, v56, v57\nv_cvt_pk_bf16_f32 v69, v58, v59\nv_exp_f32 v60, v60\nv_exp_f32 v61, v61\n" \
  "v_mfma_f32_32x32x16_bf16 v[16:31], v[80:83], v[64:67], v[16:31]\nv_exp_f32 v62, v62\nv_exp_f32 v63, v63\nv_cvt_pk_bf16_f32 v70, v60, v61\n" \
  "v_cvt_pk_bf16_f32 v71, v62, v63\nv_exp_f32 v32, v32\nv_exp_f32 v33, v33\nv_mfma_f32_32x32x16_bf16 v[16:31], v[84:87], v[68:71], v[16:31]\n" \
  "s_cmp_eq_u32 s76, 0\ns_cbranch_scc0 .Lk2t_%=\ns_lshl_b32 s61, %[b], 3\ns_add_u32 s61, s61, 81920\nv_mov_b64 v[108:109], %[ep]\ns_mov_b64 exec, 1\n" \
  "buffer_store_dwordx2 v[108:109], off, %[rf], s61 sc1\ns_mov_b64 exec, -1\n.Lk2t_%=:\ns_mov_b64 exec, s[52:53]\n" \
  "ds_read_b128 v[80:83], v130 offset:2560\nds_read_b128 v[84:87], v130 offset:3200\ns_mov_b64 exec, -1\nv_exp_f32 v34, v34\nv_exp_f32 v35, v35\n" \
  "v_cvt_pk_bf16_f32 v64, v32, v33\nv_cvt_pk_bf16_f32 v65, v34, v35\nv_exp_f32 v36, v36\nv_exp_f32 v37, v37\n" \
  "v_mfma_f32_32x32x16_bf16 v[48:63], v[72:75], v[96:99], 0\nv_exp_f32 v38, v38\nv_exp_f32 v39, v39\nv_cvt_pk_bf16_f32 v66, v36, v37\n" \
  "v_cvt_pk_bf16_f32 v67, v38, v39\nv_exp_f32 v40, v40\nv_mfma_f32_32x32x16_bf16 v[48:63], v[76:79], v[100:103], v[48:63]\n" \
  "ds_read_b128 v[72:75], v128 offset:5120\nds_read_b64 v[76:77], v129 offset:5120\nv_exp_f32 v41, v41\nv_exp_f32 v42, v42\nv_exp_f32 v43, v43\n" \
  "v_cvt_pk_bf16_f32 v68, v40, v41\nv_cvt_pk_bf16_f32 v69, v42, v43\nv_exp_f32 v44, v44\nv_exp_f32 v45, v45\ns_waitcnt lgkmcnt(2)\n" \
  "v_mfma_f32_32x32x16_bf16 v[0:15], v[80:83], v[64:67], v[0:15]\nv_exp_f32 v46, v46\nv_exp_f32 v47, v47\nv_cvt_pk_bf16_f32 v70, v44, v45\n" \
  "v_cvt_pk_bf16_f32 v71, v46, v47\nv_exp_f32 v48, v48\nv_exp_f32 v49, v49\nv_mfma_f32_32x32x16_bf16 v[0:15], v[84:87], v[68:71], v[0:15]\n" \
  "v_exp_f32 v50, v50\nv_exp_f32 v51, v51\nv_cvt_pk_bf16_f32 v64, v48, v49\nv_cvt_pk_bf16_f32 v65, v50, v51\nv_exp_f32 v52, v52\nv_exp_f32 v53, v53\n" \
  "s_waitcnt lgkmcnt(0)\nv_mfma_f32_32x32x16_bf16 v[32:47], v[72:75], v[88:91], 0\nv_exp_f32 v54, v54\nv_exp_f32 v55, v55\n" \
  "v_cvt_pk_bf16_f32 v66, v52, v53\nv_cvt_pk_bf16_f32 v67, v54, v55\nv_exp_f32 v56, v56\n" \
  "v_mfma_f32_32x32x16_bf16 v[32:47], v[76:79], v[92:95], v[32:47]\nv_exp_f32 v57, v57\nv_exp_f32 v58, v58\nv_exp_f32 v59, v59\n" \
  "v_cvt_pk_bf16_f32 v68, v56, v57\nv_cvt_pk_bf16_f32 v69, v58, v59\nv_exp_f32 v60, v60\nv_exp_f32 v61, v61\n" \
  "v_mfma_f32_32x32x16_bf16 v[16:31], v[80:83], v[64:67], v[16:31]\nv_exp_f32 v62, v62\nv_exp_f32 v63, v63\nv_cvt_pk_bf16_f32 v70, v60, v61\n" \
  "v_cvt_pk_bf16_f32 v71, v62, v63\nv_exp_f32 v32, v32\nv_exp_f32 v33, v33\nv_mfma_f32_32x32x16_bf16 v[16:31], v[84:87], v[68:71], v[16:31]\n" \
  "s_mov_b64 exec, s[52:53]\nds_read_b128 v[80:83], v130 offset:5120\nds_read_b128 v[84:87], v130 offset:5760\ns_mov_b64 exec, -1\n" \
  "v_exp_f32 v34, v34\nv_exp_f32 v35, v35\nv_cvt_pk_bf16_f32 v64, v32, v33\nv_cvt_pk_bf16_f32 v65, v34, v35\nv_exp_f32 v36, v36\nv_exp_f32 v37, v37\n" \
  "v_mfma_f32_32x32x16_bf16 v[48:63], v[72:75], v[96:99], 0\nv_exp_f32 v38, v38\nv_exp_f32 v39, v39\nv_cvt_pk_bf16_f32 v66, v36, v37\n" \
  "v_cvt_pk_bf16_f32 v67, v38, v39\nv_exp_f32 v40, v40\nv_mfma_f32_32x32x16_bf16 v[48:63], v[76:79], v[100:103], v[48:63]\n" \
  "ds_read_b128 v[72:75], v128 offset:7680\nds_read_b64 v[76:77], v129 offset:7680\nv_exp_f32 v41, v41\nv_exp_f32 v42, v42\nv_exp_f32 v43, v43\n" \
  "v_cvt_pk_bf16_f32 v68, v40, v41\nv_cvt_pk_bf16_f32 v69, v42, v43\nv_exp_f32 v44, v44\nv_exp_f32 v45, v45\ns_waitcnt lgkmcnt(2)\n" \
  "v_mfma_f32_32x32x16_bf16 v[0:15], v[80:83], v[64:67], v[0:15]\nv_exp_f32 v46, v46\nv_exp_f32 v47, v47\nv_cvt_pk_bf16_f32 v70, v44, v45\n" \
  "v_cvt_pk_bf16_f32 v71, v46, v47\nv_exp_f32 v48, v48\nv_exp_f32 v49, v49\nv_mfma_f32_32x32x16_bf16 v[0:15], v[84:87], v[68:71], v[0:15]\n" \
  "v_exp_f32 v50, v50\nv_exp_f32 v51, v51\nv_cvt_pk_bf16_f32 v64, v48, v49\nv_cvt_pk_bf16_f32 v65, v50, v51\nv_exp_f32 v52, v52\nv_exp_f32 v53, v53\n" \
  "s_waitcnt lgkmcnt(0)\nv_mfma_f32_32x32x16_bf16 v[32:47], v[72:75], v[88:91], 0\nv_exp_f32 v54, v54\nv_exp_f32 v55, v55\n" \
  "v_cvt_pk_bf16_f32 v66, v52, v53\nv_cvt_pk_bf16_f32 v67, v54, v55\nv_exp_f32 v56, v56\n" \
  "v_mfma_f32_32x32x16_bf16 v[32:47], v[76:79], v[92:95], v[32:47]\nv_exp_f32 v57, v57\nv_exp_f32 v58, v58\nv_exp_f32 v59, v59\n" \
  "v_cvt_pk_bf16_f32 v68, v56, v57\nv_cvt_pk_bf16_f32 v69, v58, v59\nv_exp_f32 v60, v60\nv_exp_f32 v61, v61\n" \
  "v_mfma_f32_32x32x16_bf16 v[16:31], v[80:83], v[64:67], v[16:31]\nv_exp_f32 v62, v62\nv_exp_f32 v63, v63\nv_cvt_pk_bf16_f32 v70, v60, v61\n" \
  "v_cvt_pk_bf16_f32 v71, v62, v63\nv_exp_f32 v32, v32\nv_exp_f32 v33, v33\nv_mfma_f32_32x32x16_bf16 v[16:31], v[84:87], v[68:71], v[16:31]\n" \
  "s_cmp_eq_u32 s54, 0\ns_cbranch_scc1 .Lk2m_%=\ns_mov_b32 exec_hi, 0\nbuffer_load_dword v108, v125, s[40:43], s75 offen offset:0 sc1\n" \
  "buffer_load_dword v109, v125, s[40:43], s75 offen offset:512 sc1\ns_mov_b32 exec_hi, -1\n.Lk2m_%=:\ns_mov_b64 exec, s[52:53]\n" \
  "ds_read_b128 v[80:83], v130 offset:7680\nds_read_b128 v[84:87], v130 offset:8320\ns_mov_b64 exec, -1\nv_exp_f32 v34, v34\nv_exp_f32 v35, v35\n" \
  "v_cvt_pk_bf16_f32 v64, v32, v33\nv_cvt_pk_bf16_f32 v65, v34, v35\nv_exp_f32 v36, v36\nv_exp_f32 v37, v37\n" \
  "v_mfma_f32_32x32x16_bf16 v[48:63], v[72:75], v[96:99], 0\nv_exp_f32 v38, v38\nv_exp_f32 v39, v39\nv_cvt_pk_bf16_f32 v66, v36, v37\n" \
  "v_cvt_pk_bf16_f32 v67, v38, v39\nv_exp_f32 v40, v40\nv_mfma_f32_32x32x16_bf16 v[48:63], v[76:79], v[100:103], v[48:63]\n" \
  "ds_read_b128 v[72:75], v128 offset:10240\nds_read_b64 v[76:77], v129 offset:10240\nv_exp_f32 v41, v41\nv_exp_f32 v42, v42\nv_exp_f32 v43, v43\n" \
  "v_cvt_pk_bf16_f32 v68, v40, v41\nv_cvt_pk_bf16_f32 v69, v42, v43\nv_exp_f32 v44, v44\nv_exp_f32 v45, v45\ns_waitcnt lgkmcnt(2)\n" \
  "v_mfma_f32_32x32x16_bf16 v[0:15], v[80:83], v[64:67], v[0:15]\nv_exp_f32 v46, v46\nv_exp_f32 v47, v47\nv_cvt_pk_bf16_f32 v70, v44, v45\n" \
  "v_cvt_pk_bf16_f32 v71, v46, v47\nv_exp_f32 v48, v48\nv_exp_f32 v49, v49\nv_mfma_f32_32x32x16_bf16 v[0:15], v[84:87], v[68:71], v[0:15]\n" \
  "v_exp_f32 v50, v50\nv_exp_f32 v51, v51\nv_cvt_pk_bf16_f32 v64, v48, v49\nv_cvt_pk_bf16_f32 v65, v50, v51\nv_exp_f32 v52, v52\nv_exp_f32 v53, v53\n" \
  "s_waitcnt lgkmcnt(0)\nv_mfma_f32_32x32x16_bf16 v[32:47], v[72:75], v[88:91], 0\nv_exp_f32 v54, v54\nv_exp_f32 v55, v55\n" \
  "v_cvt_pk_bf16_f32 v66, v52, v53\nv_cvt_pk_bf16_f32 v67, v54, v55\nv_exp_f32 v56, v56\n" \
  "v_mfma_f32_32x32x16_bf16 v[32:47], v[76:79], v[92:95], v[32:47]\nv_exp_f32 v57, v57\nv_exp_f32 v58, v58\nv_exp_f32 v59, v59\n" \
  "v_cvt_pk_bf16_f32 v68, v56, v57\nv_cvt_pk_bf16_f32 v69, v58, v59\nv_exp_f32 v60, v60\nv_exp_f32 v61, v61\n" \
  "v_mfma_f32_32x32x16_bf16 v[16:31], v[80:83], v[64:67], v[16:31]\nv_exp_f32 v62, v62\nv_exp_f32 v63, v63\nv_cvt_pk_bf16_f32 v70, v60, v61\n" \
  "v_cvt_pk_bf16_f32 v71, v62, v63\nv_exp_f32 v32, v32\nv_exp_f32 v33, v33\nv_mfma_f32_32x32x16_bf16 v[16:31], v[84:87], v[68:71], v[16:31]\n" \
  "s_mov_b64 exec, s[52:53]\nds_read_b128 v[80:83], v130 offset:10240\nds_read_b128 v[84:87], v130 offset:10880\ns_mov_b64 exec, -1\n" \
  "v_exp_f32 v34, v34\nv_exp_f32 v35, v35\nv_cvt_pk_bf16_f32 v64, v32, v33\nv_cvt_pk_bf16_f32 v65, v34, v35\nv_exp_f32 v36, v36\nv_exp_f32 v37, v37\n" \
  "v_mfma_f32_32x32x16_bf16 v[48:63], v[72:75], v[96:99], 0\nv_exp_f32 v38, v38\nv_exp_f32 v39, v39\nv_cvt_pk_bf16_f32 v66, v36, v37\n" \
  "v_cvt_pk_bf16_f32 v67, v38, v39\nv_exp_f32 v40, v40\nv_mfma_f32_32x32x16_bf16 v[48:63], v[76:79], v[100:103], v[48:63]\n" \
  "ds_read_b128 v[72:75], v128 offset:12800\nds_read_b64 v[76:77], v129 offset:12800\nv_exp_f32 v41, v41\nv_exp_f32 v42, v42\nv_exp_f32 v43, v43\n" \
  "v_cvt_pk_bf16_f32 v68, v40, v41\nv_cvt_pk_bf16_f32 v69, v42, v43\nv_exp_f32 v44, v44\nv_exp_f32 v45, v45\ns_waitcnt lgkmcnt(2)\n" \
  "v_mfma_f32_32x32x16_bf16 v[0:15], v[80:83], v[64:67], v[0:15]\nv_exp_f32 v46, v46\nv_exp_f32 v47, v47\nv_cvt_pk_bf16_f32 v70, v44, v45\n" \
  "v_cvt_pk_bf16_f32 v71, v46, v47\nv_exp_f32 v48, v48\nv_exp_f32 v49, v49\nv_mfma_f32_32x32x16_bf16 v[0:15], v[84:87], v[68:71], v[0:15]\n" \
  "v_exp_f32 v50, v50\nv_exp_f32 v51, v51\nv_cvt_pk_bf16_f32 v64, v48, v49\nv_cvt_pk_bf16_f32 v65, v50, v51\nv_exp_f32 v52, v52\nv_exp_f32 v53, v53\n" \
  "s_waitcnt lgkmcnt(0)\nv_mfma_f32_32x32x16_bf16 v[32:47], v[72:75], v[88:91], 0\nv_exp_f32 v54, v54\nv_exp_f32 v55, v55\n" \
  "v_cvt_pk_bf16_f32 v66, v52, v53\nv_cvt_pk_bf16_f32 v67, v54, v55\nv_exp_f32 v56, v56\n" \
  "v_mfma_f32_32x32x16_bf16 v[32:47], v[76:79], v[92:95], v[32:47]\nv_exp_f32 v57, v57\nv_exp_f32 v58, v58\nv_exp_f32 v59, v59\n" \
  "v_cvt_pk_bf16_f32 v68, v56, v57\nv_cvt_pk_bf16_f32 v69, v58, v59\nv_exp_f32 v60, v60\nv_exp_f32 v61, v61\n" \
  "v_mfma_f32_32x32x16_bf16 v[16:31], v[80:83], v[64:67], v[16:31]\nv_exp_f32 v62, v62\nv_exp_f32 v63, v63\nv_cvt_pk_bf16_f32 v70, v60, v61\n" \
  "v_cvt_pk_bf16_f32 v71, v62, v63\nv_exp_f32 v32, v32\nv_exp_f32 v33, v33\nv_mfma_f32_32x32x16_bf16 v[16:31], v[84:87], v[68:71], v[16:31]\n" \
  "s_cmp_eq_u32 s54, 0\ns_cbranch_scc0 .Lk2z_%=\nv_lshlrev_b32 v108, 3, v127\ns_getpc_b64 s[44:45]\n.Lk2d_%=:\ns_add_u32 s44, s44, .Ltail-.Lk2d_%=\n" \
  "s_addc_u32 s45, s45, 0\ns_mov_b32 s46, k_end-.Ltail\ns_lshl_b32 s61, s60, 13\nbuffer_load_dword v132, v108, s[44:47], s61 offen\n.Lk2z_%=:\n" \
  "s_mov_b64 exec, s[52:53]\nds_read_b128 v[80:83], v130 offset:12800\nds_read_b128 v[84:87], v130 offset:13440\ns_mov_b64 exec, -1\n" \
  "v_exp_f32 v34, v34\nv_exp_f32 v35, v35\nv_cvt_pk_bf16_f32 v64, v32, v33\nv_cvt_pk_bf16_f32 v65, v34, v35\nv_exp_f32 v36, v36\nv_exp_f32 v37, v37\n" \
  "v_mfma_f32_32x32x16_bf16 v[48:63], v[72:75], v[96:99], 0\nv_exp_f32 v38, v38\nv_exp_f32 v39, v39\nv_cvt_pk_bf16_f32 v66, v36, v37\n" \
  "v_cvt_pk_bf16_f32 v67, v38, v39\nv_exp_f32 v40, v40\nv_mfma_f32_32x32x16_bf16 v[48:63], v[76:79], v[100:103], v[48:63]\n" \
  "ds_read_b128 v[72:75], v128 offset:15360\nds_read_b64 v[76:77], v129 offset:15360\nv_exp_f32 v41, v41\nv_exp_f32 v42, v42\nv_exp_f32 v43, v43\n" \
  "v_cvt_pk_bf16_f32 v68, v40, v41\nv_cvt_pk_bf16_f32 v69, v42, v43\nv_exp_f32 v44, v44\nv_exp_f32 v45, v45\ns_waitcnt lgkmcnt(2)\n" \
  "v_mfma_f32_32x32x16_bf16 v[0:15], v[80:83], v[64:67], v[0:15]\nv_exp_f32 v46, v46\nv_exp_f32 v47, v47\nv_cvt_pk_bf16_f32 v70, v44, v45\n" \
  "v_cvt_pk_bf16_f32 v71, v46, v47\nv_exp_f32 v48, v48\nv_exp_f32 v49, v49\nv_mfma_f32_32x32x16_bf16 v[0:15], v[84:87], v[68:71], v[0:15]\n" \
  "v_exp_f32 v50, v50\nv_exp_f32 v51, v51\nv_cvt_pk_bf16_f32 v64, v48, v49\nv_cvt_pk_bf16_f32 v65, v50, v51\nv_exp_f32 v52, v52\nv_exp_f32 v53, v53\n" \
  "s_waitcnt lgkmcnt(0)\nv_mfma_f32_32x32x16_bf16 v[32:47], v[72:75], v[88:91], 0\nv_exp_f32 v54, v54\nv_exp_f32 v55, v55\n" \
  "v_cvt_pk_bf16_f32 v66, v52, v53\nv_cvt_pk_bf16_f32 v67, v54, v55\nv_exp_f32 v56, v56\n" \
  "v_mfma_f32_32x32x16_bf16 v[32:47], v[76:79], v[92:95], v[32:47]\nv_exp_f32 v57, v57\nv_exp_f32 v58, v58\nv_exp_f32 v59, v59\n" \
  "v_cvt_pk_bf16_f32 v68, v56, v57\nv_cvt_pk_bf16_f32 v69, v58, v59\nv_exp_f32 v60, v60\nv_exp_f32 v61, v61\n" \
  "v_mfma_f32_32x32x16_bf16 v[16:31], v[80:83], v[64:67], v[16:31]\nv_exp_f32 v62, v62\nv_exp_f32 v63, v63\nv_cvt_pk_bf16_f32 v70, v60, v61\n" \
  "v_cvt_pk_bf16_f32 v71, v62, v63\nv_exp_f32 v32, v32\nv_exp_f32 v33, v33\nv_mfma_f32_32x32x16_bf16 v[16:31], v[84:87], v[68:71], v[16:31]\n" \
  "s_mov_b64 exec, s[52:53]\nds_read_b128 v[80:83], v130 offset:15360\nds_read_b128 v[84:87], v130 offset:16000\ns_mov_b64 exec, -1\n" \
  "v_exp_f32 v34, v34\nv_exp_f32 v35, v35\nv_cvt_pk_bf16_f32 v64, v32, v33\nv_cvt_pk_bf16_f32 v65, v34, v35\nv_exp_f32 v36, v36\nv_exp_f32 v37, v37\n" \
  "v_mfma_f32_32x32x16_bf16 v[48:63], v[72:75], v[96:99], 0\nv_exp_f32 v38, v38\nv_exp_f32 v39, v39\nv_cvt_pk_bf16_f32 v66, v36, v37\n" \
  "v_cvt_pk_bf16_f32 v67, v38, v39\nv_exp_f32 v40, v40\nv_mfma_f32_32x32x16_bf16 v[48:63], v[76:79], v[100:103], v[48:63]\n" \
  "ds_read_b128 v[72:75], v128 offset:17920\nds_read_b64 v[76:77], v129 offset:17920\nv_exp_f32 v41, v41\nv_exp_f32 v42, v42\nv_exp_f32 v43, v43\n" \
  "v_cvt_pk_bf16_f32 v68, v40, v41\nv_cvt_pk_bf16_f32 v69, v42, v43\nv_exp_f32 v44, v44\nv_exp_f32 v45, v45\ns_waitcnt lgkmcnt(2)\n" \
  "v_mfma_f32_32x32x16_bf16 v[0:15], v[80:83], v[64:67], v[0:15]\nv_exp_f32 v46, v46\nv_exp_f32 v47, v47\nv_cvt_pk_bf16_f32 v70, v44, v45\n" \
  "v_cvt_pk_bf16_f32 v71, v46, v47\nv_exp_f32 v48, v48\nv_exp_f32 v49, v49\nv_mfma_f32_32x32x16_bf16 v[0:15], v[84:87], v[68:71], v[0:15]\n" \
  "v_exp_f32 v50, v50\nv_exp_f32 v51, v51\nv_cvt_pk_bf16_f32 v64, v48, v49\nv_cvt_pk_bf16_f32 v65, v50, v51\nv_exp_f32 v52, v52\nv_exp_f32 v53, v53\n" \
  "s_waitcnt lgkmcnt(0)\nv_mfma_f32_32x32x16_bf16 v[32:47], v[72:75], v[88:91], 0\nv_exp_f32 v54, v54\nv_exp_f32 v55, v55\n" \
  "v_cvt_pk_bf16_f32 v66, v52, v53\nv_cvt_pk_bf16_f32 v67, v54, v55\nv_exp_f32 v56, v56\n" \
  "v_mfma_f32_32x32x16_bf16 v[32:47], v[76:79], v[92:95], v[32:47]\nv_exp_f32 v57, v57\nv_exp_f32 v58, v58\nv_exp_f32 v59, v59\n" \
  "v_cvt_pk_bf16_f32 v68, v56, v57\nv_cvt_pk_bf16_f32 v69, v58, v59\nv_exp_f32 v60, v60\nv_exp_f32 v61, v61\n" \
  "v_mfma_f32_32x32x16_bf16 v[16:31], v[80:83], v[64:67], v[16:31]\nv_exp_f32 v62, v62\nv_exp_f32 v63, v63\nv_cvt_pk_bf16_f32 v70, v60, v61\n" \
  "v_cvt_pk_bf16_f32 v71, v62, v63\nv_exp_f32 v32, v32\nv_exp_f32 v33, v33\nv_mfma_f32_32x32x16_bf16 v[16:31], v[84:87], v[68:71], v[16:31]\n" \
  "s_mov_b64 exec, s[52:53]\nds_read_b128 v[80:83], v130 offset:17920\nds_read_b128 v[84:87], v130 offset:18560\ns_mov_b64 exec, -1\n" \
  "v_exp_f32 v34, v34\nv_exp_f32 v35, v35\nv_cvt_pk_bf16_f32 v64, v32, v33\nv_cvt_pk_bf16_f32 v65, v34, v35\nv_exp_f32 v36, v36\nv_exp_f32 v37, v37\n" \
  "v_mfma_f32_32x32x16_bf16 v[48:63], v[72:75], v[96:99], 0\nv_exp_f32 v38, v38\nv_exp_f32 v39, v39\nv_cvt_pk_bf16_f32 v66, v36, v37\n" \
  "v_cvt_pk_bf16_f32 v67, v38, v39\nv_exp_f32 v40, v40\nv_mfma_f32_32x32x16_bf16 v[48:63], v[76:79], v[100:103], v[48:63]\n" \
  "ds_read_b128 v[72:75], v128 offset:20480\nds_read_b64 v[76:77], v129 offset:20480\nv_exp_f32 v41, v41\nv_exp_f32 v42, v42\nv_exp_f32 v43, v43\n" \
  "v_cvt_pk_bf16_f32 v68, v40, v41\nv_cvt_pk_bf16_f32 v69, v42, v43\nv_exp_f32 v44, v44\nv_exp_f32 v45, v45\ns_waitcnt lgkmcnt(2)\n" \
  "v_mfma_f32_32x32x16_bf16 v[0:15], v[80:83], v[64:67], v[0:15]\nv_exp_f32 v46, v46\nv_exp_f32 v47, v47\nv_cvt_pk_bf16_f32 v70, v44, v45\n" \
  "v_cvt_pk_bf16_f32 v71, v46, v47\nv_exp_f32 v48, v48\nv_exp_f32 v49, v49\nv_mfma_f32_32x32x16_bf16 v[0:15], v[84:87], v[68:71], v[0:15]\n" \
  "v_exp_f32 v50, v50\nv_exp_f32 v51, v51\nv_cvt_pk_bf16_f32 v64, v48, v49\nv_cvt_pk_bf16_f32 v65, v50, v51\nv_exp_f32 v52, v52\nv_exp_f32 v53, v53\n" \
  "s_waitcnt lgkmcnt(0)\nv_mfma_f32_32x32x16_bf16 v[32:47], v[72:75], v[88:91], 0\nv_exp_f32 v54, v54\nv_exp_f32 v55, v55\n" \
  "v_cvt_pk_bf16_f32 v66, v52, v53\nv_cvt_pk_bf16_f32 v67, v54, v55\nv_exp_f32 v56, v56\n" \
  "v_mfma_f32_32x32x16_bf16 v[32:47], v[76:79], v[92:95], v[32:47]\nv_exp_f32 v57, v57\nv_exp_f32 v58, v58\nv_exp_f32 v59, v59\n" \
  "v_cvt_pk_bf16_f32 v68, v56, v57\nv_cvt_pk_bf16_f32 v69, v58, v59\nv_exp_f32 v60, v60\nv_exp_f32 v61, v61\n" \
  "v_mfma_f32_32x32x16_bf16 v[16:31], v[80:83], v[64:67], v[16:31]\nv_exp_f32 v62, v62\nv_exp_f32 v63, v63\nv_cvt_pk_bf16_f32 v70, v60, v61\n" \
  "v_cvt_pk_bf16_f32 v71, v62, v63\nv_exp_f32 v32, v32\nv_exp_f32 v33, v33\nv_mfma_f32_32x32x16_bf16 v[16:31], v[84:87], v[68:71], v[16:31]\n" \
  "s_mov_b64 exec, s[52:53]\nds_read_b128 v[80:83], v130 offset:20480\nds_read_b128 v[84:87], v130 offset:21120\ns_mov_b64 exec, -1\n" \
  "v_exp_f32 v34, v34\nv_exp_f32 v35, v35\nv_cvt_pk_bf16_f32 v64, v32, v33\nv_cvt_pk_bf16_f32 v65, v34, v35\nv_exp_f32 v36, v36\nv_exp_f32 v37, v37\n" \
  "v_mfma_f32_32x32x16_bf16 v[48:63], v[72:75], v[96:99], 0\nv_exp_f32 v38, v38\nv_exp_f32 v39, v39\nv_cvt_pk_bf16_f32 v66, v36, v37\n" \
  "v_cvt_pk_bf16_f32 v67, v38, v39\nv_exp_f32 v40, v40\nv_mfma_f32_32x32x16_bf16 v[48:63], v[76:79], v[100:103], v[48:63]\n" \
  "ds_read_b128 v[72:75], v128 offset:23040\nds_read_b64 v[76:77], v129 offset:23040\nv_exp_f32 v41, v41\nv_exp_f32 v42, v42\nv_exp_f32 v43, v43\n" \
  "v_cvt_pk_bf16_f32 v68, v40, v41\nv_cvt_pk_bf16_f32 v69, v42, v43\nv_exp_f32 v44, v44\nv_exp_f32 v45, v45\ns_waitcnt lgkmcnt(2)\n" \
  "v_mfma_f32_32x32x16_bf16 v[0:15], v[80:83], v[64:67], v[0:15]\nv_exp_f32 v46, v46\nv_exp_f32 v47, v47\nv_cvt_pk_bf16_f32 v70, v44, v45\n" \
  "v_cvt_pk_bf16_f32 v71, v46, v47\nv_exp_f32 v48, v48\nv_exp_f32 v49, v49\nv_mfma_f32_32x32x16_bf16 v[0:15], v[84:87], v[68:71], v[0:15]\n" \
  "v_exp_f32 v50, v50\nv_exp_f32 v51, v51\nv_cvt_pk_bf16_f32 v64, v48, v49\nv_cvt_pk_bf16_f32 v65, v50, v51\nv_exp_f32 v52, v52\nv_exp_f32 v53, v53\n" \
  "s_waitcnt lgkmcnt(0)\nv_mfma_f32_32x32x16_bf16 v[32:47], v[72:75], v[88:91], 0\nv_exp_f32 v54, v54\nv_exp_f32 v55, v55\n" \
  "v_cvt_pk_bf16_f32 v66, v52, v53\nv_cvt_pk_bf16_f32 v67, v54, v55\nv_exp_f32 v56, v56\n" \
  "v_mfma_f32_32x32x16_bf16 v[32:47], v[76:79], v[92:95], v[32:47]\nv_exp_f32 v57, v57\nv_exp_f32 v58, v58\nv_exp_f32 v59, v59\n" \
  "v_cvt_pk_bf16_f32 v68, v56, v57\nv_cvt_pk_bf16_f32 v69, v58, v59\nv_exp_f32 v60, v60\nv_exp_f32 v61, v61\n" \
  "v_mfma_f32_32x32x16_bf16 v[16:31], v[80:83], v[64:67], v[16:31]\nv_exp_f32 v62, v62\nv_exp_f32 v63, v63\nv_cvt_pk_bf16_f32 v70, v60, v61\n" \
  "v_cvt_pk_bf16_f32 v71, v62, v63\nv_exp_f32 v32, v32\nv_exp_f32 v33, v33\nv_mfma_f32_32x32x16_bf16 v[16:31], v[84:87], v[68:71], v[16:31]\n" \
  "s_mov_b64 exec, s[52:53]\nds_read_b128 v[80:83], v130 offset:23040\nds_read_b128 v[84:87], v130 offset:23680\ns_mov_b64 exec, -1\n" \
  "v_exp_f32 v34, v34\nv_exp_f32 v35, v35\nv_cvt_pk_bf16_f32 v64, v32, v33\nv_cvt_pk_bf16_f32 v65, v34, v35\nv_exp_f32 v36, v36\nv_exp_f32 v37, v37\n" \
  "v_mfma_f32_32x32x16_bf16 v[48:63], v[72:75], v[96:99], 0\nv_exp_f32 v38, v38\nv_exp_f32 v39, v39\nv_cvt_pk_bf16_f32 v66, v36, v37\n" \
  "v_cvt_pk_bf16_f32 v67, v38, v39\nv_exp_f32 v40, v40\nv_mfma_f32_32x32x16_bf16 v[48:63], v[76:79], v[100:103], v[48:63]\n" \
  "ds_read_b128 v[72:75], v128 offset:25600\nds_read_b64 v[76:77], v129 offset:25600\nv_exp_f32 v41, v41\nv_exp_f32 v42, v42\nv_exp_f32 v43, v43\n" \
  "v_cvt_pk_bf16_f32 v68, v40, v41\nv_cvt_pk_bf16_f32 v69, v42, v43\nv_exp_f32 v44, v44\nv_exp_f32 v45, v45\ns_waitcnt lgkmcnt(2)\n" \
  "v_mfma_f32_32x32x16_bf16 v[0:15], v[80:83], v[64:67], v[0:15]\nv_exp_f32 v46, v46\nv_exp_f32 v47, v47\nv_cvt_pk_bf16_f32 v70, v44, v45\n" \
  "v_cvt_pk_bf16_f32 v71, v46, v47\nv_exp_f32 v48, v48\nv_exp_f32 v49, v49\nv_mfma_f32_32x32x16_bf16 v[0:15], v[84:87], v[68:71], v[0:15]\n" \
  "v_exp_f32 v50, v50\nv_exp_f32 v51, v51\nv_cvt_pk_bf16_f32 v64, v48, v49\nv_cvt_pk_bf16_f32 v65, v50, v51\nv_exp_f32 v52, v52\nv_exp_f32 v53, v53\n" \
  "s_waitcnt lgkmcnt(0)\nv_mfma_f32_32x32x16_bf16 v[32:47], v[72:75], v[88:91], 0\nv_exp_f32 v54, v54\nv_exp_f32 v55, v55\n" \
  "v_cvt_pk_bf16_f32 v66, v52, v53\nv_cvt_pk_bf16_f32 v67, v54, v55\nv_exp_f32 v56, v56\n" \
  "v_mfma_f32_32x32x16_bf16 v[32:47], v[76:79], v[92:95], v[32:47]\nv_exp_f32 v57, v57\nv_exp_f32 v58, v58\nv_exp_f32 v59, v59\n" \
  "v_cvt_pk_bf16_f32 v68, v56, v57\nv_cvt_pk_bf16_f32 v69, v58, v59\nv_exp_f32 v60, v60\nv_exp_f32 v61, v61\n" \
  "v_mfma_f32_32x32x16_bf16 v[16:31], v[80:83], v[64:67], v[16:31]\nv_exp_f32 v62, v62\nv_exp_f32 v63, v63\nv_cvt_pk_bf16_f32 v70, v60, v61\n" \
  "v_cvt_pk_bf16_f32 v71, v62, v63\nv_exp_f32 v32, v32\nv_exp_f32 v33, v33\nv_mfma_f32_32x32x16_bf16 v[16:31], v[84:87], v[68:71], v[16:31]\n" \
  "s_mov_b64 exec, s[52:53]\nds_read_b128 v[80:83], v130 offset:25600\nds_read_b128 v[84:87], v130 offset:26240\ns_mov_b64 exec, -1\n" \
  "v_exp_f32 v34, v34\nv_exp_f32 v35, v35\nv_cvt_pk_bf16_f32 v64, v32, v33\nv_cvt_pk_bf16_f32 v65, v34, v35\nv_exp_f32 v36, v36\nv_exp_f32 v37, v37\n" \
  "v_mfma_f32_32x32x16_bf16 v[48:63], v[72:75], v[96:99], 0\nv_exp_f32 v38, v38\nv_exp_f32 v39, v39\nv_cvt_pk_bf16_f32 v66, v36, v37\n" \
  "v_cvt_pk_bf16_f32 v67, v38, v39\nv_exp_f32 v40, v40\nv_mfma_f32_32x32x16_bf16 v[48:63], v[76:79], v[100:103], v[48:63]\n" \
  "ds_read_b128 v[72:75], v128 offset:28160\nds_read_b64 v[76:77], v129 offset:28160\nv_exp_f32 v41, v41\nv_exp_f32 v42, v42\nv_exp_f32 v43, v43\n" \
  "v_cvt_pk_bf16_f32 v68, v40, v41\nv_cvt_pk_bf16_f32 v69, v42, v43\nv_exp_f32 v44, v44\nv_exp_f32 v45, v45\ns_waitcnt lgkmcnt(2)\n" \
  "v_mfma_f32_32x32x16_bf16 v[0:15], v[80:83], v[64:67], v[0:15]\nv_exp_f32 v46, v46\nv_exp_f32 v47, v47\nv_cvt_pk_bf16_f32 v70, v44, v45\n" \
  "v_cvt_pk_bf16_f32 v71, v46, v47\nv_exp_f32 v48, v48\nv_exp_f32 v49, v49\nv_mfma_f32_32x32x16_bf16 v[0:15], v[84:87], v[68:71], v[0:15]\n" \
  "v_exp_f32 v50, v50\nv_exp_f32 v51, v51\nv_cvt_pk_bf16_f32 v64, v48, v49\nv_cvt_pk_bf16_f32 v65, v50, v51\nv_exp_f32 v52, v52\nv_exp_f32 v53, v53\n" \
  "s_waitcnt lgkmcnt(0)\nv_mfma_f32_32x32x16_bf16 v[32:47], v[72:75], v[88:91], 0\nv_exp_f32 v54, v54\nv_exp_f32 v55, v55\n" \
  "v_cvt_pk_bf16_f32 v66, v52, v53\nv_cvt_pk_bf16_f32 v67, v54, v55\nv_exp_f32 v56, v56\n" \
  "v_mfma_f32_32x32x16_bf16 v[32:47], v[76:79], v[92:95], v[32:47]\nv_exp_f32 v57, v57\nv_exp_f32 v58, v58\nv_exp_f32 v59, v59\n" \
  "v_cvt_pk_bf16_f32 v68, v56, v57\nv_cvt_pk_bf16_f32 v69, v58, v59\nv_exp_f32 v60, v60\nv_exp_f32 v61, v61\n" \
  "v_mfma_f32_32x32x16_bf16 v[16:31], v[80:83], v[64:67], v[16:31]\nv_exp_f32 v62, v62\nv_exp_f32 v63, v63\nv_cvt_pk_bf16_f32 v70, v60, v61\n" \
  "v_cvt_pk_bf16_f32 v71, v62, v63\nv_exp_f32 v32, v32\nv_exp_f32 v33, v33\nv_mfma_f32_32x32x16_bf16 v[16:31], v[84:87], v[68:71], v[16:31]\n" \
  "s_mov_b64 exec, s[52:53]\nds_read_b128 v[80:83], v130 offset:28160\nds_read_b128 v[84:87], v130 offset:28800\ns_mov_b64 exec, -1\n" \
  "v_exp_f32 v34, v34\nv_exp_f32 v35, v35\nv_cvt_pk_bf16_f32 v64, v32, v33\nv_cvt_pk_bf16_f32 v65, v34, v35\nv_exp_f32 v36, v36\nv_exp_f32 v37, v37\n" \
  "v_mfma_f32_32x32x16_bf16 v[48:63], v[72:75], v[96:99], 0\nv_exp_f32 v38, v38\nv_exp_f32 v39, v39\nv_cvt_pk_bf16_f32 v66, v36, v37\n" \
  "v_cvt_pk_bf16_f32 v67, v38, v39\nv_exp_f32 v40, v40\nv_mfma_f32_32x32x16_bf16 v[48:63], v[76:79], v[100:103], v[48:63]\n" \
  "ds_read_b128 v[72:75], v128 offset:30720\nds_read_b64 v[76:77], v129 offset:30720\nv_exp_f32 v41, v41\nv_exp_f32 v42, v42\nv_exp_f32 v43, v43\n" \
  "v_cvt_pk_bf16_f32 v68, v40, v41\nv_cvt_pk_bf16_f32 v69, v42, v43\nv_exp_f32 v44, v44\nv_exp_f32 v45, v45\ns_waitcnt lgkmcnt(2)\n" \
  "v_mfma_f32_32x32x16_bf16 v[0:15], v[80:83], v[64:67], v[0:15]\nv_exp_f32 v46, v46\nv_exp_f32 v47, v47\nv_cvt_pk_bf16_f32 v70, v44, v45\n" \
  "v_cvt_pk_bf16_f32 v71, v46, v47\nv_exp_f32 v48, v48\nv_exp_f32 v49, v49\nv_mfma_f32_32x32x16_bf16 v[0:15], v[84:87], v[68:71], v[0:15]\n" \
  "v_exp_f32 v50, v50\nv_exp_f32 v51, v51\nv_cvt_pk_bf16_f32 v64, v48, v49\nv_cvt_pk_bf16_f32 v65, v50, v51\nv_exp_f32 v52, v52\nv_exp_f32 v53, v53\n" \
  "s_waitcnt lgkmcnt(0)\nv_mfma_f32_32x32x16_bf16 v[32:47], v[72:75], v[88:91], 0\nv_exp_f32 v54, v54\nv_exp_f32 v55, v55\n" \
  "v_cvt_pk_bf16_f32 v66, v52, v53\nv_cvt_pk_bf16_f32 v67, v54, v55\nv_exp_f32 v56, v56\n" \
  "v_mfma_f32_32x32x16_bf16 v[32:47], v[76:79], v[92:95], v[32:47]\nv_exp_f32 v57, v57\nv_exp_f32 v58, v58\nv_exp_f32 v59, v59\n" \
  "v_cvt_pk_bf16_f32 v68, v56, v57\nv_cvt_pk_bf16_f32 v69, v58, v59\nv_exp_f32 v60, v60\nv_exp_f32 v61, v61\n" \
  "v_mfma_f32_32x32x16_bf16 v[16:31], v[80:83], v[64:67], v[16:31]\nv_exp_f32 v62, v62\nv_exp_f32 v63, v63\nv_cvt_pk_bf16_f32 v70, v60, v61\n" \
  "v_cvt_pk_bf16_f32 v71, v62, v63\nv_exp_f32 v32, v32\nv_exp_f32 v33, v33\nv_mfma_f32_32x32x16_bf16 v[16:31], v[84:87], v[68:71], v[16:31]\n" \
  "s_lshl_b32 s70, s60, 9\ns_add_u32 s70, s70, 81920\nv_lshrrev_b32 v114, 1, v127\ns_cmp_eq_u32 s54, 0\ns_cbranch_scc0 .Lk2o_%=\n" \
  "buffer_load_dwordx2 v[112:113], v114, %[rf], s70 offen sc1\n.Lk2o_%=:\ns_mov_b64 exec, s[52:53]\nds_read_b128 v[80:83], v130 offset:30720\n" \
  "ds_read_b128 v[84:87], v130 offset:31360\ns_mov_b64 exec, -1\nv_exp_f32 v34, v34\nv_exp_f32 v35, v35\nv_cvt_pk_bf16_f32 v64, v32, v33\n" \
  "v_cvt_pk_bf16_f32 v65, v34, v35\nv_exp_f32 v36, v36\nv_exp_f32 v37, v37\nv_mfma_f32_32x32x16_bf16 v[48:63], v[72:75], v[96:99], 0\n" \
  "v_exp_f32 v38, v38\nv_exp_f32 v39, v39\nv_cvt_pk_bf16_f32 v66, v36, v37\nv_cvt_pk_bf16_f32 v67, v38, v39\nv_exp_f32 v40, v40\n" \
  "v_mfma_f32_32x32x16_bf16 v[48:63], v[76:79], v[100:103], v[48:63]\nds_read_b128 v[72:75], v128 offset:33280\n" \
  "ds_read_b64 v[76:77], v129 offset:33280\nv_exp_f32 v41, v41\nv_exp_f32 v42, v42\nv_exp_f32 v43, v43\nv_cvt_pk_bf16_f32 v68, v40, v41\n" \
  "v_cvt_pk_bf16_f32 v69, v42, v43\nv_exp_f32 v44, v44\nv_exp_f32 v45, v45\ns_waitcnt lgkmcnt(2)\n" \
  "v_mfma_f32_32x32x16_bf16 v[0:15], v[80:83], v[64:67], v[0:15]\nv_exp_f32 v46, v46\nv_exp_f32 v47, v47\nv_cvt_pk_bf16_f32 v70, v44, v45\n" \
  "v_cvt_pk_bf16_f32 v71, v46, v47\nv_exp_f32 v48, v48\nv_exp_f32 v49, v49\nv_mfma_f32_32x32x16_bf16 v[0:15], v[84:87], v[68:71], v[0:15]\n" \
  "v_exp_f32 v50, v50\nv_exp_f32 v51, v51\nv_cvt_pk_bf16_f32 v64, v48, v49\nv_cvt_pk_bf16_f32 v65, v50, v51\nv_exp_f32 v52, v52\nv_exp_f32 v53, v53\n" \
  "s_waitcnt lgkmcnt(0)\nv_mfma_f32_32x32x16_bf16 v[32:47], v[72:75], v[88:91], 0\nv_exp_f32 v54, v54\nv_exp_f32 v55, v55\n" \
  "v_cvt_pk_bf16_f32 v66, v52, v53\nv_cvt_pk_bf16_f32 v67, v54, v55\nv_exp_f32 v56, v56\n" \
  "v_mfma_f32_32x32x16_bf16 v[32:47], v[76:79], v[92:95], v[32:47]\nv_exp_f32 v57, v57\nv_exp_f32 v58, v58\nv_exp_f32 v59, v59\n" \
  "v_cvt_pk_bf16_f32 v68, v56, v57\nv_cvt_pk_bf16_f32 v69, v58, v59\nv_exp_f32 v60, v60\nv_exp_f32 v61, v61\n" \
  "v_mfma_f32_32x32x16_bf16 v[16:31], v[80:83], v[64:67], v[16:31]\nv_exp_f32 v62, v62\nv_exp_f32 v63, v63\nv_cvt_pk_bf16_f32 v70, v60, v61\n" \
  "v_cvt_pk_bf16_f32 v71, v62, v63\nv_exp_f32 v32, v32\nv_exp_f32 v33, v33\nv_mfma_f32_32x32x16_bf16 v[16:31], v[84:87], v[68:71], v[16:31]\n" \
  "s_cmp_eq_u32 s54, 0\ns_cbranch_scc1 .Lk2u_%=\ns_waitcnt vmcnt(0)\ns_barrier\nds_read_b128 v[144:147], v126 offset:0\n" \
  "ds_read_b128 v[148:151], v126 offset:1024\nds_read_b128 v[152:155], v126 offset:2048\nds_read_b128 v[156:159], v126 offset:3072\n" \
  "ds_read_b128 v[160:163], v126 offset:4096\nds_read_b128 v[164:167], v126 offset:5120\n.Lk2u_%=:\ns_mov_b64 exec, s[52:53]\n" \
  "ds_read_b128 v[80:83], v130 offset:33280\nds_read_b128 v[84:87], v130 offset:33920\ns_mov_b64 exec, -1\nv_exp_f32 v34, v34\nv_exp_f32 v35, v35\n" \
  "v_cvt_pk_bf16_f32 v64, v32, v33\nv_cvt_pk_bf16_f32 v65, v34, v35\nv_exp_f32 v36, v36\nv_exp_f32 v37, v37\n" \
  "v_mfma_f32_32x32x16_bf16 v[48:63], v[72:75], v[96:99], 0\nv_exp_f32 v38, v38\nv_exp_f32 v39, v39\nv_cvt_pk_bf16_f32 v66, v36, v37\n" \
  "v_cvt_pk_bf16_f32 v67, v38, v39\nv_exp_f32 v40, v40\nv_mfma_f32_32x32x16_bf16 v[48:63], v[76:79], v[100:103], v[48:63]\n" \
  "ds_read_b128 v[72:75], v128 offset:35840\nds_read_b64 v[76:77], v129 offset:35840\nv_exp_f32 v41, v41\nv_exp_f32 v42, v42\nv_exp_f32 v43, v43\n" \
  "v_cvt_pk_bf16_f32 v68, v40, v41\nv_cvt_pk_bf16_f32 v69, v42, v43\nv_exp_f32 v44, v44\nv_exp_f32 v45, v45\ns_waitcnt lgkmcnt(2)\n" \
  "v_mfma_f32_32x32x16_bf16 v[0:15], v[80:83], v[64:67], v[0:15]\nv_exp_f32 v46, v46\nv_exp_f32 v47, v47\nv_cvt_pk_bf16_f32 v70, v44, v45\n" \
  "v_cvt_pk_bf16_f32 v71, v46, v47\nv_exp_f32 v48, v48\nv_exp_f32 v49, v49\nv_mfma_f32_32x32x16_bf16 v[0:15], v[84:87], v[68:71], v[0:15]\n" \
  "v_exp_f32 v50, v50\nv_exp_f32 v51, v51\nv_cvt_pk_bf16_f32 v64, v48, v49\nv_cvt_pk_bf16_f32 v65, v50, v51\nv_exp_f32 v52, v52\nv_exp_f32 v53, v53\n" \
  "s_waitcnt lgkmcnt(0)\nv_mfma_f32_32x32x16_bf16 v[32:47], v[72:75], v[88:91], 0\nv_exp_f32 v54, v54\nv_exp_f32 v55, v55\n" \
  "v_cvt_pk_bf16_f32 v66, v52, v53\nv_cvt_pk_bf16_f32 v67, v54, v55\nv_exp_f32 v56, v56\n" \
  "v_mfma_f32_32x32x16_bf16 v[32:47], v[76:79], v[92:95], v[32:47]\nv_exp_f32 v57, v57\nv_exp_f32 v58, v58\nv_exp_f32 v59, v59\n" \
  "v_cvt_pk_bf16_f32 v68, v56, v57\nv_cvt_pk_bf16_f32 v69, v58, v59\nv_exp_f32 v60, v60\nv_exp_f32 v61, v61\n" \
  "v_mfma_f32_32x32x16_bf16 v[16:31], v[80:83], v[64:67], v[16:31]\nv_exp_f32 v62, v62\nv_exp_f32 v63, v63\nv_cvt_pk_bf16_f32 v70, v60, v61\n" \
  "v_cvt_pk_bf16_f32 v71, v62, v63\nv_exp_f32 v32, v32\nv_exp_f32 v33, v33\nv_mfma_f32_32x32x16_bf16 v[16:31], v[84:87], v[68:71], v[16:31]\n" \
  "s_mov_b64 exec, s[52:53]\nds_read_b128 v[80:83], v130 offset:35840\nds_read_b128 v[84:87], v130 offset:36480\ns_mov_b64 exec, -1\n" \
  "v_exp_f32 v34, v34\nv_exp_f32 v35, v35\nv_cvt_pk_bf16_f32 v64, v32, v33\nv_cvt_pk_bf16_f32 v65, v34, v35\nv_exp_f32 v36, v36\nv_exp_f32 v37, v37\n" \
  "v_mfma_f32_32x32x16_bf16 v[48:63], v[72:75], v[96:99], 0\nv_exp_f32 v38, v38\nv_exp_f32 v39, v39\nv_cvt_pk_bf16_f32 v66, v36, v37\n" \
  "v_cvt_pk_bf16_f32 v67, v38, v39\nv_exp_f32 v40, v40\nv_mfma_f32_32x32x16_bf16 v[48:63], v[76:79], v[100:103], v[48:63]\n" \
  "ds_read_b128 v[72:75], v128 offset:38400\nds_read_b64 v[76:77], v129 offset:38400\nv_exp_f32 v41, v41\nv_exp_f32 v42, v42\nv_exp_f32 v43, v43\n" \
  "v_cvt_pk_bf16_f32 v68, v40, v41\nv_cvt_pk_bf16_f32 v69, v42, v43\nv_exp_f32 v44, v44\nv_exp_f32 v45, v45\ns_waitcnt lgkmcnt(2)\n" \
  "v_mfma_f32_32x32x16_bf16 v[0:15], v[80:83], v[64:67], v[0:15]\nv_exp_f32 v46, v46\nv_exp_f32 v47, v47\nv_cvt_pk_bf16_f32 v70, v44, v45\n" \
  "v_cvt_pk_bf16_f32 v71, v46, v47\nv_exp_f32 v48, v48\nv_exp_f32 v49, v49\nv_mfma_f32_32x32x16_bf16 v[0:15], v[84:87], v[68:71], v[0:15]\n" \
  "v_exp_f32 v50, v50\nv_exp_f32 v51, v51\nv_cvt_pk_bf16_f32 v64, v48, v49\nv_cvt_pk_bf16_f32 v65, v50, v51\nv_exp_f32 v52, v52\nv_exp_f32 v53, v53\n" \
  "s_waitcnt lgkmcnt(0)\nv_mfma_f32_32x32x16_bf16 v[32:47], v[72:75], v[88:91], 0\nv_exp_f32 v54, v54\nv_exp_f32 v55, v55\n" \
  "v_cvt_pk_bf16_f32 v66, v52, v53\nv_cvt_pk_bf16_f32 v67, v54, v55\nv_exp_f32 v56, v56\n" \
  "v_mfma_f32_32x32x16_bf16 v[32:47], v[76:79], v[92:95], v[32:47]\nv_exp_f32 v57, v57\nv_exp_f32 v58, v58\nv_exp_f32 v59, v59\n" \
  "v_cvt_pk_bf16_f32 v68, v56, v57\nv_cvt_pk_bf16_f32 v69, v58, v59\nv_exp_f32 v60, v60\nv_exp_f32 v61, v61\n" \
  "v_mfma_f32_32x32x16_bf16 v[16:31], v[80:83], v[64:67], v[16:31]\nv_exp_f32 v62, v62\nv_exp_f32 v63, v63\nv_cvt_pk_bf16_f32 v70, v60, v61\n" \
  "v_cvt_pk_bf16_f32 v71, v62, v63\nv_exp_f32 v32, v32\nv_exp_f32 v33, v33\nv_mfma_f32_32x32x16_bf16 v[16:31], v[84:87], v[68:71], v[16:31]\n" \
  "s_cmp_eq_u32 s54, 0\ns_cbranch_scc0 .Lk2y_%=\ns_mov_b32 s61, 0x40000\ns_waitcnt vmcnt(0)\n.Lk2v_%=:\nv_cmp_ne_u64 vcc, %[ep], v[112:113]\ns_nop 0\n" \
  "s_cbranch_vccz .Lk2y_%=\ns_sleep 8\nbuffer_load_dwordx2 v[112:113], v114, %[rf], s70 offen sc1\ns_sub_u32 s61, s61, 1\ns_waitcnt vmcnt(0)\n" \
  "s_cbranch_scc0 .Lk2v_%=\n.Lk2y_%=:\ns_mov_b64 exec, s[52:53]\nds_read_b128 v[80:83], v130 offset:38400\nds_read_b128 v[84:87], v130 offset:39040\n" \
  "s_mov_b64 exec, -1\nv_exp_f32 v34, v34\nv_exp_f32 v35, v35\nv_cvt_pk_bf16_f32 v64, v32, v33\nv_cvt_pk_bf16_f32 v65, v34, v35\nv_exp_f32 v36, v36\n" \
  "v_exp_f32 v37, v37\nv_mfma_f32_32x32x16_bf16 v[48:63], v[72:75], v[96:99], 0\nv_exp_f32 v38, v38\nv_exp_f32 v39, v39\n" \
  "v_cvt_pk_bf16_f32 v66, v36, v37\nv_cvt_pk_bf16_f32 v67, v38, v39\nv_exp_f32 v40, v40\n" \
  "v_mfma_f32_32x32x16_bf16 v[48:63], v[76:79], v[100:103], v[48:63]\nv_exp_f32 v41, v41\nv_exp_f32 v42, v42\nv_exp_f32 v43, v43\n" \
  "v_cvt_pk_bf16_f32 v68, v40, v41\nv_cvt_pk_bf16_f32 v69, v42, v43\nv_exp_f32 v44, v44\nv_exp_f32 v45, v45\ns_waitcnt lgkmcnt(0)\n" \
  "v_mfma_f32_32x32x16_bf16 v[0:15], v[80:83], v[64:67], v[0:15]\nv_exp_f32 v46, v46\nv_exp_f32 v47, v47\nv_cvt_pk_bf16_f32 v70, v44, v45\n" \
  "v_cvt_pk_bf16_f32 v71, v46, v47\nv_exp_f32 v48, v48\nv_exp_f32 v49, v49\nv_mfma_f32_32x32x16_bf16 v[0:15], v[84:87], v[68:71], v[0:15]\n" \
  "v_exp_f32 v50, v50\nv_exp_f32 v51, v51\nv_cvt_pk_bf16_f32 v64, v48, v49\nv_cvt_pk_bf16_f32 v65, v50, v51\nv_exp_f32 v52, v52\nv_exp_f32 v53, v53\n" \
  "v_exp_f32 v54, v54\nv_exp_f32 v55, v55\nv_cvt_pk_bf16_f32 v66, v52, v53\nv_cvt_pk_bf16_f32 v67, v54, v55\nv_exp_f32 v56, v56\nv_exp_f32 v57, v57\n" \
  "v_exp_f32 v58, v58\nv_exp_f32 v59, v59\nv_cvt_pk_bf16_f32 v68, v56, v57\nv_cvt_pk_bf16_f32 v69, v58, v59\nv_exp_f32 v60, v60\nv_exp_f32 v61, v61\n" \
  "v_mfma_f32_32x32x16_bf16 v[16:31], v[80:83], v[64:67], v[16:31]\nv_exp_f32 v62, v62\nv_exp_f32 v63, v63\nv_cvt_pk_bf16_f32 v70, v60, v61\n" \
  "v_cvt_pk_bf16_f32 v71, v62, v63\ns_nop 1\nv_mfma_f32_32x32x16_bf16 v[16:31], v[84:87], v[68:71], v[16:31]\ns_cmp_eq_u32 s54, 0\n" \
  "s_cbranch_scc1 .Lk2e_%=\ns_barrier\nv_pk_add_f32 v[8:9], v[8:9], v[152:153]\nv_pk_add_f32 v[10:11], v[10:11], v[154:155]\nv_mov_b32 v174, v8\n" \
  "v_pk_add_f32 v[0:1], v[0:1], v[144:145]\nv_pk_add_f32 v[2:3], v[2:3], v[146:147]\nv_permlane32_swap_b32 v8, v174\nv_rcp_f32 v176, v174\n" \
  "v_pk_add_f32 v[4:5], v[4:5], v[148:149]\nv_pk_add_f32 v[6:7], v[6:7], v[150:151]\nv_pk_mul_f32 v[0:1], v[0:1], v[176:177] op_sel_hi:[1,0]\n" \
  "v_pk_mul_f32 v[4:5], v[4:5], v[176:177] op_sel_hi:[1,0]\nv_pk_mul_f32 v[2:3], v[2:3], v[176:177] op_sel_hi:[1,0]\n" \
  "v_pk_mul_f32 v[6:7], v[6:7], v[176:177] op_sel_hi:[1,0]\nv_cvt_pk_bf16_f32 v168, v0, v1\nv_cvt_pk_bf16_f32 v170, v4, v5\n" \
  "v_cvt_pk_bf16_f32 v169, v2, v3\nv_cvt_pk_bf16_f32 v171, v6, v7\nv_pk_mul_f32 v[8:9], v[8:9], v[176:177] op_sel_hi:[1,0]\n" \
  "v_pk_mul_f32 v[10:11], v[10:11], v[176:177] op_sel_hi:[1,0]\nv_permlane32_swap_b32 v168, v170\nv_permlane32_swap_b32 v169, v171\n" \
  "v_cvt_pk_bf16_f32 v172, v8, v9\nv_cvt_pk_bf16_f32 v173, v10, v11\nbuffer_store_dwordx4 v[168:171], v124, s[40:43], s74 offen offset:0 sc1\n" \
  "s_mov_b32 exec_hi, 0\nbuffer_store_dwordx2 v[172:173], v125, s[40:43], s75 offen offset:0 sc1\ns_mov_b32 exec_hi, -1\n" \
  "v_pk_add_f32 v[24:25], v[24:25], v[164:165]\nv_pk_add_f32 v[26:27], v[26:27], v[166:167]\nv_mov_b32 v184, v24\n" \
  "v_pk_add_f32 v[16:17], v[16:17], v[156:157]\nv_pk_add_f32 v[18:19], v[18:19], v[158:159]\nv_permlane32_swap_b32 v24, v184\nv_rcp_f32 v186, v184\n" \
  "v_pk_add_f32 v[20:21], v[20:21], v[160:161]\nv_pk_add_f32 v[22:23], v[22:23], v[162:163]\n" \
  "v_pk_mul_f32 v[16:17], v[16:17], v[186:187] op_sel_hi:[1,0]\nv_pk_mul_f32 v[20:21], v[20:21], v[186:187] op_sel_hi:[1,0]\n" \
  "v_pk_mul_f32 v[18:19], v[18:19], v[186:187] op_sel_hi:[1,0]\nv_pk_mul_f32 v[22:23], v[22:23], v[186:187] op_sel_hi:[1,0]\n" \
  "v_cvt_pk_bf16_f32 v178, v16, v17\nv_cvt_pk_bf16_f32 v180, v20, v21\nv_cvt_pk_bf16_f32 v179, v18, v19\nv_cvt_pk_bf16_f32 v181, v22, v23\n" \
  "v_pk_mul_f32 v[24:25], v[24:25], v[186:187] op_sel_hi:[1,0]\nv_pk_mul_f32 v[26:27], v[26:27], v[186:187] op_sel_hi:[1,0]\n" \
  "v_permlane32_swap_b32 v178, v180\nv_permlane32_swap_b32 v179, v181\nv_cvt_pk_bf16_f32 v182, v24, v25\nv_cvt_pk_bf16_f32 v183, v26, v27\n" \
  "buffer_store_dwordx4 v[178:181], v124, s[40:43], s74 offen offset:512 sc1\ns_mov_b32 exec_hi, 0\n" \
  "buffer_store_dwordx2 v[182:183], v125, s[40:43], s75 offen offset:512 sc1\ns_mov_b32 exec_hi, -1\nv_mov_b32 v0, 0\n" \
  "v_dot2c_f32_bf16 v0, v168, v168\nv_dot2c_f32_bf16 v0, v169, v169\nv_dot2c_f32_bf16 v0, v170, v170\nv_dot2c_f32_bf16 v0, v171, v171\n" \
  "v_dot2c_f32_bf16 v0, v172, v172\nv_dot2c_f32_bf16 v0, v173, v173\nv_cmp_nge_f32 vcc, 0x71800000, v174\ns_mov_b64 %[bad], vcc\n" \
  "v_cmp_nle_f32 vcc, 0x0d800000, v174\ns_or_b64 %[bad], %[bad], vcc\nv_cmp_nge_f32 vcc, 0x7e800000, v0\ns_or_b64 %[bad], %[bad], vcc\n" \
  "v_mov_b32 v16, 0\nv_dot2c_f32_bf16 v16, v178, v178\nv_dot2c_f32_bf16 v16, v179, v179\nv_dot2c_f32_bf16 v16, v180, v180\n" \
  "v_dot2c_f32_bf16 v16, v181, v181\nv_dot2c_f32_bf16 v16, v182, v182\nv_dot2c_f32_bf16 v16, v183, v183\nv_cmp_nge_f32 vcc, 0x71800000, v184\n" \
  "s_mov_b64 %[bad1], vcc\nv_cmp_nle_f32 vcc, 0x0d800000, v184\ns_or_b64 %[bad1], %[bad1], vcc\nv_cmp_nge_f32 vcc, 0x7e800000, v16\n" \
  "s_or_b64 %[bad1], %[bad1], vcc\ns_branch .Lk2x_%=\n.Lk2e_%=:\nds_write_b128 v126, v[0:3] offset:0\nds_write_b128 v126, v[4:7] offset:1024\n" \
  "ds_write_b128 v126, v[8:11] offset:2048\ns_nop 7\ns_nop 1\nds_write_b128 v126, v[16:19] offset:3072\nds_write_b128 v126, v[20:23] offset:4096\n" \
  "ds_write_b128 v126, v[24:27] offset:5120\ns_waitcnt vmcnt(0) lgkmcnt(0)\ns_barrier\n.Lk2x_%=:\ns_mov_b32 %[w], s76\n.Ltail:\n"
#define S10(p) "s" #p "0", "s" #p "1", "s" #p "2", "s" #p "3", "s" #p "4", "s" #p "5", "s" #p "6", "s" #p "7", "s" #p "8", "s" #p "9"
#define V10(p) "v" #p "0", "v" #p "1", "v" #p "2", "v" #p "3", "v" #p "4", "v" #p "5", "v" #p "6", "v" #p "7", "v" #p "8", "v" #p "9"

__device__ __forceinline__ void k2_redo(int head, int tile) {
  const int lane = lane_id(), r = lane & 31, hh = lane >> 5;
  __builtin_amdgcn_fence(__ATOMIC_ACQUIRE, "agent");
  const char *kv = wsg + WS_REC + (size_t)(32 + head) * REC_HEAD, *qrec = wsg + WS_REC + (size_t)(head * 64 + tile) * REC;
  const unsigned aK = hh * 512 + r * 16, aKh = 1024 + r * 8, aV = REC_V + hh * REC_VH + r * 16, vc = r == 20 ? 0x3f803f80u : 0u;
  const bf16x8 ql = *(const bf16x8*)(qrec + aK);
  const u32x2 t = *(const u32x2*)(qrec + aKh);
  const bf16x8 qh = bx8(hh ? u32x4{} : u32x4{t.x, t.y, 0u, 0u});
  float m = -__builtin_inff();
  f32x16 oo = {};
  for (int t = 0; t < 64; ++t) {
    const char* q = kv + t * REC;
    const u32x2 kh = *(const u32x2*)(q + aKh);
    f32x16 s = {};
    s = MFMA32(*(const bf16x8*)(q + aK), ql, s);
    s = MFMA32(bx8(u32x4{kh.x, kh.y, 0u, 0u}), qh, s);
    float mn = fmaxf(m, max16(s));
    float c = __builtin_amdgcn_exp2f(m - mn);
    m = mn;
#pragma unroll
    for (int i = 0; i < 16; ++i) { s[i] -= mn; oo[i] *= c; }
    unsigned pp[8];
#pragma unroll
    for (int g = 0; g < 4; ++g) exp4(s, g, pp + 2 * g);
    u32x4 a0 = {vc, vc, vc, vc}, a1 = a0;
    if (r < 20) { a0 = *(const u32x4*)(q + aV); a1 = *(const u32x4*)(q + aV + REC_VS); }
    oo = MFMA32(bx8(a0), px8(pp), oo);
    oo = MFMA32(bx8(a1), px8(pp + 4), oo);
  }
  const float inv = 1.f / __shfl(oo[8], r + 32);
  const i32x4 so = mk_srd(wsg + WS_O, S_ * D_ * 2);
  WAIT_V(0);
#pragma unroll
  for (int g = 0; g < 3; ++g) {
    if (g < 2 || hh == 0) {
      const int col = head * 20 + (g < 2 ? 8 * g + 4 * hh + 4 * (head & 1) : head & 1 ? 0 : 16);
      u32x2 v = {pk2(oo[4 * g] * inv, oo[4 * g + 1] * inv), pk2(oo[4 * g + 2] * inv, oo[4 * g + 3] * inv)};
      st8(v, so, (col >> 3) * 32768 + (tile * 32 + r) * 16 + (col & 7) * 2, 0);
    }
  }
}

#define K3_ASM \
  "v_mov_b64 v[0:1], 0\nv_mov_b64 v[2:3], 0\nv_mov_b64 v[4:5], 0\nv_mov_b64 v[6:7], 0\nv_mov_b64 v[8:9], 0\nv_mov_b64 v[10:11], 0\n" \
  "v_mov_b64 v[12:13], 0\nv_mov_b64 v[14:15], 0\nv_mov_b64 v[16:17], 0\nv_mov_b64 v[18:19], 0\nv_mov_b64 v[20:21], 0\nv_mov_b64 v[22:23], 0\n" \
  "v_mov_b64 v[24:25], 0\nv_mov_b64 v[26:27], 0\nv_mov_b64 v[28:29], 0\nv_mov_b64 v[30:31], 0\nv_mov_b64 v[32:33], 0\nv_mov_b64 v[34:35], 0\n" \
  "v_mov_b64 v[36:37], 0\nv_mov_b64 v[38:39], 0\nv_mov_b64 v[180:181], 0\nv_mov_b64 v[182:183], 0\ns_barrier\ns_barrier\n" \
  "ds_read_b64_tr_b16 v[48:49], v154 offset:0\nds_read_b64_tr_b16 v[50:51], v155 offset:0\nds_read_b128 v[44:47], v153 offset:0\n" \
  "ds_read_b128 v[40:43], v152 offset:0\nds_read_b64_tr_b16 v[52:53], v154 offset:32\nds_read_b64_tr_b16 v[54:55], v155 offset:32\n" \
  "ds_read_b64_tr_b16 v[56:57], v154 offset:64\nds_read_b64_tr_b16 v[58:59], v155 offset:64\nds_read_b64_tr_b16 v[60:61], v154 offset:96\n" \
  "ds_read_b64_tr_b16 v[62:63], v155 offset:96\nds_read_b64_tr_b16 v[64:65], v154 offset:128\nds_read_b64_tr_b16 v[66:67], v155 offset:128\n" \
  "ds_read_b64_tr_b16 v[76:77], v154 offset:5120\nds_read_b64_tr_b16 v[78:79], v155 offset:5120\nds_read_b128 v[72:75], v153 offset:4096\n" \
  "ds_read_b128 v[68:71], v152 offset:4096\nds_read_b64_tr_b16 v[80:81], v154 offset:5152\nds_read_b64_tr_b16 v[82:83], v155 offset:5152\n" \
  "ds_read_b64_tr_b16 v[84:85], v154 offset:5184\nds_read_b64_tr_b16 v[86:87], v155 offset:5184\nds_read_b64_tr_b16 v[88:89], v154 offset:5216\n" \
  "ds_read_b64_tr_b16 v[90:91], v155 offset:5216\nds_read_b64_tr_b16 v[92:93], v154 offset:5248\nds_read_b64_tr_b16 v[94:95], v155 offset:5248\n" \
  ".Lk3_%=:\ns_waitcnt lgkmcnt(15)\nv_mfma_f32_16x16x32_bf16 v[20:23], v[48:51], v[44:47], v[20:23]\n" \
  "v_mfma_f32_16x16x32_bf16 v[0:3], v[48:51], v[40:43], v[0:3]\nv_mfma_f32_16x16x32_bf16 v[24:27], v[52:55], v[44:47], v[24:27]\n" \
  "v_mfma_f32_16x16x32_bf16 v[4:7], v[52:55], v[40:43], v[4:7]\nv_mfma_f32_16x16x32_bf16 v[28:31], v[56:59], v[44:47], v[28:31]\n" \
  "v_mfma_f32_16x16x32_bf16 v[8:11], v[56:59], v[40:43], v[8:11]\ns_waitcnt lgkmcnt(14)\n" \
  "v_mfma_f32_16x16x32_bf16 v[32:35], v[60:63], v[44:47], v[32:35]\nv_mfma_f32_16x16x32_bf16 v[12:15], v[60:63], v[40:43], v[12:15]\n" \
  "s_waitcnt lgkmcnt(12)\nv_mfma_f32_16x16x32_bf16 v[36:39], v[64:67], v[44:47], v[36:39]\n" \
  "v_mfma_f32_16x16x32_bf16 v[16:19], v[64:67], v[40:43], v[16:19]\ns_waitcnt lgkmcnt(9)\n" \
  "v_mfma_f32_16x16x32_bf16 v[20:23], v[76:79], v[72:75], v[20:23]\ns_waitcnt lgkmcnt(8)\n" \
  "v_mfma_f32_16x16x32_bf16 v[0:3], v[76:79], v[68:71], v[0:3]\ns_waitcnt lgkmcnt(6)\n" \
  "v_mfma_f32_16x16x32_bf16 v[24:27], v[80:83], v[72:75], v[24:27]\nv_mfma_f32_16x16x32_bf16 v[4:7], v[80:83], v[68:71], v[4:7]\n" \
  "s_waitcnt lgkmcnt(0)\ns_barrier\nv_mfma_f32_16x16x32_bf16 v[28:31], v[84:87], v[72:75], v[28:31]\n" \
  "ds_read_b64_tr_b16 v[104:105], v154 offset:20480\nds_read_b64_tr_b16 v[106:107], v155 offset:20480\nds_read_b128 v[100:103], v153 offset:16384\n" \
  "ds_read_b128 v[96:99], v152 offset:16384\nv_mfma_f32_16x16x32_bf16 v[8:11], v[84:87], v[68:71], v[8:11]\n" \
  "ds_read_b64_tr_b16 v[108:109], v154 offset:20512\nds_read_b64_tr_b16 v[110:111], v155 offset:20512\n" \
  "ds_read_b64_tr_b16 v[112:113], v154 offset:20544\nds_read_b64_tr_b16 v[114:115], v155 offset:20544\n" \
  "v_mfma_f32_16x16x32_bf16 v[32:35], v[88:91], v[72:75], v[32:35]\nds_read_b64_tr_b16 v[116:117], v154 offset:20576\n" \
  "ds_read_b64_tr_b16 v[118:119], v155 offset:20576\nds_read_b64_tr_b16 v[120:121], v154 offset:20608\n" \
  "ds_read_b64_tr_b16 v[122:123], v155 offset:20608\nv_mfma_f32_16x16x32_bf16 v[12:15], v[88:91], v[68:71], v[12:15]\n" \
  "ds_read_b64_tr_b16 v[132:133], v154 offset:25600\nds_read_b64_tr_b16 v[134:135], v155 offset:25600\nds_read_b128 v[128:131], v153 offset:20480\n" \
  "ds_read_b128 v[124:127], v152 offset:20480\nv_mfma_f32_16x16x32_bf16 v[36:39], v[92:95], v[72:75], v[36:39]\n" \
  "ds_read_b64_tr_b16 v[136:137], v154 offset:25632\nds_read_b64_tr_b16 v[138:139], v155 offset:25632\n" \
  "ds_read_b64_tr_b16 v[140:141], v154 offset:25664\nds_read_b64_tr_b16 v[142:143], v155 offset:25664\n" \
  "v_mfma_f32_16x16x32_bf16 v[16:19], v[92:95], v[68:71], v[16:19]\nds_read_b64_tr_b16 v[144:145], v154 offset:25696\n" \
  "ds_read_b64_tr_b16 v[146:147], v155 offset:25696\nds_read_b64_tr_b16 v[148:149], v154 offset:25728\n" \
  "ds_read_b64_tr_b16 v[150:151], v155 offset:25728\ns_cmp_lg_u32 %[cnt], 0\ns_cbranch_scc1 .Lk3r_%=\n" \
  "buffer_load_dwordx4 v[172:175], v169, %[rr], %[sr] offen offset:0\nbuffer_load_dwordx4 v[176:179], v169, %[rr], %[sr] offen offset:64\n" \
  "s_mov_b32 exec_hi, 0\nbuffer_load_dwordx4 v[180:183], v169, %[rr], %[sr] offen offset:128\ns_mov_b32 exec_hi, -1\n.Lk3r_%=:\n" \
  "s_waitcnt lgkmcnt(15)\nv_mfma_f32_16x16x32_bf16 v[20:23], v[104:107], v[100:103], v[20:23]\nv_xor_b32 v152, 0x8000, v152\n" \
  "v_mfma_f32_16x16x32_bf16 v[0:3], v[104:107], v[96:99], v[0:3]\nv_xor_b32 v153, 0x8000, v153\n" \
  "v_mfma_f32_16x16x32_bf16 v[24:27], v[108:111], v[100:103], v[24:27]\nv_add_u32 v154, %[db], v154\n" \
  "v_mfma_f32_16x16x32_bf16 v[4:7], v[108:111], v[96:99], v[4:7]\nv_add_u32 v155, %[db], v155\n" \
  "v_mfma_f32_16x16x32_bf16 v[28:31], v[112:115], v[100:103], v[28:31]\ns_mul_i32 %[db], %[db], -2\n" \
  "v_mfma_f32_16x16x32_bf16 v[8:11], v[112:115], v[96:99], v[8:11]\ns_waitcnt lgkmcnt(14)\n" \
  "v_mfma_f32_16x16x32_bf16 v[32:35], v[116:119], v[100:103], v[32:35]\nv_mfma_f32_16x16x32_bf16 v[12:15], v[116:119], v[96:99], v[12:15]\n" \
  "s_waitcnt lgkmcnt(12)\nv_mfma_f32_16x16x32_bf16 v[36:39], v[120:123], v[100:103], v[36:39]\n" \
  "v_mfma_f32_16x16x32_bf16 v[16:19], v[120:123], v[96:99], v[16:19]\ns_waitcnt lgkmcnt(9)\n" \
  "v_mfma_f32_16x16x32_bf16 v[20:23], v[132:135], v[128:131], v[20:23]\ns_waitcnt lgkmcnt(8)\n" \
  "v_mfma_f32_16x16x32_bf16 v[0:3], v[132:135], v[124:127], v[0:3]\ns_waitcnt lgkmcnt(6)\n" \
  "v_mfma_f32_16x16x32_bf16 v[24:27], v[136:139], v[128:131], v[24:27]\nv_mfma_f32_16x16x32_bf16 v[4:7], v[136:139], v[124:127], v[4:7]\n" \
  "s_waitcnt lgkmcnt(0)\ns_barrier\nv_mfma_f32_16x16x32_bf16 v[28:31], v[140:143], v[128:131], v[28:31]\nds_read_b64_tr_b16 v[48:49], v154 offset:0\n" \
  "ds_read_b64_tr_b16 v[50:51], v155 offset:0\nds_read_b128 v[44:47], v153 offset:0\nds_read_b128 v[40:43], v152 offset:0\n" \
  "v_mfma_f32_16x16x32_bf16 v[8:11], v[140:143], v[124:127], v[8:11]\nds_read_b64_tr_b16 v[52:53], v154 offset:32\n" \
  "ds_read_b64_tr_b16 v[54:55], v155 offset:32\nds_read_b64_tr_b16 v[56:57], v154 offset:64\nds_read_b64_tr_b16 v[58:59], v155 offset:64\n" \
  "v_mfma_f32_16x16x32_bf16 v[32:35], v[144:147], v[128:131], v[32:35]\nds_read_b64_tr_b16 v[60:61], v154 offset:96\n" \
  "ds_read_b64_tr_b16 v[62:63], v155 offset:96\nds_read_b64_tr_b16 v[64:65], v154 offset:128\nds_read_b64_tr_b16 v[66:67], v155 offset:128\n" \
  "v_mfma_f32_16x16x32_bf16 v[12:15], v[144:147], v[124:127], v[12:15]\nds_read_b64_tr_b16 v[76:77], v154 offset:5120\n" \
  "ds_read_b64_tr_b16 v[78:79], v155 offset:5120\nds_read_b128 v[72:75], v153 offset:4096\nds_read_b128 v[68:71], v152 offset:4096\n" \
  "v_mfma_f32_16x16x32_bf16 v[36:39], v[148:151], v[128:131], v[36:39]\nds_read_b64_tr_b16 v[80:81], v154 offset:5152\n" \
  "ds_read_b64_tr_b16 v[82:83], v155 offset:5152\nds_read_b64_tr_b16 v[84:85], v154 offset:5184\nds_read_b64_tr_b16 v[86:87], v155 offset:5184\n" \
  "v_mfma_f32_16x16x32_bf16 v[16:19], v[148:151], v[124:127], v[16:19]\nds_read_b64_tr_b16 v[88:89], v154 offset:5216\n" \
  "ds_read_b64_tr_b16 v[90:91], v155 offset:5216\nds_read_b64_tr_b16 v[92:93], v154 offset:5248\nds_read_b64_tr_b16 v[94:95], v155 offset:5248\n" \
  "s_add_u32 %[cnt], %[cnt], -1\ns_cbranch_scc1 .Lk3_%=\ns_waitcnt lgkmcnt(15)\nv_mfma_f32_16x16x32_bf16 v[20:23], v[48:51], v[44:47], v[20:23]\n" \
  "v_mfma_f32_16x16x32_bf16 v[24:27], v[52:55], v[44:47], v[24:27]\nv_mfma_f32_16x16x32_bf16 v[28:31], v[56:59], v[44:47], v[28:31]\n" \
  "s_waitcnt lgkmcnt(14)\nv_mfma_f32_16x16x32_bf16 v[32:35], v[60:63], v[44:47], v[32:35]\ns_waitcnt lgkmcnt(12)\n" \
  "v_mfma_f32_16x16x32_bf16 v[36:39], v[64:67], v[44:47], v[36:39]\ns_waitcnt lgkmcnt(9)\n" \
  "v_mfma_f32_16x16x32_bf16 v[20:23], v[76:79], v[72:75], v[20:23]\ns_waitcnt lgkmcnt(6)\n" \
  "v_mfma_f32_16x16x32_bf16 v[24:27], v[80:83], v[72:75], v[24:27]\ns_waitcnt lgkmcnt(4)\n" \
  "v_mfma_f32_16x16x32_bf16 v[28:31], v[84:87], v[72:75], v[28:31]\ns_waitcnt lgkmcnt(2)\n" \
  "v_mfma_f32_16x16x32_bf16 v[32:35], v[88:91], v[72:75], v[32:35]\nds_write_b128 v166, v[20:23] offset:0\ns_waitcnt lgkmcnt(1)\n" \
  "v_mfma_f32_16x16x32_bf16 v[36:39], v[92:95], v[72:75], v[36:39]\nds_write_b128 v166, v[24:27] offset:1024\n" \
  "v_mfma_f32_16x16x32_bf16 v[0:3], v[48:51], v[40:43], v[0:3]\nds_write_b128 v166, v[28:31] offset:2048\n" \
  "v_mfma_f32_16x16x32_bf16 v[4:7], v[52:55], v[40:43], v[4:7]\nds_write_b128 v166, v[32:35] offset:3072\n" \
  "v_mfma_f32_16x16x32_bf16 v[8:11], v[56:59], v[40:43], v[8:11]\nds_write_b128 v166, v[36:39] offset:4096\n" \
  "v_mfma_f32_16x16x32_bf16 v[12:15], v[60:63], v[40:43], v[12:15]\nv_mfma_f32_16x16x32_bf16 v[16:19], v[64:67], v[40:43], v[16:19]\n" \
  "s_waitcnt lgkmcnt(0)\ns_barrier\nds_read_b128 v[116:119], v167 offset:0\nds_read_b128 v[120:123], v167 offset:1024\n" \
  "ds_read_b128 v[124:127], v167 offset:2048\nds_read_b128 v[128:131], v167 offset:3072\nds_read_b128 v[132:135], v167 offset:4096\n" \
  "v_mfma_f32_16x16x32_bf16 v[0:3], v[76:79], v[68:71], v[0:3]\nv_mfma_f32_16x16x32_bf16 v[4:7], v[80:83], v[68:71], v[4:7]\n" \
  "v_mfma_f32_16x16x32_bf16 v[8:11], v[84:87], v[68:71], v[8:11]\nv_mfma_f32_16x16x32_bf16 v[12:15], v[88:91], v[68:71], v[12:15]\n" \
  "v_mfma_f32_16x16x32_bf16 v[16:19], v[92:95], v[68:71], v[16:19]\ns_waitcnt vmcnt(0)\n" \
  "v_mfma_f32_16x16x32_bf16 v[0:3], v[156:159], v[172:175], v[0:3]\nv_mfma_f32_16x16x32_bf16 v[4:7], v[160:163], v[172:175], v[4:7]\n" \
  "v_mfma_f32_16x16x32_bf16 v[8:11], v[156:159], v[176:179], v[8:11]\nv_mfma_f32_16x16x32_bf16 v[12:15], v[160:163], v[176:179], v[12:15]\n" \
  "v_mfma_f32_16x16x32_bf16 v[16:19], v[156:159], v[180:183], v[16:19]\ns_nop 1\ns_waitcnt lgkmcnt(4)\nv_pk_add_f32 v[0:1], v[0:1], v[116:117]\n" \
  "v_pk_add_f32 v[2:3], v[2:3], v[118:119]\nv_cvt_pk_bf16_f32 v240, v0, v1\nv_cvt_pk_bf16_f32 v241, v2, v3\ns_waitcnt lgkmcnt(3)\n" \
  "v_pk_add_f32 v[4:5], v[4:5], v[120:121]\nv_pk_add_f32 v[6:7], v[6:7], v[122:123]\nv_cvt_pk_bf16_f32 v242, v4, v5\nv_cvt_pk_bf16_f32 v243, v6, v7\n" \
  "s_waitcnt lgkmcnt(2)\nv_pk_add_f32 v[8:9], v[8:9], v[124:125]\nv_pk_add_f32 v[10:11], v[10:11], v[126:127]\nv_cvt_pk_bf16_f32 v244, v8, v9\n" \
  "v_cvt_pk_bf16_f32 v245, v10, v11\ns_waitcnt lgkmcnt(1)\nv_pk_add_f32 v[12:13], v[12:13], v[128:129]\nv_pk_add_f32 v[14:15], v[14:15], v[130:131]\n" \
  "v_cvt_pk_bf16_f32 v246, v12, v13\nv_cvt_pk_bf16_f32 v247, v14, v15\ns_waitcnt lgkmcnt(0)\ns_nop 1\nv_pk_add_f32 v[16:17], v[16:17], v[132:133]\n" \
  "v_pk_add_f32 v[18:19], v[18:19], v[134:135]\nv_cvt_pk_bf16_f32 v248, v16, v17\nv_cvt_pk_bf16_f32 v249, v18, v19\nv_mov_b32 v96, 0\n" \
  "v_mov_b32 v97, 0\nv_permlane16_swap_b32 v240, v242\nv_permlane16_swap_b32 v241, v243\n" \
  "buffer_store_dwordx4 v[240:243], v168, %[ro], %[so0] offen sc1\nv_permlane16_swap_b32 v244, v246\nv_permlane16_swap_b32 v245, v247\n" \
  "buffer_store_dwordx4 v[244:247], v168, %[ro], %[so1] offen sc1\nv_dot2c_f32_bf16 v96, 0x3f803f80, v248\nv_dot2c_f32_bf16 v97, v248, v248\n" \
  "v_dot2c_f32_bf16 v96, 0x3f803f80, v249\nv_dot2c_f32_bf16 v97, v249, v249\nv_permlane16_swap_b32 v248, v250\nv_permlane16_swap_b32 v249, v251\n" \
  "s_mov_b64 exec, %[me]\nbuffer_store_dwordx4 v[248:251], v168, %[ro], %[so2] offen sc1\ns_mov_b64 exec, -1\nv_dot2c_f32_bf16 v96, 0x3f803f80, v240\n" \
  "v_dot2c_f32_bf16 v97, v240, v240\nv_dot2c_f32_bf16 v96, 0x3f803f80, v241\nv_dot2c_f32_bf16 v97, v241, v241\n" \
  "v_dot2c_f32_bf16 v96, 0x3f803f80, v242\nv_dot2c_f32_bf16 v97, v242, v242\nv_dot2c_f32_bf16 v96, 0x3f803f80, v243\n" \
  "v_dot2c_f32_bf16 v97, v243, v243\nv_dot2c_f32_bf16 v96, 0x3f803f80, v244\nv_dot2c_f32_bf16 v97, v244, v244\n" \
  "v_dot2c_f32_bf16 v96, 0x3f803f80, v245\nv_dot2c_f32_bf16 v97, v245, v245\nv_dot2c_f32_bf16 v96, 0x3f803f80, v246\n" \
  "v_dot2c_f32_bf16 v97, v246, v246\nv_dot2c_f32_bf16 v96, 0x3f803f80, v247\nv_dot2c_f32_bf16 v97, v247, v247\ns_nop 3\n" \
  "v_permlane32_swap_b32 v96, v97\nv_add_f32 v96, v96, v97\nv_mov_b32 v97, v96\ns_nop 1\nv_permlane16_swap_b32 v96, v97\nv_add_f32 v96, v96, v97\n" \
  "v_mov_b32 v97, v96\ns_nop 1\nv_permlane32_swap_b32 v96, v97\ns_mov_b64 exec, 0xffff\nbuffer_store_dwordx2 v[96:97], v170, %[rs], %[ss] offen sc1\n" \
  "s_mov_b64 exec, 0xf\nv_mov_b32 v98, %[e]\nv_mbcnt_lo_u32_b32 v99, -1, 0\nv_lshlrev_b32 v99, 2, v99\ns_waitcnt vmcnt(0)\n" \
  "buffer_store_dword v98, v99, %[rf], %[fo] offen sc1\ns_mov_b64 exec, -1\ns_nop 1\nv_permlane16_swap_b32 v240, v242\n" \
  "v_permlane16_swap_b32 v241, v243\nv_permlane16_swap_b32 v244, v246\nv_permlane16_swap_b32 v245, v247\nv_permlane16_swap_b32 v248, v250\n" \
  "v_permlane16_swap_b32 v249, v251\n"
#define K5_ASM \
  "v_mov_b64 v[20:21], 0\nv_mov_b64 v[22:23], 0\nv_mov_b64 v[24:25], 0\nv_mov_b64 v[26:27], 0\nv_mov_b64 v[28:29], 0\nv_mov_b64 v[30:31], 0\n" \
  "v_mov_b64 v[32:33], 0\nv_mov_b64 v[34:35], 0\nv_mov_b64 v[36:37], 0\nv_mov_b64 v[38:39], 0\nv_lshlrev_b32 v0, 16, v240\n" \
  "v_and_b32 v1, 0xffff0000, v240\nv_lshlrev_b32 v2, 16, v241\nv_and_b32 v3, 0xffff0000, v241\nv_lshlrev_b32 v4, 16, v242\n" \
  "v_and_b32 v5, 0xffff0000, v242\nv_lshlrev_b32 v6, 16, v243\nv_and_b32 v7, 0xffff0000, v243\nv_lshlrev_b32 v8, 16, v244\n" \
  "v_and_b32 v9, 0xffff0000, v244\nv_lshlrev_b32 v10, 16, v245\nv_and_b32 v11, 0xffff0000, v245\nv_lshlrev_b32 v12, 16, v246\n" \
  "v_and_b32 v13, 0xffff0000, v246\nv_lshlrev_b32 v14, 16, v247\nv_and_b32 v15, 0xffff0000, v247\nv_lshlrev_b32 v16, 16, v248\n" \
  "v_and_b32 v17, 0xffff0000, v248\nv_lshlrev_b32 v18, 16, v249\nv_and_b32 v19, 0xffff0000, v249\ns_barrier\n" \
  "ds_read_b64_tr_b16 v[48:49], v154 offset:0\nds_read_b64_tr_b16 v[50:51], v155 offset:0\nds_read_b128 v[44:47], v153 offset:0\n" \
  "ds_read_b128 v[40:43], v152 offset:0\nds_read_b64_tr_b16 v[52:53], v154 offset:32\nds_read_b64_tr_b16 v[54:55], v155 offset:32\n" \
  "ds_read_b64_tr_b16 v[56:57], v154 offset:64\nds_read_b64_tr_b16 v[58:59], v155 offset:64\nds_read_b64_tr_b16 v[60:61], v154 offset:96\n" \
  "ds_read_b64_tr_b16 v[62:63], v155 offset:96\nds_read_b64_tr_b16 v[64:65], v154 offset:128\nds_read_b64_tr_b16 v[66:67], v155 offset:128\n" \
  "ds_read_b64_tr_b16 v[76:77], v154 offset:5120\nds_read_b64_tr_b16 v[78:79], v155 offset:5120\nds_read_b128 v[72:75], v153 offset:4096\n" \
  "ds_read_b128 v[68:71], v152 offset:4096\nds_read_b64_tr_b16 v[80:81], v154 offset:5152\nds_read_b64_tr_b16 v[82:83], v155 offset:5152\n" \
  "ds_read_b64_tr_b16 v[84:85], v154 offset:5184\nds_read_b64_tr_b16 v[86:87], v155 offset:5184\nds_read_b64_tr_b16 v[88:89], v154 offset:5216\n" \
  "ds_read_b64_tr_b16 v[90:91], v155 offset:5216\nds_read_b64_tr_b16 v[92:93], v154 offset:5248\nds_read_b64_tr_b16 v[94:95], v155 offset:5248\n" \
  ".Lk3_%=:\ns_waitcnt lgkmcnt(15)\nv_mfma_f32_16x16x32_bf16 v[20:23], v[48:51], v[44:47], v[20:23]\n" \
  "v_mfma_f32_16x16x32_bf16 v[0:3], v[48:51], v[40:43], v[0:3]\nv_mfma_f32_16x16x32_bf16 v[24:27], v[52:55], v[44:47], v[24:27]\n" \
  "v_mfma_f32_16x16x32_bf16 v[4:7], v[52:55], v[40:43], v[4:7]\nv_mfma_f32_16x16x32_bf16 v[28:31], v[56:59], v[44:47], v[28:31]\n" \
  "v_mfma_f32_16x16x32_bf16 v[8:11], v[56:59], v[40:43], v[8:11]\ns_waitcnt lgkmcnt(14)\n" \
  "v_mfma_f32_16x16x32_bf16 v[32:35], v[60:63], v[44:47], v[32:35]\nv_mfma_f32_16x16x32_bf16 v[12:15], v[60:63], v[40:43], v[12:15]\n" \
  "s_waitcnt lgkmcnt(12)\nv_mfma_f32_16x16x32_bf16 v[36:39], v[64:67], v[44:47], v[36:39]\n" \
  "v_mfma_f32_16x16x32_bf16 v[16:19], v[64:67], v[40:43], v[16:19]\ns_waitcnt lgkmcnt(9)\n" \
  "v_mfma_f32_16x16x32_bf16 v[20:23], v[76:79], v[72:75], v[20:23]\ns_waitcnt lgkmcnt(8)\n" \
  "v_mfma_f32_16x16x32_bf16 v[0:3], v[76:79], v[68:71], v[0:3]\ns_waitcnt lgkmcnt(6)\n" \
  "v_mfma_f32_16x16x32_bf16 v[24:27], v[80:83], v[72:75], v[24:27]\nv_mfma_f32_16x16x32_bf16 v[4:7], v[80:83], v[68:71], v[4:7]\n" \
  "s_waitcnt lgkmcnt(0)\ns_barrier\nv_mfma_f32_16x16x32_bf16 v[28:31], v[84:87], v[72:75], v[28:31]\n" \
  "ds_read_b64_tr_b16 v[104:105], v154 offset:20480\nds_read_b64_tr_b16 v[106:107], v155 offset:20480\nds_read_b128 v[100:103], v153 offset:16384\n" \
  "ds_read_b128 v[96:99], v152 offset:16384\nv_mfma_f32_16x16x32_bf16 v[8:11], v[84:87], v[68:71], v[8:11]\n" \
  "ds_read_b64_tr_b16 v[108:109], v154 offset:20512\nds_read_b64_tr_b16 v[110:111], v155 offset:20512\n" \
  "ds_read_b64_tr_b16 v[112:113], v154 offset:20544\nds_read_b64_tr_b16 v[114:115], v155 offset:20544\n" \
  "v_mfma_f32_16x16x32_bf16 v[32:35], v[88:91], v[72:75], v[32:35]\nds_read_b64_tr_b16 v[116:117], v154 offset:20576\n" \
  "ds_read_b64_tr_b16 v[118:119], v155 offset:20576\nds_read_b64_tr_b16 v[120:121], v154 offset:20608\n" \
  "ds_read_b64_tr_b16 v[122:123], v155 offset:20608\nv_mfma_f32_16x16x32_bf16 v[12:15], v[88:91], v[68:71], v[12:15]\n" \
  "ds_read_b64_tr_b16 v[132:133], v154 offset:25600\nds_read_b64_tr_b16 v[134:135], v155 offset:25600\nds_read_b128 v[128:131], v153 offset:20480\n" \
  "ds_read_b128 v[124:127], v152 offset:20480\nv_mfma_f32_16x16x32_bf16 v[36:39], v[92:95], v[72:75], v[36:39]\n" \
  "ds_read_b64_tr_b16 v[136:137], v154 offset:25632\nds_read_b64_tr_b16 v[138:139], v155 offset:25632\n" \
  "ds_read_b64_tr_b16 v[140:141], v154 offset:25664\nds_read_b64_tr_b16 v[142:143], v155 offset:25664\n" \
  "v_mfma_f32_16x16x32_bf16 v[16:19], v[92:95], v[68:71], v[16:19]\nds_read_b64_tr_b16 v[144:145], v154 offset:25696\n" \
  "ds_read_b64_tr_b16 v[146:147], v155 offset:25696\nds_read_b64_tr_b16 v[148:149], v154 offset:25728\n" \
  "ds_read_b64_tr_b16 v[150:151], v155 offset:25728\ns_cmp_lg_u32 %[cnt], 4\ns_cbranch_scc1 .Lk3r_%=\ns_waitcnt vmcnt(0)\ns_mov_b64 exec, 0xf\n" \
  "buffer_store_dword v236, v237, %[rf], %[fo] offen sc1\ns_mov_b64 exec, -1\n.Lk3r_%=:\ns_waitcnt lgkmcnt(15)\n" \
  "v_mfma_f32_16x16x32_bf16 v[20:23], v[104:107], v[100:103], v[20:23]\nv_xor_b32 v152, 0x8000, v152\n" \
  "v_mfma_f32_16x16x32_bf16 v[0:3], v[104:107], v[96:99], v[0:3]\nv_xor_b32 v153, 0x8000, v153\n" \
  "v_mfma_f32_16x16x32_bf16 v[24:27], v[108:111], v[100:103], v[24:27]\nv_add_u32 v154, %[db], v154\n" \
  "v_mfma_f32_16x16x32_bf16 v[4:7], v[108:111], v[96:99], v[4:7]\nv_add_u32 v155, %[db], v155\n" \
  "v_mfma_f32_16x16x32_bf16 v[28:31], v[112:115], v[100:103], v[28:31]\ns_mul_i32 %[db], %[db], -1\n" \
  "v_mfma_f32_16x16x32_bf16 v[8:11], v[112:115], v[96:99], v[8:11]\ns_waitcnt lgkmcnt(14)\n" \
  "v_mfma_f32_16x16x32_bf16 v[32:35], v[116:119], v[100:103], v[32:35]\nv_mfma_f32_16x16x32_bf16 v[12:15], v[116:119], v[96:99], v[12:15]\n" \
  "s_waitcnt lgkmcnt(12)\nv_mfma_f32_16x16x32_bf16 v[36:39], v[120:123], v[100:103], v[36:39]\n" \
  "v_mfma_f32_16x16x32_bf16 v[16:19], v[120:123], v[96:99], v[16:19]\ns_waitcnt lgkmcnt(9)\n" \
  "v_mfma_f32_16x16x32_bf16 v[20:23], v[132:135], v[128:131], v[20:23]\ns_waitcnt lgkmcnt(8)\n" \
  "v_mfma_f32_16x16x32_bf16 v[0:3], v[132:135], v[124:127], v[0:3]\ns_waitcnt lgkmcnt(6)\n" \
  "v_mfma_f32_16x16x32_bf16 v[24:27], v[136:139], v[128:131], v[24:27]\nv_mfma_f32_16x16x32_bf16 v[4:7], v[136:139], v[124:127], v[4:7]\n" \
  "s_waitcnt lgkmcnt(0)\ns_barrier\nv_mfma_f32_16x16x32_bf16 v[28:31], v[140:143], v[128:131], v[28:31]\nds_read_b64_tr_b16 v[48:49], v154 offset:0\n" \
  "ds_read_b64_tr_b16 v[50:51], v155 offset:0\nds_read_b128 v[44:47], v153 offset:0\nds_read_b128 v[40:43], v152 offset:0\n" \
  "v_mfma_f32_16x16x32_bf16 v[8:11], v[140:143], v[124:127], v[8:11]\nds_read_b64_tr_b16 v[52:53], v154 offset:32\n" \
  "ds_read_b64_tr_b16 v[54:55], v155 offset:32\nds_read_b64_tr_b16 v[56:57], v154 offset:64\nds_read_b64_tr_b16 v[58:59], v155 offset:64\n" \
  "v_mfma_f32_16x16x32_bf16 v[32:35], v[144:147], v[128:131], v[32:35]\nds_read_b64_tr_b16 v[60:61], v154 offset:96\n" \
  "ds_read_b64_tr_b16 v[62:63], v155 offset:96\nds_read_b64_tr_b16 v[64:65], v154 offset:128\nds_read_b64_tr_b16 v[66:67], v155 offset:128\n" \
  "v_mfma_f32_16x16x32_bf16 v[12:15], v[144:147], v[124:127], v[12:15]\nds_read_b64_tr_b16 v[76:77], v154 offset:5120\n" \
  "ds_read_b64_tr_b16 v[78:79], v155 offset:5120\nds_read_b128 v[72:75], v153 offset:4096\nds_read_b128 v[68:71], v152 offset:4096\n" \
  "v_mfma_f32_16x16x32_bf16 v[36:39], v[148:151], v[128:131], v[36:39]\nds_read_b64_tr_b16 v[80:81], v154 offset:5152\n" \
  "ds_read_b64_tr_b16 v[82:83], v155 offset:5152\nds_read_b64_tr_b16 v[84:85], v154 offset:5184\nds_read_b64_tr_b16 v[86:87], v155 offset:5184\n" \
  "v_mfma_f32_16x16x32_bf16 v[16:19], v[148:151], v[124:127], v[16:19]\nds_read_b64_tr_b16 v[88:89], v154 offset:5216\n" \
  "ds_read_b64_tr_b16 v[90:91], v155 offset:5216\nds_read_b64_tr_b16 v[92:93], v154 offset:5248\nds_read_b64_tr_b16 v[94:95], v155 offset:5248\n" \
  "s_add_u32 %[cnt], %[cnt], -1\ns_cbranch_scc1 .Lk3_%=\ns_waitcnt lgkmcnt(15)\nv_mfma_f32_16x16x32_bf16 v[20:23], v[48:51], v[44:47], v[20:23]\n" \
  "v_mfma_f32_16x16x32_bf16 v[0:3], v[48:51], v[40:43], v[0:3]\nv_mfma_f32_16x16x32_bf16 v[24:27], v[52:55], v[44:47], v[24:27]\n" \
  "v_mfma_f32_16x16x32_bf16 v[4:7], v[52:55], v[40:43], v[4:7]\nv_mfma_f32_16x16x32_bf16 v[28:31], v[56:59], v[44:47], v[28:31]\n" \
  "v_mfma_f32_16x16x32_bf16 v[8:11], v[56:59], v[40:43], v[8:11]\ns_waitcnt lgkmcnt(14)\n" \
  "v_mfma_f32_16x16x32_bf16 v[32:35], v[60:63], v[44:47], v[32:35]\nv_mfma_f32_16x16x32_bf16 v[12:15], v[60:63], v[40:43], v[12:15]\n" \
  "s_waitcnt lgkmcnt(12)\nv_mfma_f32_16x16x32_bf16 v[36:39], v[64:67], v[44:47], v[36:39]\n" \
  "v_mfma_f32_16x16x32_bf16 v[16:19], v[64:67], v[40:43], v[16:19]\ns_waitcnt lgkmcnt(9)\n" \
  "v_mfma_f32_16x16x32_bf16 v[20:23], v[76:79], v[72:75], v[20:23]\ns_waitcnt lgkmcnt(8)\n" \
  "v_mfma_f32_16x16x32_bf16 v[0:3], v[76:79], v[68:71], v[0:3]\ns_waitcnt lgkmcnt(6)\n" \
  "v_mfma_f32_16x16x32_bf16 v[24:27], v[80:83], v[72:75], v[24:27]\nv_mfma_f32_16x16x32_bf16 v[4:7], v[80:83], v[68:71], v[4:7]\n" \
  "s_waitcnt lgkmcnt(0)\ns_barrier\nv_mfma_f32_16x16x32_bf16 v[28:31], v[84:87], v[72:75], v[28:31]\n" \
  "ds_read_b64_tr_b16 v[104:105], v154 offset:20480\nds_read_b64_tr_b16 v[106:107], v155 offset:20480\nds_read_b128 v[100:103], v153 offset:16384\n" \
  "ds_read_b128 v[96:99], v152 offset:16384\nv_mfma_f32_16x16x32_bf16 v[8:11], v[84:87], v[68:71], v[8:11]\n" \
  "ds_read_b64_tr_b16 v[108:109], v154 offset:20512\nds_read_b64_tr_b16 v[110:111], v155 offset:20512\n" \
  "ds_read_b64_tr_b16 v[112:113], v154 offset:20544\nds_read_b64_tr_b16 v[114:115], v155 offset:20544\n" \
  "v_mfma_f32_16x16x32_bf16 v[32:35], v[88:91], v[72:75], v[32:35]\nds_read_b64_tr_b16 v[116:117], v154 offset:20576\n" \
  "ds_read_b64_tr_b16 v[118:119], v155 offset:20576\nds_read_b64_tr_b16 v[120:121], v154 offset:20608\n" \
  "ds_read_b64_tr_b16 v[122:123], v155 offset:20608\nv_mfma_f32_16x16x32_bf16 v[12:15], v[88:91], v[68:71], v[12:15]\n" \
  "ds_read_b64_tr_b16 v[132:133], v154 offset:25600\nds_read_b64_tr_b16 v[134:135], v155 offset:25600\nds_read_b128 v[128:131], v153 offset:20480\n" \
  "ds_read_b128 v[124:127], v152 offset:20480\nv_mfma_f32_16x16x32_bf16 v[36:39], v[92:95], v[72:75], v[36:39]\n" \
  "ds_read_b64_tr_b16 v[136:137], v154 offset:25632\nds_read_b64_tr_b16 v[138:139], v155 offset:25632\n" \
  "ds_read_b64_tr_b16 v[140:141], v154 offset:25664\nds_read_b64_tr_b16 v[142:143], v155 offset:25664\n" \
  "v_mfma_f32_16x16x32_bf16 v[16:19], v[92:95], v[68:71], v[16:19]\nds_read_b64_tr_b16 v[144:145], v154 offset:25696\n" \
  "ds_read_b64_tr_b16 v[146:147], v155 offset:25696\nds_read_b64_tr_b16 v[148:149], v154 offset:25728\n" \
  "ds_read_b64_tr_b16 v[150:151], v155 offset:25728\ns_waitcnt lgkmcnt(15)\nv_mfma_f32_16x16x32_bf16 v[20:23], v[104:107], v[100:103], v[20:23]\n" \
  "v_mfma_f32_16x16x32_bf16 v[24:27], v[108:111], v[100:103], v[24:27]\nv_mfma_f32_16x16x32_bf16 v[28:31], v[112:115], v[100:103], v[28:31]\n" \
  "s_waitcnt lgkmcnt(14)\nv_mfma_f32_16x16x32_bf16 v[32:35], v[116:119], v[100:103], v[32:35]\ns_waitcnt lgkmcnt(12)\n" \
  "v_mfma_f32_16x16x32_bf16 v[36:39], v[120:123], v[100:103], v[36:39]\ns_waitcnt lgkmcnt(9)\n" \
  "v_mfma_f32_16x16x32_bf16 v[20:23], v[132:135], v[128:131], v[20:23]\ns_waitcnt lgkmcnt(6)\n" \
  "v_mfma_f32_16x16x32_bf16 v[24:27], v[136:139], v[128:131], v[24:27]\ns_waitcnt lgkmcnt(4)\n" \
  "v_mfma_f32_16x16x32_bf16 v[28:31], v[140:143], v[128:131], v[28:31]\ns_waitcnt lgkmcnt(2)\n" \
  "v_mfma_f32_16x16x32_bf16 v[32:35], v[144:147], v[128:131], v[32:35]\nds_write_b128 v166, v[20:23] offset:0\ns_waitcnt lgkmcnt(1)\n" \
  "v_mfma_f32_16x16x32_bf16 v[36:39], v[148:151], v[128:131], v[36:39]\nds_write_b128 v166, v[24:27] offset:1024\n" \
  "v_mfma_f32_16x16x32_bf16 v[0:3], v[104:107], v[96:99], v[0:3]\nds_write_b128 v166, v[28:31] offset:2048\n" \
  "v_mfma_f32_16x16x32_bf16 v[4:7], v[108:111], v[96:99], v[4:7]\nds_write_b128 v166, v[32:35] offset:3072\n" \
  "v_mfma_f32_16x16x32_bf16 v[8:11], v[112:115], v[96:99], v[8:11]\nds_write_b128 v166, v[36:39] offset:4096\n" \
  "v_mfma_f32_16x16x32_bf16 v[12:15], v[116:119], v[96:99], v[12:15]\nv_mfma_f32_16x16x32_bf16 v[16:19], v[120:123], v[96:99], v[16:19]\n" \
  "s_waitcnt lgkmcnt(0)\ns_barrier\nds_read_b128 v[60:63], v167 offset:0\nds_read_b128 v[64:67], v167 offset:1024\n" \
  "ds_read_b128 v[68:71], v167 offset:2048\nds_read_b128 v[72:75], v167 offset:3072\nds_read_b128 v[76:79], v167 offset:4096\n" \
  "v_mfma_f32_16x16x32_bf16 v[0:3], v[132:135], v[124:127], v[0:3]\nv_mfma_f32_16x16x32_bf16 v[4:7], v[136:139], v[124:127], v[4:7]\n" \
  "v_mfma_f32_16x16x32_bf16 v[8:11], v[140:143], v[124:127], v[8:11]\nv_mfma_f32_16x16x32_bf16 v[12:15], v[144:147], v[124:127], v[12:15]\n" \
  "v_mfma_f32_16x16x32_bf16 v[16:19], v[148:151], v[124:127], v[16:19]\ns_nop 7\ns_waitcnt lgkmcnt(4)\nv_pk_add_f32 v[0:1], v[0:1], v[60:61]\n" \
  "v_pk_add_f32 v[2:3], v[2:3], v[62:63]\nv_cvt_pk_bf16_f32 v80, v0, v1\nv_cvt_pk_bf16_f32 v81, v2, v3\ns_waitcnt lgkmcnt(3)\n" \
  "v_pk_add_f32 v[4:5], v[4:5], v[64:65]\nv_pk_add_f32 v[6:7], v[6:7], v[66:67]\nv_cvt_pk_bf16_f32 v82, v4, v5\nv_cvt_pk_bf16_f32 v83, v6, v7\n" \
  "s_waitcnt lgkmcnt(2)\nv_pk_add_f32 v[8:9], v[8:9], v[68:69]\nv_pk_add_f32 v[10:11], v[10:11], v[70:71]\nv_cvt_pk_bf16_f32 v84, v8, v9\n" \
  "v_cvt_pk_bf16_f32 v85, v10, v11\ns_waitcnt lgkmcnt(1)\nv_pk_add_f32 v[12:13], v[12:13], v[72:73]\nv_pk_add_f32 v[14:15], v[14:15], v[74:75]\n" \
  "v_cvt_pk_bf16_f32 v86, v12, v13\nv_cvt_pk_bf16_f32 v87, v14, v15\ns_waitcnt lgkmcnt(0)\ns_nop 1\nv_pk_add_f32 v[16:17], v[16:17], v[76:77]\n" \
  "v_pk_add_f32 v[18:19], v[18:19], v[78:79]\nv_cvt_pk_bf16_f32 v88, v16, v17\nv_cvt_pk_bf16_f32 v89, v18, v19\nv_permlane16_swap_b32 v80, v82\n" \
  "v_permlane16_swap_b32 v81, v83\nbuffer_store_dwordx4 v[80:83], v168, %[ro], %[so0] offen\nv_permlane16_swap_b32 v84, v86\n" \
  "v_permlane16_swap_b32 v85, v87\nbuffer_store_dwordx4 v[84:87], v168, %[ro], %[so0] offen offset:64\nv_permlane16_swap_b32 v88, v90\n" \
  "v_permlane16_swap_b32 v89, v91\ns_mov_b64 exec, %[me]\nbuffer_store_dwordx4 v[88:91], v168, %[ro], %[so0] offen offset:128\ns_mov_b64 exec, -1\n"
#define P5_ASM \
  "s_add_u32 m0, %[lb], 0x1e000\ns_nop 0\nbuffer_load_dwordx4 %[a0], %[sw], %[gb] offen offset:0 sc1 lds\n" \
  "buffer_load_dwordx4 %[a0], %[sw], %[gb] offen offset:1024 sc1 lds\nbuffer_load_dwordx4 %[a0], %[sw], %[gb] offen offset:2048 sc1 lds\n" \
  "buffer_load_dwordx4 %[a0], %[sw], %[gb] offen offset:3072 sc1 lds\ns_add_u32 m0, %[lb], 0x1f000\ns_nop 0\n" \
  "buffer_load_dwordx4 %[b4], %[sw], %[gb] offen offset:0 sc1 lds\ns_add_u32 %[gb], %[gb], 0x5000\ns_add_u32 m0, %[lb], 0x23000\ns_nop 0\n" \
  "buffer_load_dwordx4 %[a0], %[sw], %[gb] offen offset:0 sc1 lds\nbuffer_load_dwordx4 %[a0], %[sw], %[gb] offen offset:1024 sc1 lds\n" \
  "buffer_load_dwordx4 %[a0], %[sw], %[gb] offen offset:2048 sc1 lds\nbuffer_load_dwordx4 %[a0], %[sw], %[gb] offen offset:3072 sc1 lds\n" \
  "s_add_u32 m0, %[lb], 0x24000\ns_nop 0\nbuffer_load_dwordx4 %[b4], %[sw], %[gb] offen offset:0 sc1 lds\ns_add_u32 %[gb], %[gb], 0x5000\n" \
  "s_add_u32 m0, %[lb], 0x10000\ns_nop 0\nbuffer_load_dwordx4 %[a0], %[sw], %[gb] offen offset:0 sc1 lds\n" \
  "buffer_load_dwordx4 %[a0], %[sw], %[gb] offen offset:1024 sc1 lds\nbuffer_load_dwordx4 %[a0], %[sw], %[gb] offen offset:2048 sc1 lds\n" \
  "buffer_load_dwordx4 %[a0], %[sw], %[gb] offen offset:3072 sc1 lds\ns_add_u32 m0, %[lb], 0x11000\ns_nop 0\n" \
  "buffer_load_dwordx4 %[b4], %[sw], %[gb] offen offset:0 sc1 lds\ns_sleep 24\nv_mov_b64 %[pv], %[ep]\ns_mov_b64 exec, 0xff\nv_mov_b64 %[pv], 0\n" \
  "buffer_atomic_or_x2 %[pv], %[vf], %[rf], %[fi] offen sc0 sc1\ns_mov_b64 exec, -1\ns_waitcnt vmcnt(0)\ns_mov_b32 %[t], 0x40000\n.Lq0_%=:\n" \
  "v_cmp_ne_u64 vcc, %[ep], %[pv]\ns_nop 0\ns_cbranch_vccz .Lq0_%=x\ns_sleep 2\ns_mov_b64 exec, 0xff\nv_mov_b64 %[pv], 0\n" \
  "buffer_atomic_or_x2 %[pv], %[vf], %[rf], %[fi] offen sc0 sc1\ns_mov_b64 exec, -1\ns_sub_u32 %[t], %[t], 1\ns_waitcnt vmcnt(0)\n" \
  "s_cbranch_scc0 .Lq0_%=\n.Lq0_%=x:\ns_add_u32 %[fi], %[fi], 512\ns_add_u32 m0, %[la], 0x8000\ns_nop 0\n" \
  "buffer_load_dwordx4 %[a0], %[sa], %[ga] offen offset:0 sc1 lds\nbuffer_load_dwordx4 %[a1], %[sa], %[ga] offen offset:1024 sc1 lds\n" \
  "buffer_load_dwordx4 %[a2], %[sa], %[ga] offen offset:2048 sc1 lds\nbuffer_load_dwordx4 %[a3], %[sa], %[ga] offen offset:3072 sc1 lds\n" \
  "s_add_u32 %[ga], %[ga], 0x80000\ns_add_u32 m0, %[la], 0xc000\ns_nop 0\nbuffer_load_dwordx4 %[a0], %[sa], %[ga] offen offset:0 sc1 lds\n" \
  "buffer_load_dwordx4 %[a1], %[sa], %[ga] offen offset:1024 sc1 lds\nbuffer_load_dwordx4 %[a2], %[sa], %[ga] offen offset:2048 sc1 lds\n" \
  "buffer_load_dwordx4 %[a3], %[sa], %[ga] offen offset:3072 sc1 lds\ns_add_u32 %[ga], %[ga], 0x80000\ns_add_u32 m0, %[la], 0x0\ns_nop 0\n" \
  "buffer_load_dwordx4 %[a0], %[sa], %[ga] offen offset:0 sc1 lds\nbuffer_load_dwordx4 %[a1], %[sa], %[ga] offen offset:1024 sc1 lds\n" \
  "buffer_load_dwordx4 %[a2], %[sa], %[ga] offen offset:2048 sc1 lds\nbuffer_load_dwordx4 %[a3], %[sa], %[ga] offen offset:3072 sc1 lds\n" \
  "s_waitcnt vmcnt(8)\ns_barrier\ns_mov_b64 exec, 0xff\nv_mov_b64 %[pv], 0\nbuffer_atomic_or_x2 %[pv], %[vf], %[rf], %[fi] offen sc0 sc1\n" \
  "s_mov_b64 exec, -1\ns_add_u32 %[ga], %[ga], 0x80000\ns_add_u32 m0, %[la], 0x4000\ns_nop 0\n" \
  "buffer_load_dwordx4 %[a0], %[sa], %[ga] offen offset:0 sc1 lds\nbuffer_load_dwordx4 %[a1], %[sa], %[ga] offen offset:1024 sc1 lds\n" \
  "buffer_load_dwordx4 %[a2], %[sa], %[ga] offen offset:2048 sc1 lds\nbuffer_load_dwordx4 %[a3], %[sa], %[ga] offen offset:3072 sc1 lds\n" \
  "s_add_u32 %[gb], %[gb], 0x5000\ns_add_u32 m0, %[lb], 0x15000\ns_nop 0\nbuffer_load_dwordx4 %[a0], %[sw], %[gb] offen offset:0 sc1 lds\n" \
  "buffer_load_dwordx4 %[a0], %[sw], %[gb] offen offset:1024 sc1 lds\nbuffer_load_dwordx4 %[a0], %[sw], %[gb] offen offset:2048 sc1 lds\n" \
  "buffer_load_dwordx4 %[a0], %[sw], %[gb] offen offset:3072 sc1 lds\ns_add_u32 m0, %[lb], 0x16000\ns_nop 0\n" \
  "buffer_load_dwordx4 %[b4], %[sw], %[gb] offen offset:0 sc1 lds\ns_waitcnt vmcnt(14)\ns_barrier\ns_waitcnt vmcnt(9)\ns_mov_b32 %[t], 0x40000\n" \
  ".Lq1_%=:\nv_cmp_ne_u64 vcc, %[ep], %[pv]\ns_nop 0\ns_cbranch_vccz .Lq1_%=x\ns_mov_b64 exec, 0xff\nv_mov_b64 %[pv], 0\n" \
  "buffer_atomic_or_x2 %[pv], %[vf], %[rf], %[fi] offen sc0 sc1\ns_mov_b64 exec, -1\ns_sub_u32 %[t], %[t], 1\ns_waitcnt vmcnt(0)\n" \
  "s_cbranch_scc0 .Lq1_%=\n.Lq1_%=x:\ns_add_u32 %[fi], %[fi], 512\ns_add_u32 %[ga], %[ga], 0x80000\ns_add_u32 m0, %[la], 0x8000\ns_nop 0\n" \
  "buffer_load_dwordx4 %[a0], %[sa], %[ga] offen offset:0 sc1 lds\nbuffer_load_dwordx4 %[a1], %[sa], %[ga] offen offset:1024 sc1 lds\n" \
  "s_waitcnt vmcnt(12)\ns_barrier\nbuffer_load_dwordx4 %[a2], %[sa], %[ga] offen offset:2048 sc1 lds\n" \
  "buffer_load_dwordx4 %[a3], %[sa], %[ga] offen offset:3072 sc1 lds\ns_add_u32 %[gb], %[gb], 0x5000\ns_add_u32 m0, %[lb], 0x1e000\ns_nop 0\n" \
  "buffer_load_dwordx4 %[a0], %[sw], %[gb] offen offset:0 sc1 lds\nbuffer_load_dwordx4 %[a0], %[sw], %[gb] offen offset:1024 sc1 lds\n" \
  "buffer_load_dwordx4 %[a0], %[sw], %[gb] offen offset:2048 sc1 lds\nbuffer_load_dwordx4 %[a0], %[sw], %[gb] offen offset:3072 sc1 lds\n" \
  "s_add_u32 m0, %[lb], 0x1f000\ns_nop 0\nbuffer_load_dwordx4 %[b4], %[sw], %[gb] offen offset:0 sc1 lds\ns_add_u32 %[ga], %[ga], 0x80000\n" \
  "s_add_u32 m0, %[la], 0xc000\ns_nop 0\nbuffer_load_dwordx4 %[a0], %[sa], %[ga] offen offset:0 sc1 lds\n" \
  "buffer_load_dwordx4 %[a1], %[sa], %[ga] offen offset:1024 sc1 lds\ns_waitcnt vmcnt(11)\ns_barrier\n" \
  "buffer_load_dwordx4 %[a2], %[sa], %[ga] offen offset:2048 sc1 lds\nbuffer_load_dwordx4 %[a3], %[sa], %[ga] offen offset:3072 sc1 lds\n" \
  "s_add_u32 %[gb], %[gb], 0x5000\ns_add_u32 m0, %[lb], 0x23000\ns_nop 0\nbuffer_load_dwordx4 %[a0], %[sw], %[gb] offen offset:0 sc1 lds\n" \
  "buffer_load_dwordx4 %[a0], %[sw], %[gb] offen offset:1024 sc1 lds\nbuffer_load_dwordx4 %[a0], %[sw], %[gb] offen offset:2048 sc1 lds\n" \
  "buffer_load_dwordx4 %[a0], %[sw], %[gb] offen offset:3072 sc1 lds\ns_add_u32 m0, %[lb], 0x24000\ns_nop 0\n" \
  "buffer_load_dwordx4 %[b4], %[sw], %[gb] offen offset:0 sc1 lds\ns_add_u32 %[ga], %[ga], 0x80000\ns_add_u32 m0, %[la], 0x0\ns_nop 0\n" \
  "buffer_load_dwordx4 %[a0], %[sa], %[ga] offen offset:0 sc1 lds\nbuffer_load_dwordx4 %[a1], %[sa], %[ga] offen offset:1024 sc1 lds\n" \
  "s_waitcnt vmcnt(11)\ns_barrier\ns_mov_b64 exec, 0xff\nv_mov_b64 %[pv], 0\nbuffer_atomic_or_x2 %[pv], %[vf], %[rf], %[fi] offen sc0 sc1\n" \
  "s_mov_b64 exec, -1\nbuffer_load_dwordx4 %[a2], %[sa], %[ga] offen offset:2048 sc1 lds\n" \
  "buffer_load_dwordx4 %[a3], %[sa], %[ga] offen offset:3072 sc1 lds\ns_add_u32 %[gb], %[gb], 0x5000\ns_add_u32 m0, %[lb], 0x10000\ns_nop 0\n" \
  "buffer_load_dwordx4 %[a0], %[sw], %[gb] offen offset:0 sc1 lds\nbuffer_load_dwordx4 %[a0], %[sw], %[gb] offen offset:1024 sc1 lds\n" \
  "buffer_load_dwordx4 %[a0], %[sw], %[gb] offen offset:2048 sc1 lds\nbuffer_load_dwordx4 %[a0], %[sw], %[gb] offen offset:3072 sc1 lds\n" \
  "s_add_u32 m0, %[lb], 0x11000\ns_nop 0\nbuffer_load_dwordx4 %[b4], %[sw], %[gb] offen offset:0 sc1 lds\ns_add_u32 %[ga], %[ga], 0x80000\n" \
  "s_add_u32 m0, %[la], 0x4000\ns_nop 0\nbuffer_load_dwordx4 %[a0], %[sa], %[ga] offen offset:0 sc1 lds\n" \
  "buffer_load_dwordx4 %[a1], %[sa], %[ga] offen offset:1024 sc1 lds\ns_waitcnt vmcnt(12)\ns_barrier\n" \
  "buffer_load_dwordx4 %[a2], %[sa], %[ga] offen offset:2048 sc1 lds\nbuffer_load_dwordx4 %[a3], %[sa], %[ga] offen offset:3072 sc1 lds\n" \
  "s_add_u32 %[gb], %[gb], 0x5000\ns_add_u32 m0, %[lb], 0x15000\ns_nop 0\nbuffer_load_dwordx4 %[a0], %[sw], %[gb] offen offset:0 sc1 lds\n" \
  "buffer_load_dwordx4 %[a0], %[sw], %[gb] offen offset:1024 sc1 lds\nbuffer_load_dwordx4 %[a0], %[sw], %[gb] offen offset:2048 sc1 lds\n" \
  "buffer_load_dwordx4 %[a0], %[sw], %[gb] offen offset:3072 sc1 lds\ns_add_u32 m0, %[lb], 0x16000\ns_nop 0\n" \
  "buffer_load_dwordx4 %[b4], %[sw], %[gb] offen offset:0 sc1 lds\ns_waitcnt vmcnt(16)\ns_mov_b32 %[t], 0x40000\n.Lq2_%=:\n" \
  "v_cmp_ne_u64 vcc, %[ep], %[pv]\ns_nop 0\ns_cbranch_vccz .Lq2_%=x\ns_mov_b64 exec, 0xff\nv_mov_b64 %[pv], 0\n" \
  "buffer_atomic_or_x2 %[pv], %[vf], %[rf], %[fi] offen sc0 sc1\ns_mov_b64 exec, -1\ns_sub_u32 %[t], %[t], 1\ns_waitcnt vmcnt(0)\n" \
  "s_cbranch_scc0 .Lq2_%=\n.Lq2_%=x:\ns_add_u32 %[fi], %[fi], 512\ns_add_u32 %[ga], %[ga], 0x80000\ns_add_u32 m0, %[la], 0x8000\ns_nop 0\n" \
  "buffer_load_dwordx4 %[a0], %[sa], %[ga] offen offset:0 sc1 lds\nbuffer_load_dwordx4 %[a1], %[sa], %[ga] offen offset:1024 sc1 lds\n" \
  "s_waitcnt vmcnt(11)\ns_barrier\nbuffer_load_dwordx4 %[a2], %[sa], %[ga] offen offset:2048 sc1 lds\n" \
  "buffer_load_dwordx4 %[a3], %[sa], %[ga] offen offset:3072 sc1 lds\ns_add_u32 %[gb], %[gb], 0x5000\ns_add_u32 m0, %[lb], 0x1e000\ns_nop 0\n" \
  "buffer_load_dwordx4 %[a0], %[sw], %[gb] offen offset:0 sc1 lds\nbuffer_load_dwordx4 %[a0], %[sw], %[gb] offen offset:1024 sc1 lds\n" \
  "buffer_load_dwordx4 %[a0], %[sw], %[gb] offen offset:2048 sc1 lds\nbuffer_load_dwordx4 %[a0], %[sw], %[gb] offen offset:3072 sc1 lds\n" \
  "s_add_u32 m0, %[lb], 0x1f000\ns_nop 0\nbuffer_load_dwordx4 %[b4], %[sw], %[gb] offen offset:0 sc1 lds\ns_add_u32 %[ga], %[ga], 0x80000\n" \
  "s_add_u32 m0, %[la], 0xc000\ns_nop 0\nbuffer_load_dwordx4 %[a0], %[sa], %[ga] offen offset:0 sc1 lds\n" \
  "buffer_load_dwordx4 %[a1], %[sa], %[ga] offen offset:1024 sc1 lds\ns_waitcnt vmcnt(11)\ns_barrier\n" \
  "buffer_load_dwordx4 %[a2], %[sa], %[ga] offen offset:2048 sc1 lds\nbuffer_load_dwordx4 %[a3], %[sa], %[ga] offen offset:3072 sc1 lds\n" \
  "s_add_u32 %[gb], %[gb], 0x5000\ns_add_u32 m0, %[lb], 0x23000\ns_nop 0\nbuffer_load_dwordx4 %[a0], %[sw], %[gb] offen offset:0 sc1 lds\n" \
  "buffer_load_dwordx4 %[a0], %[sw], %[gb] offen offset:1024 sc1 lds\nbuffer_load_dwordx4 %[a0], %[sw], %[gb] offen offset:2048 sc1 lds\n" \
  "buffer_load_dwordx4 %[a0], %[sw], %[gb] offen offset:3072 sc1 lds\ns_add_u32 m0, %[lb], 0x24000\ns_nop 0\n" \
  "buffer_load_dwordx4 %[b4], %[sw], %[gb] offen offset:0 sc1 lds\ns_add_u32 %[ga], %[ga], 0x80000\ns_add_u32 m0, %[la], 0x0\ns_nop 0\n" \
  "buffer_load_dwordx4 %[a0], %[sa], %[ga] offen offset:0 sc1 lds\nbuffer_load_dwordx4 %[a1], %[sa], %[ga] offen offset:1024 sc1 lds\n" \
  "s_waitcnt vmcnt(11)\ns_barrier\nbuffer_load_dwordx4 %[a2], %[sa], %[ga] offen offset:2048 sc1 lds\n" \
  "buffer_load_dwordx4 %[a3], %[sa], %[ga] offen offset:3072 sc1 lds\ns_add_u32 %[gb], %[gb], 0x5000\ns_add_u32 m0, %[lb], 0x10000\ns_nop 0\n" \
  "buffer_load_dwordx4 %[a0], %[sw], %[gb] offen offset:0 sc1 lds\nbuffer_load_dwordx4 %[a0], %[sw], %[gb] offen offset:1024 sc1 lds\n" \
  "buffer_load_dwordx4 %[a0], %[sw], %[gb] offen offset:2048 sc1 lds\nbuffer_load_dwordx4 %[a0], %[sw], %[gb] offen offset:3072 sc1 lds\n" \
  "s_add_u32 m0, %[lb], 0x11000\ns_nop 0\nbuffer_load_dwordx4 %[b4], %[sw], %[gb] offen offset:0 sc1 lds\ns_add_u32 %[ga], %[ga], 0x80000\n" \
  "s_add_u32 m0, %[la], 0x4000\ns_nop 0\nbuffer_load_dwordx4 %[a0], %[sa], %[ga] offen offset:0 sc1 lds\n" \
  "buffer_load_dwordx4 %[a1], %[sa], %[ga] offen offset:1024 sc1 lds\ns_waitcnt vmcnt(11)\ns_barrier\n" \
  "buffer_load_dwordx4 %[a2], %[sa], %[ga] offen offset:2048 sc1 lds\nbuffer_load_dwordx4 %[a3], %[sa], %[ga] offen offset:3072 sc1 lds\n" \
  "s_add_u32 %[gb], %[gb], 0x5000\ns_add_u32 m0, %[lb], 0x15000\ns_nop 0\nbuffer_load_dwordx4 %[a0], %[sw], %[gb] offen offset:0 sc1 lds\n" \
  "buffer_load_dwordx4 %[a0], %[sw], %[gb] offen offset:1024 sc1 lds\nbuffer_load_dwordx4 %[a0], %[sw], %[gb] offen offset:2048 sc1 lds\n" \
  "buffer_load_dwordx4 %[a0], %[sw], %[gb] offen offset:3072 sc1 lds\ns_add_u32 m0, %[lb], 0x16000\ns_nop 0\n" \
  "buffer_load_dwordx4 %[b4], %[sw], %[gb] offen offset:0 sc1 lds\ns_waitcnt vmcnt(9)\ns_barrier\ns_waitcnt vmcnt(0)\ns_barrier\ns_barrier\n"
#define POLL3_ASM \
  "s_sleep 12\ns_mov_b32 %[t], 0x40000\nv_mov_b64 %[pv], %[ep]\nv_mov_b64 %[pw], %[ep]\n.Lq%=:\ns_mov_b32 exec_hi, 0\nv_mov_b64 %[pv], 0\n" \
  "buffer_atomic_or_x2 %[pv], %[vf], %[rf], %[fi] offen sc0 sc1\ns_mov_b32 exec_hi, -1\ns_waitcnt vmcnt(0)\nv_cmp_ne_u64 vcc, %[ep], %[pv]\ns_nop 0\n" \
  "s_cbranch_vccz .Lx%=\ns_sleep 2\ns_sub_u32 %[t], %[t], 1\ns_cbranch_scc0 .Lq%=\n.Lx%=:\n"
#define PUB2_ASM \
  "s_mov_b64 exec, 0xf\nv_mov_b32 v2, %[e]\nv_mbcnt_lo_u32_b32 v3, -1, 0\nv_lshlrev_b32 v3, 2, v3\ns_waitcnt vmcnt(0)\n" \
  "buffer_store_dword v2, v3, %[rf], %[fo] offen sc1\ns_mov_b64 exec, -1\ns_nop 1\n"
#define K4_ASM \
  "v_mov_b64 v[0:1], 0\nv_mov_b64 v[2:3], 0\nv_mov_b64 v[4:5], 0\nv_mov_b64 v[6:7], 0\nv_mov_b64 v[8:9], 0\nv_mov_b64 v[10:11], 0\n" \
  "v_mov_b64 v[12:13], 0\nv_mov_b64 v[14:15], 0\nv_mov_b64 v[16:17], 0\nv_mov_b64 v[18:19], 0\nv_mov_b64 v[20:21], 0\nv_mov_b64 v[22:23], 0\n" \
  "v_mov_b64 v[24:25], 0\nv_mov_b64 v[26:27], 0\nv_mov_b64 v[28:29], 0\nv_mov_b64 v[30:31], 0\nv_mov_b64 v[32:33], 0\nv_mov_b64 v[34:35], 0\n" \
  "v_mov_b64 v[36:37], 0\nv_mov_b64 v[38:39], 0\nv_mov_b64 v[40:41], 0\nv_mov_b64 v[42:43], 0\nv_mov_b64 v[44:45], 0\nv_mov_b64 v[46:47], 0\n" \
  "v_mov_b64 v[48:49], 0\nv_mov_b64 v[50:51], 0\nv_mov_b64 v[52:53], 0\nv_mov_b64 v[54:55], 0\nv_mov_b64 v[56:57], 0\nv_mov_b64 v[58:59], 0\n" \
  "v_mov_b64 v[60:61], 0\nv_mov_b64 v[62:63], 0\nv_mov_b64 v[64:65], 0\nv_mov_b64 v[66:67], 0\nv_mov_b64 v[68:69], 0\nv_mov_b64 v[70:71], 0\n" \
  "v_mov_b64 v[72:73], 0\nv_mov_b64 v[74:75], 0\nv_mov_b64 v[76:77], 0\nv_mov_b64 v[78:79], 0\nv_mov_b64 v[80:81], 0\nv_mov_b64 v[82:83], 0\n" \
  "v_mov_b64 v[84:85], 0\nv_mov_b64 v[86:87], 0\nv_mov_b64 v[88:89], 0\nv_mov_b64 v[90:91], 0\nv_mov_b64 v[92:93], 0\nv_mov_b64 v[94:95], 0\n" \
  "v_mov_b32 v234, 0xc0135761\nv_mov_b32 v235, 0xbdd2d3e8\nv_mbcnt_lo_u32_b32 v237, -1, 0\nv_mov_b32 v236, %[e]\nv_lshlrev_b32 v237, 2, v237\n" \
  "s_cmp_lg_u32 %[pw], 0\ns_cbranch_scc1 .Lwk4_%=x\ns_mov_b32 %[pw], 0x40000\n.Lwk4_%=:\nbuffer_load_dwordx2 v[96:97], %[vf], %[rf], %[fi] offen sc1\n" \
  "s_sub_u32 %[pw], %[pw], 1\ns_cbranch_scc1 .Lwk4_%=x\ns_waitcnt vmcnt(0)\nv_cmp_ne_u64 vcc, %[ep], v[96:97]\ns_nop 0\ns_cbranch_vccnz .Lwk4_%=\n" \
  ".Lwk4_%=x:\ns_waitcnt vmcnt(0)\ns_barrier\ns_barrier\nds_read_b64_tr_b16 v[112:113], v176 offset:0\n" \
  "ds_read_b64_tr_b16 v[114:115], v176 offset:768\nds_read_b128 v[96:99], v178 offset:0\nds_read_b128 v[100:103], v178 offset:256\n" \
  "ds_read_b128 v[104:107], v178 offset:512\nds_read_b128 v[108:111], v178 offset:768\nds_read_b64_tr_b16 v[116:117], v177 offset:0\n" \
  "ds_read_b64_tr_b16 v[118:119], v177 offset:768\nds_read_b64_tr_b16 v[120:121], v176 offset:64\nds_read_b64_tr_b16 v[122:123], v176 offset:832\n" \
  "ds_read_b64_tr_b16 v[124:125], v176 offset:12288\nds_read_b64_tr_b16 v[126:127], v176 offset:13056\n" \
  "ds_read_b64_tr_b16 v[128:129], v177 offset:12288\nds_read_b64_tr_b16 v[130:131], v177 offset:13056\n" \
  "ds_read_b64_tr_b16 v[132:133], v176 offset:12352\nds_read_b64_tr_b16 v[134:135], v176 offset:13120\n.Lk4_%=:\ns_waitcnt lgkmcnt(13)\n" \
  "v_mfma_f32_16x16x32_bf16 v[0:3], v[112:115], v[96:99], v[0:3]\ns_waitcnt lgkmcnt(12)\n" \
  "v_mfma_f32_16x16x32_bf16 v[4:7], v[112:115], v[100:103], v[4:7]\ns_waitcnt lgkmcnt(11)\n" \
  "v_mfma_f32_16x16x32_bf16 v[8:11], v[112:115], v[104:107], v[8:11]\ns_waitcnt lgkmcnt(10)\n" \
  "v_mfma_f32_16x16x32_bf16 v[12:15], v[112:115], v[108:111], v[12:15]\ns_waitcnt lgkmcnt(8)\n" \
  "v_mfma_f32_16x16x32_bf16 v[16:19], v[116:119], v[96:99], v[16:19]\nds_read_b64_tr_b16 v[152:153], v176 offset:6144\n" \
  "v_mfma_f32_16x16x32_bf16 v[20:23], v[116:119], v[100:103], v[20:23]\nds_read_b64_tr_b16 v[154:155], v176 offset:6912\n" \
  "v_mfma_f32_16x16x32_bf16 v[24:27], v[116:119], v[104:107], v[24:27]\nds_read_b128 v[136:139], v178 offset:8192\n" \
  "v_mfma_f32_16x16x32_bf16 v[28:31], v[116:119], v[108:111], v[28:31]\nds_read_b128 v[140:143], v178 offset:8448\ns_waitcnt lgkmcnt(10)\n" \
  "v_mfma_f32_16x16x32_bf16 v[32:35], v[120:123], v[96:99], v[32:35]\nds_read_b128 v[144:147], v178 offset:8704\n" \
  "v_mfma_f32_16x16x32_bf16 v[36:39], v[120:123], v[100:103], v[36:39]\nds_read_b128 v[148:151], v178 offset:8960\n" \
  "v_mfma_f32_16x16x32_bf16 v[40:43], v[120:123], v[104:107], v[40:43]\nds_read_b64_tr_b16 v[156:157], v177 offset:6144\n" \
  "v_mfma_f32_16x16x32_bf16 v[44:47], v[120:123], v[108:111], v[44:47]\nds_read_b64_tr_b16 v[158:159], v177 offset:6912\ns_waitcnt lgkmcnt(12)\n" \
  "v_mfma_f32_16x16x32_bf16 v[48:51], v[124:127], v[96:99], v[48:51]\nds_read_b64_tr_b16 v[160:161], v176 offset:6208\n" \
  "v_mfma_f32_16x16x32_bf16 v[52:55], v[124:127], v[100:103], v[52:55]\nds_read_b64_tr_b16 v[162:163], v176 offset:6976\n" \
  "v_mfma_f32_16x16x32_bf16 v[56:59], v[124:127], v[104:107], v[56:59]\nds_read_b64_tr_b16 v[164:165], v176 offset:18432\n" \
  "v_mfma_f32_16x16x32_bf16 v[60:63], v[124:127], v[108:111], v[60:63]\nds_read_b64_tr_b16 v[166:167], v176 offset:19200\ns_waitcnt lgkmcnt(14)\n" \
  "v_mfma_f32_16x16x32_bf16 v[64:67], v[128:131], v[96:99], v[64:67]\nds_read_b64_tr_b16 v[168:169], v177 offset:18432\n" \
  "v_mfma_f32_16x16x32_bf16 v[68:71], v[128:131], v[100:103], v[68:71]\nds_read_b64_tr_b16 v[170:171], v177 offset:19200\n" \
  "v_mfma_f32_16x16x32_bf16 v[72:75], v[128:131], v[104:107], v[72:75]\nds_read_b64_tr_b16 v[172:173], v176 offset:18496\n" \
  "v_mfma_f32_16x16x32_bf16 v[76:79], v[128:131], v[108:111], v[76:79]\nds_read_b64_tr_b16 v[174:175], v176 offset:19264\ns_waitcnt lgkmcnt(15)\n" \
  "v_mfma_f32_16x16x32_bf16 v[80:83], v[132:135], v[96:99], v[80:83]\nv_mfma_f32_16x16x32_bf16 v[84:87], v[132:135], v[100:103], v[84:87]\n" \
  "v_mfma_f32_16x16x32_bf16 v[88:91], v[132:135], v[104:107], v[88:91]\nv_mfma_f32_16x16x32_bf16 v[92:95], v[132:135], v[108:111], v[92:95]\n" \
  "s_waitcnt lgkmcnt(13)\nv_mfma_f32_16x16x32_bf16 v[0:3], v[152:155], v[136:139], v[0:3]\ns_waitcnt lgkmcnt(12)\n" \
  "v_mfma_f32_16x16x32_bf16 v[4:7], v[152:155], v[140:143], v[4:7]\ns_waitcnt lgkmcnt(11)\n" \
  "v_mfma_f32_16x16x32_bf16 v[8:11], v[152:155], v[144:147], v[8:11]\ns_waitcnt lgkmcnt(10)\n" \
  "v_mfma_f32_16x16x32_bf16 v[12:15], v[152:155], v[148:151], v[12:15]\ns_waitcnt lgkmcnt(0)\ns_barrier\n" \
  "v_mfma_f32_16x16x32_bf16 v[16:19], v[156:159], v[136:139], v[16:19]\nds_read_b64_tr_b16 v[112:113], v176 offset:24576\n" \
  "v_mfma_f32_16x16x32_bf16 v[20:23], v[156:159], v[140:143], v[20:23]\nds_read_b64_tr_b16 v[114:115], v176 offset:25344\n" \
  "v_mfma_f32_16x16x32_bf16 v[24:27], v[156:159], v[144:147], v[24:27]\nds_read_b128 v[96:99], v178 offset:16384\n" \
  "v_mfma_f32_16x16x32_bf16 v[28:31], v[156:159], v[148:151], v[28:31]\nds_read_b128 v[100:103], v178 offset:16640\n" \
  "v_mfma_f32_16x16x32_bf16 v[32:35], v[160:163], v[136:139], v[32:35]\nds_read_b128 v[104:107], v178 offset:16896\n" \
  "v_mfma_f32_16x16x32_bf16 v[36:39], v[160:163], v[140:143], v[36:39]\nds_read_b128 v[108:111], v178 offset:17152\n" \
  "v_mfma_f32_16x16x32_bf16 v[40:43], v[160:163], v[144:147], v[40:43]\nds_read_b64_tr_b16 v[116:117], v177 offset:24576\n" \
  "v_mfma_f32_16x16x32_bf16 v[44:47], v[160:163], v[148:151], v[44:47]\nds_read_b64_tr_b16 v[118:119], v177 offset:25344\n" \
  "v_mfma_f32_16x16x32_bf16 v[48:51], v[164:167], v[136:139], v[48:51]\nds_read_b64_tr_b16 v[120:121], v176 offset:24640\n" \
  "v_mfma_f32_16x16x32_bf16 v[52:55], v[164:167], v[140:143], v[52:55]\nds_read_b64_tr_b16 v[122:123], v176 offset:25408\n" \
  "v_mfma_f32_16x16x32_bf16 v[56:59], v[164:167], v[144:147], v[56:59]\nds_read_b64_tr_b16 v[124:125], v176 offset:36864\n" \
  "v_mfma_f32_16x16x32_bf16 v[60:63], v[164:167], v[148:151], v[60:63]\nds_read_b64_tr_b16 v[126:127], v176 offset:37632\n" \
  "v_mfma_f32_16x16x32_bf16 v[64:67], v[168:171], v[136:139], v[64:67]\nds_read_b64_tr_b16 v[128:129], v177 offset:36864\n" \
  "v_mfma_f32_16x16x32_bf16 v[68:71], v[168:171], v[140:143], v[68:71]\nds_read_b64_tr_b16 v[130:131], v177 offset:37632\n" \
  "v_mfma_f32_16x16x32_bf16 v[72:75], v[168:171], v[144:147], v[72:75]\nds_read_b64_tr_b16 v[132:133], v176 offset:36928\n" \
  "v_mfma_f32_16x16x32_bf16 v[76:79], v[168:171], v[148:151], v[76:79]\nds_read_b64_tr_b16 v[134:135], v176 offset:37696\n" \
  "v_mfma_f32_16x16x32_bf16 v[80:83], v[172:175], v[136:139], v[80:83]\nv_mfma_f32_16x16x32_bf16 v[84:87], v[172:175], v[140:143], v[84:87]\n" \
  "v_mfma_f32_16x16x32_bf16 v[88:91], v[172:175], v[144:147], v[88:91]\nv_mfma_f32_16x16x32_bf16 v[92:95], v[172:175], v[148:151], v[92:95]\n" \
  "s_waitcnt lgkmcnt(13)\nv_mfma_f32_16x16x32_bf16 v[0:3], v[112:115], v[96:99], v[0:3]\ns_waitcnt lgkmcnt(12)\n" \
  "v_mfma_f32_16x16x32_bf16 v[4:7], v[112:115], v[100:103], v[4:7]\ns_waitcnt lgkmcnt(11)\n" \
  "v_mfma_f32_16x16x32_bf16 v[8:11], v[112:115], v[104:107], v[8:11]\ns_waitcnt lgkmcnt(10)\n" \
  "v_mfma_f32_16x16x32_bf16 v[12:15], v[112:115], v[108:111], v[12:15]\ns_waitcnt lgkmcnt(8)\n" \
  "v_mfma_f32_16x16x32_bf16 v[16:19], v[116:119], v[96:99], v[16:19]\nds_read_b64_tr_b16 v[152:153], v176 offset:30720\n" \
  "v_mfma_f32_16x16x32_bf16 v[20:23], v[116:119], v[100:103], v[20:23]\nds_read_b64_tr_b16 v[154:155], v176 offset:31488\n" \
  "v_mfma_f32_16x16x32_bf16 v[24:27], v[116:119], v[104:107], v[24:27]\nds_read_b128 v[136:139], v178 offset:24576\n" \
  "v_mfma_f32_16x16x32_bf16 v[28:31], v[116:119], v[108:111], v[28:31]\nds_read_b128 v[140:143], v178 offset:24832\ns_waitcnt lgkmcnt(10)\n" \
  "v_mfma_f32_16x16x32_bf16 v[32:35], v[120:123], v[96:99], v[32:35]\nds_read_b128 v[144:147], v178 offset:25088\n" \
  "v_mfma_f32_16x16x32_bf16 v[36:39], v[120:123], v[100:103], v[36:39]\nds_read_b128 v[148:151], v178 offset:25344\n" \
  "v_mfma_f32_16x16x32_bf16 v[40:43], v[120:123], v[104:107], v[40:43]\nds_read_b64_tr_b16 v[156:157], v177 offset:30720\n" \
  "v_mfma_f32_16x16x32_bf16 v[44:47], v[120:123], v[108:111], v[44:47]\nds_read_b64_tr_b16 v[158:159], v177 offset:31488\ns_waitcnt lgkmcnt(12)\n" \
  "v_mfma_f32_16x16x32_bf16 v[48:51], v[124:127], v[96:99], v[48:51]\nds_read_b64_tr_b16 v[160:161], v176 offset:30784\n" \
  "v_mfma_f32_16x16x32_bf16 v[52:55], v[124:127], v[100:103], v[52:55]\nds_read_b64_tr_b16 v[162:163], v176 offset:31552\n" \
  "v_mfma_f32_16x16x32_bf16 v[56:59], v[124:127], v[104:107], v[56:59]\nds_read_b64_tr_b16 v[164:165], v176 offset:43008\n" \
  "v_mfma_f32_16x16x32_bf16 v[60:63], v[124:127], v[108:111], v[60:63]\nds_read_b64_tr_b16 v[166:167], v176 offset:43776\ns_waitcnt lgkmcnt(14)\n" \
  "v_mfma_f32_16x16x32_bf16 v[64:67], v[128:131], v[96:99], v[64:67]\nds_read_b64_tr_b16 v[168:169], v177 offset:43008\n" \
  "v_mfma_f32_16x16x32_bf16 v[68:71], v[128:131], v[100:103], v[68:71]\nds_read_b64_tr_b16 v[170:171], v177 offset:43776\n" \
  "v_mfma_f32_16x16x32_bf16 v[72:75], v[128:131], v[104:107], v[72:75]\nds_read_b64_tr_b16 v[172:173], v176 offset:43072\n" \
  "v_mfma_f32_16x16x32_bf16 v[76:79], v[128:131], v[108:111], v[76:79]\nds_read_b64_tr_b16 v[174:175], v176 offset:43840\ns_waitcnt lgkmcnt(15)\n" \
  "v_mfma_f32_16x16x32_bf16 v[80:83], v[132:135], v[96:99], v[80:83]\nv_xor_b32 v178, 0x8000, v178\n" \
  "v_mfma_f32_16x16x32_bf16 v[84:87], v[132:135], v[100:103], v[84:87]\nv_add_u32 v176, %[db], v176\n" \
  "v_mfma_f32_16x16x32_bf16 v[88:91], v[132:135], v[104:107], v[88:91]\nv_add_u32 v177, %[db], v177\n" \
  "v_mfma_f32_16x16x32_bf16 v[92:95], v[132:135], v[108:111], v[92:95]\ns_sub_u32 %[db], 0, %[db]\ns_waitcnt lgkmcnt(13)\n" \
  "v_mfma_f32_16x16x32_bf16 v[0:3], v[152:155], v[136:139], v[0:3]\ns_waitcnt lgkmcnt(12)\n" \
  "v_mfma_f32_16x16x32_bf16 v[4:7], v[152:155], v[140:143], v[4:7]\ns_waitcnt lgkmcnt(11)\n" \
  "v_mfma_f32_16x16x32_bf16 v[8:11], v[152:155], v[144:147], v[8:11]\ns_waitcnt lgkmcnt(10)\n" \
  "v_mfma_f32_16x16x32_bf16 v[12:15], v[152:155], v[148:151], v[12:15]\ns_waitcnt lgkmcnt(0)\ns_barrier\n" \
  "v_mfma_f32_16x16x32_bf16 v[16:19], v[156:159], v[136:139], v[16:19]\nds_read_b64_tr_b16 v[112:113], v176 offset:0\n" \
  "v_mfma_f32_16x16x32_bf16 v[20:23], v[156:159], v[140:143], v[20:23]\nds_read_b64_tr_b16 v[114:115], v176 offset:768\n" \
  "v_mfma_f32_16x16x32_bf16 v[24:27], v[156:159], v[144:147], v[24:27]\nds_read_b128 v[96:99], v178 offset:0\n" \
  "v_mfma_f32_16x16x32_bf16 v[28:31], v[156:159], v[148:151], v[28:31]\nds_read_b128 v[100:103], v178 offset:256\n" \
  "v_mfma_f32_16x16x32_bf16 v[32:35], v[160:163], v[136:139], v[32:35]\nds_read_b128 v[104:107], v178 offset:512\n" \
  "v_mfma_f32_16x16x32_bf16 v[36:39], v[160:163], v[140:143], v[36:39]\nds_read_b128 v[108:111], v178 offset:768\n" \
  "v_mfma_f32_16x16x32_bf16 v[40:43], v[160:163], v[144:147], v[40:43]\nds_read_b64_tr_b16 v[116:117], v177 offset:0\n" \
  "v_mfma_f32_16x16x32_bf16 v[44:47], v[160:163], v[148:151], v[44:47]\nds_read_b64_tr_b16 v[118:119], v177 offset:768\n" \
  "v_mfma_f32_16x16x32_bf16 v[48:51], v[164:167], v[136:139], v[48:51]\nds_read_b64_tr_b16 v[120:121], v176 offset:64\n" \
  "v_mfma_f32_16x16x32_bf16 v[52:55], v[164:167], v[140:143], v[52:55]\nds_read_b64_tr_b16 v[122:123], v176 offset:832\n" \
  "v_mfma_f32_16x16x32_bf16 v[56:59], v[164:167], v[144:147], v[56:59]\nds_read_b64_tr_b16 v[124:125], v176 offset:12288\n" \
  "v_mfma_f32_16x16x32_bf16 v[60:63], v[164:167], v[148:151], v[60:63]\nds_read_b64_tr_b16 v[126:127], v176 offset:13056\n" \
  "v_mfma_f32_16x16x32_bf16 v[64:67], v[168:171], v[136:139], v[64:67]\nds_read_b64_tr_b16 v[128:129], v177 offset:12288\n" \
  "v_mfma_f32_16x16x32_bf16 v[68:71], v[168:171], v[140:143], v[68:71]\nds_read_b64_tr_b16 v[130:131], v177 offset:13056\n" \
  "v_mfma_f32_16x16x32_bf16 v[72:75], v[168:171], v[144:147], v[72:75]\nds_read_b64_tr_b16 v[132:133], v176 offset:12352\n" \
  "v_mfma_f32_16x16x32_bf16 v[76:79], v[168:171], v[148:151], v[76:79]\nds_read_b64_tr_b16 v[134:135], v176 offset:13120\n" \
  "v_mfma_f32_16x16x32_bf16 v[80:83], v[172:175], v[136:139], v[80:83]\nv_mfma_f32_16x16x32_bf16 v[84:87], v[172:175], v[140:143], v[84:87]\n" \
  "v_mfma_f32_16x16x32_bf16 v[88:91], v[172:175], v[144:147], v[88:91]\nv_mfma_f32_16x16x32_bf16 v[92:95], v[172:175], v[148:151], v[92:95]\n" \
  "s_add_u32 %[cnt], %[cnt], -1\ns_cbranch_scc1 .Lk4_%=\ns_waitcnt lgkmcnt(13)\nv_mfma_f32_16x16x32_bf16 v[0:3], v[112:115], v[96:99], v[0:3]\n" \
  "s_waitcnt lgkmcnt(12)\nv_mfma_f32_16x16x32_bf16 v[4:7], v[112:115], v[100:103], v[4:7]\ns_waitcnt lgkmcnt(11)\n" \
  "v_mfma_f32_16x16x32_bf16 v[8:11], v[112:115], v[104:107], v[8:11]\ns_waitcnt lgkmcnt(10)\n" \
  "v_mfma_f32_16x16x32_bf16 v[12:15], v[112:115], v[108:111], v[12:15]\ns_waitcnt lgkmcnt(8)\n" \
  "v_mfma_f32_16x16x32_bf16 v[16:19], v[116:119], v[96:99], v[16:19]\nds_read_b64_tr_b16 v[152:153], v176 offset:6144\n" \
  "v_mfma_f32_16x16x32_bf16 v[20:23], v[116:119], v[100:103], v[20:23]\nds_read_b64_tr_b16 v[154:155], v176 offset:6912\n" \
  "v_mfma_f32_16x16x32_bf16 v[24:27], v[116:119], v[104:107], v[24:27]\nds_read_b128 v[136:139], v178 offset:8192\n" \
  "v_mfma_f32_16x16x32_bf16 v[28:31], v[116:119], v[108:111], v[28:31]\nds_read_b128 v[140:143], v178 offset:8448\ns_waitcnt lgkmcnt(10)\n" \
  "v_mfma_f32_16x16x32_bf16 v[32:35], v[120:123], v[96:99], v[32:35]\nds_read_b128 v[144:147], v178 offset:8704\n" \
  "v_mfma_f32_16x16x32_bf16 v[36:39], v[120:123], v[100:103], v[36:39]\nds_read_b128 v[148:151], v178 offset:8960\n" \
  "v_mfma_f32_16x16x32_bf16 v[40:43], v[120:123], v[104:107], v[40:43]\nds_read_b64_tr_b16 v[156:157], v177 offset:6144\n" \
  "v_mfma_f32_16x16x32_bf16 v[44:47], v[120:123], v[108:111], v[44:47]\nds_read_b64_tr_b16 v[158:159], v177 offset:6912\ns_waitcnt lgkmcnt(12)\n" \
  "v_mfma_f32_16x16x32_bf16 v[48:51], v[124:127], v[96:99], v[48:51]\nds_read_b64_tr_b16 v[160:161], v176 offset:6208\n" \
  "v_mfma_f32_16x16x32_bf16 v[52:55], v[124:127], v[100:103], v[52:55]\nds_read_b64_tr_b16 v[162:163], v176 offset:6976\n" \
  "v_mfma_f32_16x16x32_bf16 v[56:59], v[124:127], v[104:107], v[56:59]\nds_read_b64_tr_b16 v[164:165], v176 offset:18432\n" \
  "v_mfma_f32_16x16x32_bf16 v[60:63], v[124:127], v[108:111], v[60:63]\nds_read_b64_tr_b16 v[166:167], v176 offset:19200\ns_waitcnt lgkmcnt(14)\n" \
  "v_mfma_f32_16x16x32_bf16 v[64:67], v[128:131], v[96:99], v[64:67]\nds_read_b64_tr_b16 v[168:169], v177 offset:18432\n" \
  "v_mfma_f32_16x16x32_bf16 v[68:71], v[128:131], v[100:103], v[68:71]\nds_read_b64_tr_b16 v[170:171], v177 offset:19200\n" \
  "v_mfma_f32_16x16x32_bf16 v[72:75], v[128:131], v[104:107], v[72:75]\nds_read_b64_tr_b16 v[172:173], v176 offset:18496\n" \
  "v_mfma_f32_16x16x32_bf16 v[76:79], v[128:131], v[108:111], v[76:79]\nds_read_b64_tr_b16 v[174:175], v176 offset:19264\ns_waitcnt lgkmcnt(15)\n" \
  "v_mfma_f32_16x16x32_bf16 v[80:83], v[132:135], v[96:99], v[80:83]\nv_mfma_f32_16x16x32_bf16 v[84:87], v[132:135], v[100:103], v[84:87]\n" \
  "v_mfma_f32_16x16x32_bf16 v[88:91], v[132:135], v[104:107], v[88:91]\nv_mfma_f32_16x16x32_bf16 v[92:95], v[132:135], v[108:111], v[92:95]\n" \
  "s_waitcnt lgkmcnt(13)\nv_mfma_f32_16x16x32_bf16 v[0:3], v[152:155], v[136:139], v[0:3]\ns_waitcnt lgkmcnt(12)\n" \
  "v_mfma_f32_16x16x32_bf16 v[4:7], v[152:155], v[140:143], v[4:7]\ns_waitcnt lgkmcnt(11)\n" \
  "v_mfma_f32_16x16x32_bf16 v[8:11], v[152:155], v[144:147], v[8:11]\ns_waitcnt lgkmcnt(10)\n" \
  "v_mfma_f32_16x16x32_bf16 v[12:15], v[152:155], v[148:151], v[12:15]\ns_waitcnt lgkmcnt(0)\ns_barrier\n" \
  "v_mfma_f32_16x16x32_bf16 v[16:19], v[156:159], v[136:139], v[16:19]\nds_read_b96 v[182:184], v179 offset:0\n" \
  "ds_read_b96 v[186:188], v179 offset:256\nv_mfma_f32_16x16x32_bf16 v[20:23], v[156:159], v[140:143], v[20:23]\n" \
  "ds_read_b96 v[190:192], v179 offset:512\nds_read_b96 v[194:196], v179 offset:768\n" \
  "v_mfma_f32_16x16x32_bf16 v[24:27], v[156:159], v[144:147], v[24:27]\nds_read_b32 v198, v200 offset:0\nds_read_b32 v199, v200 offset:448\n" \
  "v_mfma_f32_16x16x32_bf16 v[28:31], v[156:159], v[148:151], v[28:31]\nds_read_b64_tr_b16 v[112:113], v176 offset:24576\n" \
  "v_mfma_f32_16x16x32_bf16 v[32:35], v[160:163], v[136:139], v[32:35]\nds_read_b64_tr_b16 v[114:115], v176 offset:25344\n" \
  "v_mfma_f32_16x16x32_bf16 v[36:39], v[160:163], v[140:143], v[36:39]\nds_read_b128 v[96:99], v178 offset:16384\n" \
  "v_mfma_f32_16x16x32_bf16 v[40:43], v[160:163], v[144:147], v[40:43]\nds_read_b128 v[100:103], v178 offset:16640\n" \
  "v_mfma_f32_16x16x32_bf16 v[44:47], v[160:163], v[148:151], v[44:47]\nds_read_b128 v[104:107], v178 offset:16896\ns_waitcnt lgkmcnt(5)\n" \
  "v_mfma_f32_16x16x32_bf16 v[48:51], v[164:167], v[136:139], v[48:51]\nds_read_b128 v[108:111], v178 offset:17152\n" \
  "v_fmac_f32_dpp v0, v198, v184 row_newbcast:0 row_mask:0xf bank_mask:0xf\nv_mfma_f32_16x16x32_bf16 v[52:55], v[164:167], v[140:143], v[52:55]\n" \
  "ds_read_b64_tr_b16 v[116:117], v177 offset:24576\nv_fmac_f32_dpp v1, v198, v184 row_newbcast:1 row_mask:0xf bank_mask:0xf\n" \
  "v_mfma_f32_16x16x32_bf16 v[56:59], v[164:167], v[144:147], v[56:59]\nds_read_b64_tr_b16 v[118:119], v177 offset:25344\n" \
  "v_fmac_f32_dpp v2, v198, v184 row_newbcast:2 row_mask:0xf bank_mask:0xf\nv_mfma_f32_16x16x32_bf16 v[60:63], v[164:167], v[148:151], v[60:63]\n" \
  "ds_read_b64_tr_b16 v[120:121], v176 offset:24640\nv_fmac_f32_dpp v3, v198, v184 row_newbcast:3 row_mask:0xf bank_mask:0xf\n" \
  "v_mfma_f32_16x16x32_bf16 v[64:67], v[168:171], v[136:139], v[64:67]\nds_read_b64_tr_b16 v[122:123], v176 offset:25408\n" \
  "v_fmac_f32_dpp v4, v198, v188 row_newbcast:0 row_mask:0xf bank_mask:0xf\nv_mfma_f32_16x16x32_bf16 v[68:71], v[168:171], v[140:143], v[68:71]\n" \
  "ds_read_b64_tr_b16 v[124:125], v176 offset:36864\nv_fmac_f32_dpp v5, v198, v188 row_newbcast:1 row_mask:0xf bank_mask:0xf\n" \
  "v_mfma_f32_16x16x32_bf16 v[72:75], v[168:171], v[144:147], v[72:75]\nds_read_b64_tr_b16 v[126:127], v176 offset:37632\n" \
  "v_fmac_f32_dpp v6, v198, v188 row_newbcast:2 row_mask:0xf bank_mask:0xf\nv_mfma_f32_16x16x32_bf16 v[76:79], v[168:171], v[148:151], v[76:79]\n" \
  "ds_read_b64_tr_b16 v[128:129], v177 offset:36864\nv_fmac_f32_dpp v7, v198, v188 row_newbcast:3 row_mask:0xf bank_mask:0xf\n" \
  "v_mfma_f32_16x16x32_bf16 v[80:83], v[172:175], v[136:139], v[80:83]\nds_read_b64_tr_b16 v[130:131], v177 offset:37632\n" \
  "v_fmac_f32_dpp v48, v198, v184 row_newbcast:12 row_mask:0xf bank_mask:0xf\nv_mfma_f32_16x16x32_bf16 v[84:87], v[172:175], v[140:143], v[84:87]\n" \
  "ds_read_b64_tr_b16 v[132:133], v176 offset:36928\nv_fmac_f32_dpp v49, v198, v184 row_newbcast:13 row_mask:0xf bank_mask:0xf\n" \
  "v_mfma_f32_16x16x32_bf16 v[88:91], v[172:175], v[144:147], v[88:91]\nds_read_b64_tr_b16 v[134:135], v176 offset:37696\n" \
  "v_fmac_f32_dpp v50, v198, v184 row_newbcast:14 row_mask:0xf bank_mask:0xf\nv_mfma_f32_16x16x32_bf16 v[92:95], v[172:175], v[148:151], v[92:95]\n" \
  "v_fmac_f32_dpp v51, v198, v184 row_newbcast:15 row_mask:0xf bank_mask:0xf\n" \
  "v_fmac_f32_dpp v52, v198, v188 row_newbcast:12 row_mask:0xf bank_mask:0xf\ns_waitcnt lgkmcnt(13)\n" \
  "v_mfma_f32_16x16x32_bf16 v[0:3], v[112:115], v[96:99], v[0:3]\nv_fmac_f32_dpp v53, v198, v188 row_newbcast:13 row_mask:0xf bank_mask:0xf\n" \
  "v_fmac_f32_dpp v54, v198, v188 row_newbcast:14 row_mask:0xf bank_mask:0xf\ns_waitcnt lgkmcnt(12)\n" \
  "v_mfma_f32_16x16x32_bf16 v[4:7], v[112:115], v[100:103], v[4:7]\nv_fmac_f32_dpp v55, v198, v188 row_newbcast:15 row_mask:0xf bank_mask:0xf\n" \
  "v_fmac_f32_dpp v56, v198, v192 row_newbcast:12 row_mask:0xf bank_mask:0xf\ns_waitcnt lgkmcnt(11)\n" \
  "v_mfma_f32_16x16x32_bf16 v[8:11], v[112:115], v[104:107], v[8:11]\nv_fmac_f32_dpp v57, v198, v192 row_newbcast:13 row_mask:0xf bank_mask:0xf\n" \
  "v_fmac_f32_dpp v58, v198, v192 row_newbcast:14 row_mask:0xf bank_mask:0xf\ns_waitcnt lgkmcnt(10)\n" \
  "v_mfma_f32_16x16x32_bf16 v[12:15], v[112:115], v[108:111], v[12:15]\nv_fmac_f32_dpp v59, v198, v192 row_newbcast:15 row_mask:0xf bank_mask:0xf\n" \
  "v_fmac_f32_dpp v60, v198, v196 row_newbcast:12 row_mask:0xf bank_mask:0xf\ns_waitcnt lgkmcnt(8)\n" \
  "v_mfma_f32_16x16x32_bf16 v[16:19], v[116:119], v[96:99], v[16:19]\nds_read_b64_tr_b16 v[152:153], v176 offset:30720\n" \
  "v_fmac_f32_dpp v61, v198, v196 row_newbcast:13 row_mask:0xf bank_mask:0xf\nv_mfma_f32_16x16x32_bf16 v[20:23], v[116:119], v[100:103], v[20:23]\n" \
  "ds_read_b64_tr_b16 v[154:155], v176 offset:31488\nv_fmac_f32_dpp v62, v198, v196 row_newbcast:14 row_mask:0xf bank_mask:0xf\n" \
  "v_mfma_f32_16x16x32_bf16 v[24:27], v[116:119], v[104:107], v[24:27]\nds_read_b128 v[136:139], v178 offset:24576\n" \
  "v_fmac_f32_dpp v63, v198, v196 row_newbcast:15 row_mask:0xf bank_mask:0xf\nv_mfma_f32_16x16x32_bf16 v[28:31], v[116:119], v[108:111], v[28:31]\n" \
  "ds_read_b128 v[140:143], v178 offset:24832\nv_fmac_f32_dpp v8, v198, v192 row_newbcast:0 row_mask:0xf bank_mask:0xf\ns_waitcnt lgkmcnt(10)\n" \
  "v_mfma_f32_16x16x32_bf16 v[32:35], v[120:123], v[96:99], v[32:35]\nds_read_b128 v[144:147], v178 offset:25088\n" \
  "v_fmac_f32_dpp v9, v198, v192 row_newbcast:1 row_mask:0xf bank_mask:0xf\nv_mfma_f32_16x16x32_bf16 v[36:39], v[120:123], v[100:103], v[36:39]\n" \
  "ds_read_b128 v[148:151], v178 offset:25344\nv_fmac_f32_dpp v10, v198, v192 row_newbcast:2 row_mask:0xf bank_mask:0xf\n" \
  "v_mfma_f32_16x16x32_bf16 v[40:43], v[120:123], v[104:107], v[40:43]\nds_read_b64_tr_b16 v[156:157], v177 offset:30720\n" \
  "v_fmac_f32_dpp v11, v198, v192 row_newbcast:3 row_mask:0xf bank_mask:0xf\nv_mfma_f32_16x16x32_bf16 v[44:47], v[120:123], v[108:111], v[44:47]\n" \
  "ds_read_b64_tr_b16 v[158:159], v177 offset:31488\nv_fmac_f32_dpp v12, v198, v196 row_newbcast:0 row_mask:0xf bank_mask:0xf\ns_waitcnt lgkmcnt(12)\n" \
  "v_mfma_f32_16x16x32_bf16 v[48:51], v[124:127], v[96:99], v[48:51]\nds_read_b64_tr_b16 v[160:161], v176 offset:30784\n" \
  "v_fmac_f32_dpp v13, v198, v196 row_newbcast:1 row_mask:0xf bank_mask:0xf\nv_mfma_f32_16x16x32_bf16 v[52:55], v[124:127], v[100:103], v[52:55]\n" \
  "ds_read_b64_tr_b16 v[162:163], v176 offset:31552\nv_fmac_f32_dpp v14, v198, v196 row_newbcast:2 row_mask:0xf bank_mask:0xf\n" \
  "v_mfma_f32_16x16x32_bf16 v[56:59], v[124:127], v[104:107], v[56:59]\nds_read_b64_tr_b16 v[164:165], v176 offset:43008\n" \
  "v_fmac_f32_dpp v15, v198, v196 row_newbcast:3 row_mask:0xf bank_mask:0xf\nv_mfma_f32_16x16x32_bf16 v[60:63], v[124:127], v[108:111], v[60:63]\n" \
  "ds_read_b64_tr_b16 v[166:167], v176 offset:43776\nv_fmac_f32_dpp v16, v198, v184 row_newbcast:4 row_mask:0xf bank_mask:0xf\ns_waitcnt lgkmcnt(14)\n" \
  "v_mfma_f32_16x16x32_bf16 v[64:67], v[128:131], v[96:99], v[64:67]\nds_read_b64_tr_b16 v[168:169], v177 offset:43008\n" \
  "v_fmac_f32_dpp v17, v198, v184 row_newbcast:5 row_mask:0xf bank_mask:0xf\nv_mfma_f32_16x16x32_bf16 v[68:71], v[128:131], v[100:103], v[68:71]\n" \
  "ds_read_b64_tr_b16 v[170:171], v177 offset:43776\nv_fmac_f32_dpp v18, v198, v184 row_newbcast:6 row_mask:0xf bank_mask:0xf\n" \
  "v_mfma_f32_16x16x32_bf16 v[72:75], v[128:131], v[104:107], v[72:75]\nds_read_b64_tr_b16 v[172:173], v176 offset:43072\n" \
  "v_fmac_f32_dpp v19, v198, v184 row_newbcast:7 row_mask:0xf bank_mask:0xf\nv_mfma_f32_16x16x32_bf16 v[76:79], v[128:131], v[108:111], v[76:79]\n" \
  "ds_read_b64_tr_b16 v[174:175], v176 offset:43840\nv_fmac_f32_dpp v20, v198, v188 row_newbcast:4 row_mask:0xf bank_mask:0xf\ns_waitcnt lgkmcnt(15)\n" \
  "v_mfma_f32_16x16x32_bf16 v[80:83], v[132:135], v[96:99], v[80:83]\nv_fmac_f32_dpp v21, v198, v188 row_newbcast:5 row_mask:0xf bank_mask:0xf\n" \
  "v_fmac_f32_dpp v22, v198, v188 row_newbcast:6 row_mask:0xf bank_mask:0xf\nv_mfma_f32_16x16x32_bf16 v[84:87], v[132:135], v[100:103], v[84:87]\n" \
  "v_fmac_f32_dpp v23, v198, v188 row_newbcast:7 row_mask:0xf bank_mask:0xf\n" \
  "v_fmac_f32_dpp v64, v199, v184 row_newbcast:0 row_mask:0xf bank_mask:0xf\nv_mfma_f32_16x16x32_bf16 v[88:91], v[132:135], v[104:107], v[88:91]\n" \
  "v_fmac_f32_dpp v65, v199, v184 row_newbcast:1 row_mask:0xf bank_mask:0xf\n" \
  "v_fmac_f32_dpp v66, v199, v184 row_newbcast:2 row_mask:0xf bank_mask:0xf\nv_mfma_f32_16x16x32_bf16 v[92:95], v[132:135], v[108:111], v[92:95]\n" \
  "v_fmac_f32_dpp v67, v199, v184 row_newbcast:3 row_mask:0xf bank_mask:0xf\n" \
  "v_fmac_f32_dpp v68, v199, v188 row_newbcast:0 row_mask:0xf bank_mask:0xf\ns_waitcnt lgkmcnt(0)\n" \
  "v_mfma_f32_16x16x32_bf16 v[0:3], v[152:155], v[136:139], v[0:3]\nv_fmac_f32_dpp v69, v199, v188 row_newbcast:1 row_mask:0xf bank_mask:0xf\n" \
  "v_fmac_f32_dpp v70, v199, v188 row_newbcast:2 row_mask:0xf bank_mask:0xf\nv_mfma_f32_16x16x32_bf16 v[4:7], v[152:155], v[140:143], v[4:7]\n" \
  "v_fmac_f32_dpp v71, v199, v188 row_newbcast:3 row_mask:0xf bank_mask:0xf\n" \
  "v_fmac_f32_dpp v24, v198, v192 row_newbcast:4 row_mask:0xf bank_mask:0xf\nv_mfma_f32_16x16x32_bf16 v[48:51], v[164:167], v[136:139], v[48:51]\n" \
  "v_fmac_f32_dpp v25, v198, v192 row_newbcast:5 row_mask:0xf bank_mask:0xf\n" \
  "v_fmac_f32_dpp v26, v198, v192 row_newbcast:6 row_mask:0xf bank_mask:0xf\nv_mfma_f32_16x16x32_bf16 v[52:55], v[164:167], v[140:143], v[52:55]\n" \
  "v_fmac_f32_dpp v27, v198, v192 row_newbcast:7 row_mask:0xf bank_mask:0xf\n" \
  "v_fmac_f32_dpp v28, v198, v196 row_newbcast:4 row_mask:0xf bank_mask:0xf\nv_mfma_f32_16x16x32_bf16 v[8:11], v[152:155], v[144:147], v[8:11]\n" \
  "v_fmac_f32_dpp v29, v198, v196 row_newbcast:5 row_mask:0xf bank_mask:0xf\n" \
  "v_fmac_f32_dpp v30, v198, v196 row_newbcast:6 row_mask:0xf bank_mask:0xf\nv_mfma_f32_16x16x32_bf16 v[12:15], v[152:155], v[148:151], v[12:15]\n" \
  "v_fmac_f32_dpp v31, v198, v196 row_newbcast:7 row_mask:0xf bank_mask:0xf\n" \
  "v_fmac_f32_dpp v72, v199, v192 row_newbcast:0 row_mask:0xf bank_mask:0xf\nv_mfma_f32_16x16x32_bf16 v[56:59], v[164:167], v[144:147], v[56:59]\n" \
  "v_fmac_f32_dpp v73, v199, v192 row_newbcast:1 row_mask:0xf bank_mask:0xf\n" \
  "v_fmac_f32_dpp v74, v199, v192 row_newbcast:2 row_mask:0xf bank_mask:0xf\nv_mfma_f32_16x16x32_bf16 v[60:63], v[164:167], v[148:151], v[60:63]\n" \
  "v_fmac_f32_dpp v75, v199, v192 row_newbcast:3 row_mask:0xf bank_mask:0xf\n" \
  "v_fmac_f32_dpp v76, v199, v196 row_newbcast:0 row_mask:0xf bank_mask:0xf\nv_mfma_f32_16x16x32_bf16 v[16:19], v[156:159], v[136:139], v[16:19]\n" \
  "v_fmac_f32_dpp v77, v199, v196 row_newbcast:1 row_mask:0xf bank_mask:0xf\n" \
  "v_fmac_f32_dpp v78, v199, v196 row_newbcast:2 row_mask:0xf bank_mask:0xf\nv_mfma_f32_16x16x32_bf16 v[20:23], v[156:159], v[140:143], v[20:23]\n" \
  "v_fmac_f32_dpp v79, v199, v196 row_newbcast:3 row_mask:0xf bank_mask:0xf\n" \
  "v_fmac_f32_dpp v32, v198, v184 row_newbcast:8 row_mask:0xf bank_mask:0xf\nv_mfma_f32_16x16x32_bf16 v[64:67], v[168:171], v[136:139], v[64:67]\n" \
  "v_fmac_f32_dpp v33, v198, v184 row_newbcast:9 row_mask:0xf bank_mask:0xf\n" \
  "v_fmac_f32_dpp v34, v198, v184 row_newbcast:10 row_mask:0xf bank_mask:0xf\nv_mfma_f32_16x16x32_bf16 v[68:71], v[168:171], v[140:143], v[68:71]\n" \
  "v_fmac_f32_dpp v35, v198, v184 row_newbcast:11 row_mask:0xf bank_mask:0xf\n" \
  "v_fmac_f32_dpp v36, v198, v188 row_newbcast:8 row_mask:0xf bank_mask:0xf\nv_mfma_f32_16x16x32_bf16 v[24:27], v[156:159], v[144:147], v[24:27]\n" \
  "v_fmac_f32_dpp v37, v198, v188 row_newbcast:9 row_mask:0xf bank_mask:0xf\n" \
  "v_fmac_f32_dpp v38, v198, v188 row_newbcast:10 row_mask:0xf bank_mask:0xf\nv_mfma_f32_16x16x32_bf16 v[28:31], v[156:159], v[148:151], v[28:31]\n" \
  "v_fmac_f32_dpp v39, v198, v188 row_newbcast:11 row_mask:0xf bank_mask:0xf\n" \
  "v_fmac_f32_dpp v80, v199, v184 row_newbcast:4 row_mask:0xf bank_mask:0xf\nv_mfma_f32_16x16x32_bf16 v[72:75], v[168:171], v[144:147], v[72:75]\n" \
  "v_fmac_f32_dpp v81, v199, v184 row_newbcast:5 row_mask:0xf bank_mask:0xf\n" \
  "v_fmac_f32_dpp v82, v199, v184 row_newbcast:6 row_mask:0xf bank_mask:0xf\nv_mfma_f32_16x16x32_bf16 v[76:79], v[168:171], v[148:151], v[76:79]\n" \
  "v_fmac_f32_dpp v83, v199, v184 row_newbcast:7 row_mask:0xf bank_mask:0xf\n" \
  "v_fmac_f32_dpp v84, v199, v188 row_newbcast:4 row_mask:0xf bank_mask:0xf\nv_mfma_f32_16x16x32_bf16 v[32:35], v[160:163], v[136:139], v[32:35]\n" \
  "v_fmac_f32_dpp v85, v199, v188 row_newbcast:5 row_mask:0xf bank_mask:0xf\n" \
  "v_fmac_f32_dpp v86, v199, v188 row_newbcast:6 row_mask:0xf bank_mask:0xf\nv_mfma_f32_16x16x32_bf16 v[36:39], v[160:163], v[140:143], v[36:39]\n" \
  "v_fmac_f32_dpp v40, v198, v192 row_newbcast:8 row_mask:0xf bank_mask:0xf\n" \
  "v_fmac_f32_dpp v41, v198, v192 row_newbcast:9 row_mask:0xf bank_mask:0xf\nv_mfma_f32_16x16x32_bf16 v[80:83], v[172:175], v[136:139], v[80:83]\n" \
  "v_fmac_f32_dpp v44, v198, v196 row_newbcast:8 row_mask:0xf bank_mask:0xf\n" \
  "v_fmac_f32_dpp v45, v198, v196 row_newbcast:9 row_mask:0xf bank_mask:0xf\nv_mfma_f32_16x16x32_bf16 v[84:87], v[172:175], v[140:143], v[84:87]\n" \
  "v_fmac_f32_dpp v88, v199, v192 row_newbcast:4 row_mask:0xf bank_mask:0xf\n" \
  "v_fmac_f32_dpp v89, v199, v192 row_newbcast:5 row_mask:0xf bank_mask:0xf\nv_mfma_f32_16x16x32_bf16 v[40:43], v[160:163], v[144:147], v[40:43]\n" \
  "v_fmac_f32_dpp v92, v199, v196 row_newbcast:4 row_mask:0xf bank_mask:0xf\n" \
  "v_fmac_f32_dpp v93, v199, v196 row_newbcast:5 row_mask:0xf bank_mask:0xf\nv_mfma_f32_16x16x32_bf16 v[44:47], v[160:163], v[148:151], v[44:47]\n" \
  "v_mfma_f32_16x16x32_bf16 v[88:91], v[172:175], v[144:147], v[88:91]\nv_mfma_f32_16x16x32_bf16 v[92:95], v[172:175], v[148:151], v[92:95]\n" \
  "v_pk_mul_f32 v[0:1], v[0:1], v[182:183] op_sel_hi:[1,0]\nv_pk_mul_f32 v[2:3], v[2:3], v[182:183] op_sel_hi:[1,0]\n" \
  "v_pk_mul_f32 v[4:5], v[4:5], v[186:187] op_sel_hi:[1,0]\nv_pk_mul_f32 v[6:7], v[6:7], v[186:187] op_sel_hi:[1,0]\n" \
  "v_pk_mul_f32 v[96:97], v[0:1], v[0:1]\nv_pk_mul_f32 v[98:99], v[2:3], v[2:3]\nv_pk_mul_f32 v[100:101], v[4:5], v[4:5]\n" \
  "v_pk_mul_f32 v[102:103], v[6:7], v[6:7]\nv_fmac_f32_dpp v87, v199, v188 row_newbcast:7 row_mask:0xf bank_mask:0xf\n" \
  "v_fmac_f32_dpp v42, v198, v192 row_newbcast:10 row_mask:0xf bank_mask:0xf\n" \
  "v_fmac_f32_dpp v43, v198, v192 row_newbcast:11 row_mask:0xf bank_mask:0xf\n" \
  "v_fmac_f32_dpp v46, v198, v196 row_newbcast:10 row_mask:0xf bank_mask:0xf\n" \
  "v_fmac_f32_dpp v47, v198, v196 row_newbcast:11 row_mask:0xf bank_mask:0xf\n" \
  "v_fmac_f32_dpp v90, v199, v192 row_newbcast:6 row_mask:0xf bank_mask:0xf\n" \
  "v_fmac_f32_dpp v91, v199, v192 row_newbcast:7 row_mask:0xf bank_mask:0xf\n" \
  "v_fmac_f32_dpp v94, v199, v196 row_newbcast:6 row_mask:0xf bank_mask:0xf\n" \
  "v_fmac_f32_dpp v95, v199, v196 row_newbcast:7 row_mask:0xf bank_mask:0xf\n" \
  "v_pk_fma_f32 v[96:97], v[96:97], v[234:235], v[234:235] op_sel:[0,1,0] op_sel_hi:[1,1,0]\n" \
  "v_pk_fma_f32 v[98:99], v[98:99], v[234:235], v[234:235] op_sel:[0,1,0] op_sel_hi:[1,1,0]\n" \
  "v_pk_fma_f32 v[100:101], v[100:101], v[234:235], v[234:235] op_sel:[0,1,0] op_sel_hi:[1,1,0]\n" \
  "v_pk_fma_f32 v[102:103], v[102:103], v[234:235], v[234:235] op_sel:[0,1,0] op_sel_hi:[1,1,0]\nv_pk_mul_f32 v[96:97], v[96:97], v[0:1]\n" \
  "v_pk_mul_f32 v[98:99], v[98:99], v[2:3]\nv_pk_mul_f32 v[100:101], v[100:101], v[4:5]\nv_pk_mul_f32 v[102:103], v[102:103], v[6:7]\n" \
  "v_exp_f32 v96, v96\nv_exp_f32 v97, v97\nv_exp_f32 v98, v98\nv_exp_f32 v99, v99\nv_exp_f32 v100, v100\nv_exp_f32 v101, v101\nv_exp_f32 v102, v102\n" \
  "v_exp_f32 v103, v103\nv_pk_mul_f32 v[48:49], v[48:49], v[0:1]\nv_pk_mul_f32 v[50:51], v[50:51], v[2:3]\nv_pk_mul_f32 v[52:53], v[52:53], v[4:5]\n" \
  "v_pk_mul_f32 v[54:55], v[54:55], v[6:7]\nv_pk_fma_f32 v[96:97], v[96:97], v[182:183], v[182:183] op_sel:[0,1,1]\n" \
  "v_pk_fma_f32 v[98:99], v[98:99], v[182:183], v[182:183] op_sel:[0,1,1]\n" \
  "v_pk_fma_f32 v[100:101], v[100:101], v[186:187], v[186:187] op_sel:[0,1,1]\n" \
  "v_pk_fma_f32 v[102:103], v[102:103], v[186:187], v[186:187] op_sel:[0,1,1]\nv_rcp_f32 v96, v96\nv_rcp_f32 v97, v97\nv_rcp_f32 v98, v98\n" \
  "v_rcp_f32 v99, v99\nv_rcp_f32 v100, v100\nv_rcp_f32 v101, v101\nv_rcp_f32 v102, v102\nv_rcp_f32 v103, v103\n" \
  "v_pk_mul_f32 v[48:49], v[48:49], v[96:97]\nv_pk_mul_f32 v[50:51], v[50:51], v[98:99]\nv_pk_mul_f32 v[52:53], v[52:53], v[100:101]\n" \
  "v_pk_mul_f32 v[54:55], v[54:55], v[102:103]\nv_cvt_pk_bf16_f32 v104, v48, v49\nv_cvt_pk_bf16_f32 v105, v50, v51\nv_cvt_pk_bf16_f32 v106, v52, v53\n" \
  "v_cvt_pk_bf16_f32 v107, v54, v55\nv_pk_mul_f32 v[8:9], v[8:9], v[190:191] op_sel_hi:[1,0]\n" \
  "v_pk_mul_f32 v[10:11], v[10:11], v[190:191] op_sel_hi:[1,0]\nv_permlane16_swap_b32 v104, v106\nv_permlane16_swap_b32 v105, v107\n" \
  "buffer_store_dwordx4 v[104:107], v181, %[ro], %[so0] offen offset:0 sc1\nv_pk_mul_f32 v[12:13], v[12:13], v[194:195] op_sel_hi:[1,0]\n" \
  "v_pk_mul_f32 v[14:15], v[14:15], v[194:195] op_sel_hi:[1,0]\nv_pk_mul_f32 v[96:97], v[8:9], v[8:9]\nv_pk_mul_f32 v[98:99], v[10:11], v[10:11]\n" \
  "v_pk_mul_f32 v[100:101], v[12:13], v[12:13]\nv_pk_mul_f32 v[102:103], v[14:15], v[14:15]\n" \
  "v_pk_fma_f32 v[96:97], v[96:97], v[234:235], v[234:235] op_sel:[0,1,0] op_sel_hi:[1,1,0]\n" \
  "v_pk_fma_f32 v[98:99], v[98:99], v[234:235], v[234:235] op_sel:[0,1,0] op_sel_hi:[1,1,0]\n" \
  "v_pk_fma_f32 v[100:101], v[100:101], v[234:235], v[234:235] op_sel:[0,1,0] op_sel_hi:[1,1,0]\n" \
  "v_pk_fma_f32 v[102:103], v[102:103], v[234:235], v[234:235] op_sel:[0,1,0] op_sel_hi:[1,1,0]\nv_pk_mul_f32 v[96:97], v[96:97], v[8:9]\n" \
  "v_pk_mul_f32 v[98:99], v[98:99], v[10:11]\nv_pk_mul_f32 v[100:101], v[100:101], v[12:13]\nv_pk_mul_f32 v[102:103], v[102:103], v[14:15]\n" \
  "v_exp_f32 v96, v96\nv_exp_f32 v97, v97\nv_exp_f32 v98, v98\nv_exp_f32 v99, v99\nv_exp_f32 v100, v100\nv_exp_f32 v101, v101\nv_exp_f32 v102, v102\n" \
  "v_exp_f32 v103, v103\nv_pk_mul_f32 v[56:57], v[56:57], v[8:9]\nv_pk_mul_f32 v[58:59], v[58:59], v[10:11]\n" \
  "v_pk_mul_f32 v[60:61], v[60:61], v[12:13]\nv_pk_mul_f32 v[62:63], v[62:63], v[14:15]\n" \
  "v_pk_fma_f32 v[96:97], v[96:97], v[190:191], v[190:191] op_sel:[0,1,1]\nv_pk_fma_f32 v[98:99], v[98:99], v[190:191], v[190:191] op_sel:[0,1,1]\n" \
  "v_pk_fma_f32 v[100:101], v[100:101], v[194:195], v[194:195] op_sel:[0,1,1]\n" \
  "v_pk_fma_f32 v[102:103], v[102:103], v[194:195], v[194:195] op_sel:[0,1,1]\nv_rcp_f32 v96, v96\nv_rcp_f32 v97, v97\nv_rcp_f32 v98, v98\n" \
  "v_rcp_f32 v99, v99\nv_rcp_f32 v100, v100\nv_rcp_f32 v101, v101\nv_rcp_f32 v102, v102\nv_rcp_f32 v103, v103\n" \
  "v_pk_mul_f32 v[56:57], v[56:57], v[96:97]\nv_pk_mul_f32 v[58:59], v[58:59], v[98:99]\nv_pk_mul_f32 v[60:61], v[60:61], v[100:101]\n" \
  "v_pk_mul_f32 v[62:63], v[62:63], v[102:103]\nv_cvt_pk_bf16_f32 v108, v56, v57\nv_cvt_pk_bf16_f32 v109, v58, v59\nv_cvt_pk_bf16_f32 v110, v60, v61\n" \
  "v_cvt_pk_bf16_f32 v111, v62, v63\nv_pk_mul_f32 v[16:17], v[16:17], v[182:183] op_sel_hi:[1,0]\n" \
  "v_pk_mul_f32 v[18:19], v[18:19], v[182:183] op_sel_hi:[1,0]\nv_permlane16_swap_b32 v108, v110\nv_permlane16_swap_b32 v109, v111\n" \
  "buffer_store_dwordx4 v[108:111], v181, %[ro], %[so0] offen offset:512 sc1\nv_pk_mul_f32 v[20:21], v[20:21], v[186:187] op_sel_hi:[1,0]\n" \
  "v_pk_mul_f32 v[22:23], v[22:23], v[186:187] op_sel_hi:[1,0]\nv_pk_mul_f32 v[96:97], v[16:17], v[16:17]\nv_pk_mul_f32 v[98:99], v[18:19], v[18:19]\n" \
  "v_pk_mul_f32 v[100:101], v[20:21], v[20:21]\nv_pk_mul_f32 v[102:103], v[22:23], v[22:23]\n" \
  "v_pk_fma_f32 v[96:97], v[96:97], v[234:235], v[234:235] op_sel:[0,1,0] op_sel_hi:[1,1,0]\n" \
  "v_pk_fma_f32 v[98:99], v[98:99], v[234:235], v[234:235] op_sel:[0,1,0] op_sel_hi:[1,1,0]\n" \
  "v_pk_fma_f32 v[100:101], v[100:101], v[234:235], v[234:235] op_sel:[0,1,0] op_sel_hi:[1,1,0]\n" \
  "v_pk_fma_f32 v[102:103], v[102:103], v[234:235], v[234:235] op_sel:[0,1,0] op_sel_hi:[1,1,0]\nv_pk_mul_f32 v[96:97], v[96:97], v[16:17]\n" \
  "v_pk_mul_f32 v[98:99], v[98:99], v[18:19]\nv_pk_mul_f32 v[100:101], v[100:101], v[20:21]\nv_pk_mul_f32 v[102:103], v[102:103], v[22:23]\n" \
  "v_exp_f32 v96, v96\nv_exp_f32 v97, v97\nv_exp_f32 v98, v98\nv_exp_f32 v99, v99\nv_exp_f32 v100, v100\nv_exp_f32 v101, v101\nv_exp_f32 v102, v102\n" \
  "v_exp_f32 v103, v103\nv_pk_mul_f32 v[64:65], v[64:65], v[16:17]\nv_pk_mul_f32 v[66:67], v[66:67], v[18:19]\n" \
  "v_pk_mul_f32 v[68:69], v[68:69], v[20:21]\nv_pk_mul_f32 v[70:71], v[70:71], v[22:23]\n" \
  "v_pk_fma_f32 v[96:97], v[96:97], v[182:183], v[182:183] op_sel:[0,1,1]\nv_pk_fma_f32 v[98:99], v[98:99], v[182:183], v[182:183] op_sel:[0,1,1]\n" \
  "v_pk_fma_f32 v[100:101], v[100:101], v[186:187], v[186:187] op_sel:[0,1,1]\n" \
  "v_pk_fma_f32 v[102:103], v[102:103], v[186:187], v[186:187] op_sel:[0,1,1]\nv_rcp_f32 v96, v96\nv_rcp_f32 v97, v97\nv_rcp_f32 v98, v98\n" \
  "v_rcp_f32 v99, v99\nv_rcp_f32 v100, v100\nv_rcp_f32 v101, v101\nv_rcp_f32 v102, v102\nv_rcp_f32 v103, v103\n" \
  "v_pk_mul_f32 v[64:65], v[64:65], v[96:97]\nv_pk_mul_f32 v[66:67], v[66:67], v[98:99]\nv_pk_mul_f32 v[68:69], v[68:69], v[100:101]\n" \
  "v_pk_mul_f32 v[70:71], v[70:71], v[102:103]\nv_cvt_pk_bf16_f32 v112, v64, v65\nv_cvt_pk_bf16_f32 v113, v66, v67\nv_cvt_pk_bf16_f32 v114, v68, v69\n" \
  "v_cvt_pk_bf16_f32 v115, v70, v71\nv_pk_mul_f32 v[24:25], v[24:25], v[190:191] op_sel_hi:[1,0]\n" \
  "v_pk_mul_f32 v[26:27], v[26:27], v[190:191] op_sel_hi:[1,0]\nv_permlane16_swap_b32 v112, v114\nv_permlane16_swap_b32 v113, v115\n" \
  "buffer_store_dwordx4 v[112:115], v181, %[ro], %[so1] offen offset:0 sc1\nv_pk_mul_f32 v[28:29], v[28:29], v[194:195] op_sel_hi:[1,0]\n" \
  "v_pk_mul_f32 v[30:31], v[30:31], v[194:195] op_sel_hi:[1,0]\nv_pk_mul_f32 v[96:97], v[24:25], v[24:25]\nv_pk_mul_f32 v[98:99], v[26:27], v[26:27]\n" \
  "v_pk_mul_f32 v[100:101], v[28:29], v[28:29]\nv_pk_mul_f32 v[102:103], v[30:31], v[30:31]\n" \
  "v_pk_fma_f32 v[96:97], v[96:97], v[234:235], v[234:235] op_sel:[0,1,0] op_sel_hi:[1,1,0]\n" \
  "v_pk_fma_f32 v[98:99], v[98:99], v[234:235], v[234:235] op_sel:[0,1,0] op_sel_hi:[1,1,0]\n" \
  "v_pk_fma_f32 v[100:101], v[100:101], v[234:235], v[234:235] op_sel:[0,1,0] op_sel_hi:[1,1,0]\n" \
  "v_pk_fma_f32 v[102:103], v[102:103], v[234:235], v[234:235] op_sel:[0,1,0] op_sel_hi:[1,1,0]\nv_pk_mul_f32 v[96:97], v[96:97], v[24:25]\n" \
  "v_pk_mul_f32 v[98:99], v[98:99], v[26:27]\nv_pk_mul_f32 v[100:101], v[100:101], v[28:29]\nv_pk_mul_f32 v[102:103], v[102:103], v[30:31]\n" \
  "v_exp_f32 v96, v96\nv_exp_f32 v97, v97\nv_exp_f32 v98, v98\nv_exp_f32 v99, v99\nv_exp_f32 v100, v100\nv_exp_f32 v101, v101\nv_exp_f32 v102, v102\n" \
  "v_exp_f32 v103, v103\nv_pk_mul_f32 v[72:73], v[72:73], v[24:25]\nv_pk_mul_f32 v[74:75], v[74:75], v[26:27]\n" \
  "v_pk_mul_f32 v[76:77], v[76:77], v[28:29]\nv_pk_mul_f32 v[78:79], v[78:79], v[30:31]\n" \
  "v_pk_fma_f32 v[96:97], v[96:97], v[190:191], v[190:191] op_sel:[0,1,1]\nv_pk_fma_f32 v[98:99], v[98:99], v[190:191], v[190:191] op_sel:[0,1,1]\n" \
  "v_pk_fma_f32 v[100:101], v[100:101], v[194:195], v[194:195] op_sel:[0,1,1]\n" \
  "v_pk_fma_f32 v[102:103], v[102:103], v[194:195], v[194:195] op_sel:[0,1,1]\nv_rcp_f32 v96, v96\nv_rcp_f32 v97, v97\nv_rcp_f32 v98, v98\n" \
  "v_rcp_f32 v99, v99\nv_rcp_f32 v100, v100\nv_rcp_f32 v101, v101\nv_rcp_f32 v102, v102\nv_rcp_f32 v103, v103\n" \
  "v_pk_mul_f32 v[72:73], v[72:73], v[96:97]\nv_pk_mul_f32 v[74:75], v[74:75], v[98:99]\nv_pk_mul_f32 v[76:77], v[76:77], v[100:101]\n" \
  "v_pk_mul_f32 v[78:79], v[78:79], v[102:103]\nv_cvt_pk_bf16_f32 v116, v72, v73\nv_cvt_pk_bf16_f32 v117, v74, v75\nv_cvt_pk_bf16_f32 v118, v76, v77\n" \
  "v_cvt_pk_bf16_f32 v119, v78, v79\nv_pk_mul_f32 v[32:33], v[32:33], v[182:183] op_sel_hi:[1,0]\n" \
  "v_pk_mul_f32 v[34:35], v[34:35], v[182:183] op_sel_hi:[1,0]\nv_permlane16_swap_b32 v116, v118\nv_permlane16_swap_b32 v117, v119\n" \
  "buffer_store_dwordx4 v[116:119], v181, %[ro], %[so1] offen offset:512 sc1\ns_waitcnt vmcnt(2)\ns_mov_b64 exec, 0xf\n" \
  "buffer_store_dword v236, v237, %[rf], %[fo] offen sc1\ns_mov_b64 exec, -1\ns_add_u32 %[fo], %[fo], 512\n" \
  "v_pk_mul_f32 v[36:37], v[36:37], v[186:187] op_sel_hi:[1,0]\nv_pk_mul_f32 v[38:39], v[38:39], v[186:187] op_sel_hi:[1,0]\n" \
  "v_pk_mul_f32 v[96:97], v[32:33], v[32:33]\nv_pk_mul_f32 v[98:99], v[34:35], v[34:35]\nv_pk_mul_f32 v[100:101], v[36:37], v[36:37]\n" \
  "v_pk_mul_f32 v[102:103], v[38:39], v[38:39]\nv_pk_fma_f32 v[96:97], v[96:97], v[234:235], v[234:235] op_sel:[0,1,0] op_sel_hi:[1,1,0]\n" \
  "v_pk_fma_f32 v[98:99], v[98:99], v[234:235], v[234:235] op_sel:[0,1,0] op_sel_hi:[1,1,0]\n" \
  "v_pk_fma_f32 v[100:101], v[100:101], v[234:235], v[234:235] op_sel:[0,1,0] op_sel_hi:[1,1,0]\n" \
  "v_pk_fma_f32 v[102:103], v[102:103], v[234:235], v[234:235] op_sel:[0,1,0] op_sel_hi:[1,1,0]\nv_pk_mul_f32 v[96:97], v[96:97], v[32:33]\n" \
  "v_pk_mul_f32 v[98:99], v[98:99], v[34:35]\nv_pk_mul_f32 v[100:101], v[100:101], v[36:37]\nv_pk_mul_f32 v[102:103], v[102:103], v[38:39]\n" \
  "v_exp_f32 v96, v96\nv_exp_f32 v97, v97\nv_exp_f32 v98, v98\nv_exp_f32 v99, v99\nv_exp_f32 v100, v100\nv_exp_f32 v101, v101\nv_exp_f32 v102, v102\n" \
  "v_exp_f32 v103, v103\nv_pk_mul_f32 v[80:81], v[80:81], v[32:33]\nv_pk_mul_f32 v[82:83], v[82:83], v[34:35]\n" \
  "v_pk_mul_f32 v[84:85], v[84:85], v[36:37]\nv_pk_mul_f32 v[86:87], v[86:87], v[38:39]\n" \
  "v_pk_fma_f32 v[96:97], v[96:97], v[182:183], v[182:183] op_sel:[0,1,1]\nv_pk_fma_f32 v[98:99], v[98:99], v[182:183], v[182:183] op_sel:[0,1,1]\n" \
  "v_pk_fma_f32 v[100:101], v[100:101], v[186:187], v[186:187] op_sel:[0,1,1]\n" \
  "v_pk_fma_f32 v[102:103], v[102:103], v[186:187], v[186:187] op_sel:[0,1,1]\nv_rcp_f32 v96, v96\nv_rcp_f32 v97, v97\nv_rcp_f32 v98, v98\n" \
  "v_rcp_f32 v99, v99\nv_rcp_f32 v100, v100\nv_rcp_f32 v101, v101\nv_rcp_f32 v102, v102\nv_rcp_f32 v103, v103\n" \
  "v_pk_mul_f32 v[80:81], v[80:81], v[96:97]\nv_pk_mul_f32 v[82:83], v[82:83], v[98:99]\nv_pk_mul_f32 v[84:85], v[84:85], v[100:101]\n" \
  "v_pk_mul_f32 v[86:87], v[86:87], v[102:103]\nv_cvt_pk_bf16_f32 v120, v80, v81\nv_cvt_pk_bf16_f32 v121, v82, v83\nv_cvt_pk_bf16_f32 v122, v84, v85\n" \
  "v_cvt_pk_bf16_f32 v123, v86, v87\nv_pk_mul_f32 v[40:41], v[40:41], v[190:191] op_sel_hi:[1,0]\n" \
  "v_pk_mul_f32 v[42:43], v[42:43], v[190:191] op_sel_hi:[1,0]\nv_permlane16_swap_b32 v120, v122\nv_permlane16_swap_b32 v121, v123\n" \
  "buffer_store_dwordx4 v[120:123], v181, %[ro], %[so2] offen offset:0 sc1\nv_pk_mul_f32 v[44:45], v[44:45], v[194:195] op_sel_hi:[1,0]\n" \
  "v_pk_mul_f32 v[46:47], v[46:47], v[194:195] op_sel_hi:[1,0]\nv_pk_mul_f32 v[96:97], v[40:41], v[40:41]\nv_pk_mul_f32 v[98:99], v[42:43], v[42:43]\n" \
  "v_pk_mul_f32 v[100:101], v[44:45], v[44:45]\nv_pk_mul_f32 v[102:103], v[46:47], v[46:47]\n" \
  "v_pk_fma_f32 v[96:97], v[96:97], v[234:235], v[234:235] op_sel:[0,1,0] op_sel_hi:[1,1,0]\n" \
  "v_pk_fma_f32 v[98:99], v[98:99], v[234:235], v[234:235] op_sel:[0,1,0] op_sel_hi:[1,1,0]\n" \
  "v_pk_fma_f32 v[100:101], v[100:101], v[234:235], v[234:235] op_sel:[0,1,0] op_sel_hi:[1,1,0]\n" \
  "v_pk_fma_f32 v[102:103], v[102:103], v[234:235], v[234:235] op_sel:[0,1,0] op_sel_hi:[1,1,0]\nv_pk_mul_f32 v[96:97], v[96:97], v[40:41]\n" \
  "v_pk_mul_f32 v[98:99], v[98:99], v[42:43]\nv_pk_mul_f32 v[100:101], v[100:101], v[44:45]\nv_pk_mul_f32 v[102:103], v[102:103], v[46:47]\n" \
  "v_exp_f32 v96, v96\nv_exp_f32 v97, v97\nv_exp_f32 v98, v98\nv_exp_f32 v99, v99\nv_exp_f32 v100, v100\nv_exp_f32 v101, v101\nv_exp_f32 v102, v102\n" \
  "v_exp_f32 v103, v103\nv_pk_mul_f32 v[88:89], v[88:89], v[40:41]\nv_pk_mul_f32 v[90:91], v[90:91], v[42:43]\n" \
  "v_pk_mul_f32 v[92:93], v[92:93], v[44:45]\nv_pk_mul_f32 v[94:95], v[94:95], v[46:47]\n" \
  "v_pk_fma_f32 v[96:97], v[96:97], v[190:191], v[190:191] op_sel:[0,1,1]\nv_pk_fma_f32 v[98:99], v[98:99], v[190:191], v[190:191] op_sel:[0,1,1]\n" \
  "v_pk_fma_f32 v[100:101], v[100:101], v[194:195], v[194:195] op_sel:[0,1,1]\n" \
  "v_pk_fma_f32 v[102:103], v[102:103], v[194:195], v[194:195] op_sel:[0,1,1]\nv_rcp_f32 v96, v96\nv_rcp_f32 v97, v97\nv_rcp_f32 v98, v98\n" \
  "v_rcp_f32 v99, v99\nv_rcp_f32 v100, v100\nv_rcp_f32 v101, v101\nv_rcp_f32 v102, v102\nv_rcp_f32 v103, v103\n" \
  "v_pk_mul_f32 v[88:89], v[88:89], v[96:97]\nv_pk_mul_f32 v[90:91], v[90:91], v[98:99]\nv_pk_mul_f32 v[92:93], v[92:93], v[100:101]\n" \
  "v_pk_mul_f32 v[94:95], v[94:95], v[102:103]\nv_cvt_pk_bf16_f32 v124, v88, v89\nv_cvt_pk_bf16_f32 v125, v90, v91\nv_cvt_pk_bf16_f32 v126, v92, v93\n" \
  "v_cvt_pk_bf16_f32 v127, v94, v95\ns_nop 1\nv_permlane16_swap_b32 v124, v126\nv_permlane16_swap_b32 v125, v127\n" \
  "buffer_store_dwordx4 v[124:127], v181, %[ro], %[so2] offen offset:512 sc1\ns_waitcnt vmcnt(3)\ns_mov_b64 exec, 0xf\n" \
  "buffer_store_dword v236, v237, %[rf], %[fo] offen sc1\ns_mov_b64 exec, -1\ns_add_u32 %[fo], %[fo], 512\n"
#define K3_CLOBBER "memory", "scc", "vcc", V10(), V10(1), V10(2), V10(3), V10(4), V10(5), V10(6), V10(7), V10(8), V10(9), V10(10), V10(11), V10(12), V10(13), \
  V10(14), "v150", "v151", "v172", "v173", "v174", "v175", "v176", "v177", "v178", "v179", "v180", "v181", "v182", "v183"

extern "C" __global__ __launch_bounds__(512, 2) void k_all(char* fl, const u16* __restrict__ x, u16* out, const u16* wo, const u16* wg, const u16* wu,
                                                           const u16* wd, const u16* __restrict__ wqkv) {
  char* __restrict__ ws = wsg;
  __shared__ __attribute__((aligned(1024))) char smem[REC_HEAD];
  asm volatile("" ::"v"((LDS3 char*)smem));
  const int b = blockIdx.x;
  unsigned wraw = __builtin_amdgcn_readfirstlane(threadIdx.x >> 6);
  const unsigned e1 = k1_qkv(x, wqkv, fl, wraw) + 1;
  const i32x4 rf = flag_srd(fl);
  const unsigned long long ep = (unsigned long long)e1 << 32 | e1;
  unsigned tid0 = wraw * 64 + lane_id();
  unsigned pa;
  {
    const unsigned l = tid0 & 63, hd = (b & 7) * 4 + ((b >> 3) & 3), g = l >> 3, id = (b & 1) * 30 + (b >> 3), c1 = id >> 2, k = c1 / 5, j = c1 - 5 * k;
    const unsigned h = j * 32 / 5 + (l >> 1);
    pa = wraw == 0 ? F_K1 + hd * 768 + (g > 5 ? 5 : g) * 128 + (g < 2 ? b >> 5 : l & 7) * 16
       : b < 240 && l < 16 && ((h * 20 + (l & 1) * 19) >> 7) == j ? F_K1 + h * 768 + (k * 2 + (l & 1)) * 128 + (((b & 7) >> 1) * 4 + (id & 3)) * 8 : 0x80000000u;
  }
  unsigned long long bad, bad1;

  asm volatile(K2_ASM
               : [tid] "+{v0}"(tid0), [bad] "=&s"(bad), [bad1] "=&s"(bad1), [w] "=&s"(wraw)
               : [b] "s"(b), [rf] "s"(rf), [ep] "s"(ep), [pa] "v"(pa), [ka] "s"(__builtin_amdgcn_kernarg_segment_ptr())
               : "memory", "scc", "vcc", "v1", "v2", "v3", "v4", "v5", "v6", "v7", "v8", "v9", V10(1), V10(2), V10(3), V10(4), V10(5), V10(6), V10(7), V10(8), V10(9),
                 V10(10), V10(11), V10(12), V10(13), V10(14), V10(15), V10(16), V10(17), V10(18), S10(4), S10(5), S10(6), S10(7), S10(8));
  const int xcd = b & 7, bi = b >> 3, wave = wraw - 4;
  const int rt = xcd * 4 + (bi & 3), ct = bi >> 2, m0 = rt * 64, n0 = ct * 80;
  const int rt2 = (xcd >> 1) * 4 + (bi & 3), ct2 = (xcd & 1) * 8 + (bi >> 2), m2 = rt2 * 128;
  if (wraw >= 4) {
    if (bad) k2_redo(rt, ct * 8 + wave * 2);
    if (bad1) k2_redo(rt, ct * 8 + wave * 2 + 1);

    asm volatile(PUB2_ASM ::[rf] "s"(rf), [e] "s"(e1), [fo] "s"((4096 + (ct * 4 + wave) * 32 + rt) * FS) : "memory", "v2", "v3");

    const int wm = wave & 1, wk = wave >> 1;
    u32x4 h0, h1, h2;
    u32x2 fv;
    unsigned fo = (1024 + (rt2 * 2 + (wave >> 1)) * 96 + (ct2 & 3) * 8 + (ct2 >> 2) * 2 + (wave & 1)) * FS;
    {
      const int lane = lane_id(), g = lane >> 4, c = lane & 15, lr = wm * 32 + wk * 16 + c;
      const unsigned bb = 122880 + (wk * 64 + 8 * g + ((lane >> 2) & 3)) * 160 + (lane & 3) * 8, a0 = (wk * 8 + g) * 1024 + lr * 16;
      u32x4 ad = {a0, a0 ^ 256, bb + (g & 1) * 640, bb + ((g & 1) ^ 1) * 640};
      const u32x2 xa = {16384u + lane * 16 + wave * 5120, 16384u + lane * 16 + (wave ^ 2) * 5120};
      unsigned cnt = 1, db = -20480;
      const unsigned one = (c & 1) ? 0x3f800000u : 0x3f80u, e = ((c >> 3) - g) * 4 + ((c >> 1) & 3);
      const u32x4 s0 = {e == 0 ? one : 0u, e == 1 ? one : 0u, e == 2 ? one : 0u, e == 3 ? one : 0u};
      const u32x4 s1 = {e == -8u ? one : 0u, e == -7u ? one : 0u, e == -6u ? one : 0u, e == -5u ? one : 0u};
      const unsigned so = ct * 10 * 32768 + m0 * 16;
      asm volatile(K3_ASM
                   : "+{v[152:155]}"(ad), [cnt] "+s"(cnt), [db] "+s"(db), "={v[240:243]}"(h0), "={v[244:247]}"(h1), "={v[248:251]}"(h2)
                   : "{v[166:167]}"(xa), "{v168}"(((g & 1) * 2 + (g >> 1)) * 32768 + lr * 16), "{v169}"(lr * 1280 + g * 16), "{v170}"(lr * 8),
                     "{v[156:159]}"(s0), "{v[160:163]}"(s1),
                     [rr] "s"(mk_srd(x, S_ * D_ * 2)), [sr] "s"(m0 * 1280 + n0 * 2), [ro] "s"(mk_srd(ws + WS_H1, S_ * D_ * 2)), [so0] "s"(so),
                     [so1] "s"(so + 131072), [so2] "s"(so + 262144), [me] "s"(0x0000ffff0000ffffull), [rs] "s"(mk_srd(ws + WS_ST, S_ * 64)),
                     [ss] "s"(m0 * 8 + ct * 16384), [rf] "s"(rf), [e] "s"(e1), [fo] "s"(((rt >> 1) * 64 + ((rt & 1) * 8 + ct) * 4 + wave) * FS)
                   : K3_CLOBBER);


    }

    {
      const int lane = lane_id(), g = lane >> 4, c = lane & 15, wm = wave >> 1, wn = wave & 1;
      unsigned aA = g * 2048 + (wm * 64 + c) * 16;
      const unsigned bb = 65536 + (8 * g + ((lane >> 2) & 3)) * 192 + (lane & 3) * 8;
      u32x2 aB = {bb + ((wn * 3) ^ (g & 1)) * 32, bb + ((wn * 3 + 1) ^ (g & 1)) * 32};
      const unsigned so = (ct2 * 4 + wn * 2) * 32768 + (m2 + wm * 64) * 16;
#define CTL 30
      unsigned cnt = 3, db = 49152, pw = wave, to = (bi >> 2) * 245760 + ((bi & 3) * 4 + wave) * (CTL * 128);
      const unsigned vc = 114688 + 2048 + wn * 192 + (c >> 2) * 64 + (c >= 12) * 192 + g * 16 + (c & 3) * 4;
      asm volatile(K4_ASM
                   : "+{v178}"(aA), "+{v[176:177]}"(aB), [cnt] "+s"(cnt), [db] "+s"(db), [pw] "+s"(pw), [fo] "+s"(fo), "={v[236:237]}"(fv), [to] "+s"(to),
                     "+{v[240:243]}"(h0), "+{v[244:247]}"(h1), "+{v[248:251]}"(h2)
                   : "{v179}"(114688 + (wm * 64 + c) * 16), "{v200}"(vc), "{v181}"((g >> 1) * 32768 + (lane & 31) * 16),
                     [ro] "s"(mk_srd(ws + WS_HH, S_ * F_ * 2)), [so0] "s"(so), [so1] "s"(so + 64 * 32768), [so2] "s"(so + 128 * 32768),
                     [rf] "s"(rf), [vf] "v"(lane * FS), [fi] "s"(rt2 * 64 * FS), [e] "s"(e1), [ep] "s"(ep),
                     "{v205}"(lane < CTL ? lane * 128 : 0x80000000u), [sd] "s"(mk_srd(ws + WS_PK + PK_D, PK_SIZE - PK_D))
                   : "memory", "scc", "vcc", V10(), V10(1), V10(2), V10(3), V10(4), V10(5), V10(6), V10(7), V10(8), V10(9), V10(10), V10(11), V10(12), V10(13),
                     V10(14), V10(15), V10(16), "v170", "v171", "v172", "v173", "v174", "v175", "v182", "v183", "v184", "v185", "v186", "v187", "v188",
                     "v189", V10(19), "v204", "v234", "v235");


    }

    {
      const int lane = lane_id(), g = lane >> 4, c = lane & 15, lr = wm * 32 + wk * 16 + c;
      const unsigned bb = 122880 + (wk * 64 + 8 * g + ((lane >> 2) & 3)) * 160 + (lane & 3) * 8, a0 = 32768 + (wk * 8 + g) * 1024 + lr * 16;
      u32x4 ad = {a0, a0 ^ 256, bb + (g & 1) * 640, bb + ((g & 1) ^ 1) * 640};
      const u32x2 xa = {32768u + lane * 16 + wave * 5120, 32768u + lane * 16 + (wave ^ 2) * 5120};
      unsigned cnt = 4, db = -57344;
      asm volatile(K5_ASM
                   : "+{v[152:155]}"(ad), [cnt] "+s"(cnt), [db] "+s"(db), [fo] "+s"(fo)
                   : "{v[166:167]}"(xa), "{v168}"(lr * 1280 + (g & 1) * 32 + (g >> 1) * 16), "{v[240:243]}"(h0), "{v[244:247]}"(h1), "{v[248:251]}"(h2),
                     [ro] "s"(mk_srd(out, S_ * D_ * 2)), [so0] "s"(m0 * 1280 + n0 * 2), [me] "s"(0x0000ffff0000ffffull),
                     [rf] "s"(rf), "{v[236:237]}"(fv)
                   : K3_CLOBBER);


    }
    return;
  }
  const int pw = wraw, lane = lane_id(), tid = pw * 64 + lane;
  const unsigned vL = lane * 16;
  {
    const int ks = ct >= 5 ? ct - 5 : ct;
    const i32x4 so = mk_srd(ws + WS_O, S_ * D_ * 2), sb = mk_srd(ws + WS_PK + PK_O, D_ * D_ * 2);
    auto kk = [&](int s) { return ks + s >= 5 ? ks + s - 5 : ks + s; };
    auto A3 = [&](int s) { for (int i = 0; i < 4; ++i) dma16s((s & 3) * 16384 + (pw * 4 + i) * 1024, vL, so, (kk(s) * 16 + pw * 4 + i) * 32768 + m0 * 16); };
    auto B3 = [&](int s) { for (int i = 0; i < 5; ++i) dma16s(102400 + (s + 1) % 3 * 20480 + (pw * 5 + i) * 1024, vL, sb, (ct * 5 + kk(s)) * 20480 + (pw * 5 + i) * 1024); };
    B3(0); B3(1); B3(2);
#define TW 0
    unsigned w0 = 0, w1 = 0, w2 = 0;
    if (TW && bi * 4 + pw < (TW == 1 ? 80 : 114)) {
      const int u = (bi * 4 + pw) * 3, o = (u + (u < 240 ? (b & 1) * 240 : 240)) * 8192;
      asm volatile("buffer_load_dword %0, %3, %4, %5 offen\n\tbuffer_load_dword %1, %3, %4, %6 offen\n\tbuffer_load_dword %2, %3, %4, %7 offen"
                   : "=&v"(w0), "=&v"(w1), "=&v"(w2) : "v"(lane * 128), "s"(mk_srd(ws + WS_PK, PK_D)), "s"(o), "s"(o + 8192), "s"(o + 16384));
    }
    asm volatile("" ::"s"(so), "s"(kk(0) * 16 * 32768 + pw * 4 * 32768 + m0 * 16), "s"(kk(1) * 16 * 32768 + pw * 4 * 32768 + m0 * 16));
    BARRIER();

    unsigned long long pv = 0, pw2 = 0;
    if (pw == 0) {
      unsigned t;
      asm volatile(POLL3_ASM : [t] "=&s"(t), [pv] "=&v"(pv), [pw] "=&v"(pw2) : [rf] "s"(rf), [ep] "s"(ep), [vf] "v"((lane & 31) * FS), [fi] "s"((4096 + rt * 32) * FS) : "memory", "scc", "vcc");
    }

    BARRIER();

    A3(0); A3(1); A3(2); A3(3);
    WAIT_V(12); BARRIER();
    asm volatile("" ::"v"(w0), "v"(w1), "v"(w2), "v"(pv), "v"(pw2));
    WAIT_V(8); BARRIER();
    A3(4); B3(3);
    WAIT_V(13); BARRIER();
    B3(4);
  }
  const i32x4 sa = mk_srd(ws + WS_H1, S_ * D_ * 2), sw = mk_srd(ws + WS_PK + PK_G, PK_O);
  auto getA = [&](int s, int i) { const int p = pw * 4 + i; dma16s((s & 3) * 16384 + p * 1024, vL, sa, (s * 8 + (p >> 1)) * 32768 + (m2 + (p & 1) * 64) * 16); };
  auto getB = [&](int s, int i) { const int q = pw * 6 + i; dma16s(65536 + (s & 3) * 24576 + q * 1024, vL, sw, (ct2 * 10 + s) * 24576 + q * 1024); };
  auto allA = [&](int s) { getA(s, 0); getA(s, 1); getA(s, 2); getA(s, 3); };
  auto allB = [&](int s) { getB(s, 0); getB(s, 1); getB(s, 2); getB(s, 3); getB(s, 4); getB(s, 5); };
  auto all = [&](int s) { getA(s, 0); getB(s, 0); getA(s, 1); getB(s, 1); getA(s, 2); getB(s, 2); getA(s, 3); getB(s, 3); getB(s, 4); getB(s, 5); };
#define NB4 2
#define LA4 3
#define PB0 1
#define PB1 2
  float cs;
  {
    float c[10];
    const i32x4 sc = mk_srd(ws + WS_CS + ct2 * 8192, 7680);
    WAIT_V(5); BARRIER();
#pragma unroll
    for (int k = 0; k < 10; ++k)
      asm volatile("buffer_load_dword %0, %1, %2, %3 offen offset:%4 sc1" : "=v"(c[k]) : "v"(tid * 4), "s"(sc), "s"((k / 5) * 3840), "i"((k % 5) * 768));
    if (PB0 == 0) allB(0);
    WAIT_V(PB0 == 0 ? 16 : 10); BARRIER();
    if (PB0 == 1) allB(0);
    if (NB4 > 1 && PB1 == 1) allB(1);
    BARRIER();
    if (NB4 > 1 && PB1 == 2) allB(1);
    if (NB4 > 2) allB(2);
    if (NB4 > 3) allB(3);
    asm volatile("s_waitcnt vmcnt(%10)" : "+v"(c[0]), "+v"(c[1]), "+v"(c[2]), "+v"(c[3]), "+v"(c[4]), "+v"(c[5]), "+v"(c[6]), "+v"(c[7]), "+v"(c[8]), "+v"(c[9]) : "i"(6 * NB4));
    cs = ((c[0] + c[1]) + (c[2] + c[3])) + ((c[4] + c[5]) + (c[6] + c[7])) + (c[8] + c[9]);
    asm volatile("" : "+v"(cs));
  }
  auto nst = [](int s) { return s > 9 ? 0 : s < NB4 ? 4 : 10; };

  BARRIER();

  allA(0);
  if (NB4 > 1) allA(1); else all(1);
  if (NB4 > 2) allA(2);

  WAIT_V(nst(1) + (NB4 > 2 ? 4 : 0)); BARRIER();
  if (NB4 <= 2) all(2);
#define TN 2
#define T0 7
#define TL 30
#define TOFF ((bi >> 2) * 245760 + (((s - T0) * 4 + (bi & 3)) * 4 + pw) * (TL * 128))
  f32x2 t0, t1, t2, t3;
  unsigned p0 = 0;
  auto tch = [](int s) { return s >= T0 && s < T0 + TN ? 1 : 0; };
  auto newer = [&](int s) {
    int n = 0;
    for (int q = s + 2; q < s + LA4; ++q) n += nst(q);
    for (int q = s + 1 - LA4; q < s; ++q) n += tch(q) + (q == 6 && q > s + 1 - LA4 ? 4 : 0);
    return n;
  };
  if (LA4 > 3) { if (NB4 > 3) allA(3); else all(3); }
  const unsigned vT = lane < TL ? lane * 128 : 0x80000000u;
  const i32x4 sd = mk_srd(ws + WS_PK + PK_D, PK_SIZE - PK_D);
#pragma unroll
  for (int s = 0; s < 9; ++s) {

    if (s == 8) WAIT_VL(newer(s)); else WAIT_V(newer(s));

    BARRIER();

    if (s == 6)
      asm volatile("buffer_load_dwordx2 %0, %4, %5, %6 offen sc1\n\tbuffer_load_dwordx2 %1, %4, %5, %7 offen sc1\n\t"
                   "buffer_load_dwordx2 %2, %4, %5, %8 offen sc1\n\tbuffer_load_dwordx2 %3, %4, %5, %9 offen sc1"
                   : "=&v"(t0), "=&v"(t1), "=&v"(t2), "=&v"(t3) : "v"((lane & 31) * 8 + (lane >> 5) * 65536), "s"(mk_srd(ws + WS_ST, S_ * 64)), "s"((m2 + pw * 32) * 8),
                     "s"((m2 + pw * 32) * 8 + 16384), "s"((m2 + pw * 32) * 8 + 32768), "s"((m2 + pw * 32) * 8 + 49152));
    if (s + LA4 < NB4) allA(s + LA4); else if (s + LA4 < 10) all(s + LA4);
    if (tch(s))
      asm volatile("buffer_load_dword %0, %1, %2, %3 offen" : "+v"(p0) : "v"(vT), "s"(sd), "s"(TOFF));
    if (s == 7) {
      asm volatile("s_waitcnt vmcnt(%4)" : "+v"(t0), "+v"(t1), "+v"(t2), "+v"(t3) : "i"(nst(6 + LA4) + tch(6) + tch(7)) : "memory");

      float a0 = t0[0], a1 = t0[1], a2 = t1[0], a3 = t1[1];
      asm volatile("v_add_f32 %0, %0, %2\n\tv_add_f32 %1, %1, %3\n\tv_add_f32 %2, %4, %6\n\tv_add_f32 %3, %5, %7\n\t"
                   "v_add_f32 %0, %0, %2\n\tv_add_f32 %1, %1, %3\n\ts_nop 1\n\tv_permlane32_swap_b32 %0, %1\n\tv_add_f32 %0, %0, %1\n\t"
                   "v_mov_b32 %1, %0\n\ts_nop 1\n\tv_permlane32_swap_b32 %0, %1\n\tv_mul_f32 %2, 0xbacccccd, %0\n\tv_mul_f32_e64 %3, -%2, %2\n\t"
                   "v_fmamk_f32 %1, %1, 0x3acccccd, %3\n\tv_max_f32 %1, 0, %1\n\tv_add_f32 %1, 0x3727c5ac, %1\n\tv_rsq_f32 %0, %1\n\ts_nop 0\n\t"
                   "v_mul_f32 %1, %1, %0"
                   : "+v"(a0), "+v"(a1), "+v"(a2), "+v"(a3) : "v"(t2[0]), "v"(t2[1]), "v"(t3[0]), "v"(t3[1]));

      ldsw<f32x4>(114688 + (pw * 32 + lane % 32) * 16, f32x4{a0, a1, a2, a3});
      ldsw<float>(114688 + 2048 + tid * 4, cs);
      asm volatile("" ::: "memory");
    }
  }

  unsigned ga = pw * 4 * 32768 + m0 * 16, gb = ct * 12 * 20480 + pw * 5120, fi = (1024 + rt * 96 + pw * 8) * FS, t, tv;
  unsigned to = (bi >> 2) * 245760 + ((TN * 4 + (bi & 3)) * 4 + pw) * (TL * 128);
  unsigned long long pv;
  asm volatile(P5_ASM : [ga] "+s"(ga), [gb] "+s"(gb), [fi] "+s"(fi), [t] "=&s"(t), [pv] "=&v"(pv), [tv] "=&v"(tv), [to] "+s"(to)
               : [la] "s"(pw * 4096), [lb] "s"(pw * 5120), [a0] "v"(vL), [a1] "v"(vL + 31744), [a2] "v"(vL + 2 * 31744), [a3] "v"(vL + 3 * 31744), [b4] "v"(vL + 4096),
                 [sa] "s"(mk_srd(ws + WS_HH, S_ * F_ * 2)), [sw] "s"(sd), [rf] "s"(rf), [vf] "v"((lane & 7) * FS), [vw] "v"(vT), [ep] "s"(ep), "v"(p0),
                 [vt] "v"(lane < 16 ? (lane >> 2) * 0x80000 + (lane & 3) * 32768 + ct * 128 : 0x80000000u) : "memory", "scc", "vcc");

  if (b == 0 && tid == 0) asm volatile("buffer_store_dword %0, off, %1, %2 sc1" ::"v"(e1), "s"(rf), "s"(F_EP));

}

extern "C" __global__ void k_end() {}

extern "C" void kernel_launch(void* const* d_in, const int* in_sizes, int n_in, void* d_out, int out_size, void* d_ws,
                              size_t ws_size, hipStream_t stream) {
  const u16* x = (const u16*)d_in[0];
  const u16* wqkv = (const u16*)d_in[1];
  const u16* wo = (const u16*)d_in[2];
  const u16* wg = (const u16*)d_in[4];
  const u16* wu = (const u16*)d_in[5];
  const u16* wd = (const u16*)d_in[6];
  k_all<<<256, 512, 0, stream>>>((char*)d_ws, x, (u16*)d_out, wo, wg, wu, wd, wqkv);
}
```
